```python
import torch

def _softmax(x, axis=-1):
    return torch.softmax(x, dim=axis)

def _gelu(x):
    return torch.nn.functional.gelu(x, approximate='tanh')

def _silu(x):
    return torch.nn.functional.silu(x)

def _sigmoid(x):
    return torch.sigmoid(x)

def _layernorm(x, axis=-1, eps=1e-05):
    m = x.mean(dim=axis, keepdim=True)
    v = x.var(dim=axis, keepdim=True, unbiased=False)
    return (x - m) / torch.sqrt(v + eps)

def _scatter_add(tgt, idx, src, axis=0):
    return tgt.clone().index_add_(axis, idx, src)

def _segment_sum(src, seg, n):
    out = torch.zeros((n,) + src.shape[1:], dtype=src.dtype)
    return out.index_add_(0, seg, src)

def _pad(x, pw):
    flat = tuple((v for pair in reversed(pw) for v in pair))
    return torch.nn.functional.pad(x, flat)
kernel_name = 'interleave_layers__sliding_local_dilated__memory_aug_recurrent_mem_tr__prompt'

def setup_inputs():

    _w = lambda a: (lambda t: t.to(torch.bfloat16) if t.is_floating_point() else t)(torch.as_tensor(a))

    def _A_setup():
        B, S, D = (1, 2048, 576)
        return {'x': _w((lambda *s: torch.randn(*s, dtype=torch.bfloat16) * 0.05)(B, S, D)), 'wqkv': _w((lambda *s: torch.randn(*s, dtype=torch.bfloat16) * 0.05)(D, 3 * D)), 'wo': _w((lambda *s: torch.randn(*s, dtype=torch.bfloat16) * 0.05)(D, D))}
    _A_in = _A_setup()

    def _B_setup():
        B, S, Dm = (1, 2048, 576)
        H, Dh = (32, 128)
        M, seg = (8, 32)
        seg = min(seg, S)
        S = S // seg * seg
        return {'x': _w((lambda *s: torch.randn(*s, dtype=torch.bfloat16) * 0.05)(B, S, Dm)), 'mem0': _w((lambda *s: torch.randn(*s, dtype=torch.bfloat16) * 0.8)(B, M, Dm)), 'Wq': _w((lambda *s: torch.randn(*s, dtype=torch.bfloat16) * 1.6)(Dm, H, Dh) * Dm ** (-0.5)), 'Wk': _w((lambda *s: torch.randn(*s, dtype=torch.bfloat16) * 1.6)(Dm, H, Dh) * Dm ** (-0.5)), 'Wv': _w((lambda *s: torch.randn(*s, dtype=torch.bfloat16) * 0.8)(Dm, H, Dh) * Dm ** (-0.5)), 'Wo': _w((lambda *s: torch.randn(*s, dtype=torch.bfloat16) * 0.8)(H, Dh, Dm) * (H * Dh) ** (-0.5)), 'seg_len': seg}
    _B_in = _B_setup()
    return {'A_x': _A_in['x'], 'A_wqkv': _A_in['wqkv'], 'A_wo': _A_in['wo'], 'B_x': _B_in['x'], 'B_mem0': _B_in['mem0'], 'B_Wq': _B_in['Wq'], 'B_Wk': _B_in['Wk'], 'B_Wv': _B_in['Wv'], 'B_Wo': _B_in['Wo'], 'B_seg_len': _B_in['seg_len']}

def reference(A_x, A_wqkv, A_wo, B_x, B_mem0, B_Wq, B_Wk, B_Wv, B_Wo, B_seg_len):

    def _A_ref(x, wqkv, wo):
        B, S, D = x.shape
        H = 32
        Dh = D // H
        W, dil = (8, 2)
        qkv = (lambda x, *d: x.permute(*d))((x @ wqkv).reshape(B, S, 3, H, Dh), 2, 0, 3, 1, 4)
        q, k, v = (qkv[0], qkv[1], qkv[2])
        scale = Dh ** (-0.5)
        pos = torch.arange(S)
        off = torch.arange(W) * dil
        idx = pos[:, None] - off[None, :]
        mask = idx >= 0
        idx_c = torch.where(mask, idx, 0).reshape(-1)
        kg = k[:, :, idx_c, :].reshape(B, H, S, W, Dh)
        vg = v[:, :, idx_c, :].reshape(B, H, S, W, Dh)
        logits = torch.einsum('bhsd,bhswd->bhsw', q, kg) * scale
        logits = torch.where(mask[None, None, :, :], logits, -1000000000.0)
        attn = _softmax(logits, axis=-1)
        o = torch.einsum('bhsw,bhswd->bhsd', attn, vg)
        return (lambda x, *d: x.permute(*d))(o, 0, 2, 1, 3).reshape(B, S, D) @ wo

    def _B_ref(x, mem0, Wq, Wk, Wv, Wo, seg_len):
        B, S, Dm = x.shape
        H, Dh = (Wq.shape[1], Wq.shape[2])
        M = mem0.shape[1]
        scale = Dh ** (-0.5)
        n_seg = S // seg_len
        L = M + seg_len
        idx = torch.arange(L)
        mask = (idx[:, None] < M) | (idx[:, None] >= idx[None, :])
        mem = mem0
        outs = []
        for s in range(n_seg):
            xs = x[:, s * seg_len:(s + 1) * seg_len, :].clone()
            h = torch.concatenate([mem, xs], axis=1)
            q = torch.einsum('btd,dhe->bhte', h, Wq)
            k = torch.einsum('btd,dhe->bhte', h, Wk)
            v = torch.einsum('btd,dhe->bhte', h, Wv)
            scores = torch.einsum('bhqe,bhke->bhqk', q, k) * scale
            scores = torch.where(mask[None, None], scores, -1000000000.0)
            p = _softmax(scores, axis=-1)
            o = torch.einsum('bhqk,bhke->bhqe', p, v)
            o = torch.einsum('bhte,hed->btd', o, Wo)
            mem = o[:, :M, :].clone()
            outs.append(o[:, M:, :])
        return torch.concatenate(outs, axis=1)
    x = A_x
    x = x + _A_ref(_layernorm(x, axis=-1), A_wqkv, A_wo)
    x = x + _B_ref(_layernorm(x, axis=-1), B_mem0, B_Wq, B_Wk, B_Wv, B_Wo, B_seg_len)
    x = x + _A_ref(_layernorm(x, axis=-1), A_wqkv, A_wo)
    x = x + _B_ref(_layernorm(x, axis=-1), B_mem0, B_Wq, B_Wk, B_Wv, B_Wo, B_seg_len)
    return x
```

```cpp
#include <hip/hip_runtime.h>
#include <hip/hip_bf16.h>
#include <stdint.h>
#include <type_traits>

typedef unsigned short u16;
typedef unsigned u32;
typedef unsigned long long u64;
using bf16x8 = __attribute__((ext_vector_type(8))) short;
using bf16x4 = __attribute__((ext_vector_type(4))) short;
using f32x4 = __attribute__((ext_vector_type(4))) float;
using f32x2 = __attribute__((ext_vector_type(2))) float;
using v4u = __attribute__((ext_vector_type(4))) unsigned;
using v2u = __attribute__((ext_vector_type(2))) unsigned;
typedef __bf16 bfv2 __attribute__((ext_vector_type(2)));
#define GAS __attribute__((address_space(1)))
typedef GAS unsigned gu32;
#define RLX_AGENT __ATOMIC_RELAXED, __HIP_MEMORY_SCOPE_AGENT
typedef __amdgpu_buffer_rsrc_t rsrc_t;

constexpr int S_ = 2048, D_ = 576, NSEG = 64, SEG = 32, NB = 4096, NQA = 1728;

constexpr u32 XB = S_ * D_ * 2;
constexpr u32 OFF_ABORT = 0u;
constexpr u32 OFF_PAR = 64u;
constexpr u32 OFF_CC = 128u;
constexpr u32 OFF_Z = 256u;
constexpr u32 OFF_Z2 = 384u;
constexpr u32 OFF_CH = 512u;
constexpr u32 OFF_CXNA = 1024u;
constexpr u32 OFF_FKVM = 12288u;
constexpr u32 OFF_SCT = 151552u;
constexpr u32 OFF_STAT = 290816u;
constexpr u32 SYNC_BYTES = 880640u;
constexpr u32 CX = 4u;
constexpr u32 OFF_PROG = 884736u;
constexpr u32 OFF_ACK = 1015808u;
constexpr u32 OFF_ACC = 1048576u;
constexpr u32 OFF_XNA = 11862016u;
constexpr u32 OFF_XNB = 16580608u;
constexpr u32 OFF_X = 21299200u;
constexpr u32 OFF_OA = 28377088u;
constexpr u32 OFF_PB = 33095680u;
constexpr u32 OFF_OB = 70844416u;
constexpr u32 OFF_QKV = 104398848u;
constexpr u32 OFF_KVM = 208207872u;
constexpr u32 OFF_DBG = 224985088u;
constexpr u32 OFF_JUNK = 225574912u;
constexpr u32 WS_BYTES = 230326272u;
constexpr u32 ACC_SLOT = 163840;
__device__ __forceinline__ void role_chain(char* ws, const u16* Wq, const u16* Wk, const u16* Wv, const u16* Wo, u32 id, u32 ack) {
    asm volatile(
        "s_mov_b64 s[36:37], %0\n"
        "s_mov_b32 s38, 0xdba8000\n"
        "s_mov_b32 s39, 0x00020000\n"
        "s_mov_b32 s42, 0x480000\n"
        "s_mov_b32 s43, 0x00020000\n"
        "s_and_b32 s45, %5, 3\n"
        "s_bfe_u32 s46, %5, 0x10002\n"
        "s_bfe_u32 s47, %5, 0x50003\n"
        "s_bfe_u32 s48, %5, 0x10008\n"
        "s_mov_b32 s63, 0\n"
        "v_mbcnt_lo_u32_b32 v128, -1, 0\n"
        "v_mbcnt_hi_u32_b32 v128, -1, v128\n"
        "v_and_b32 v129, 15, v128\n"
        "v_lshrrev_b32 v130, 4, v128\n"
        "v_lshlrev_b32 v131, 1, v130\n"
        "v_xor_b32 v131, v131, v129\n"
        "v_lshlrev_b32 v131, 4, v131\n"
        "v_lshl_add_u32 v134, v130, 13, v131\n"
        "v_xor_b32 v135, 0x80, v134\n"
        "v_and_b32 v131, 0xc0, v131\n"
        "v_lshl_or_b32 v140, v131, 14, v134\n"
        "v_xor_b32 v141, 0x200080, v140\n"
        "v_lshrrev_b32 v131, 2, v129\n"
        "v_lshl_add_u32 v132, v130, 3, v131\n"
        "v_lshlrev_b32 v136, 8, v132\n"
        "v_and_b32 v132, 1, v130\n"
        "v_lshl_or_b32 v131, v132, 2, v131\n"
        "v_lshlrev_b32 v131, 1, v131\n"
        "v_bfe_u32 v132, v129, 1, 1\n"
        "s_lshl_b32 s80, s45, 2\n"
        "v_or_b32 v132, s80, v132\n"
        "v_xor_b32 v131, v131, v132\n"
        "v_lshl_add_u32 v136, v131, 4, v136\n"
        "v_and_b32 v132, 1, v129\n"
        "v_lshl_add_u32 v136, v132, 3, v136\n"
        "v_xor_b32 v137, 32, v136\n"
        "s_lshl_b32 s49, s45, 17\n"
        "s_lshl_b32 s80, s47, 8\n"
        "s_add_u32 s49, s49, s80\n"
        "s_lshl_b32 s50, s45, 12\n"
        "s_bfe_u32 s80, s47, 0x10004\n"
        "s_xor_b32 s83, s80, 1\n"
        "s_lshl_b32 s67, s83, 19\n"
        "s_lshl_b32 s68, s80, 19\n"
        "s_add_u32 s67, s67, s49\n"
        "s_add_u32 s68, s68, s49\n"
        "s_mov_b64 s[40:41], %3\n"
        "v_xor_b32 v142, 0x0, v140\n"
        "v_xor_b32 v143, 0x0, v141\n"
        "s_add_u32 m0, s50, 0\n"
        "s_add_u32 s80, s67, 0\n"
        "buffer_load_dwordx4 v142, s[40:43], s80 offen lds\n"
        "s_add_u32 m0, s50, 1024\n"
        "s_add_u32 s80, s67, 32768\n"
        "buffer_load_dwordx4 v142, s[40:43], s80 offen lds\n"
        "s_add_u32 m0, s50, 2048\n"
        "s_add_u32 s80, s67, 65536\n"
        "buffer_load_dwordx4 v143, s[40:43], s80 offen lds\n"
        "s_add_u32 m0, s50, 3072\n"
        "s_add_u32 s80, s67, 98304\n"
        "buffer_load_dwordx4 v143, s[40:43], s80 offen lds\n"
        "v_xor_b32 v142, 0x0, v140\n"
        "v_xor_b32 v143, 0x0, v141\n"
        "s_add_u32 m0, s50, 16384\n"
        "s_add_u32 s80, s68, 0\n"
        "buffer_load_dwordx4 v142, s[40:43], s80 offen lds\n"
        "s_add_u32 m0, s50, 17408\n"
        "s_add_u32 s80, s68, 32768\n"
        "buffer_load_dwordx4 v142, s[40:43], s80 offen lds\n"
        "s_add_u32 m0, s50, 18432\n"
        "s_add_u32 s80, s68, 65536\n"
        "buffer_load_dwordx4 v143, s[40:43], s80 offen lds\n"
        "s_add_u32 m0, s50, 19456\n"
        "s_add_u32 s80, s68, 98304\n"
        "buffer_load_dwordx4 v143, s[40:43], s80 offen lds\n"
        "v_xor_b32 v142, 0x100000, v140\n"
        "v_xor_b32 v143, 0x100000, v141\n"
        "s_add_u32 m0, s50, 32768\n"
        "s_add_u32 s80, s67, 0\n"
        "buffer_load_dwordx4 v142, s[40:43], s80 offen lds\n"
        "s_add_u32 m0, s50, 33792\n"
        "s_add_u32 s80, s67, 32768\n"
        "buffer_load_dwordx4 v142, s[40:43], s80 offen lds\n"
        "s_add_u32 m0, s50, 34816\n"
        "s_add_u32 s80, s67, 65536\n"
        "buffer_load_dwordx4 v143, s[40:43], s80 offen lds\n"
        "s_add_u32 m0, s50, 35840\n"
        "s_add_u32 s80, s67, 98304\n"
        "buffer_load_dwordx4 v143, s[40:43], s80 offen lds\n"
        "s_mov_b64 s[40:41], %3\n"
        "v_xor_b32 v142, 0x100000, v140\n"
        "v_xor_b32 v143, 0x100000, v141\n"
        "s_add_u32 m0, s50, 49152\n"
        "s_add_u32 s80, s68, 0\n"
        "buffer_load_dwordx4 v142, s[40:43], s80 offen lds\n"
        "s_add_u32 m0, s50, 50176\n"
        "s_add_u32 s80, s68, 32768\n"
        "buffer_load_dwordx4 v142, s[40:43], s80 offen lds\n"
        "s_add_u32 m0, s50, 51200\n"
        "s_add_u32 s80, s68, 65536\n"
        "buffer_load_dwordx4 v143, s[40:43], s80 offen lds\n"
        "s_add_u32 m0, s50, 52224\n"
        "s_add_u32 s80, s68, 98304\n"
        "buffer_load_dwordx4 v143, s[40:43], s80 offen lds\n"
        "v_xor_b32 v142, 0x200000, v140\n"
        "v_xor_b32 v143, 0x200000, v141\n"
        "s_add_u32 m0, s50, 65536\n"
        "s_add_u32 s80, s67, 0\n"
        "buffer_load_dwordx4 v142, s[40:43], s80 offen lds\n"
        "s_add_u32 m0, s50, 66560\n"
        "s_add_u32 s80, s67, 32768\n"
        "buffer_load_dwordx4 v142, s[40:43], s80 offen lds\n"
        "s_add_u32 m0, s50, 67584\n"
        "s_add_u32 s80, s67, 65536\n"
        "buffer_load_dwordx4 v143, s[40:43], s80 offen lds\n"
        "s_add_u32 m0, s50, 68608\n"
        "s_add_u32 s80, s67, 98304\n"
        "buffer_load_dwordx4 v143, s[40:43], s80 offen lds\n"
        "v_xor_b32 v142, 0x200000, v140\n"
        "v_xor_b32 v143, 0x200000, v141\n"
        "s_add_u32 m0, s50, 81920\n"
        "s_add_u32 s80, s68, 0\n"
        "buffer_load_dwordx4 v142, s[40:43], s80 offen lds\n"
        "s_add_u32 m0, s50, 82944\n"
        "s_add_u32 s80, s68, 32768\n"
        "buffer_load_dwordx4 v142, s[40:43], s80 offen lds\n"
        "s_add_u32 m0, s50, 83968\n"
        "s_add_u32 s80, s68, 65536\n"
        "buffer_load_dwordx4 v143, s[40:43], s80 offen lds\n"
        "s_add_u32 m0, s50, 84992\n"
        "s_add_u32 s80, s68, 98304\n"
        "buffer_load_dwordx4 v143, s[40:43], s80 offen lds\n"
        "s_mov_b64 s[40:41], %3\n"
        "v_xor_b32 v142, 0x300000, v140\n"
        "v_xor_b32 v143, 0x300000, v141\n"
        "s_add_u32 m0, s50, 98304\n"
        "s_add_u32 s80, s67, 0\n"
        "buffer_load_dwordx4 v142, s[40:43], s80 offen lds\n"
        "s_add_u32 m0, s50, 99328\n"
        "s_add_u32 s80, s67, 32768\n"
        "buffer_load_dwordx4 v142, s[40:43], s80 offen lds\n"
        "s_add_u32 m0, s50, 100352\n"
        "s_add_u32 s80, s67, 65536\n"
        "buffer_load_dwordx4 v143, s[40:43], s80 offen lds\n"
        "s_add_u32 m0, s50, 101376\n"
        "s_add_u32 s80, s67, 98304\n"
        "buffer_load_dwordx4 v143, s[40:43], s80 offen lds\n"
        "v_xor_b32 v142, 0x300000, v140\n"
        "v_xor_b32 v143, 0x300000, v141\n"
        "s_add_u32 m0, s50, 114688\n"
        "s_add_u32 s80, s68, 0\n"
        "buffer_load_dwordx4 v142, s[40:43], s80 offen lds\n"
        "s_add_u32 m0, s50, 115712\n"
        "s_add_u32 s80, s68, 32768\n"
        "buffer_load_dwordx4 v142, s[40:43], s80 offen lds\n"
        "s_add_u32 m0, s50, 116736\n"
        "s_add_u32 s80, s68, 65536\n"
        "buffer_load_dwordx4 v143, s[40:43], s80 offen lds\n"
        "s_add_u32 m0, s50, 117760\n"
        "s_add_u32 s80, s68, 98304\n"
        "buffer_load_dwordx4 v143, s[40:43], s80 offen lds\n"
        "s_add_u32 m0, s50, 131072\n"
        "s_add_u32 s80, s49, 4194304\n"
        "buffer_load_dwordx4 v134, s[40:43], s80 offen lds\n"
        "s_add_u32 m0, s50, 132096\n"
        "s_add_u32 s80, s49, 4227072\n"
        "buffer_load_dwordx4 v134, s[40:43], s80 offen lds\n"
        "s_add_u32 m0, s50, 133120\n"
        "s_add_u32 s80, s49, 4259840\n"
        "buffer_load_dwordx4 v135, s[40:43], s80 offen lds\n"
        "s_add_u32 m0, s50, 134144\n"
        "s_add_u32 s80, s49, 4292608\n"
        "buffer_load_dwordx4 v135, s[40:43], s80 offen lds\n"
        "v_lshrrev_b32 v131, 3, v129\n"
        "v_lshlrev_b32 v131, 5, v131\n"
        "v_and_b32 v132, 3, v129\n"
        "v_lshl_add_u32 v131, v132, 3, v131\n"
        "v_and_b32 v132, 4, v129\n"
        "v_add_u32 v131, v131, v132\n"
        "s_movk_i32 s80, 4608\n"
        "v_mad_u32_u24 v138, v130, s80, v131\n"
        "s_and_b32 s81, s47, 1\n"
        "s_mul_i32 s81, s81, 6\n"
        "v_xor_b32 v131, s81, v129\n"
        "v_and_b32 v131, 14, v131\n"
        "v_lshlrev_b32 v131, 1, v131\n"
        "v_mad_u32_u24 v127, v130, s80, v131\n"
        "s_mul_i32 s51, s47, 147456\n"
        "s_lshl2_add_u32 s80, s46, s45\n"
        "s_lshl_b32 s80, s80, 7\n"
        "s_add_u32 s52, s51, s80\n"
        "s_and_b32 s80, s45, 1\n"
        "s_lshl_b32 s81, s80, 6\n"
        "s_sub_u32 s52, s52, s81\n"
        "s_lshl1_add_u32 s80, s46, s80\n"
        "s_lshl_b32 s80, s80, 5\n"
        "s_add_u32 s51, s51, s80\n"
        "s_add_u32 s51, s51, 1024\n"
        "s_and_b32 s80, s47, 2\n"
        "s_lshl_b32 s80, s80, 6\n"
        "s_add_u32 s52, s52, s80\n"
        "s_xor_b32 s66, s52, 128\n"
        "s_mov_b32 s53, 0x05040100\n"
        "s_mov_b32 s54, 0x07060302\n"
        "v_and_b32 v131, 1, v129\n"
        "s_mov_b32 s80, 0x02020202\n"
        "v_mul_lo_u32 v131, v131, s80\n"
        "v_add_u32 v139, s53, v131\n"
        "v_lshlrev_b32 v131, 4, v128\n"
        "s_and_b32 s80, s45, 1\n"
        "s_lshl_b32 s80, s80, 12\n"
        "s_add_u32 s84, s80, 152576\n"
        "s_mov_b64 s[40:41], %4\n"
        "s_add_u32 s80, s51, 0\n"
        "buffer_load_dword v64, v127, s[40:43], s80 offen offset:0\n"
        "buffer_load_dword v66, v127, s[40:43], s80 offen offset:1152\n"
        "buffer_load_dword v68, v127, s[40:43], s80 offen offset:2304\n"
        "buffer_load_dword v70, v127, s[40:43], s80 offen offset:3456\n"
        "s_add_u32 s80, s51, 18432\n"
        "buffer_load_dword v65, v127, s[40:43], s80 offen offset:0\n"
        "buffer_load_dword v67, v127, s[40:43], s80 offen offset:1152\n"
        "buffer_load_dword v69, v127, s[40:43], s80 offen offset:2304\n"
        "buffer_load_dword v71, v127, s[40:43], s80 offen offset:3456\n"
        "s_add_u32 s80, s51, 36864\n"
        "buffer_load_dword v72, v127, s[40:43], s80 offen offset:0\n"
        "buffer_load_dword v74, v127, s[40:43], s80 offen offset:1152\n"
        "buffer_load_dword v76, v127, s[40:43], s80 offen offset:2304\n"
        "buffer_load_dword v78, v127, s[40:43], s80 offen offset:3456\n"
        "s_add_u32 s80, s51, 55296\n"
        "buffer_load_dword v73, v127, s[40:43], s80 offen offset:0\n"
        "buffer_load_dword v75, v127, s[40:43], s80 offen offset:1152\n"
        "buffer_load_dword v77, v127, s[40:43], s80 offen offset:2304\n"
        "buffer_load_dword v79, v127, s[40:43], s80 offen offset:3456\n"
        "s_add_u32 s80, s51, 73728\n"
        "buffer_load_dword v80, v127, s[40:43], s80 offen offset:0\n"
        "buffer_load_dword v82, v127, s[40:43], s80 offen offset:1152\n"
        "buffer_load_dword v84, v127, s[40:43], s80 offen offset:2304\n"
        "buffer_load_dword v86, v127, s[40:43], s80 offen offset:3456\n"
        "s_add_u32 s80, s51, 92160\n"
        "buffer_load_dword v81, v127, s[40:43], s80 offen offset:0\n"
        "buffer_load_dword v83, v127, s[40:43], s80 offen offset:1152\n"
        "buffer_load_dword v85, v127, s[40:43], s80 offen offset:2304\n"
        "buffer_load_dword v87, v127, s[40:43], s80 offen offset:3456\n"
        "s_add_u32 s80, s51, 110592\n"
        "buffer_load_dword v88, v127, s[40:43], s80 offen offset:0\n"
        "buffer_load_dword v90, v127, s[40:43], s80 offen offset:1152\n"
        "buffer_load_dword v92, v127, s[40:43], s80 offen offset:2304\n"
        "buffer_load_dword v94, v127, s[40:43], s80 offen offset:3456\n"
        "s_add_u32 s80, s51, 129024\n"
        "buffer_load_dword v89, v127, s[40:43], s80 offen offset:0\n"
        "buffer_load_dword v91, v127, s[40:43], s80 offen offset:1152\n"
        "buffer_load_dword v93, v127, s[40:43], s80 offen offset:2304\n"
        "buffer_load_dword v95, v127, s[40:43], s80 offen offset:3456\n"
        "v_add_u32 v132, 0, v136\n"
        "v_add_u32 v133, 0, v137\n"
        "s_waitcnt vmcnt(56)\n"
        "s_barrier\n"
        "ds_read_b64_tr_b16 a[0:1], v132 offset:0\n"
        "ds_read_b64_tr_b16 a[2:3], v132 offset:1024\n"
        "ds_read_b64_tr_b16 a[4:5], v132 offset:8192\n"
        "ds_read_b64_tr_b16 a[6:7], v132 offset:9216\n"
        "ds_read_b64_tr_b16 a[8:9], v133 offset:0\n"
        "ds_read_b64_tr_b16 a[10:11], v133 offset:1024\n"
        "ds_read_b64_tr_b16 a[12:13], v133 offset:8192\n"
        "ds_read_b64_tr_b16 a[14:15], v133 offset:9216\n"
        "ds_read_b64_tr_b16 a[16:17], v132 offset:16384\n"
        "ds_read_b64_tr_b16 a[18:19], v132 offset:17408\n"
        "ds_read_b64_tr_b16 a[20:21], v132 offset:24576\n"
        "ds_read_b64_tr_b16 a[22:23], v132 offset:25600\n"
        "ds_read_b64_tr_b16 a[24:25], v133 offset:16384\n"
        "ds_read_b64_tr_b16 a[26:27], v133 offset:17408\n"
        "ds_read_b64_tr_b16 a[28:29], v133 offset:24576\n"
        "ds_read_b64_tr_b16 a[30:31], v133 offset:25600\n"
        "ds_read_b64_tr_b16 a[32:33], v132 offset:32768\n"
        "ds_read_b64_tr_b16 a[34:35], v132 offset:33792\n"
        "ds_read_b64_tr_b16 a[36:37], v132 offset:40960\n"
        "ds_read_b64_tr_b16 a[38:39], v132 offset:41984\n"
        "ds_read_b64_tr_b16 a[40:41], v133 offset:32768\n"
        "ds_read_b64_tr_b16 a[42:43], v133 offset:33792\n"
        "ds_read_b64_tr_b16 a[44:45], v133 offset:40960\n"
        "ds_read_b64_tr_b16 a[46:47], v133 offset:41984\n"
        "s_mov_b64 s[40:41], %4\n"
        "s_add_u32 s80, s52, 0\n"
        "buffer_load_dword v0, v138, s[40:43], s80 offen offset:0\n"
        "buffer_load_dword v2, v138, s[40:43], s80 offen offset:1152\n"
        "buffer_load_dword v4, v138, s[40:43], s80 offen offset:2304\n"
        "buffer_load_dword v6, v138, s[40:43], s80 offen offset:3456\n"
        "s_add_u32 s80, s52, 18432\n"
        "buffer_load_dword v1, v138, s[40:43], s80 offen offset:0\n"
        "buffer_load_dword v3, v138, s[40:43], s80 offen offset:1152\n"
        "buffer_load_dword v5, v138, s[40:43], s80 offen offset:2304\n"
        "buffer_load_dword v7, v138, s[40:43], s80 offen offset:3456\n"
        "s_add_u32 s80, s52, 36864\n"
        "buffer_load_dword v8, v138, s[40:43], s80 offen offset:0\n"
        "buffer_load_dword v10, v138, s[40:43], s80 offen offset:1152\n"
        "buffer_load_dword v12, v138, s[40:43], s80 offen offset:2304\n"
        "buffer_load_dword v14, v138, s[40:43], s80 offen offset:3456\n"
        "s_add_u32 s80, s52, 55296\n"
        "buffer_load_dword v9, v138, s[40:43], s80 offen offset:0\n"
        "buffer_load_dword v11, v138, s[40:43], s80 offen offset:1152\n"
        "buffer_load_dword v13, v138, s[40:43], s80 offen offset:2304\n"
        "buffer_load_dword v15, v138, s[40:43], s80 offen offset:3456\n"
        "s_add_u32 s80, s52, 73728\n"
        "buffer_load_dword v16, v138, s[40:43], s80 offen offset:0\n"
        "buffer_load_dword v18, v138, s[40:43], s80 offen offset:1152\n"
        "buffer_load_dword v20, v138, s[40:43], s80 offen offset:2304\n"
        "buffer_load_dword v22, v138, s[40:43], s80 offen offset:3456\n"
        "s_add_u32 s80, s52, 92160\n"
        "buffer_load_dword v17, v138, s[40:43], s80 offen offset:0\n"
        "buffer_load_dword v19, v138, s[40:43], s80 offen offset:1152\n"
        "buffer_load_dword v21, v138, s[40:43], s80 offen offset:2304\n"
        "buffer_load_dword v23, v138, s[40:43], s80 offen offset:3456\n"
        "s_add_u32 s80, s52, 110592\n"
        "buffer_load_dword v24, v138, s[40:43], s80 offen offset:0\n"
        "buffer_load_dword v26, v138, s[40:43], s80 offen offset:1152\n"
        "buffer_load_dword v28, v138, s[40:43], s80 offen offset:2304\n"
        "buffer_load_dword v30, v138, s[40:43], s80 offen offset:3456\n"
        "s_add_u32 s80, s52, 129024\n"
        "buffer_load_dword v25, v138, s[40:43], s80 offen offset:0\n"
        "buffer_load_dword v27, v138, s[40:43], s80 offen offset:1152\n"
        "buffer_load_dword v29, v138, s[40:43], s80 offen offset:2304\n"
        "buffer_load_dword v31, v138, s[40:43], s80 offen offset:3456\n"
        "v_add_u32 v132, 49152, v136\n"
        "v_add_u32 v133, 49152, v137\n"
        "s_waitcnt vmcnt(60)\n"
        "s_barrier\n"
        "ds_read_b64_tr_b16 a[48:49], v132 offset:0\n"
        "ds_read_b64_tr_b16 a[50:51], v132 offset:1024\n"
        "ds_read_b64_tr_b16 a[52:53], v132 offset:8192\n"
        "ds_read_b64_tr_b16 a[54:55], v132 offset:9216\n"
        "ds_read_b64_tr_b16 a[56:57], v133 offset:0\n"
        "ds_read_b64_tr_b16 a[58:59], v133 offset:1024\n"
        "ds_read_b64_tr_b16 a[60:61], v133 offset:8192\n"
        "ds_read_b64_tr_b16 a[62:63], v133 offset:9216\n"
        "ds_read_b64_tr_b16 a[64:65], v132 offset:16384\n"
        "ds_read_b64_tr_b16 a[66:67], v132 offset:17408\n"
        "ds_read_b64_tr_b16 a[68:69], v132 offset:24576\n"
        "ds_read_b64_tr_b16 a[70:71], v132 offset:25600\n"
        "ds_read_b64_tr_b16 a[72:73], v133 offset:16384\n"
        "ds_read_b64_tr_b16 a[74:75], v133 offset:17408\n"
        "ds_read_b64_tr_b16 a[76:77], v133 offset:24576\n"
        "ds_read_b64_tr_b16 a[78:79], v133 offset:25600\n"
        "ds_read_b64_tr_b16 a[80:81], v132 offset:32768\n"
        "ds_read_b64_tr_b16 a[82:83], v132 offset:33792\n"
        "ds_read_b64_tr_b16 a[84:85], v132 offset:40960\n"
        "ds_read_b64_tr_b16 a[86:87], v132 offset:41984\n"
        "ds_read_b64_tr_b16 a[88:89], v133 offset:32768\n"
        "ds_read_b64_tr_b16 a[90:91], v133 offset:33792\n"
        "ds_read_b64_tr_b16 a[92:93], v133 offset:40960\n"
        "ds_read_b64_tr_b16 a[94:95], v133 offset:41984\n"
        "s_mov_b64 s[40:41], %4\n"
        "s_add_u32 s80, s66, 0\n"
        "buffer_load_dword v32, v138, s[40:43], s80 offen offset:0\n"
        "buffer_load_dword v34, v138, s[40:43], s80 offen offset:1152\n"
        "buffer_load_dword v36, v138, s[40:43], s80 offen offset:2304\n"
        "buffer_load_dword v38, v138, s[40:43], s80 offen offset:3456\n"
        "s_add_u32 s80, s66, 18432\n"
        "buffer_load_dword v33, v138, s[40:43], s80 offen offset:0\n"
        "buffer_load_dword v35, v138, s[40:43], s80 offen offset:1152\n"
        "buffer_load_dword v37, v138, s[40:43], s80 offen offset:2304\n"
        "buffer_load_dword v39, v138, s[40:43], s80 offen offset:3456\n"
        "s_add_u32 s80, s66, 36864\n"
        "buffer_load_dword v40, v138, s[40:43], s80 offen offset:0\n"
        "buffer_load_dword v42, v138, s[40:43], s80 offen offset:1152\n"
        "buffer_load_dword v44, v138, s[40:43], s80 offen offset:2304\n"
        "buffer_load_dword v46, v138, s[40:43], s80 offen offset:3456\n"
        "s_add_u32 s80, s66, 55296\n"
        "buffer_load_dword v41, v138, s[40:43], s80 offen offset:0\n"
        "buffer_load_dword v43, v138, s[40:43], s80 offen offset:1152\n"
        "buffer_load_dword v45, v138, s[40:43], s80 offen offset:2304\n"
        "buffer_load_dword v47, v138, s[40:43], s80 offen offset:3456\n"
        "s_add_u32 s80, s66, 73728\n"
        "buffer_load_dword v48, v138, s[40:43], s80 offen offset:0\n"
        "buffer_load_dword v50, v138, s[40:43], s80 offen offset:1152\n"
        "buffer_load_dword v52, v138, s[40:43], s80 offen offset:2304\n"
        "buffer_load_dword v54, v138, s[40:43], s80 offen offset:3456\n"
        "s_add_u32 s80, s66, 92160\n"
        "buffer_load_dword v49, v138, s[40:43], s80 offen offset:0\n"
        "buffer_load_dword v51, v138, s[40:43], s80 offen offset:1152\n"
        "buffer_load_dword v53, v138, s[40:43], s80 offen offset:2304\n"
        "buffer_load_dword v55, v138, s[40:43], s80 offen offset:3456\n"
        "s_add_u32 s80, s66, 110592\n"
        "buffer_load_dword v56, v138, s[40:43], s80 offen offset:0\n"
        "buffer_load_dword v58, v138, s[40:43], s80 offen offset:1152\n"
        "buffer_load_dword v60, v138, s[40:43], s80 offen offset:2304\n"
        "buffer_load_dword v62, v138, s[40:43], s80 offen offset:3456\n"
        "s_add_u32 s80, s66, 129024\n"
        "buffer_load_dword v57, v138, s[40:43], s80 offen offset:0\n"
        "buffer_load_dword v59, v138, s[40:43], s80 offen offset:1152\n"
        "buffer_load_dword v61, v138, s[40:43], s80 offen offset:2304\n"
        "buffer_load_dword v63, v138, s[40:43], s80 offen offset:3456\n"
        "v_add_u32 v132, 98304, v136\n"
        "v_add_u32 v133, 98304, v137\n"
        "s_waitcnt vmcnt(60)\n"
        "s_barrier\n"
        "ds_read_b64_tr_b16 a[96:97], v132 offset:0\n"
        "ds_read_b64_tr_b16 a[98:99], v132 offset:1024\n"
        "ds_read_b64_tr_b16 a[100:101], v132 offset:8192\n"
        "ds_read_b64_tr_b16 a[102:103], v132 offset:9216\n"
        "ds_read_b64_tr_b16 a[104:105], v133 offset:0\n"
        "ds_read_b64_tr_b16 a[106:107], v133 offset:1024\n"
        "ds_read_b64_tr_b16 a[108:109], v133 offset:8192\n"
        "ds_read_b64_tr_b16 a[110:111], v133 offset:9216\n"
        "ds_read_b64_tr_b16 a[112:113], v132 offset:16384\n"
        "ds_read_b64_tr_b16 a[114:115], v132 offset:17408\n"
        "ds_read_b64_tr_b16 a[116:117], v132 offset:24576\n"
        "ds_read_b64_tr_b16 a[118:119], v132 offset:25600\n"
        "ds_read_b64_tr_b16 a[120:121], v133 offset:16384\n"
        "ds_read_b64_tr_b16 a[122:123], v133 offset:17408\n"
        "ds_read_b64_tr_b16 a[124:125], v133 offset:24576\n"
        "ds_read_b64_tr_b16 a[126:127], v133 offset:25600\n"
        "ds_read_b64_tr_b16 a[128:129], v132 offset:32768\n"
        "ds_read_b64_tr_b16 a[130:131], v132 offset:33792\n"
        "ds_read_b64_tr_b16 a[132:133], v132 offset:40960\n"
        "ds_read_b64_tr_b16 a[134:135], v132 offset:41984\n"
        "ds_read_b64_tr_b16 a[136:137], v133 offset:32768\n"
        "ds_read_b64_tr_b16 a[138:139], v133 offset:33792\n"
        "ds_read_b64_tr_b16 a[140:141], v133 offset:40960\n"
        "ds_read_b64_tr_b16 a[142:143], v133 offset:41984\n"
        "s_waitcnt lgkmcnt(0)\n"
        "s_barrier\n"
        "s_mov_b64 s[40:41], %1\n"
        "v_xor_b32 v142, 0x0, v140\n"
        "v_xor_b32 v143, 0x0, v141\n"
        "s_add_u32 m0, s50, 0\n"
        "s_add_u32 s80, s67, 0\n"
        "buffer_load_dwordx4 v142, s[40:43], s80 offen lds\n"
        "s_add_u32 m0, s50, 1024\n"
        "s_add_u32 s80, s67, 32768\n"
        "buffer_load_dwordx4 v142, s[40:43], s80 offen lds\n"
        "s_add_u32 m0, s50, 2048\n"
        "s_add_u32 s80, s67, 65536\n"
        "buffer_load_dwordx4 v143, s[40:43], s80 offen lds\n"
        "s_add_u32 m0, s50, 3072\n"
        "s_add_u32 s80, s67, 98304\n"
        "buffer_load_dwordx4 v143, s[40:43], s80 offen lds\n"
        "v_xor_b32 v142, 0x0, v140\n"
        "v_xor_b32 v143, 0x0, v141\n"
        "s_add_u32 m0, s50, 16384\n"
        "s_add_u32 s80, s68, 0\n"
        "buffer_load_dwordx4 v142, s[40:43], s80 offen lds\n"
        "s_add_u32 m0, s50, 17408\n"
        "s_add_u32 s80, s68, 32768\n"
        "buffer_load_dwordx4 v142, s[40:43], s80 offen lds\n"
        "s_add_u32 m0, s50, 18432\n"
        "s_add_u32 s80, s68, 65536\n"
        "buffer_load_dwordx4 v143, s[40:43], s80 offen lds\n"
        "s_add_u32 m0, s50, 19456\n"
        "s_add_u32 s80, s68, 98304\n"
        "buffer_load_dwordx4 v143, s[40:43], s80 offen lds\n"
        "v_xor_b32 v142, 0x100000, v140\n"
        "v_xor_b32 v143, 0x100000, v141\n"
        "s_add_u32 m0, s50, 32768\n"
        "s_add_u32 s80, s67, 0\n"
        "buffer_load_dwordx4 v142, s[40:43], s80 offen lds\n"
        "s_add_u32 m0, s50, 33792\n"
        "s_add_u32 s80, s67, 32768\n"
        "buffer_load_dwordx4 v142, s[40:43], s80 offen lds\n"
        "s_add_u32 m0, s50, 34816\n"
        "s_add_u32 s80, s67, 65536\n"
        "buffer_load_dwordx4 v143, s[40:43], s80 offen lds\n"
        "s_add_u32 m0, s50, 35840\n"
        "s_add_u32 s80, s67, 98304\n"
        "buffer_load_dwordx4 v143, s[40:43], s80 offen lds\n"
        "s_mov_b64 s[40:41], %1\n"
        "v_xor_b32 v142, 0x100000, v140\n"
        "v_xor_b32 v143, 0x100000, v141\n"
        "s_add_u32 m0, s50, 49152\n"
        "s_add_u32 s80, s68, 0\n"
        "buffer_load_dwordx4 v142, s[40:43], s80 offen lds\n"
        "s_add_u32 m0, s50, 50176\n"
        "s_add_u32 s80, s68, 32768\n"
        "buffer_load_dwordx4 v142, s[40:43], s80 offen lds\n"
        "s_add_u32 m0, s50, 51200\n"
        "s_add_u32 s80, s68, 65536\n"
        "buffer_load_dwordx4 v143, s[40:43], s80 offen lds\n"
        "s_add_u32 m0, s50, 52224\n"
        "s_add_u32 s80, s68, 98304\n"
        "buffer_load_dwordx4 v143, s[40:43], s80 offen lds\n"
        "v_xor_b32 v142, 0x200000, v140\n"
        "v_xor_b32 v143, 0x200000, v141\n"
        "s_add_u32 m0, s50, 65536\n"
        "s_add_u32 s80, s67, 0\n"
        "buffer_load_dwordx4 v142, s[40:43], s80 offen lds\n"
        "s_add_u32 m0, s50, 66560\n"
        "s_add_u32 s80, s67, 32768\n"
        "buffer_load_dwordx4 v142, s[40:43], s80 offen lds\n"
        "s_add_u32 m0, s50, 67584\n"
        "s_add_u32 s80, s67, 65536\n"
        "buffer_load_dwordx4 v143, s[40:43], s80 offen lds\n"
        "s_add_u32 m0, s50, 68608\n"
        "s_add_u32 s80, s67, 98304\n"
        "buffer_load_dwordx4 v143, s[40:43], s80 offen lds\n"
        "v_xor_b32 v142, 0x200000, v140\n"
        "v_xor_b32 v143, 0x200000, v141\n"
        "s_add_u32 m0, s50, 81920\n"
        "s_add_u32 s80, s68, 0\n"
        "buffer_load_dwordx4 v142, s[40:43], s80 offen lds\n"
        "s_add_u32 m0, s50, 82944\n"
        "s_add_u32 s80, s68, 32768\n"
        "buffer_load_dwordx4 v142, s[40:43], s80 offen lds\n"
        "s_add_u32 m0, s50, 83968\n"
        "s_add_u32 s80, s68, 65536\n"
        "buffer_load_dwordx4 v143, s[40:43], s80 offen lds\n"
        "s_add_u32 m0, s50, 84992\n"
        "s_add_u32 s80, s68, 98304\n"
        "buffer_load_dwordx4 v143, s[40:43], s80 offen lds\n"
        "s_mov_b64 s[40:41], %2\n"
        "v_xor_b32 v142, 0x0, v140\n"
        "v_xor_b32 v143, 0x0, v141\n"
        "s_add_u32 m0, s50, 98304\n"
        "s_add_u32 s80, s67, 0\n"
        "buffer_load_dwordx4 v142, s[40:43], s80 offen lds\n"
        "s_add_u32 m0, s50, 99328\n"
        "s_add_u32 s80, s67, 32768\n"
        "buffer_load_dwordx4 v142, s[40:43], s80 offen lds\n"
        "s_add_u32 m0, s50, 100352\n"
        "s_add_u32 s80, s67, 65536\n"
        "buffer_load_dwordx4 v143, s[40:43], s80 offen lds\n"
        "s_add_u32 m0, s50, 101376\n"
        "s_add_u32 s80, s67, 98304\n"
        "buffer_load_dwordx4 v143, s[40:43], s80 offen lds\n"
        "v_xor_b32 v142, 0x0, v140\n"
        "v_xor_b32 v143, 0x0, v141\n"
        "s_add_u32 m0, s50, 114688\n"
        "s_add_u32 s80, s68, 0\n"
        "buffer_load_dwordx4 v142, s[40:43], s80 offen lds\n"
        "s_add_u32 m0, s50, 115712\n"
        "s_add_u32 s80, s68, 32768\n"
        "buffer_load_dwordx4 v142, s[40:43], s80 offen lds\n"
        "s_add_u32 m0, s50, 116736\n"
        "s_add_u32 s80, s68, 65536\n"
        "buffer_load_dwordx4 v143, s[40:43], s80 offen lds\n"
        "s_add_u32 m0, s50, 117760\n"
        "s_add_u32 s80, s68, 98304\n"
        "buffer_load_dwordx4 v143, s[40:43], s80 offen lds\n"
        "v_xor_b32 v142, 0x200000, v140\n"
        "v_xor_b32 v143, 0x200000, v141\n"
        "s_add_u32 m0, s50, 131072\n"
        "s_add_u32 s80, s68, 0\n"
        "buffer_load_dwordx4 v142, s[40:43], s80 offen lds\n"
        "s_add_u32 m0, s50, 132096\n"
        "s_add_u32 s80, s68, 32768\n"
        "buffer_load_dwordx4 v142, s[40:43], s80 offen lds\n"
        "s_add_u32 m0, s50, 133120\n"
        "s_add_u32 s80, s68, 65536\n"
        "buffer_load_dwordx4 v143, s[40:43], s80 offen lds\n"
        "s_add_u32 m0, s50, 134144\n"
        "s_add_u32 s80, s68, 98304\n"
        "buffer_load_dwordx4 v143, s[40:43], s80 offen lds\n"
        "s_waitcnt vmcnt(63)\n"
        "v_perm_b32 v96, v65, v64, v139\n"
        "s_waitcnt vmcnt(63)\n"
        "v_perm_b32 v97, v67, v66, v139\n"
        "s_waitcnt vmcnt(63)\n"
        "v_perm_b32 v98, v69, v68, v139\n"
        "s_waitcnt vmcnt(63)\n"
        "v_perm_b32 v99, v71, v70, v139\n"
        "s_waitcnt vmcnt(63)\n"
        "v_perm_b32 v100, v73, v72, v139\n"
        "s_waitcnt vmcnt(63)\n"
        "v_perm_b32 v101, v75, v74, v139\n"
        "s_waitcnt vmcnt(63)\n"
        "v_perm_b32 v102, v77, v76, v139\n"
        "s_waitcnt vmcnt(63)\n"
        "v_perm_b32 v103, v79, v78, v139\n"
        "s_waitcnt vmcnt(63)\n"
        "v_perm_b32 v104, v81, v80, v139\n"
        "s_waitcnt vmcnt(63)\n"
        "v_perm_b32 v105, v83, v82, v139\n"
        "s_waitcnt vmcnt(63)\n"
        "v_perm_b32 v106, v85, v84, v139\n"
        "s_waitcnt vmcnt(63)\n"
        "v_perm_b32 v107, v87, v86, v139\n"
        "s_waitcnt vmcnt(63)\n"
        "v_perm_b32 v108, v89, v88, v139\n"
        "s_waitcnt vmcnt(63)\n"
        "v_perm_b32 v109, v91, v90, v139\n"
        "s_waitcnt vmcnt(63)\n"
        "v_perm_b32 v110, v93, v92, v139\n"
        "s_waitcnt vmcnt(63)\n"
        "v_perm_b32 v111, v95, v94, v139\n"
        "v_add_u32 v127, s84, v131\n"
        "ds_write_b128 v127, v[96:99] offset:0\n"
        "ds_write_b128 v127, v[100:103] offset:1024\n"
        "ds_write_b128 v127, v[104:107] offset:2048\n"
        "ds_write_b128 v127, v[108:111] offset:3072\n"
        "s_waitcnt vmcnt(63)\n"
        "v_perm_b32 v144, v1, v0, s53\n"
        "v_perm_b32 v152, v1, v0, s54\n"
        "s_waitcnt vmcnt(63)\n"
        "v_perm_b32 v145, v3, v2, s53\n"
        "v_perm_b32 v153, v3, v2, s54\n"
        "s_waitcnt vmcnt(63)\n"
        "v_perm_b32 v146, v5, v4, s53\n"
        "v_perm_b32 v154, v5, v4, s54\n"
        "s_waitcnt vmcnt(63)\n"
        "v_perm_b32 v147, v7, v6, s53\n"
        "v_perm_b32 v155, v7, v6, s54\n"
        "s_waitcnt vmcnt(63)\n"
        "v_perm_b32 v148, v9, v8, s53\n"
        "v_perm_b32 v156, v9, v8, s54\n"
        "s_waitcnt vmcnt(63)\n"
        "v_perm_b32 v149, v11, v10, s53\n"
        "v_perm_b32 v157, v11, v10, s54\n"
        "s_waitcnt vmcnt(63)\n"
        "v_perm_b32 v150, v13, v12, s53\n"
        "v_perm_b32 v158, v13, v12, s54\n"
        "s_waitcnt vmcnt(63)\n"
        "v_perm_b32 v151, v15, v14, s53\n"
        "v_perm_b32 v159, v15, v14, s54\n"
        "s_waitcnt vmcnt(63)\n"
        "v_perm_b32 v160, v17, v16, s53\n"
        "v_perm_b32 v168, v17, v16, s54\n"
        "s_waitcnt vmcnt(63)\n"
        "v_perm_b32 v161, v19, v18, s53\n"
        "v_perm_b32 v169, v19, v18, s54\n"
        "s_waitcnt vmcnt(63)\n"
        "v_perm_b32 v162, v21, v20, s53\n"
        "v_perm_b32 v170, v21, v20, s54\n"
        "s_waitcnt vmcnt(63)\n"
        "v_perm_b32 v163, v23, v22, s53\n"
        "v_perm_b32 v171, v23, v22, s54\n"
        "s_waitcnt vmcnt(63)\n"
        "v_perm_b32 v164, v25, v24, s53\n"
        "v_perm_b32 v172, v25, v24, s54\n"
        "s_waitcnt vmcnt(63)\n"
        "v_perm_b32 v165, v27, v26, s53\n"
        "v_perm_b32 v173, v27, v26, s54\n"
        "s_waitcnt vmcnt(63)\n"
        "v_perm_b32 v166, v29, v28, s53\n"
        "v_perm_b32 v174, v29, v28, s54\n"
        "s_waitcnt vmcnt(63)\n"
        "v_perm_b32 v167, v31, v30, s53\n"
        "v_perm_b32 v175, v31, v30, s54\n"
        "s_waitcnt lgkmcnt(0)\n"
        "s_waitcnt vmcnt(63)\n"
        "v_perm_b32 v96, v33, v32, s53\n"
        "v_perm_b32 v104, v33, v32, s54\n"
        "s_waitcnt vmcnt(62)\n"
        "v_perm_b32 v97, v35, v34, s53\n"
        "v_perm_b32 v105, v35, v34, s54\n"
        "s_waitcnt vmcnt(61)\n"
        "v_perm_b32 v98, v37, v36, s53\n"
        "v_perm_b32 v106, v37, v36, s54\n"
        "s_waitcnt vmcnt(60)\n"
        "v_perm_b32 v99, v39, v38, s53\n"
        "v_perm_b32 v107, v39, v38, s54\n"
        "s_waitcnt vmcnt(55)\n"
        "v_perm_b32 v100, v41, v40, s53\n"
        "v_perm_b32 v108, v41, v40, s54\n"
        "s_waitcnt vmcnt(54)\n"
        "v_perm_b32 v101, v43, v42, s53\n"
        "v_perm_b32 v109, v43, v42, s54\n"
        "s_waitcnt vmcnt(53)\n"
        "v_perm_b32 v102, v45, v44, s53\n"
        "v_perm_b32 v110, v45, v44, s54\n"
        "s_waitcnt vmcnt(52)\n"
        "v_perm_b32 v103, v47, v46, s53\n"
        "v_perm_b32 v111, v47, v46, s54\n"
        "s_waitcnt vmcnt(47)\n"
        "v_perm_b32 v112, v49, v48, s53\n"
        "v_perm_b32 v120, v49, v48, s54\n"
        "s_waitcnt vmcnt(46)\n"
        "v_perm_b32 v113, v51, v50, s53\n"
        "v_perm_b32 v121, v51, v50, s54\n"
        "s_waitcnt vmcnt(45)\n"
        "v_perm_b32 v114, v53, v52, s53\n"
        "v_perm_b32 v122, v53, v52, s54\n"
        "s_waitcnt vmcnt(44)\n"
        "v_perm_b32 v115, v55, v54, s53\n"
        "v_perm_b32 v123, v55, v54, s54\n"
        "s_waitcnt vmcnt(39)\n"
        "v_perm_b32 v116, v57, v56, s53\n"
        "v_perm_b32 v124, v57, v56, s54\n"
        "s_waitcnt vmcnt(38)\n"
        "v_perm_b32 v117, v59, v58, s53\n"
        "v_perm_b32 v125, v59, v58, s54\n"
        "s_waitcnt vmcnt(37)\n"
        "v_perm_b32 v118, v61, v60, s53\n"
        "v_perm_b32 v126, v61, v60, s54\n"
        "s_waitcnt vmcnt(36)\n"
        "v_perm_b32 v119, v63, v62, s53\n"
        "v_perm_b32 v127, v63, v62, s54\n"
        "v_add_u32 v132, 0, v136\n"
        "v_add_u32 v133, 0, v137\n"
        "s_waitcnt vmcnt(24)\n"
        "s_barrier\n"
        "ds_read_b64_tr_b16 a[144:145], v132 offset:0\n"
        "ds_read_b64_tr_b16 a[146:147], v132 offset:1024\n"
        "ds_read_b64_tr_b16 a[148:149], v132 offset:8192\n"
        "ds_read_b64_tr_b16 a[150:151], v132 offset:9216\n"
        "ds_read_b64_tr_b16 a[152:153], v133 offset:0\n"
        "ds_read_b64_tr_b16 a[154:155], v133 offset:1024\n"
        "ds_read_b64_tr_b16 a[156:157], v133 offset:8192\n"
        "ds_read_b64_tr_b16 a[158:159], v133 offset:9216\n"
        "ds_read_b64_tr_b16 a[160:161], v132 offset:16384\n"
        "ds_read_b64_tr_b16 a[162:163], v132 offset:17408\n"
        "ds_read_b64_tr_b16 a[164:165], v132 offset:24576\n"
        "ds_read_b64_tr_b16 a[166:167], v132 offset:25600\n"
        "ds_read_b64_tr_b16 a[168:169], v133 offset:16384\n"
        "ds_read_b64_tr_b16 a[170:171], v133 offset:17408\n"
        "ds_read_b64_tr_b16 a[172:173], v133 offset:24576\n"
        "ds_read_b64_tr_b16 a[174:175], v133 offset:25600\n"
        "ds_read_b64_tr_b16 a[176:177], v132 offset:32768\n"
        "ds_read_b64_tr_b16 a[178:179], v132 offset:33792\n"
        "ds_read_b64_tr_b16 a[180:181], v132 offset:40960\n"
        "ds_read_b64_tr_b16 a[182:183], v132 offset:41984\n"
        "ds_read_b64_tr_b16 a[184:185], v133 offset:32768\n"
        "ds_read_b64_tr_b16 a[186:187], v133 offset:33792\n"
        "ds_read_b64_tr_b16 a[188:189], v133 offset:40960\n"
        "ds_read_b64_tr_b16 a[190:191], v133 offset:41984\n"
        "s_waitcnt lgkmcnt(0)\n"
        "s_barrier\n"
        "s_mov_b64 s[40:41], %2\n"
        "v_xor_b32 v142, 0x300000, v140\n"
        "v_xor_b32 v143, 0x300000, v141\n"
        "s_add_u32 m0, s50, 0\n"
        "s_add_u32 s80, s67, 0\n"
        "buffer_load_dwordx4 v142, s[40:43], s80 offen lds\n"
        "s_add_u32 m0, s50, 1024\n"
        "s_add_u32 s80, s67, 32768\n"
        "buffer_load_dwordx4 v142, s[40:43], s80 offen lds\n"
        "s_add_u32 m0, s50, 2048\n"
        "s_add_u32 s80, s67, 65536\n"
        "buffer_load_dwordx4 v143, s[40:43], s80 offen lds\n"
        "s_add_u32 m0, s50, 3072\n"
        "s_add_u32 s80, s67, 98304\n"
        "buffer_load_dwordx4 v143, s[40:43], s80 offen lds\n"
        "v_xor_b32 v142, 0x300000, v140\n"
        "v_xor_b32 v143, 0x300000, v141\n"
        "s_add_u32 m0, s50, 16384\n"
        "s_add_u32 s80, s68, 0\n"
        "buffer_load_dwordx4 v142, s[40:43], s80 offen lds\n"
        "s_add_u32 m0, s50, 17408\n"
        "s_add_u32 s80, s68, 32768\n"
        "buffer_load_dwordx4 v142, s[40:43], s80 offen lds\n"
        "s_add_u32 m0, s50, 18432\n"
        "s_add_u32 s80, s68, 65536\n"
        "buffer_load_dwordx4 v143, s[40:43], s80 offen lds\n"
        "s_add_u32 m0, s50, 19456\n"
        "s_add_u32 s80, s68, 98304\n"
        "buffer_load_dwordx4 v143, s[40:43], s80 offen lds\n"
        "s_add_u32 m0, s50, 32768\n"
        "s_add_u32 s80, s49, 4194304\n"
        "buffer_load_dwordx4 v134, s[40:43], s80 offen lds\n"
        "s_add_u32 m0, s50, 33792\n"
        "s_add_u32 s80, s49, 4227072\n"
        "buffer_load_dwordx4 v134, s[40:43], s80 offen lds\n"
        "s_add_u32 m0, s50, 34816\n"
        "s_add_u32 s80, s49, 4259840\n"
        "buffer_load_dwordx4 v135, s[40:43], s80 offen lds\n"
        "s_add_u32 m0, s50, 35840\n"
        "s_add_u32 s80, s49, 4292608\n"
        "buffer_load_dwordx4 v135, s[40:43], s80 offen lds\n"
        "v_add_u32 v132, 49152, v136\n"
        "v_add_u32 v133, 49152, v137\n"
        "s_waitcnt vmcnt(24)\n"
        "s_barrier\n"
        "ds_read_b64_tr_b16 a[192:193], v132 offset:0\n"
        "ds_read_b64_tr_b16 a[194:195], v132 offset:1024\n"
        "ds_read_b64_tr_b16 a[196:197], v132 offset:8192\n"
        "ds_read_b64_tr_b16 a[198:199], v132 offset:9216\n"
        "ds_read_b64_tr_b16 a[200:201], v133 offset:0\n"
        "ds_read_b64_tr_b16 a[202:203], v133 offset:1024\n"
        "ds_read_b64_tr_b16 a[204:205], v133 offset:8192\n"
        "ds_read_b64_tr_b16 a[206:207], v133 offset:9216\n"
        "ds_read_b64_tr_b16 a[208:209], v132 offset:16384\n"
        "ds_read_b64_tr_b16 a[210:211], v132 offset:17408\n"
        "ds_read_b64_tr_b16 a[212:213], v132 offset:24576\n"
        "ds_read_b64_tr_b16 a[214:215], v132 offset:25600\n"
        "ds_read_b64_tr_b16 a[216:217], v133 offset:16384\n"
        "ds_read_b64_tr_b16 a[218:219], v133 offset:17408\n"
        "ds_read_b64_tr_b16 a[220:221], v133 offset:24576\n"
        "ds_read_b64_tr_b16 a[222:223], v133 offset:25600\n"
        "ds_read_b64_tr_b16 a[224:225], v132 offset:32768\n"
        "ds_read_b64_tr_b16 a[226:227], v132 offset:33792\n"
        "ds_read_b64_tr_b16 a[228:229], v132 offset:40960\n"
        "ds_read_b64_tr_b16 a[230:231], v132 offset:41984\n"
        "ds_read_b64_tr_b16 a[232:233], v133 offset:32768\n"
        "ds_read_b64_tr_b16 a[234:235], v133 offset:33792\n"
        "ds_read_b64_tr_b16 a[236:237], v133 offset:40960\n"
        "ds_read_b64_tr_b16 a[238:239], v133 offset:41984\n"
        "s_waitcnt lgkmcnt(0)\n"
        "s_barrier\n"
        "s_mov_b64 s[40:41], %1\n"
        "v_xor_b32 v142, 0x300000, v140\n"
        "v_xor_b32 v143, 0x300000, v141\n"
        "s_add_u32 m0, s50, 49152\n"
        "s_add_u32 s80, s67, 0\n"
        "buffer_load_dwordx4 v142, s[40:43], s80 offen lds\n"
        "s_add_u32 m0, s50, 50176\n"
        "s_add_u32 s80, s67, 32768\n"
        "buffer_load_dwordx4 v142, s[40:43], s80 offen lds\n"
        "s_add_u32 m0, s50, 51200\n"
        "s_add_u32 s80, s67, 65536\n"
        "buffer_load_dwordx4 v143, s[40:43], s80 offen lds\n"
        "s_add_u32 m0, s50, 52224\n"
        "s_add_u32 s80, s67, 98304\n"
        "buffer_load_dwordx4 v143, s[40:43], s80 offen lds\n"
        "v_xor_b32 v142, 0x300000, v140\n"
        "v_xor_b32 v143, 0x300000, v141\n"
        "s_add_u32 m0, s50, 65536\n"
        "s_add_u32 s80, s68, 0\n"
        "buffer_load_dwordx4 v142, s[40:43], s80 offen lds\n"
        "s_add_u32 m0, s50, 66560\n"
        "s_add_u32 s80, s68, 32768\n"
        "buffer_load_dwordx4 v142, s[40:43], s80 offen lds\n"
        "s_add_u32 m0, s50, 67584\n"
        "s_add_u32 s80, s68, 65536\n"
        "buffer_load_dwordx4 v143, s[40:43], s80 offen lds\n"
        "s_add_u32 m0, s50, 68608\n"
        "s_add_u32 s80, s68, 98304\n"
        "buffer_load_dwordx4 v143, s[40:43], s80 offen lds\n"
        "s_add_u32 m0, s50, 81920\n"
        "s_add_u32 s80, s49, 4194304\n"
        "buffer_load_dwordx4 v134, s[40:43], s80 offen lds\n"
        "s_add_u32 m0, s50, 82944\n"
        "s_add_u32 s80, s49, 4227072\n"
        "buffer_load_dwordx4 v134, s[40:43], s80 offen lds\n"
        "s_add_u32 m0, s50, 83968\n"
        "s_add_u32 s80, s49, 4259840\n"
        "buffer_load_dwordx4 v135, s[40:43], s80 offen lds\n"
        "s_add_u32 m0, s50, 84992\n"
        "s_add_u32 s80, s49, 4292608\n"
        "buffer_load_dwordx4 v135, s[40:43], s80 offen lds\n"
        "v_add_u32 v132, 98304, v136\n"
        "v_add_u32 v133, 98304, v137\n"
        "s_waitcnt vmcnt(24)\n"
        "s_barrier\n"
        "ds_read_b64_tr_b16 a[240:241], v132 offset:0\n"
        "ds_read_b64_tr_b16 a[242:243], v132 offset:1024\n"
        "ds_read_b64_tr_b16 a[244:245], v132 offset:8192\n"
        "ds_read_b64_tr_b16 a[246:247], v132 offset:9216\n"
        "ds_read_b64_tr_b16 a[248:249], v133 offset:0\n"
        "ds_read_b64_tr_b16 a[250:251], v133 offset:1024\n"
        "ds_read_b64_tr_b16 a[252:253], v133 offset:8192\n"
        "ds_read_b64_tr_b16 a[254:255], v133 offset:9216\n"
        "ds_read_b64_tr_b16 v[176:177], v132 offset:16384\n"
        "ds_read_b64_tr_b16 v[178:179], v132 offset:17408\n"
        "ds_read_b64_tr_b16 v[180:181], v132 offset:24576\n"
        "ds_read_b64_tr_b16 v[182:183], v132 offset:25600\n"
        "ds_read_b64_tr_b16 v[184:185], v133 offset:16384\n"
        "ds_read_b64_tr_b16 v[186:187], v133 offset:17408\n"
        "ds_read_b64_tr_b16 v[188:189], v133 offset:24576\n"
        "ds_read_b64_tr_b16 v[190:191], v133 offset:25600\n"
        "ds_read_b64_tr_b16 v[192:193], v132 offset:32768\n"
        "ds_read_b64_tr_b16 v[194:195], v132 offset:33792\n"
        "ds_read_b64_tr_b16 v[196:197], v132 offset:40960\n"
        "ds_read_b64_tr_b16 v[198:199], v132 offset:41984\n"
        "ds_read_b64_tr_b16 v[200:201], v133 offset:32768\n"
        "ds_read_b64_tr_b16 v[202:203], v133 offset:33792\n"
        "ds_read_b64_tr_b16 v[204:205], v133 offset:40960\n"
        "ds_read_b64_tr_b16 v[206:207], v133 offset:41984\n"
        "s_waitcnt lgkmcnt(0)\n"
        "s_barrier\n"
        "s_mov_b64 s[40:41], %2\n"
        "v_xor_b32 v142, 0x100000, v140\n"
        "v_xor_b32 v143, 0x100000, v141\n"
        "s_add_u32 m0, s50, 98304\n"
        "s_add_u32 s80, s67, 0\n"
        "buffer_load_dwordx4 v142, s[40:43], s80 offen lds\n"
        "s_add_u32 m0, s50, 99328\n"
        "s_add_u32 s80, s67, 32768\n"
        "buffer_load_dwordx4 v142, s[40:43], s80 offen lds\n"
        "s_add_u32 m0, s50, 100352\n"
        "s_add_u32 s80, s67, 65536\n"
        "buffer_load_dwordx4 v143, s[40:43], s80 offen lds\n"
        "s_add_u32 m0, s50, 101376\n"
        "s_add_u32 s80, s67, 98304\n"
        "buffer_load_dwordx4 v143, s[40:43], s80 offen lds\n"
        "v_xor_b32 v142, 0x100000, v140\n"
        "v_xor_b32 v143, 0x100000, v141\n"
        "s_add_u32 m0, s50, 114688\n"
        "s_add_u32 s80, s68, 0\n"
        "buffer_load_dwordx4 v142, s[40:43], s80 offen lds\n"
        "s_add_u32 m0, s50, 115712\n"
        "s_add_u32 s80, s68, 32768\n"
        "buffer_load_dwordx4 v142, s[40:43], s80 offen lds\n"
        "s_add_u32 m0, s50, 116736\n"
        "s_add_u32 s80, s68, 65536\n"
        "buffer_load_dwordx4 v143, s[40:43], s80 offen lds\n"
        "s_add_u32 m0, s50, 117760\n"
        "s_add_u32 s80, s68, 98304\n"
        "buffer_load_dwordx4 v143, s[40:43], s80 offen lds\n"
        "v_xor_b32 v142, 0x200000, v140\n"
        "v_xor_b32 v143, 0x200000, v141\n"
        "s_add_u32 m0, s50, 131072\n"
        "s_add_u32 s80, s67, 0\n"
        "buffer_load_dwordx4 v142, s[40:43], s80 offen lds\n"
        "s_add_u32 m0, s50, 132096\n"
        "s_add_u32 s80, s67, 32768\n"
        "buffer_load_dwordx4 v142, s[40:43], s80 offen lds\n"
        "s_add_u32 m0, s50, 133120\n"
        "s_add_u32 s80, s67, 65536\n"
        "buffer_load_dwordx4 v143, s[40:43], s80 offen lds\n"
        "s_add_u32 m0, s50, 134144\n"
        "s_add_u32 s80, s67, 98304\n"
        "buffer_load_dwordx4 v143, s[40:43], s80 offen lds\n"
        "v_add_u32 v132, 0, v136\n"
        "v_add_u32 v133, 0, v137\n"
        "s_waitcnt vmcnt(24)\n"
        "s_barrier\n"
        "ds_read_b64_tr_b16 v[208:209], v132 offset:0\n"
        "ds_read_b64_tr_b16 v[210:211], v132 offset:1024\n"
        "ds_read_b64_tr_b16 v[212:213], v132 offset:8192\n"
        "ds_read_b64_tr_b16 v[214:215], v132 offset:9216\n"
        "ds_read_b64_tr_b16 v[216:217], v133 offset:0\n"
        "ds_read_b64_tr_b16 v[218:219], v133 offset:1024\n"
        "ds_read_b64_tr_b16 v[220:221], v133 offset:8192\n"
        "ds_read_b64_tr_b16 v[222:223], v133 offset:9216\n"
        "ds_read_b64_tr_b16 v[224:225], v132 offset:16384\n"
        "ds_read_b64_tr_b16 v[226:227], v132 offset:17408\n"
        "ds_read_b64_tr_b16 v[228:229], v132 offset:24576\n"
        "ds_read_b64_tr_b16 v[230:231], v132 offset:25600\n"
        "ds_read_b64_tr_b16 v[232:233], v133 offset:16384\n"
        "ds_read_b64_tr_b16 v[234:235], v133 offset:17408\n"
        "ds_read_b64_tr_b16 v[236:237], v133 offset:24576\n"
        "ds_read_b64_tr_b16 v[238:239], v133 offset:25600\n"
        "ds_read_b64_tr_b16 v[240:241], v132 offset:32768\n"
        "ds_read_b64_tr_b16 v[242:243], v132 offset:33792\n"
        "ds_read_b64_tr_b16 v[244:245], v132 offset:40960\n"
        "ds_read_b64_tr_b16 v[246:247], v132 offset:41984\n"
        "ds_read_b64_tr_b16 v[248:249], v133 offset:32768\n"
        "ds_read_b64_tr_b16 v[250:251], v133 offset:33792\n"
        "ds_read_b64_tr_b16 v[252:253], v133 offset:40960\n"
        "ds_read_b64_tr_b16 v[254:255], v133 offset:41984\n"
        "v_add_u32 v132, 49152, v136\n"
        "v_add_u32 v133, 49152, v137\n"
        "s_waitcnt vmcnt(12)\n"
        "s_barrier\n"
        "ds_read_b64_tr_b16 v[0:1], v132 offset:0\n"
        "ds_read_b64_tr_b16 v[2:3], v132 offset:1024\n"
        "ds_read_b64_tr_b16 v[4:5], v132 offset:8192\n"
        "ds_read_b64_tr_b16 v[6:7], v132 offset:9216\n"
        "ds_read_b64_tr_b16 v[8:9], v133 offset:0\n"
        "ds_read_b64_tr_b16 v[10:11], v133 offset:1024\n"
        "ds_read_b64_tr_b16 v[12:13], v133 offset:8192\n"
        "ds_read_b64_tr_b16 v[14:15], v133 offset:9216\n"
        "ds_read_b64_tr_b16 v[16:17], v132 offset:16384\n"
        "ds_read_b64_tr_b16 v[18:19], v132 offset:17408\n"
        "ds_read_b64_tr_b16 v[20:21], v132 offset:24576\n"
        "ds_read_b64_tr_b16 v[22:23], v132 offset:25600\n"
        "ds_read_b64_tr_b16 v[24:25], v133 offset:16384\n"
        "ds_read_b64_tr_b16 v[26:27], v133 offset:17408\n"
        "ds_read_b64_tr_b16 v[28:29], v133 offset:24576\n"
        "ds_read_b64_tr_b16 v[30:31], v133 offset:25600\n"
        "ds_read_b64_tr_b16 v[32:33], v132 offset:32768\n"
        "ds_read_b64_tr_b16 v[34:35], v132 offset:33792\n"
        "ds_read_b64_tr_b16 v[36:37], v132 offset:40960\n"
        "ds_read_b64_tr_b16 v[38:39], v132 offset:41984\n"
        "ds_read_b64_tr_b16 v[40:41], v133 offset:32768\n"
        "ds_read_b64_tr_b16 v[42:43], v133 offset:33792\n"
        "ds_read_b64_tr_b16 v[44:45], v133 offset:40960\n"
        "ds_read_b64_tr_b16 v[46:47], v133 offset:41984\n"
        "v_add_u32 v132, 98304, v136\n"
        "v_add_u32 v133, 98304, v137\n"
        "s_waitcnt vmcnt(0)\n"
        "s_barrier\n"
        "ds_read_b64_tr_b16 v[48:49], v132 offset:0\n"
        "ds_read_b64_tr_b16 v[50:51], v132 offset:1024\n"
        "ds_read_b64_tr_b16 v[52:53], v132 offset:8192\n"
        "ds_read_b64_tr_b16 v[54:55], v132 offset:9216\n"
        "ds_read_b64_tr_b16 v[56:57], v133 offset:0\n"
        "ds_read_b64_tr_b16 v[58:59], v133 offset:1024\n"
        "ds_read_b64_tr_b16 v[60:61], v133 offset:8192\n"
        "ds_read_b64_tr_b16 v[62:63], v133 offset:9216\n"
        "ds_read_b64_tr_b16 v[64:65], v132 offset:16384\n"
        "ds_read_b64_tr_b16 v[66:67], v132 offset:17408\n"
        "ds_read_b64_tr_b16 v[68:69], v132 offset:24576\n"
        "ds_read_b64_tr_b16 v[70:71], v132 offset:25600\n"
        "ds_read_b64_tr_b16 v[72:73], v133 offset:16384\n"
        "ds_read_b64_tr_b16 v[74:75], v133 offset:17408\n"
        "ds_read_b64_tr_b16 v[76:77], v133 offset:24576\n"
        "ds_read_b64_tr_b16 v[78:79], v133 offset:25600\n"
        "ds_read_b64_tr_b16 v[80:81], v132 offset:32768\n"
        "ds_read_b64_tr_b16 v[82:83], v132 offset:33792\n"
        "ds_read_b64_tr_b16 v[84:85], v132 offset:40960\n"
        "ds_read_b64_tr_b16 v[86:87], v132 offset:41984\n"
        "ds_read_b64_tr_b16 v[88:89], v133 offset:32768\n"
        "ds_read_b64_tr_b16 v[90:91], v133 offset:33792\n"
        "ds_read_b64_tr_b16 v[92:93], v133 offset:40960\n"
        "ds_read_b64_tr_b16 v[94:95], v133 offset:41984\n"
        "s_waitcnt lgkmcnt(0)\n"
        "s_barrier\n"
        "s_lshl_b32 s80, s45, 15\n"
        "s_add_u32 s80, s80, 21504\n"
        "v_add_u32 v132, s80, v131\n"
        "v_mov_b32 v134, 0\n"
        "s_mov_b64 exec, 1\n"
        "buffer_atomic_add v134, off, s[36:39], %6 sc0\n"
        "s_mov_b64 exec, -1\n"
        "ds_write_b128 v132, v[0:3] offset:0\n"
        "ds_write_b128 v132, v[4:7] offset:1024\n"
        "ds_write_b128 v132, v[8:11] offset:2048\n"
        "ds_write_b128 v132, v[12:15] offset:3072\n"
        "ds_write_b128 v132, v[16:19] offset:4096\n"
        "ds_write_b128 v132, v[20:23] offset:5120\n"
        "ds_write_b128 v132, v[24:27] offset:6144\n"
        "ds_write_b128 v132, v[28:31] offset:7168\n"
        "ds_write_b128 v132, v[32:35] offset:8192\n"
        "ds_write_b128 v132, v[36:39] offset:9216\n"
        "ds_write_b128 v132, v[40:43] offset:10240\n"
        "ds_write_b128 v132, v[44:47] offset:11264\n"
        "ds_write_b128 v132, v[48:51] offset:12288\n"
        "ds_write_b128 v132, v[52:55] offset:13312\n"
        "ds_write_b128 v132, v[56:59] offset:14336\n"
        "ds_write_b128 v132, v[60:63] offset:15360\n"
        "ds_write_b128 v132, v[64:67] offset:16384\n"
        "ds_write_b128 v132, v[68:71] offset:17408\n"
        "ds_write_b128 v132, v[72:75] offset:18432\n"
        "ds_write_b128 v132, v[76:79] offset:19456\n"
        "ds_write_b128 v132, v[80:83] offset:20480\n"
        "ds_write_b128 v132, v[84:87] offset:21504\n"
        "ds_write_b128 v132, v[88:91] offset:22528\n"
        "ds_write_b128 v132, v[92:95] offset:23552\n"
        "ds_write_b128 v132, v[96:99] offset:24576\n"
        "ds_write_b128 v132, v[100:103] offset:25600\n"
        "ds_write_b128 v132, v[104:107] offset:26624\n"
        "ds_write_b128 v132, v[108:111] offset:27648\n"
        "ds_write_b128 v132, v[112:115] offset:28672\n"
        "ds_write_b128 v132, v[116:119] offset:29696\n"
        "ds_write_b128 v132, v[120:123] offset:30720\n"
        "ds_write_b128 v132, v[124:127] offset:31744\n"
        "s_waitcnt lgkmcnt(0)\n"
        "s_waitcnt lgkmcnt(0)\n"
        "L_hs%=:\n"
        "s_waitcnt vmcnt(0)\n"
        "v_readfirstlane_b32 s81, v134\n"
        "s_nop 0\n"
        "s_cmp_lg_u32 s81, 0\n"
        "s_cbranch_scc1 L_hs2%=\n"
        "s_add_u32 s63, s63, 1\n"
        "s_cmp_gt_u32 s63, 0x40000\n"
        "s_cbranch_scc1 L_abort%=\n"
        "s_sleep 4\n"
        "v_mov_b32 v134, 0\n"
        "s_mov_b64 exec, 1\n"
        "buffer_atomic_add v134, off, s[36:39], %6 sc0\n"
        "s_mov_b64 exec, -1\n"
        "s_branch L_hs%=\n"
        "L_hs2%=:\n"
        "v_mov_b32 v134, 2\n"
        "s_mov_b64 exec, 1\n"
        "buffer_atomic_swap v134, off, s[36:39], %6\n"
        "s_mov_b64 exec, -1\n"
        "v_mbcnt_lo_u32_b32 v0, -1, 0\n"
        "v_mbcnt_hi_u32_b32 v0, -1, v0\n"
        "v_and_b32 v1, 15, v0\n"
        "v_lshrrev_b32 v2, 4, v0\n"
        "v_lshlrev_b32 v124, 4, v0\n"
        "v_add_u32 v125, s80, v124\n"
        "s_mul_i32 s80, s45, 0\n"
        "s_add_u32 s80, s80, 0\n"
        "v_mov_b32 v138, s80\n"
        "v_bfe_u32 v3, v0, 0, 2\n"
        "s_mov_b32 s80, 16\n"
        "v_mad_u32_u24 v138, v3, s80, v138\n"
        "v_bfe_u32 v3, v0, 2, 3\n"
        "s_mov_b32 s80, 1024\n"
        "v_mad_u32_u24 v138, v3, s80, v138\n"
        "v_bfe_u32 v3, v0, 5, 1\n"
        "s_mov_b32 s80, 512\n"
        "v_mad_u32_u24 v138, v3, s80, v138\n"
        "s_mul_i32 s80, s45, 64\n"
        "s_add_u32 s80, s80, 9216\n"
        "v_mov_b32 v127, s80\n"
        "v_bfe_u32 v3, v0, 0, 2\n"
        "s_mov_b32 s80, 8\n"
        "v_mad_u32_u24 v127, v3, s80, v127\n"
        "v_bfe_u32 v3, v0, 2, 1\n"
        "s_mov_b32 s80, 4\n"
        "v_mad_u32_u24 v127, v3, s80, v127\n"
        "v_bfe_u32 v3, v0, 3, 1\n"
        "s_mov_b32 s80, 32\n"
        "v_mad_u32_u24 v127, v3, s80, v127\n"
        "v_bfe_u32 v3, v0, 4, 2\n"
        "s_mov_b32 s80, 576\n"
        "v_mad_u32_u24 v127, v3, s80, v127\n"
        "s_mul_i32 s80, s45, 2048\n"
        "s_add_u32 s80, s80, 0\n"
        "v_mov_b32 v126, s80\n"
        "v_bfe_u32 v3, v0, 0, 1\n"
        "s_mov_b32 s80, 8\n"
        "v_mad_u32_u24 v126, v3, s80, v126\n"
        "v_bfe_u32 v3, v0, 1, 1\n"
        "s_mov_b32 s80, 32\n"
        "v_mad_u32_u24 v126, v3, s80, v126\n"
        "v_bfe_u32 v3, v0, 2, 1\n"
        "s_mov_b32 s80, 16\n"
        "v_mad_u32_u24 v126, v3, s80, v126\n"
        "v_bfe_u32 v3, v0, 3, 3\n"
        "s_mov_b32 s80, 64\n"
        "v_mad_u32_u24 v126, v3, s80, v126\n"
        "s_mul_i32 s80, s45, 0\n"
        "s_add_u32 s80, s80, 9216\n"
        "v_mov_b32 v128, s80\n"
        "v_bfe_u32 v3, v0, 0, 1\n"
        "s_mov_b32 s80, 288\n"
        "v_mad_u32_u24 v128, v3, s80, v128\n"
        "v_bfe_u32 v3, v0, 2, 2\n"
        "s_mov_b32 s80, 576\n"
        "v_mad_u32_u24 v128, v3, s80, v128\n"
        "v_bfe_u32 v3, v0, 4, 2\n"
        "s_mov_b32 s80, 16\n"
        "v_mad_u32_u24 v128, v3, s80, v128\n"
        "s_mul_i32 s80, s45, 512\n"
        "s_add_u32 s80, s80, 11520\n"
        "v_mov_b32 v129, s80\n"
        "v_bfe_u32 v3, v0, 0, 2\n"
        "s_mov_b32 s80, 4\n"
        "v_mad_u32_u24 v129, v3, s80, v129\n"
        "v_bfe_u32 v3, v0, 2, 1\n"
        "s_mov_b32 s80, 16\n"
        "v_mad_u32_u24 v129, v3, s80, v129\n"
        "v_bfe_u32 v3, v0, 3, 1\n"
        "s_mov_b32 s80, 256\n"
        "v_mad_u32_u24 v129, v3, s80, v129\n"
        "v_bfe_u32 v3, v0, 4, 2\n"
        "s_mov_b32 s80, 64\n"
        "v_mad_u32_u24 v129, v3, s80, v129\n"
        "s_mul_i32 s80, s45, 512\n"
        "s_add_u32 s80, s80, 18432\n"
        "v_mov_b32 v139, s80\n"
        "v_bfe_u32 v3, v0, 0, 1\n"
        "s_mov_b32 s80, 32\n"
        "v_mad_u32_u24 v139, v3, s80, v139\n"
        "v_bfe_u32 v3, v0, 1, 2\n"
        "s_mov_b32 s80, 4\n"
        "v_mad_u32_u24 v139, v3, s80, v139\n"
        "v_bfe_u32 v3, v0, 3, 1\n"
        "s_mov_b32 s80, 256\n"
        "v_mad_u32_u24 v139, v3, s80, v139\n"
        "v_bfe_u32 v3, v0, 4, 2\n"
        "s_mov_b32 s80, 64\n"
        "v_mad_u32_u24 v139, v3, s80, v139\n"
        "s_mul_i32 s80, s45, 0\n"
        "s_add_u32 s80, s80, 11520\n"
        "v_mov_b32 v130, s80\n"
        "v_bfe_u32 v3, v0, 0, 1\n"
        "s_mov_b32 s80, 32\n"
        "v_mad_u32_u24 v130, v3, s80, v130\n"
        "v_bfe_u32 v3, v0, 1, 1\n"
        "s_mov_b32 s80, 16\n"
        "v_mad_u32_u24 v130, v3, s80, v130\n"
        "v_bfe_u32 v3, v0, 2, 1\n"
        "s_mov_b32 s80, 128\n"
        "v_mad_u32_u24 v130, v3, s80, v130\n"
        "v_bfe_u32 v3, v0, 3, 1\n"
        "s_mov_b32 s80, 64\n"
        "v_mad_u32_u24 v130, v3, s80, v130\n"
        "v_bfe_u32 v3, v0, 4, 2\n"
        "s_mov_b32 s80, 256\n"
        "v_mad_u32_u24 v130, v3, s80, v130\n"
        "s_mul_i32 s80, s45, 256\n"
        "s_add_u32 s80, s80, 13568\n"
        "v_mov_b32 v131, s80\n"
        "v_bfe_u32 v3, v0, 0, 1\n"
        "s_mov_b32 s80, 16\n"
        "v_mad_u32_u24 v131, v3, s80, v131\n"
        "v_bfe_u32 v3, v0, 2, 2\n"
        "s_mov_b32 s80, 64\n"
        "v_mad_u32_u24 v131, v3, s80, v131\n"
        "v_bfe_u32 v3, v0, 4, 1\n"
        "s_mov_b32 s80, 32\n"
        "v_mad_u32_u24 v131, v3, s80, v131\n"
        "v_bfe_u32 v3, v0, 5, 1\n"
        "s_mov_b32 s80, 8\n"
        "v_mad_u32_u24 v131, v3, s80, v131\n"
        "s_mul_i32 s80, s45, 0\n"
        "s_add_u32 s80, s80, 0\n"
        "v_mov_b32 v132, s80\n"
        "v_bfe_u32 v3, v0, 0, 1\n"
        "s_mov_b32 s80, 16\n"
        "v_mad_u32_u24 v132, v3, s80, v132\n"
        "v_bfe_u32 v3, v0, 1, 1\n"
        "s_mov_b32 s80, 256\n"
        "v_mad_u32_u24 v132, v3, s80, v132\n"
        "v_bfe_u32 v3, v0, 2, 2\n"
        "s_mov_b32 s80, 32\n"
        "v_mad_u32_u24 v132, v3, s80, v132\n"
        "v_bfe_u32 v3, v0, 4, 1\n"
        "s_mov_b32 s80, 512\n"
        "v_mad_u32_u24 v132, v3, s80, v132\n"
        "v_bfe_u32 v3, v0, 5, 1\n"
        "s_mov_b32 s80, 6144\n"
        "v_mad_u32_u24 v132, v3, s80, v132\n"
        "s_mul_i32 s80, s45, 0\n"
        "s_add_u32 s80, s80, 0\n"
        "v_mov_b32 v133, s80\n"
        "v_bfe_u32 v3, v0, 0, 3\n"
        "s_mov_b32 s80, 8\n"
        "v_mad_u32_u24 v133, v3, s80, v133\n"
        "v_bfe_u32 v3, v0, 3, 1\n"
        "s_mov_b32 s80, 512\n"
        "v_mad_u32_u24 v133, v3, s80, v133\n"
        "v_bfe_u32 v3, v0, 4, 2\n"
        "s_mov_b32 s80, 2048\n"
        "v_mad_u32_u24 v133, v3, s80, v133\n"
        "s_mul_i32 s80, s45, 256\n"
        "s_add_u32 s80, s80, 8192\n"
        "v_mov_b32 v137, s80\n"
        "v_bfe_u32 v3, v0, 0, 1\n"
        "s_mov_b32 s80, 32\n"
        "v_mad_u32_u24 v137, v3, s80, v137\n"
        "v_bfe_u32 v3, v0, 1, 1\n"
        "s_mov_b32 s80, 4\n"
        "v_mad_u32_u24 v137, v3, s80, v137\n"
        "v_bfe_u32 v3, v0, 2, 1\n"
        "s_mov_b32 s80, 16\n"
        "v_mad_u32_u24 v137, v3, s80, v137\n"
        "v_bfe_u32 v3, v0, 3, 2\n"
        "s_mov_b32 s80, 64\n"
        "v_mad_u32_u24 v137, v3, s80, v137\n"
        "v_bfe_u32 v3, v0, 5, 1\n"
        "s_mov_b32 s80, 8\n"
        "v_mad_u32_u24 v137, v3, s80, v137\n"
        "s_and_b32 s80, s47, 1\n"
        "v_bfe_u32 v3, v0, 2, 1\n"
        "v_xor_b32 v3, s80, v3\n"
        "v_mul_u32_u24 v3, 36, v3\n"
        "v_xor_b32 v137, v3, v137\n"
        "s_lshl_b32 s66, s80, 10\n"
        "v_xor_b32 v138, s66, v138\n"
        "s_mul_i32 s80, s45, 4\n"
        "s_add_u32 s80, s80, 20480\n"
        "v_mov_b32 v142, s80\n"
        "s_mul_i32 s80, s45, 0\n"
        "s_add_u32 s80, s80, 0\n"
        "v_mov_b32 v143, s80\n"
        "v_bfe_u32 v3, v0, 0, 6\n"
        "s_mov_b32 s80, 4\n"
        "v_mad_u32_u24 v143, v3, s80, v143\n"
        "v_cmp_gt_u32 vcc, 36, v143\n"
        "s_nop 1\n"
        "v_cndmask_b32 v143, 2.0, v143, vcc\n"
        "v_add_u32 v141, s84, v124\n"
        "s_mov_b32 s92, 0xcccccccc\n"
        "s_mov_b32 s93, 0xcccccccc\n"
        "s_lshl_b32 s82, s45, 12\n"
        "s_and_b32 s80, s45, 1\n"
        "s_mul_i32 s80, s80, -1408\n"
        "s_mul_i32 s100, s45, -4608\n"
        "s_add_u32 s100, s100, s80\n"
        "s_add_u32 s100, s100, 1024\n"
        "v_mov_b32 v3, 0x4444\n"
        "v_mov_b32 v4, 0xeeee\n"
        "v_cndmask_b32 v134, v3, v4, s[92:93]\n"
        "v_mov_b32 v3, 0xffff\n"
        "v_mov_b32 v4, 0xffff0000\n"
        "v_cndmask_b32 v3, v3, v4, s[92:93]\n"
        "v_cmp_gt_u32 vcc, 32, v0\n"
        "s_nop 1\n"
        "v_cndmask_b32 v135, 0, v3, vcc\n"
        "v_xor_b32 v136, v135, v3\n"
        "s_mul_i32 s80, s48, 81920\n"
        "s_add_u32 s80, s80, 884736\n"
        "s_lshl_b32 s81, s45, 14\n"
        "s_add_u32 s49, s80, s81\n"
        "s_lshl_b32 s81, s83, 13\n"
        "s_add_u32 s49, s49, s81\n"
        "s_and_b32 s81, s45, 1\n"
        "s_lshl_b32 s81, s81, 8\n"
        "s_add_u32 s50, s80, s81\n"
        "s_lshr_b32 s81, s45, 1\n"
        "s_lshl_b32 s81, s81, 13\n"
        "s_add_u32 s50, s50, s81\n"
        "s_add_u32 s50, s50, 65536\n"
        "s_add_u32 s80, s80, 163840\n"
        "s_lshl_b32 s69, s83, 11\n"
        "s_add_u32 s83, s80, s36\n"
        "s_mov_b32 s86, 0\n"
        "s_mov_b32 s34, 0\n"
        "s_mov_b32 s53, 0\n"
        "s_add_u32 s80, s80, s66\n"
        "s_lshr_b32 s81, s45, 1\n"
        "s_lshl1_add_u32 s81, s46, s81\n"
        "s_lshl_b32 s81, s81, 14\n"
        "s_add_u32 s51, s80, s81\n"
        "s_and_b32 s81, s45, 1\n"
        "s_lshl_b32 s81, s81, 8\n"
        "s_add_u32 s51, s51, s81\n"
        "s_and_b32 s81, s47, 2\n"
        "s_lshl_b32 s81, s81, 12\n"
        "s_add_u32 s51, s51, s81\n"
        "s_lshl_b32 s81, s46, 5\n"
        "s_add_u32 s81, s81, s45\n"
        "s_lshl_b32 s81, s81, 8\n"
        "s_add_u32 s81, s81, s80\n"
        "s_add_u32 s81, s81, 65536\n"
        "s_cmp_lt_u32 s45, 2\n"
        "s_cselect_b32 s52, s81, 0x40000000\n"
        "s_lshl1_add_u32 s81, s47, s46\n"
        "s_mov_b32 s99, s81\n"
        "s_mov_b32 s28, 0\n"
        "s_lshl_b32 s30, s81, 4\n"
        "s_mul_i32 s29, s48, 69632\n"
        "s_add_u32 s29, s29, 150528\n"
        "s_mul_i32 s80, s48, 2112\n"
        "s_add_u32 s80, s80, s47\n"
        "s_mul_i32 s81, s80, 24576\n"
        "s_mul_i32 s85, s45, 6144\n"
        "s_add_u32 s81, s81, s85\n"
        "s_add_u32 s81, s81, 102825984\n"
        "s_add_u32 s55, s81, 2048\n"
        "s_add_u32 s56, s81, s100\n"
        "s_add_u32 s64, s56, 12288\n"
        "s_mul_i32 s85, s46, 3072\n"
        "s_add_u32 s81, s81, s85\n"
        "s_add_u32 s54, s81, 786432\n"
        "s_lshl_b32 s80, s48, 11\n"
        "s_add_u32 s80, s80, s47\n"
        "s_lshl_b32 s80, s80, 12\n"
        "s_add_u32 s80, s80, 207945728\n"
        "s_bfe_u32 s81, %5, 0x20001\n"
        "s_cmp_eq_u32 s81, 1\n"
        "s_cselect_b32 s59, s80, 0x40000000\n"
        "s_add_u32 s80, s80, 2048\n"
        "s_mul_i32 s85, s45, 1536\n"
        "s_sub_u32 s80, s80, s85\n"
        "s_cmp_eq_u32 s46, 1\n"
        "s_cselect_b32 s60, s80, 0x40000000\n"
        "s_lshl_b32 s80, s48, 5\n"
        "s_add_u32 s80, s80, s47\n"
        "s_mul_i32 s80, s80, 544\n"
        "s_xor_b32 s81, s46, 1\n"
        "s_lshl1_add_u32 s80, s81, s80\n"
        "s_add_u32 s80, s80, s45\n"
        "s_lshl_b32 s80, s80, 2\n"
        "s_add_u32 s80, s80, 12288\n"
        "s_and_b32 s81, s59, s60\n"
        "s_bitcmp1_b32 s81, 30\n"
        "s_cselect_b32 s61, 0x40000000, s80\n"
        "s_lshl1_add_u32 s80, s48, s46\n"
        "s_lshl_b32 s57, s80, 13\n"
        "s_and_b32 s80, s47, 3\n"
        "s_lshl_b32 s80, s80, 8\n"
        "s_add_u32 s57, s57, s80\n"
        "s_add_u32 s57, s57, 884736\n"
        "s_mov_b32 s31, 0\n"
        "s_mul_i32 s80, s48, 2359296\n"
        "s_mul_i32 s81, s46, 18432\n"
        "s_add_u32 s81, s81, s82\n"
        "s_add_u32 s80, s80, s81\n"
        "s_add_u32 s58, s80, 16580608\n"
        "s_mov_b32 s82, s69\n"
        "s_mov_b32 s70, 0x41f40000\n"
        "s_mov_b32 s71, 0x41f40000\n"
        "s_mov_b32 s78, 0x41f40000\n"
        "s_mov_b32 s79, 0\n"
        "s_mov_b32 s44, -2\n"
        "s_waitcnt lgkmcnt(0)\n"
        "s_cmp_lg_u32 s45, 0\n"
        "s_cbranch_scc1 L_x1%=\n"
        "s_branch L_xf2%=\n"
        "L_xf1%=:\n"
        "s_add_u32 s63, s63, 1\n"
        "s_cmp_gt_u32 s63, 0x40000\n"
        "s_cbranch_scc1 L_abort%=\n"
        "s_sleep 2\n"
        "L_xf2%=:\n"
        "v_mov_b32 v140, 0\n"
        "buffer_atomic_add v140, v143, s[36:39], s57 offen sc0\n"
        "s_waitcnt vmcnt(0)\n"
        "v_cmp_ge_u32 vcc, s31, v140\n"
        "s_and_b32 vcc_lo, vcc_lo, 0x1ff\n"
        "s_cbranch_scc1 L_xf1%=\n"
        "L_x1%=:\n"
        "s_bitset1_b32 s49, 30\n"
        "s_bitset1_b32 s50, 30\n"
        "s_bitset1_b32 s56, 30\n"
        "s_bitset1_b32 s64, 30\n"
        "s_add_u32 s29, s29, 1024\n"
        "s_add_u32 s80, s29, s30\n"
        "v_mov_b32 v140, 1\n"
        "s_barrier\n"
        "s_memrealtime s[26:27]\n"
        "s_waitcnt lgkmcnt(0)\n"
        "s_sub_u32 s28, s28, s26\n"
        "s_cmp_eq_u32 s45, 0\n"
        "s_cbranch_scc1 L_x2_0%=\n"
        "s_cmp_eq_u32 s45, 1\n"
        "s_cbranch_scc1 L_x2_1%=\n"
        "s_cmp_eq_u32 s45, 2\n"
        "s_cbranch_scc1 L_x2_2%=\n"
        "s_cmp_eq_u32 s45, 3\n"
        "s_cbranch_scc1 L_x2_3%=\n"
        "L_light_0%=:\n"
        "v_mfma_f32_16x16x32_bf16 v[52:55], v[252:255], v[104:107], v[52:55]\n"
        "v_mfma_f32_16x16x32_bf16 v[84:87], v[96:99], a[112:115], v[84:87]\n"
        "v_mfma_f32_16x16x32_bf16 v[88:91], v[96:99], a[120:123], v[88:91]\n"
        "v_cvt_pk_bf16_f32 v48, v48, v52\n"
        "v_cvt_pk_bf16_f32 v49, v49, v53\n"
        "v_mfma_f32_16x16x32_bf16 v[84:87], v[100:103], a[116:119], v[84:87]\n"
        "v_cvt_pk_bf16_f32 v50, v50, v54\n"
        "v_cvt_pk_bf16_f32 v51, v51, v55\n"
        "v_mfma_f32_16x16x32_bf16 v[88:91], v[100:103], a[124:127], v[88:91]\n"
        "buffer_store_dwordx4 v[48:51], v124, s[36:39], s54 offen offset:1024 sc1\n"
        "v_mfma_f32_16x16x32_bf16 v[84:87], v[92:95], a[128:131], v[84:87]\n"
        "v_mfma_f32_16x16x32_bf16 v[88:91], v[92:95], a[136:139], v[88:91]\n"
        "v_mfma_f32_16x16x32_bf16 v[84:87], v[104:107], a[132:135], v[84:87]\n"
        "v_mfma_f32_16x16x32_bf16 v[88:91], v[104:107], a[140:143], v[88:91]\n"
        "v_mfma_f32_16x16x32_bf16 v[56:59], v[32:35], v[92:95], v[56:59]\n"
        "v_mfma_f32_16x16x32_bf16 v[60:63], v[40:43], v[92:95], v[60:63]\n"
        "v_cvt_pk_bf16_f32 v84, v84, v85\n"
        "v_cvt_pk_bf16_f32 v85, v86, v87\n"
        "v_mfma_f32_16x16x32_bf16 v[56:59], v[16:19], v[96:99], v[56:59]\n"
        "v_cvt_pk_bf16_f32 v86, v88, v89\n"
        "v_cvt_pk_bf16_f32 v87, v90, v91\n"
        "v_mfma_f32_16x16x32_bf16 v[60:63], v[24:27], v[96:99], v[60:63]\n"
        "buffer_store_dwordx4 v[84:87], v124, s[36:39], s54 offen offset:2048 sc1\n"
        "v_mfma_f32_16x16x32_bf16 v[56:59], v[20:23], v[100:103], v[56:59]\n"
        "v_mov_b64 v[92:93], 0\n"
        "v_mov_b64 v[94:95], 0\n"
        "v_mfma_f32_16x16x32_bf16 v[60:63], v[28:31], v[100:103], v[60:63]\n"
        "v_mov_b64 v[96:97], 0\n"
        "v_mov_b64 v[98:99], 0\n"
        "v_mfma_f32_16x16x32_bf16 v[56:59], v[36:39], v[104:107], v[56:59]\n"
        "v_mfma_f32_16x16x32_bf16 v[60:63], v[44:47], v[104:107], v[60:63]\n"
        "s_nop 7\n"
        "v_cvt_pk_bf16_f32 v100, v56, v60\n"
        "v_cvt_pk_bf16_f32 v101, v57, v61\n"
        "v_cvt_pk_bf16_f32 v102, v58, v62\n"
        "v_cvt_pk_bf16_f32 v103, v59, v63\n"
        "buffer_store_dwordx4 v[100:103], v124, s[36:39], s54 offen offset:0 sc1\n"
        "s_barrier\n"
        "s_add_u32 s69, s58, s82\n"
        "s_sub_u32 s72, s58, s82\n"
        "s_waitcnt vmcnt(6)\n"
        "buffer_load_dwordx4 v[64:67], v124, s[36:39], s69 offen offset:0 sc1\n"
        "s_waitcnt vmcnt(6)\n"
        "buffer_load_dwordx4 v[68:71], v124, s[36:39], s69 offen offset:1024 sc1\n"
        "s_waitcnt vmcnt(6)\n"
        "buffer_load_dwordx4 v[72:75], v124, s[36:39], s72 offen offset:2048 sc1\n"
        "s_waitcnt vmcnt(6)\n"
        "buffer_load_dwordx4 v[76:79], v124, s[36:39], s72 offen offset:3072 sc1\n"
        "s_mov_b32 s81, 64\n"
        "buffer_load_dword v107, off, s[36:39], s81 sc1\n"
        "s_memrealtime s[26:27]\n"
        "s_waitcnt vmcnt(5)\n"
        "s_waitcnt lgkmcnt(0)\n"
        "s_barrier\n"
        "s_add_u32 s29, s29, 1024\n"
        "s_waitcnt vmcnt(0)\n"
        "L_par_0%=:\n"
        "v_readfirstlane_b32 s80, v107\n"
        "s_nop 0\n"
        "s_cmp_lt_i32 s80, 0\n"
        "s_cbranch_scc1 L_par2_0%=\n"
        "s_add_u32 s63, s63, 1\n"
        "s_cmp_gt_u32 s63, 0x40000\n"
        "s_cbranch_scc1 L_abort%=\n"
        "s_sleep 4\n"
        "buffer_load_dword v107, off, s[36:39], s81 sc1\n"
        "s_waitcnt vmcnt(0)\n"
        "s_branch L_par_0%=\n"
        "L_par2_0%=:\n"
        "s_bfe_u32 s46, s80, 0x10018\n"
        "s_lshl_b32 s46, s46, 21\n"
        "s_mov_b32 s53, 0x200000\n"
        "s_bfe_u32 s84, s80, 0x80010\n"
        "s_sub_u32 s84, 255, s84\n"
        "s_bfe_u32 s76, s80, 0x80008\n"
        "s_sub_u32 s76, s76, 32\n"
        "s_max_i32 s76, s76, 66\n"
        "s_bfe_u32 s77, s80, 0x80000\n"
        "s_sub_u32 s77, s77, 158\n"
        "s_bitset0_b32 s49, 30\n"
        "s_bitset0_b32 s50, 30\n"
        "s_bitset0_b32 s56, 30\n"
        "s_bitset0_b32 s64, 30\n"
        "v_mov_b32 v140, 0\n"
        "s_mov_b32 s28, 1000\n"
        "s_branch L_xok_0%=\n"
        "L_x2_0%=:\n"
        "s_add_u32 s69, s58, s82\n"
        "s_sub_u32 s72, s58, s82\n"
        "buffer_load_dwordx4 v[64:67], v124, s[36:39], s69 offen offset:0 sc1\n"
        "buffer_load_dwordx4 v[68:71], v124, s[36:39], s69 offen offset:1024 sc1\n"
        "buffer_load_dwordx4 v[72:75], v124, s[36:39], s72 offen offset:2048 sc1\n"
        "buffer_load_dwordx4 v[76:79], v124, s[36:39], s72 offen offset:3072 sc1\n"
        "s_waitcnt vmcnt(0)\n"
        "s_memrealtime s[26:27]\n"
        "s_waitcnt lgkmcnt(0)\n"
        "s_add_u32 s28, s28, s26\n"
        "L_xok_0%=:\n"
        "v_mfma_f32_16x16x32_bf16 v[84:87], v[64:67], a[0:3], 0\n"
        "v_mfma_f32_16x16x32_bf16 v[88:91], v[64:67], a[8:11], 0\n"
        "v_mfma_f32_16x16x32_bf16 v[56:59], a[144:147], v[64:67], 0\n"
        "s_branch L_win_0%=\n"
        "L_retry_0%=:\n"
        "s_add_u32 s63, s63, 1\n"
        "s_cmp_gt_u32 s63, 0x40000\n"
        "s_cbranch_scc1 L_abort%=\n"
        "s_sleep 2\n"
        "s_waitcnt vmcnt(0)\n"
        "s_waitcnt lgkmcnt(0)\n"
        "buffer_load_dwordx4 v[80:83], v138, s[36:39], s50 offen offset:0 sc1\n"
        "buffer_load_dwordx4 v[64:67], v138, s[36:39], s49 offen offset:0 sc1\n"
        "buffer_load_dwordx4 v[68:71], v138, s[36:39], s49 offen offset:256 sc1\n"
        "buffer_load_dwordx4 v[72:75], v138, s[36:39], s65 offen offset:0 sc1\n"
        "buffer_load_dwordx4 v[76:79], v138, s[36:39], s65 offen offset:256 sc1\n"
        "s_waitcnt vmcnt(4)\n"
        "v_bitop3_b32 v123, 32, v80, v82 bitop3:0x80\n"
        "v_add_u32_dpp v80, v81, v80 row_half_mirror row_mask:0xf bank_mask:0xf\n"
        "v_add_u32_dpp v82, v83, v82 row_half_mirror row_mask:0xf bank_mask:0xf\n"
        "v_cvt_f32_i32 v80, v80\n"
        "v_cvt_f32_i32 v82, v82\n"
        "v_cvt_pk_bf16_f32 v52, v80, v82\n"
        "ds_write_b32 v137, v52\n"
        "s_waitcnt vmcnt(3)\n"
        "v_bitop3_b32 v123, v123, v64, v66 bitop3:0x80\n"
        "v_cvt_f32_i32 v64, v64\n"
        "v_cvt_f32_i32 v65, v65\n"
        "v_cvt_f32_i32 v66, v66\n"
        "v_cvt_f32_i32 v67, v67\n"
        "v_cvt_pk_bf16_f32 v52, v64, v65\n"
        "v_cvt_pk_bf16_f32 v53, v66, v67\n"
        "ds_write_b64 v126, v[52:53] offset:0\n"
        "s_waitcnt vmcnt(2)\n"
        "v_bitop3_b32 v123, v123, v68, v70 bitop3:0x80\n"
        "v_cvt_f32_i32 v68, v68\n"
        "v_cvt_f32_i32 v69, v69\n"
        "v_cvt_f32_i32 v70, v70\n"
        "v_cvt_f32_i32 v71, v71\n"
        "v_cvt_pk_bf16_f32 v54, v68, v69\n"
        "v_cvt_pk_bf16_f32 v55, v70, v71\n"
        "ds_write_b64 v126, v[54:55] offset:512\n"
        "s_waitcnt vmcnt(1)\n"
        "v_bitop3_b32 v123, v123, v72, v74 bitop3:0x80\n"
        "v_cvt_f32_i32 v72, v72\n"
        "v_cvt_f32_i32 v73, v73\n"
        "ds_read_b128 v[48:51], v124 offset:0\n"
        "v_cvt_f32_i32 v74, v74\n"
        "v_cvt_f32_i32 v75, v75\n"
        "v_cvt_pk_bf16_f32 v52, v72, v73\n"
        "v_cvt_pk_bf16_f32 v53, v74, v75\n"
        "ds_write_b64 v126, v[52:53] offset:1024\n"
        "s_waitcnt vmcnt(0)\n"
        "v_bitop3_b32 v123, v123, v76, v78 bitop3:0x80\n"
        "v_cmp_eq_u32 vcc, 0, v123\n"
        "v_mov_b64 v[122:123], 0\n"
        "v_cvt_f32_i32 v76, v76\n"
        "v_cvt_f32_i32 v77, v77\n"
        "v_cvt_f32_i32 v78, v78\n"
        "v_cvt_f32_i32 v79, v79\n"
        "v_cvt_pk_bf16_f32 v54, v76, v77\n"
        "v_cvt_pk_bf16_f32 v55, v78, v79\n"
        "ds_write_b64 v126, v[54:55] offset:1536\n"
        "v_mov_b64 v[92:93], 0\n"
        "v_mov_b64 v[94:95], 0\n"
        "v_mov_b64 v[96:97], 0\n"
        "v_mov_b64 v[98:99], 0\n"
        "s_waitcnt lgkmcnt(0)\n"
        "s_branch L_dec_0%=\n"
        "L_sw0_0%=:\n"
        "s_memrealtime s[26:27]\n"
        "s_waitcnt lgkmcnt(0)\n"
        "s_sub_u32 s28, s28, s26\n"
        "L_sw_0%=:\n"
        "s_add_u32 s63, s63, 1\n"
        "s_cmp_gt_u32 s63, 0x40000\n"
        "s_cbranch_scc1 L_abort%=\n"
        "s_sleep 2\n"
        "buffer_load_dword v140, v124, s[36:39], s29 offen sc1\n"
        "s_waitcnt vmcnt(0)\n"
        "v_cmp_eq_u32 vcc, 0, v140\n"
        "s_cbranch_vccnz L_sw_0%=\n"
        "s_memrealtime s[26:27]\n"
        "s_waitcnt lgkmcnt(0)\n"
        "s_add_u32 s28, s28, s26\n"
        "s_branch L_sok_0%=\n"
        ".p2align 6\n"
        "L_rot_0%=:\n"
        "v_mfma_f32_16x16x32_bf16 v[60:63], a[236:239], v[100:103], v[60:63]\n"
        "v_mfma_f32_16x16x32_bf16 v[48:51], v[196:199], v[100:103], v[48:51]\n"
        "v_mfma_f32_16x16x32_bf16 v[52:55], v[204:207], v[100:103], v[52:55]\n"
        "s_waitcnt lgkmcnt(8)\n"
        "v_mfma_f32_16x16x32_bf16 v[84:87], v[92:95], a[96:99], v[84:87]\n"
        "ds_read_b128 v[100:103], v124 offset:15360\n"
        "v_mfma_f32_16x16x32_bf16 v[88:91], v[92:95], a[104:107], v[88:91]\n"
        "v_mfma_f32_16x16x32_bf16 v[56:59], v[0:3], v[92:95], v[56:59]\n"
        "v_mfma_f32_16x16x32_bf16 v[60:63], v[8:11], v[92:95], v[60:63]\n"
        "v_mfma_f32_16x16x32_bf16 v[48:51], v[208:211], v[92:95], v[48:51]\n"
        "s_waitcnt lgkmcnt(0)\n"
        "s_barrier\n"
        "v_mfma_f32_16x16x32_bf16 v[52:55], v[216:219], v[92:95], v[52:55]\n"
        "v_mov_b64 v[116:117], 0\n"
        "v_mfma_f32_16x16x32_bf16 v[84:87], v[104:107], a[100:103], v[84:87]\n"
        "ds_read_b128 v[92:95], v124 offset:16384\n"
        "v_mov_b64 v[118:119], 0\n"
        "v_mfma_f32_16x16x32_bf16 v[88:91], v[104:107], a[108:111], v[88:91]\n"
        "v_mov_b64 v[120:121], 0\n"
        "v_mfma_f32_16x16x32_bf16 v[56:59], v[4:7], v[104:107], v[56:59]\n"
        "v_mfma_f32_16x16x32_bf16 v[60:63], v[12:15], v[104:107], v[60:63]\n"
        "v_mfma_f32_16x16x32_bf16 v[48:51], v[212:215], v[104:107], v[48:51]\n"
        "v_mfma_f32_16x16x32_bf16 v[52:55], v[220:223], v[104:107], v[52:55]\n"
        "v_mfma_f32_16x16x32_bf16 v[48:51], v[224:227], v[96:99], v[48:51]\n"
        "ds_read_b128 v[104:107], v124 offset:17408\n"
        "v_mfma_f32_16x16x32_bf16 v[52:55], v[232:235], v[96:99], v[52:55]\n"
        "v_mfma_f32_16x16x32_bf16 v[48:51], v[228:231], v[100:103], v[48:51]\n"
        "v_mfma_f32_16x16x32_bf16 v[52:55], v[236:239], v[100:103], v[52:55]\n"
        "s_waitcnt lgkmcnt(1)\n"
        "v_mfma_f32_16x16x32_bf16 v[48:51], v[240:243], v[92:95], v[48:51]\n"
        "v_mfma_f32_16x16x32_bf16 v[52:55], v[248:251], v[92:95], v[52:55]\n"
        "s_waitcnt lgkmcnt(0)\n"
        "v_mfma_f32_16x16x32_bf16 v[48:51], v[244:247], v[104:107], v[48:51]\n"
        "s_cmp_lt_i32 s44, 0\n"
        "s_cbranch_scc1 L_light_0%=\n"
        "v_mfma_f32_16x16x32_bf16 v[52:55], v[252:255], v[104:107], v[52:55]\n"
        "s_waitcnt vmcnt(4)\n"
        "v_bitop3_b32 v123, 32, v80, v82 bitop3:0x80\n"
        "s_nop 0\n"
        "v_add_u32_dpp v80, v81, v80 row_half_mirror row_mask:0xf bank_mask:0xf\n"
        "v_mfma_f32_16x16x32_bf16 v[84:87], v[96:99], a[112:115], v[84:87]\n"
        "s_nop 0\n"
        "v_add_u32_dpp v82, v83, v82 row_half_mirror row_mask:0xf bank_mask:0xf\n"
        "v_mfma_f32_16x16x32_bf16 v[88:91], v[96:99], a[120:123], v[88:91]\n"
        "v_cvt_pk_bf16_f32 v48, v48, v52\n"
        "v_cvt_pk_bf16_f32 v49, v49, v53\n"
        "s_nop 0\n"
        "v_mfma_f32_16x16x32_bf16 v[84:87], v[100:103], a[116:119], v[84:87]\n"
        "v_cvt_pk_bf16_f32 v50, v50, v54\n"
        "v_cvt_pk_bf16_f32 v51, v51, v55\n"
        "v_mfma_f32_16x16x32_bf16 v[88:91], v[100:103], a[124:127], v[88:91]\n"
        "buffer_store_dwordx4 v[48:51], v124, s[36:39], s54 offen offset:1024 sc1\n"
        "v_cvt_f32_i32 v80, v80\n"
        "v_mfma_f32_16x16x32_bf16 v[84:87], v[92:95], a[128:131], v[84:87]\n"
        "s_nop 0\n"
        "v_cvt_f32_i32 v82, v82\n"
        "v_mfma_f32_16x16x32_bf16 v[88:91], v[92:95], a[136:139], v[88:91]\n"
        "s_nop 0\n"
        "v_cvt_pk_bf16_f32 v52, v80, v82\n"
        "v_mfma_f32_16x16x32_bf16 v[84:87], v[104:107], a[132:135], v[84:87]\n"
        "s_nop 0\n"
        "v_mfma_f32_16x16x32_bf16 v[88:91], v[104:107], a[140:143], v[88:91]\n"
        "ds_write_b32 v137, v52\n"
        "s_waitcnt vmcnt(4)\n"
        "v_bitop3_b32 v123, v123, v64, v66 bitop3:0x80\n"
        "v_cvt_f32_i32 v64, v64\n"
        "v_mfma_f32_16x16x32_bf16 v[56:59], v[32:35], v[92:95], v[56:59]\n"
        "v_cvt_f32_i32 v65, v65\n"
        "v_cvt_f32_i32 v66, v66\n"
        "v_mfma_f32_16x16x32_bf16 v[60:63], v[40:43], v[92:95], v[60:63]\n"
        "v_cvt_pk_bf16_f32 v84, v84, v85\n"
        "v_cvt_pk_bf16_f32 v85, v86, v87\n"
        "v_mfma_f32_16x16x32_bf16 v[56:59], v[16:19], v[96:99], v[56:59]\n"
        "v_cvt_pk_bf16_f32 v86, v88, v89\n"
        "v_cvt_pk_bf16_f32 v87, v90, v91\n"
        "v_mfma_f32_16x16x32_bf16 v[60:63], v[24:27], v[96:99], v[60:63]\n"
        "buffer_store_dwordx4 v[84:87], v124, s[36:39], s54 offen offset:2048 sc1\n"
        "v_cvt_f32_i32 v67, v67\n"
        "v_cvt_pk_bf16_f32 v52, v64, v65\n"
        "v_mfma_f32_16x16x32_bf16 v[56:59], v[20:23], v[100:103], v[56:59]\n"
        "v_mov_b64 v[92:93], 0\n"
        "v_mov_b64 v[94:95], 0\n"
        "v_mfma_f32_16x16x32_bf16 v[60:63], v[28:31], v[100:103], v[60:63]\n"
        "v_mov_b64 v[96:97], 0\n"
        "v_mov_b64 v[98:99], 0\n"
        "v_mfma_f32_16x16x32_bf16 v[56:59], v[36:39], v[104:107], v[56:59]\n"
        "v_cvt_pk_bf16_f32 v53, v66, v67\n"
        "ds_write_b64 v126, v[52:53] offset:0\n"
        "s_waitcnt vmcnt(4)\n"
        "v_bitop3_b32 v123, v123, v68, v70 bitop3:0x80\n"
        "v_mfma_f32_16x16x32_bf16 v[60:63], v[44:47], v[104:107], v[60:63]\n"
        "v_cvt_f32_i32 v68, v68\n"
        "v_cvt_f32_i32 v69, v69\n"
        "v_cvt_f32_i32 v70, v70\n"
        "v_cvt_f32_i32 v71, v71\n"
        "v_cvt_pk_bf16_f32 v54, v68, v69\n"
        "v_cvt_pk_bf16_f32 v55, v70, v71\n"
        "ds_write_b64 v126, v[54:55] offset:512\n"
        "s_waitcnt vmcnt(3)\n"
        "v_bitop3_b32 v123, v123, v72, v74 bitop3:0x80\n"
        "v_cvt_f32_i32 v72, v72\n"
        "v_cvt_pk_bf16_f32 v100, v56, v60\n"
        "v_cvt_pk_bf16_f32 v101, v57, v61\n"
        "v_cvt_pk_bf16_f32 v102, v58, v62\n"
        "v_cvt_pk_bf16_f32 v103, v59, v63\n"
        "v_cvt_f32_i32 v73, v73\n"
        "ds_read_b128 v[48:51], v124 offset:0\n"
        "v_cvt_f32_i32 v74, v74\n"
        "v_cvt_f32_i32 v75, v75\n"
        "v_cvt_pk_bf16_f32 v52, v72, v73\n"
        "v_cvt_pk_bf16_f32 v53, v74, v75\n"
        "ds_write_b64 v126, v[52:53] offset:1024\n"
        "s_waitcnt vmcnt(2)\n"
        "v_bitop3_b32 v123, v123, v76, v78 bitop3:0x80\n"
        "v_cmp_eq_u32 vcc, 0, v123\n"
        "v_mov_b64 v[122:123], 0\n"
        "v_cvt_f32_i32 v76, v76\n"
        "v_cvt_f32_i32 v77, v77\n"
        "v_cvt_f32_i32 v78, v78\n"
        "v_cvt_f32_i32 v79, v79\n"
        "v_cvt_pk_bf16_f32 v54, v76, v77\n"
        "v_cvt_pk_bf16_f32 v55, v78, v79\n"
        "ds_write_b64 v126, v[54:55] offset:1536\n"
        "L_dec_0%=:\n"
        "s_waitcnt lgkmcnt(2)\n"
        "v_smfmac_f32_16x16x64_bf16 v[92:95], v[48:51], a[144:151], v134\n"
        "ds_read_b128 v[52:55], v124 offset:1024\n"
        "v_max3_f32 v104, |v80|, |v82|, |v64|\n"
        "v_smfmac_f32_16x16x64_bf16 v[96:99], v[48:51], a[152:159], v134\n"
        "s_cbranch_vccnz L_retry_0%=\n"
        "v_max3_f32 v104, |v65|, |v66|, v104\n"
        "v_smfmac_f32_16x16x64_bf16 v[116:119], v[48:51], a[240:247], v134\n"
        "buffer_load_dwordx4 v[88:91], v124, s[36:39], s55 offen offset:3072 sc1\n"
        "s_add_u32 s29, s29, 1024\n"
        "v_max3_f32 v104, |v67|, |v68|, v104\n"
        "v_smfmac_f32_16x16x64_bf16 v[120:123], v[48:51], a[248:255], v134\n"
        "s_waitcnt lgkmcnt(0)\n"
        "v_max3_f32 v104, |v69|, |v70|, v104\n"
        "s_barrier\n"
        "ds_read_b128 v[56:59], v124 offset:6144\n"
        "v_smfmac_f32_16x16x64_bf16 v[92:95], v[52:55], a[160:167], v134\n"
        "ds_read_b128 v[60:63], v124 offset:5120\n"
        "v_max3_f32 v104, |v71|, |v72|, v104\n"
        "v_smfmac_f32_16x16x64_bf16 v[96:99], v[52:55], a[168:175], v134\n"
        "buffer_load_dwordx4 v[84:87], v124, s[36:39], s55 offen offset:0 sc1\n"
        "ds_read_b128 v[80:83], v124 offset:7168\n"
        "v_max3_f32 v104, |v73|, |v74|, v104\n"
        "v_smfmac_f32_16x16x64_bf16 v[116:119], v[52:55], v[176:183], v134\n"
        "ds_read_b128 v[64:67], v124 offset:8192\n"
        "v_max3_f32 v104, |v75|, |v76|, v104\n"
        "v_smfmac_f32_16x16x64_bf16 v[120:123], v[52:55], v[184:191], v134\n"
        "ds_read_b128 v[68:71], v124 offset:2048\n"
        "v_max3_f32 v104, |v77|, |v78|, v104\n"
        "s_waitcnt lgkmcnt(4)\n"
        "v_smfmac_f32_16x16x64_bf16 v[92:95], v[56:59], v[0:7], v134\n"
        "ds_read_b128 v[72:75], v124 offset:3072\n"
        "v_max_f32_e64 v104, |v79|, v104\n"
        "v_smfmac_f32_16x16x64_bf16 v[96:99], v[56:59], v[8:15], v134\n"
        "ds_read_b128 v[0:3], v125 offset:12288\n"
        "v_max_f32_dpp v104, v104, v104 row_shr:1 row_mask:0xf bank_mask:0xf\n"
        "s_waitcnt lgkmcnt(3)\n"
        "v_smfmac_f32_16x16x64_bf16 v[92:95], v[60:63], a[224:231], v134\n"
        "ds_read_b128 v[76:79], v124 offset:4096\n"
        "v_max_f32_dpp v104, v104, v104 row_shr:2 row_mask:0xf bank_mask:0xf\n"
        "v_smfmac_f32_16x16x64_bf16 v[96:99], v[60:63], a[232:239], v134\n"
        "ds_read_b128 v[4:7], v125 offset:13312\n"
        "v_max_f32_dpp v104, v104, v104 row_shr:4 row_mask:0xf bank_mask:0xf\n"
        "v_smfmac_f32_16x16x64_bf16 v[92:95], v[80:83], v[16:23], v134\n"
        "ds_read_b128 v[8:11], v125 offset:14336\n"
        "v_max_f32_dpp v104, v104, v104 row_shr:8 row_mask:0xf bank_mask:0xf\n"
        "v_smfmac_f32_16x16x64_bf16 v[96:99], v[80:83], v[24:31], v134\n"
        "ds_read_b128 v[12:15], v125 offset:15360\n"
        "v_max_f32_dpp v104, v104, v104 row_bcast:15 row_mask:0xa bank_mask:0xf\n"
        "v_smfmac_f32_16x16x64_bf16 v[92:95], v[64:67], v[32:39], v134\n"
        "ds_read_b128 v[16:19], v125 offset:16384\n"
        "v_max_f32_dpp v104, v104, v104 row_bcast:31 row_mask:0xc bank_mask:0xf\n"
        "v_smfmac_f32_16x16x64_bf16 v[96:99], v[64:67], v[40:47], v134\n"
        "ds_read_b128 v[20:23], v125 offset:17408\n"
        "v_readlane_b32 s85, v104, 63\n"
        "s_waitcnt lgkmcnt(5)\n"
        "v_smfmac_f32_16x16x64_bf16 v[92:95], v[68:71], a[176:183], v134\n"
        "ds_read_b128 v[24:27], v125 offset:18432\n"
        "v_smfmac_f32_16x16x64_bf16 v[96:99], v[68:71], a[184:191], v134\n"
        "ds_read_b128 v[28:31], v125 offset:19456\n"
        "v_mov_b32 v104, s85\n"
        "v_smfmac_f32_16x16x64_bf16 v[92:95], v[72:75], a[192:199], v134\n"
        "ds_read_b128 v[32:35], v125 offset:20480\n"
        "v_smfmac_f32_16x16x64_bf16 v[96:99], v[72:75], a[200:207], v134\n"
        "ds_read_b128 v[36:39], v125 offset:21504\n"
        "s_add_u32 s69, s58, s82\n"
        "v_smfmac_f32_16x16x64_bf16 v[92:95], v[76:79], a[208:215], v134\n"
        "ds_read_b128 v[40:43], v125 offset:22528\n"
        "s_sub_u32 s72, s58, s82\n"
        "v_smfmac_f32_16x16x64_bf16 v[96:99], v[76:79], a[216:223], v134\n"
        "ds_read_b128 v[44:47], v125 offset:23552\n"
        "v_smfmac_f32_16x16x64_bf16 v[116:119], v[64:67], v[240:247], v134\n"
        "ds_write_b32 v142, v104\n"
        "v_add_f32 v92, v92, v94\n"
        "v_add_f32 v93, v93, v95\n"
        "v_smfmac_f32_16x16x64_bf16 v[120:123], v[64:67], v[248:255], v134\n"
        "v_add_f32 v96, v96, v98\n"
        "v_add_f32 v97, v97, v99\n"
        "v_cvt_pk_bf16_f32 v92, v92, v96\n"
        "v_cvt_pk_bf16_f32 v93, v93, v97\n"
        "s_waitcnt lgkmcnt(3)\n"
        "v_smfmac_f32_16x16x64_bf16 v[116:119], v[68:71], v[0:7], v134\n"
        "ds_write2_b32 v127, v92, v93 offset1:72\n"
        "v_smfmac_f32_16x16x64_bf16 v[120:123], v[68:71], v[8:15], v134\n"
        "buffer_store_dwordx4 v[100:103], v124, s[36:39], s54 offen offset:0 sc1\n"
        "v_mov_b64 v[98:99], 0\n"
        "v_smfmac_f32_16x16x64_bf16 v[116:119], v[72:75], v[16:23], v134\n"
        "v_mov_b64 v[96:97], 0\n"
        "v_smfmac_f32_16x16x64_bf16 v[120:123], v[72:75], v[24:31], v134\n"
        "s_waitcnt vmcnt(4)\n"
        "s_waitcnt lgkmcnt(0)\n"
        "v_mov_b64 v[106:107], 0\n"
        "s_barrier\n"
        "ds_read_b128 v[0:3], v128 offset:0\n"
        "v_smfmac_f32_16x16x64_bf16 v[116:119], v[76:79], v[32:39], v134\n"
        "ds_read_b128 v[24:27], v124 offset:20480\n"
        "v_mov_b64 v[104:105], 0\n"
        "v_smfmac_f32_16x16x64_bf16 v[120:123], v[76:79], v[40:47], v134\n"
        "ds_read_b128 v[4:7], v128 offset:64\n"
        "v_mov_b64 v[94:95], 0\n"
        "v_smfmac_f32_16x16x64_bf16 v[116:119], v[60:63], v[192:199], v134\n"
        "ds_read_b128 v[8:11], v128 offset:128\n"
        "v_mov_b64 v[92:93], 0\n"
        "v_smfmac_f32_16x16x64_bf16 v[120:123], v[60:63], v[200:207], v134\n"
        "ds_read_b128 v[12:15], v128 offset:192\n"
        "v_mov_b64 v[20:21], 0\n"
        "v_smfmac_f32_16x16x64_bf16 v[116:119], v[56:59], v[208:215], v134\n"
        "s_waitcnt lgkmcnt(3)\n"
        "v_max3_f32 v24, v24, v25, v26\n"
        "s_waitcnt lgkmcnt(2)\n"
        "v_smfmac_f32_16x16x64_bf16 v[104:107], v[108:111], v[0:7], v134\n"
        "v_max_f32 v24, v24, v27\n"
        "v_smfmac_f32_16x16x64_bf16 v[120:123], v[56:59], v[216:223], v134\n"
        "v_readfirstlane_b32 s84, v24\n"
        "s_waitcnt lgkmcnt(0)\n"
        "v_smfmac_f32_16x16x64_bf16 v[104:107], v[112:115], v[8:15], v134\n"
        "v_mov_b64 v[22:23], 0\n"
        "v_smfmac_f32_16x16x64_bf16 v[116:119], v[80:83], v[224:231], v134\n"
        "s_lshr_b32 s84, s84, 23\n"
        "s_add_u32 s84, s84, s101\n"
        "v_smfmac_f32_16x16x64_bf16 v[120:123], v[80:83], v[232:239], v134\n"
        "s_max_i32 s84, s84, s76\n"
        "s_min_i32 s84, s84, 186\n"
        "v_add_f32 v104, v104, v106\n"
        "v_add_f32 v105, v105, v107\n"
        "v_smfmac_f32_16x16x64_bf16 v[92:95], v[64:67], a[128:135], v134\n"
        "ds_write_b64 v131, v[104:105]\n"
        "v_add_f32 v116, v116, v118\n"
        "v_add_f32 v117, v117, v119\n"
        "v_pk_add_f32 v[120:121], v[120:121], v[122:123]\n"
        "v_cvt_pk_bf16_f32 v116, v116, v120\n"
        "v_cvt_pk_bf16_f32 v117, v117, v121\n"
        "ds_write2_b32 v129, v116, v117 offset1:8\n"
        "v_smfmac_f32_16x16x64_bf16 v[96:99], v[64:67], a[136:143], v134\n"
        "s_mul_i32 s88, s84, 0x800080\n"
        "s_mov_b32 s89, s88\n"
        "v_smfmac_f32_16x16x64_bf16 v[92:95], v[68:71], a[32:39], v134\n"
        "buffer_load_dwordx4 v[64:67], v124, s[36:39], s69 offen offset:0 sc1\n"
        "v_mov_b64 v[104:105], s[88:89]\n"
        "v_smfmac_f32_16x16x64_bf16 v[96:99], v[68:71], a[40:47], v134\n"
        "s_add_u32 s90, s88, s35\n"
        "s_mov_b32 s91, s90\n"
        "v_mov_b64 v[106:107], s[90:91]\n"
        "v_smfmac_f32_16x16x64_bf16 v[92:95], v[48:51], a[0:7], v134\n"
        "s_waitcnt lgkmcnt(0)\n"
        "v_mov_b64 v[100:101], s[88:89]\n"
        "s_barrier\n"
        "ds_read_b128 v[24:27], v130\n"
        "v_smfmac_f32_16x16x64_bf16 v[96:99], v[48:51], a[8:15], v134\n"
        "ds_read_b128 v[28:31], v130 offset:1024\n"
        "v_mov_b64 v[102:103], s[90:91]\n"
        "v_smfmac_f32_16x16x64_bf16 v[92:95], v[52:55], a[16:23], v134\n"
        "ds_read_b128 v[116:119], v124 offset:13568\n"
        "v_mov_b64 v[48:49], 0\n"
        "v_smfmac_f32_16x16x64_bf16 v[96:99], v[52:55], a[24:31], v134\n"
        "ds_read_b128 v[32:35], v141 offset:0\n"
        "v_mov_b64 v[50:51], 0\n"
        "v_smfmac_f32_16x16x64_bf16 v[92:95], v[56:59], a[96:103], v134\n"
        "ds_read_b128 v[36:39], v141 offset:1024\n"
        "v_mov_b64 v[52:53], 0\n"
        "v_smfmac_f32_16x16x64_bf16 v[96:99], v[56:59], a[104:111], v134\n"
        "ds_read_b128 v[40:43], v141 offset:2048\n"
        "v_mov_b64 v[54:55], 0\n"
        "s_waitcnt lgkmcnt(5)\n"
        "v_smfmac_f32_16x16x64_bf16 v[20:23], v[24:27], v[0:7], v134\n"
        "ds_read_b128 v[44:47], v141 offset:3072\n"
        "v_mov_b64 v[56:57], 0\n"
        "s_waitcnt lgkmcnt(4)\n"
        "v_smfmac_f32_16x16x64_bf16 v[20:23], v[28:31], v[8:15], v134\n"
        "buffer_load_dwordx4 v[68:71], v124, s[36:39], s69 offen offset:1024 sc1\n"
        "v_max3_f32 v121, v116, v117, v118\n"
        "v_smfmac_f32_16x16x64_bf16 v[92:95], v[72:75], a[48:55], v134\n"
        "ds_read_b128 v[0:3], v125 offset:24576\n"
        "v_mov_b64 v[58:59], 0\n"
        "v_smfmac_f32_16x16x64_bf16 v[96:99], v[72:75], a[56:63], v134\n"
        "buffer_load_dwordx4 v[72:75], v124, s[36:39], s72 offen offset:2048 sc1\n"
        "v_add_f32 v20, v20, v22\n"
        "v_add_f32 v21, v21, v23\n"
        "v_cndmask_b32 v120, v20, v21, s[92:93]\n"
        "v_mul_f32 v120, s74, v120\n"
        "v_max3_f32 v121, v121, v119, v120\n"
        "v_smfmac_f32_16x16x64_bf16 v[92:95], v[76:79], a[64:71], v134\n"
        "ds_read_b128 v[4:7], v125 offset:25600\n"
        "v_max_f32_dpp v121, v121, v121 quad_perm:[2,3,0,1] row_mask:0xf bank_mask:0xf\n"
        "v_mov_b32 v122, v121\n"
        "v_smfmac_f32_16x16x64_bf16 v[96:99], v[76:79], a[72:79], v134\n"
        "buffer_load_dwordx4 v[76:79], v124, s[36:39], s72 offen offset:3072 sc1\n"
        "v_permlane16_swap_b32 v121, v122\n"
        "v_max_f32 v121, v121, v122\n"
        "v_mov_b32 v122, v121\n"
        "v_smfmac_f32_16x16x64_bf16 v[92:95], v[80:83], a[112:119], v134\n"
        "ds_read_b128 v[8:11], v125 offset:26624\n"
        "v_permlane32_swap_b32 v121, v122\n"
        "v_smfmac_f32_16x16x64_bf16 v[96:99], v[80:83], a[120:127], v134\n"
        "v_max_f32 v121, v121, v122\n"
        "v_mul_f32_e64 v123, -s73, v121\n"
        "v_fma_f32 v120, v120, s73, v123\n"
        "v_fma_f32 v116, v116, s73, v123\n"
        "v_smfmac_f32_16x16x64_bf16 v[92:95], v[60:63], a[80:87], v134\n"
        "ds_read_b128 v[12:15], v125 offset:27648\n"
        "v_fma_f32 v117, v117, s73, v123\n"
        "v_fma_f32 v118, v118, s73, v123\n"
        "v_fma_f32 v119, v119, s73, v123\n"
        "v_exp_f32 v120, v120\n"
        "v_smfmac_f32_16x16x64_bf16 v[96:99], v[60:63], a[88:95], v134\n"
        "ds_read_b128 v[16:19], v125 offset:28672\n"
        "v_exp_f32 v116, v116\n"
        "v_mul_f32 v120, s74, v120\n"
        "v_exp_f32 v117, v117\n"
        "v_exp_f32 v118, v118\n"
        "v_exp_f32 v119, v119\n"
        "s_waitcnt vmcnt(5)\n"
        "v_mov_b64 v[108:109], v[86:87]\n"
        "v_mov_b64 v[112:113], v[90:91]\n"
        "v_cvt_pk_bf16_f32 v120, v120, v120\n"
        "v_cvt_pk_bf16_f32 v60, v116, v117\n"
        "v_cvt_pk_bf16_f32 v62, v118, v119\n"
        "v_and_b32 v61, v120, v135\n"
        "v_and_b32 v63, v120, v136\n"
        "v_pk_add_f32 v[92:93], v[92:93], v[94:95]\n"
        "v_pk_add_f32 v[96:97], v[96:97], v[98:99]\n"
        "v_smfmac_f32_16x16x64_bf16 v[56:59], v[60:63], v[100:107], v134\n"
        "ds_read_b128 v[20:23], v125 offset:29696\n"
        "v_cvt_pk_bf16_f32 v86, v92, v93\n"
        "v_cvt_pk_bf16_f32 v87, v92, v93\n"
        "v_mov_b64 v[90:91], v[86:87]\n"
        "v_cvt_pk_bf16_f32 v110, v96, v97\n"
        "v_cvt_pk_bf16_f32 v111, v96, v97\n"
        "v_smfmac_f32_16x16x64_bf16 v[48:51], v[60:63], v[84:91], v134\n"
        "ds_read_b128 v[24:27], v125 offset:30720\n"
        "v_mov_b64 v[114:115], v[110:111]\n"
        "v_add_f32 v56, v56, v58\n"
        "v_add_f32 v57, v57, v59\n"
        "v_smfmac_f32_16x16x64_bf16 v[52:55], v[60:63], v[108:115], v134\n"
        "ds_read_b128 v[28:31], v125 offset:31744\n"
        "s_nop 0\n"
        "v_rcp_f32 v56, v56\n"
        "v_rcp_f32 v57, v57\n"
        "v_pk_add_f32 v[48:49], v[48:49], v[50:51]\n"
        "v_pk_mul_f32 v[48:49], v[48:49], v[56:57]\n"
        "v_mov_b32 v110, v86\n"
        "v_mov_b64 v[92:93], s[70:71]\n"
        "v_pk_add_f32 v[52:53], v[52:53], v[54:55]\n"
        "v_pk_mul_f32 v[52:53], v[52:53], v[56:57]\n"
        "v_cvt_pk_bf16_f32 v48, v48, v52\n"
        "v_cvt_pk_bf16_f32 v49, v49, v53\n"
        "ds_write2_b32 v139, v48, v49 offset1:4\n"
        "v_mov_b64 v[94:95], 0\n"
        "buffer_store_dwordx2 v[110:111], v126, s[36:39], s60 offen sc1\n"
        "v_mov_b64 v[96:97], 0\n"
        "v_mov_b64 v[98:99], 0\n"
        "v_mov_b64 v[100:101], s[70:71]\n"
        "v_mov_b64 v[102:103], 0\n"
        "v_mov_b64 v[104:105], 0\n"
        "s_waitcnt lgkmcnt(0)\n"
        "v_mov_b64 v[106:107], 0\n"
        "s_barrier\n"
        "ds_read_b128 v[108:111], v124 offset:18432\n"
        "v_mov_b64 v[118:119], 0\n"
        "ds_read_b128 v[112:115], v124 offset:19456\n"
        "v_mov_b64 v[118:119], 0\n"
        "s_nop 0\n"
        "v_mov_b64 v[116:117], s[78:79]\n"
        "s_nop 0\n"
        "v_mov_b64 v[118:119], 0\n"
        "s_nop 0\n"
        "v_mov_b64 v[116:117], s[78:79]\n"
        "s_nop 0\n"
        "s_waitcnt lgkmcnt(1)\n"
        "v_smfmac_f32_16x16x64_bf16 v[116:119], v[108:111], v[32:39], v134\n"
        "s_waitcnt lgkmcnt(0)\n"
        "v_smfmac_f32_16x16x64_bf16 v[116:119], v[112:115], v[40:47], v134\n"
        "v_smfmac_f32_16x16x64_bf16 v[92:95], v[108:111], v[144:151], v134\n"
        "v_smfmac_f32_16x16x64_bf16 v[96:99], v[108:111], v[152:159], v134\n"
        "buffer_load_dword v140, v124, s[36:39], s29 offen sc1\n"
        "v_add_f32 v116, v116, v118\n"
        "v_add_f32 v117, v117, v119\n"
        "v_smfmac_f32_16x16x64_bf16 v[92:95], v[112:115], v[160:167], v134\n"
        "v_cvt_rpi_i32_f32 v118, v116\n"
        "v_bfi_b32 v118, 63, 2, v118\n"
        "v_cvt_rpi_i32_f32 v119, v117\n"
        "v_smfmac_f32_16x16x64_bf16 v[96:99], v[112:115], v[168:175], v134\n"
        "buffer_atomic_add_x2 v[118:119], v133, s[36:39], s52 offen\n"
        "v_smfmac_f32_16x16x64_bf16 v[100:103], v[108:111], v[0:7], v134\n"
        "v_add_f32 v92, v92, v94\n"
        "v_add_f32 v93, v93, v95\n"
        "v_cvt_rpi_i32_f32 v94, v92\n"
        "v_smfmac_f32_16x16x64_bf16 v[104:107], v[108:111], v[8:15], v134\n"
        "v_bfi_b32 v94, 63, 1, v94\n"
        "v_add_f32 v96, v96, v98\n"
        "v_smfmac_f32_16x16x64_bf16 v[100:103], v[112:115], v[16:23], v134\n"
        "v_cvt_rpi_i32_f32 v95, v96\n"
        "v_smfmac_f32_16x16x64_bf16 v[104:107], v[112:115], v[24:31], v134\n"
        "buffer_atomic_add_x2 v[94:95], v133, s[36:39], s51 offen\n"
        "v_add_f32 v97, v97, v99\n"
        "v_cvt_rpi_i32_f32 v98, v93\n"
        "v_bfi_b32 v98, 63, 1, v98\n"
        "v_cvt_rpi_i32_f32 v99, v97\n"
        "s_waitcnt vmcnt(7)\n"
        "v_mfma_f32_16x16x32_bf16 v[84:87], v[64:67], a[0:3], 0\n"
        "buffer_atomic_add_x2 v[98:99], v133, s[36:39], s66 offen\n"
        "v_add_f32 v100, v100, v102\n"
        "v_add_f32 v101, v101, v103\n"
        "v_add_f32 v104, v104, v106\n"
        "v_cvt_rpi_i32_f32 v102, v100\n"
        "v_bfi_b32 v102, 63, 1, v102\n"
        "v_cvt_rpi_i32_f32 v103, v104\n"
        "v_mfma_f32_16x16x32_bf16 v[88:91], v[64:67], a[8:11], 0\n"
        "buffer_atomic_add_x2 v[102:103], v133, s[36:39], s67 offen\n"
        "v_add_f32 v105, v105, v107\n"
        "v_cvt_rpi_i32_f32 v106, v101\n"
        "v_bfi_b32 v106, 63, 1, v106\n"
        "v_cvt_rpi_i32_f32 v107, v105\n"
        "v_mfma_f32_16x16x32_bf16 v[56:59], a[144:147], v[64:67], 0\n"
        "buffer_atomic_add_x2 v[106:107], v133, s[36:39], s68 offen\n"
        "L_win_0%=:\n"
        "v_mfma_f32_16x16x32_bf16 v[60:63], a[152:155], v[64:67], 0\n"
        "v_mfma_f32_16x16x32_bf16 v[48:51], a[240:243], v[64:67], 0\n"
        "ds_write_b128 v124, v[64:67] offset:0\n"
        "v_mfma_f32_16x16x32_bf16 v[52:55], a[248:251], v[64:67], 0\n"
        "s_waitcnt vmcnt(9)\n"
        "v_mfma_f32_16x16x32_bf16 v[84:87], v[68:71], a[4:7], v[84:87]\n"
        "ds_write_b128 v124, v[68:71] offset:1024\n"
        "v_mfma_f32_16x16x32_bf16 v[88:91], v[68:71], a[12:15], v[88:91]\n"
        "v_mfma_f32_16x16x32_bf16 v[56:59], a[148:151], v[68:71], v[56:59]\n"
        "s_waitcnt vmcnt(8)\n"
        "ds_write_b128 v124, v[72:75] offset:2048\n"
        "v_mfma_f32_16x16x32_bf16 v[60:63], a[156:159], v[68:71], v[60:63]\n"
        "v_mfma_f32_16x16x32_bf16 v[48:51], a[244:247], v[68:71], v[48:51]\n"
        "s_waitcnt vmcnt(7)\n"
        "ds_write_b128 v124, v[76:79] offset:3072\n"
        "v_mfma_f32_16x16x32_bf16 v[52:55], a[252:255], v[68:71], v[52:55]\n"
        "v_mfma_f32_16x16x32_bf16 v[84:87], v[72:75], a[16:19], v[84:87]\n"
        "v_mfma_f32_16x16x32_bf16 v[88:91], v[72:75], a[24:27], v[88:91]\n"
        "v_mfma_f32_16x16x32_bf16 v[56:59], a[160:163], v[72:75], v[56:59]\n"
        "v_mfma_f32_16x16x32_bf16 v[60:63], a[168:171], v[72:75], v[60:63]\n"
        "v_mfma_f32_16x16x32_bf16 v[48:51], v[176:179], v[72:75], v[48:51]\n"
        "ds_read_b128 v[0:3], v125 offset:12288\n"
        "v_mfma_f32_16x16x32_bf16 v[52:55], v[184:187], v[72:75], v[52:55]\n"
        "ds_read_b128 v[8:11], v125 offset:14336\n"
        "v_mfma_f32_16x16x32_bf16 v[84:87], v[76:79], a[20:23], v[84:87]\n"
        "s_waitcnt lgkmcnt(2)\n"
        "s_barrier\n"
        "v_mfma_f32_16x16x32_bf16 v[88:91], v[76:79], a[28:31], v[88:91]\n"
        "ds_read_b128 v[92:95], v124 offset:4096\n"
        "v_mov_b32 v116, s62\n"
        "v_mfma_f32_16x16x32_bf16 v[56:59], a[164:167], v[76:79], v[56:59]\n"
        "s_add_u32 s49, s49, 163840\n"
        "ds_read_b128 v[104:107], v124 offset:5120\n"
        "v_mfma_f32_16x16x32_bf16 v[60:63], a[172:175], v[76:79], v[60:63]\n"
        "s_add_u32 s50, s50, 163840\n"
        "ds_read_b128 v[4:7], v125 offset:13312\n"
        "v_mfma_f32_16x16x32_bf16 v[48:51], v[180:183], v[76:79], v[48:51]\n"
        "s_xor_b32 s49, s49, s86\n"
        "ds_read_b128 v[12:15], v125 offset:15360\n"
        "v_mfma_f32_16x16x32_bf16 v[52:55], v[188:191], v[76:79], v[52:55]\n"
        "s_xor_b32 s65, s49, 8192\n"
        "s_add_u32 s56, s56, 786432\n"
        "s_waitcnt lgkmcnt(3)\n"
        "v_mfma_f32_16x16x32_bf16 v[84:87], v[92:95], a[32:35], v[84:87]\n"
        "s_add_u32 s64, s64, 786432\n"
        "ds_read_b128 v[96:99], v124 offset:6144\n"
        "v_mfma_f32_16x16x32_bf16 v[88:91], v[92:95], a[40:43], v[88:91]\n"
        "ds_read_b128 v[100:103], v124 offset:7168\n"
        "v_mfma_f32_16x16x32_bf16 v[56:59], a[176:179], v[92:95], v[56:59]\n"
        "ds_read_b128 v[16:19], v125 offset:16384\n"
        "v_mfma_f32_16x16x32_bf16 v[60:63], a[184:187], v[92:95], v[60:63]\n"
        "s_add_u32 s51, s51, 163840\n"
        "ds_read_b128 v[20:23], v125 offset:17408\n"
        "v_mfma_f32_16x16x32_bf16 v[48:51], v[0:3], v[92:95], v[48:51]\n"
        "s_add_u32 s52, s52, 163840\n"
        "ds_read_b128 v[24:27], v125 offset:18432\n"
        "v_mfma_f32_16x16x32_bf16 v[52:55], v[8:11], v[92:95], v[52:55]\n"
        "ds_read_b128 v[28:31], v125 offset:19456\n"
        "s_waitcnt lgkmcnt(8)\n"
        "v_mfma_f32_16x16x32_bf16 v[84:87], v[104:107], a[36:39], v[84:87]\n"
        "s_add_u32 s83, s83, 163840\n"
        "ds_read_b128 v[92:95], v124 offset:8192\n"
        "v_mfma_f32_16x16x32_bf16 v[88:91], v[104:107], a[44:47], v[88:91]\n"
        "s_waitcnt vmcnt(6)\n"
        "s_mov_b64 exec, 1\n"
        "buffer_atomic_umax v116, off, s[36:39], s61\n"
        "s_mov_b64 exec, -1\n"
        "v_mfma_f32_16x16x32_bf16 v[56:59], a[180:183], v[104:107], v[56:59]\n"
        "s_cmp_eq_u32 s44, 63\n"
        "s_cbranch_scc1 L_end%=\n"
        "v_mfma_f32_16x16x32_bf16 v[60:63], a[188:191], v[104:107], v[60:63]\n"
        "s_xor_b32 s86, s83, s46\n"
        "ds_read_b128 v[32:35], v125 offset:20480\n"
        "s_waitcnt lgkmcnt(9)\n"
        "v_mfma_f32_16x16x32_bf16 v[48:51], v[4:7], v[104:107], v[48:51]\n"
        "s_and_b32 s86, s86, s53\n"
        "ds_read_b128 v[36:39], v125 offset:21504\n"
        "s_waitcnt lgkmcnt(9)\n"
        "v_mfma_f32_16x16x32_bf16 v[52:55], v[12:15], v[104:107], v[52:55]\n"
        "ds_read_b128 v[40:43], v125 offset:22528\n"
        "s_waitcnt lgkmcnt(9)\n"
        "v_mfma_f32_16x16x32_bf16 v[84:87], v[96:99], a[48:51], v[84:87]\n"
        "s_lshr_b32 s86, s86, 8\n"
        "ds_read_b128 v[104:107], v124 offset:9216\n"
        "v_mfma_f32_16x16x32_bf16 v[88:91], v[96:99], a[56:59], v[88:91]\n"
        "s_xor_b32 s86, s86, s34\n"
        "ds_read_b128 v[44:47], v125 offset:23552\n"
        "v_mfma_f32_16x16x32_bf16 v[56:59], a[192:195], v[96:99], v[56:59]\n"
        "s_xor_b32 s34, s34, s86\n"
        "ds_read_b128 v[0:3], v125 offset:0\n"
        "v_mfma_f32_16x16x32_bf16 v[60:63], a[200:203], v[96:99], v[60:63]\n"
        "ds_read_b128 v[4:7], v125 offset:1024\n"
        "s_waitcnt vmcnt(6)\n"
        "v_cmp_eq_u32 vcc, 0, v140\n"
        "s_waitcnt lgkmcnt(11)\n"
        "v_mfma_f32_16x16x32_bf16 v[48:51], v[16:19], v[96:99], v[48:51]\n"
        "s_cbranch_vccnz L_sw0_0%=\n"
        "L_sok_0%=:\n"
        "s_waitcnt lgkmcnt(9)\n"
        "v_mfma_f32_16x16x32_bf16 v[52:55], v[24:27], v[96:99], v[52:55]\n"
        "v_readlane_b32 s81, v140, s99\n"
        "v_max_u32_dpp v140, v140, v140 row_shr:1 row_mask:0xf bank_mask:0xf\n"
        "v_mfma_f32_16x16x32_bf16 v[84:87], v[100:103], a[52:55], v[84:87]\n"
        "ds_read_b128 v[96:99], v124 offset:10240\n"
        "v_max_u32_dpp v140, v140, v140 row_shr:2 row_mask:0xf bank_mask:0xf\n"
        "v_mfma_f32_16x16x32_bf16 v[88:91], v[100:103], a[60:63], v[88:91]\n"
        "buffer_load_dwordx4 v[108:111], v132, s[36:39], s56 offen offset:0 sc1\n"
        "v_max_u32_dpp v140, v140, v140 row_shr:4 row_mask:0xf bank_mask:0xf\n"
        "v_mfma_f32_16x16x32_bf16 v[56:59], a[196:199], v[100:103], v[56:59]\n"
        "ds_read_b128 v[8:11], v125 offset:2048\n"
        "v_max_u32_dpp v140, v140, v140 row_shr:8 row_mask:0xf bank_mask:0xf\n"
        "v_mfma_f32_16x16x32_bf16 v[60:63], a[204:207], v[100:103], v[60:63]\n"
        "buffer_load_dwordx4 v[112:115], v132, s[36:39], s64 offen offset:0 sc1\n"
        "v_max_u32_dpp v140, v140, v140 row_bcast:15 row_mask:0xa bank_mask:0xf\n"
        "v_mfma_f32_16x16x32_bf16 v[48:51], v[20:23], v[100:103], v[48:51]\n"
        "ds_read_b128 v[12:15], v125 offset:3072\n"
        "v_max_u32_dpp v140, v140, v140 row_bcast:31 row_mask:0xc bank_mask:0xf\n"
        "s_waitcnt lgkmcnt(11)\n"
        "v_mfma_f32_16x16x32_bf16 v[52:55], v[28:31], v[100:103], v[52:55]\n"
        "v_readlane_b32 s33, v140, 63\n"
        "s_lshl1_add_u32 s28, s28, s81\n"
        "s_waitcnt lgkmcnt(10)\n"
        "v_mfma_f32_16x16x32_bf16 v[84:87], v[92:95], a[64:67], v[84:87]\n"
        "ds_read_b128 v[100:103], v124 offset:11264\n"
        "s_add_u32 s28, s28, 36\n"
        "s_sub_i32 s33, s33, s28\n"
        "v_mfma_f32_16x16x32_bf16 v[88:91], v[92:95], a[72:75], v[88:91]\n"
        "s_min_i32 s33, s33, 500\n"
        "s_mov_b32 s28, 0\n"
        "L_dl_0%=:\n"
        "s_sub_i32 s33, s33, 3\n"
        "s_cmp_gt_i32 s33, 0\n"
        "s_cbranch_scc1 L_dl_0%=\n"
        "v_mfma_f32_16x16x32_bf16 v[56:59], a[208:211], v[92:95], v[56:59]\n"
        "s_xor_b32 s51, s51, s86\n"
        "buffer_load_dwordx4 v[80:83], v138, s[36:39], s50 offen offset:0 sc1\n"
        "v_mfma_f32_16x16x32_bf16 v[60:63], a[216:219], v[92:95], v[60:63]\n"
        "s_add_u32 s55, s55, 786432\n"
        "ds_read_b128 v[16:19], v125 offset:4096\n"
        "s_waitcnt lgkmcnt(11)\n"
        "v_mfma_f32_16x16x32_bf16 v[48:51], v[32:35], v[92:95], v[48:51]\n"
        "s_add_u32 s54, s54, 786432\n"
        "buffer_load_dwordx4 v[64:67], v138, s[36:39], s49 offen offset:0 sc1\n"
        "s_waitcnt lgkmcnt(9)\n"
        "v_mfma_f32_16x16x32_bf16 v[52:55], v[40:43], v[92:95], v[52:55]\n"
        "ds_read_b128 v[20:23], v125 offset:5120\n"
        "s_waitcnt lgkmcnt(9)\n"
        "v_mfma_f32_16x16x32_bf16 v[84:87], v[104:107], a[68:71], v[84:87]\n"
        "s_add_u32 s60, s60, 131072\n"
        "ds_read_b128 v[92:95], v124 offset:12288\n"
        "v_mfma_f32_16x16x32_bf16 v[88:91], v[104:107], a[76:79], v[88:91]\n"
        "s_add_u32 s58, s58, 36864\n"
        "buffer_load_dwordx4 v[68:71], v138, s[36:39], s49 offen offset:256 sc1\n"
        "v_mfma_f32_16x16x32_bf16 v[56:59], a[212:215], v[104:107], v[56:59]\n"
        "s_xor_b32 s66, s51, 1024\n"
        "ds_read_b128 v[24:27], v125 offset:6144\n"
        "v_mfma_f32_16x16x32_bf16 v[60:63], a[220:223], v[104:107], v[60:63]\n"
        "s_xor_b32 s67, s51, 8192\n"
        "buffer_load_dwordx4 v[72:75], v138, s[36:39], s65 offen offset:0 sc1\n"
        "v_mfma_f32_16x16x32_bf16 v[48:51], v[36:39], v[104:107], v[48:51]\n"
        "s_xor_b32 s68, s51, 9216\n"
        "ds_read_b128 v[28:31], v125 offset:7168\n"
        "s_waitcnt lgkmcnt(11)\n"
        "v_mfma_f32_16x16x32_bf16 v[52:55], v[44:47], v[104:107], v[52:55]\n"
        "buffer_load_dwordx4 v[76:79], v138, s[36:39], s65 offen offset:256 sc1\n"
        "s_waitcnt lgkmcnt(8)\n"
        "v_mfma_f32_16x16x32_bf16 v[84:87], v[96:99], a[80:83], v[84:87]\n"
        "s_sub_u32 s75, 127, s84\n"
        "ds_read_b128 v[104:107], v124 offset:13312\n"
        "v_mfma_f32_16x16x32_bf16 v[88:91], v[96:99], a[88:91], v[88:91]\n"
        "s_sub_u32 s101, s77, s75\n"
        "ds_read_b128 v[32:35], v125 offset:8192\n"
        "v_mfma_f32_16x16x32_bf16 v[56:59], a[224:227], v[96:99], v[56:59]\n"
        "s_mul_i32 s35, s75, 0x800080\n"
        "ds_read_b128 v[36:39], v125 offset:9216\n"
        "v_mfma_f32_16x16x32_bf16 v[60:63], a[232:235], v[96:99], v[60:63]\n"
        "s_lshl_b32 s80, s75, 23\n"
        "ds_read_b128 v[40:43], v125 offset:10240\n"
        "v_mfma_f32_16x16x32_bf16 v[48:51], v[192:195], v[96:99], v[48:51]\n"
        "s_sub_u32 s73, 0x3e0293ee, s80\n"
        "ds_read_b128 v[44:47], v125 offset:11264\n"
        "v_mfma_f32_16x16x32_bf16 v[52:55], v[200:203], v[96:99], v[52:55]\n"
        "s_sub_u32 s74, 0x3f800000, s80\n"
        "s_add_u32 s80, s75, 128\n"
        "s_waitcnt lgkmcnt(10)\n"
        "v_mfma_f32_16x16x32_bf16 v[84:87], v[100:103], a[84:87], v[84:87]\n"
        "s_lshl_b32 s80, s80, 8\n"
        "ds_read_b128 v[96:99], v124 offset:14336\n"
        "v_mfma_f32_16x16x32_bf16 v[88:91], v[100:103], a[92:95], v[88:91]\n"
        "s_add_u32 s44, s44, 1\n"
        "s_add_u32 s61, s61, 32\n"
        "s_add_u32 s62, s80, 1\n"
        "v_mfma_f32_16x16x32_bf16 v[56:59], a[228:231], v[100:103], v[56:59]\n"
        "s_branch L_rot_0%=\n"
        "L_light_1%=:\n"
        "v_mfma_f32_16x16x32_bf16 v[52:55], v[252:255], v[104:107], v[52:55]\n"
        "v_mfma_f32_16x16x32_bf16 v[84:87], v[96:99], a[112:115], v[84:87]\n"
        "v_mfma_f32_16x16x32_bf16 v[88:91], v[96:99], a[120:123], v[88:91]\n"
        "v_cvt_pk_bf16_f32 v48, v48, v52\n"
        "v_cvt_pk_bf16_f32 v49, v49, v53\n"
        "v_mfma_f32_16x16x32_bf16 v[84:87], v[100:103], a[116:119], v[84:87]\n"
        "v_cvt_pk_bf16_f32 v50, v50, v54\n"
        "v_cvt_pk_bf16_f32 v51, v51, v55\n"
        "v_mfma_f32_16x16x32_bf16 v[88:91], v[100:103], a[124:127], v[88:91]\n"
        "v_mfma_f32_16x16x32_bf16 v[84:87], v[92:95], a[128:131], v[84:87]\n"
        "buffer_store_dwordx4 v[48:51], v124, s[36:39], s54 offen offset:1024 sc1\n"
        "v_mfma_f32_16x16x32_bf16 v[88:91], v[92:95], a[136:139], v[88:91]\n"
        "v_mfma_f32_16x16x32_bf16 v[84:87], v[104:107], a[132:135], v[84:87]\n"
        "v_mfma_f32_16x16x32_bf16 v[88:91], v[104:107], a[140:143], v[88:91]\n"
        "v_mfma_f32_16x16x32_bf16 v[56:59], v[32:35], v[92:95], v[56:59]\n"
        "v_mfma_f32_16x16x32_bf16 v[60:63], v[40:43], v[92:95], v[60:63]\n"
        "v_cvt_pk_bf16_f32 v84, v84, v85\n"
        "v_cvt_pk_bf16_f32 v85, v86, v87\n"
        "v_mfma_f32_16x16x32_bf16 v[56:59], v[16:19], v[96:99], v[56:59]\n"
        "v_cvt_pk_bf16_f32 v86, v88, v89\n"
        "v_cvt_pk_bf16_f32 v87, v90, v91\n"
        "v_mfma_f32_16x16x32_bf16 v[60:63], v[24:27], v[96:99], v[60:63]\n"
        "v_mfma_f32_16x16x32_bf16 v[56:59], v[20:23], v[100:103], v[56:59]\n"
        "buffer_store_dwordx4 v[84:87], v124, s[36:39], s54 offen offset:2048 sc1\n"
        "v_mov_b64 v[92:93], 0\n"
        "v_mfma_f32_16x16x32_bf16 v[60:63], v[28:31], v[100:103], v[60:63]\n"
        "v_mov_b64 v[94:95], 0\n"
        "v_mov_b64 v[96:97], 0\n"
        "v_mfma_f32_16x16x32_bf16 v[56:59], v[36:39], v[104:107], v[56:59]\n"
        "v_mov_b64 v[98:99], 0\n"
        "v_mfma_f32_16x16x32_bf16 v[60:63], v[44:47], v[104:107], v[60:63]\n"
        "s_nop 7\n"
        "v_cvt_pk_bf16_f32 v100, v56, v60\n"
        "v_cvt_pk_bf16_f32 v101, v57, v61\n"
        "v_cvt_pk_bf16_f32 v102, v58, v62\n"
        "v_cvt_pk_bf16_f32 v103, v59, v63\n"
        "buffer_store_dwordx4 v[100:103], v124, s[36:39], s54 offen offset:0 sc1\n"
        "s_barrier\n"
        "s_add_u32 s69, s58, s82\n"
        "s_sub_u32 s72, s58, s82\n"
        "s_waitcnt vmcnt(6)\n"
        "buffer_load_dwordx4 v[64:67], v124, s[36:39], s69 offen offset:0 sc1\n"
        "s_waitcnt vmcnt(6)\n"
        "buffer_load_dwordx4 v[68:71], v124, s[36:39], s69 offen offset:1024 sc1\n"
        "s_waitcnt vmcnt(6)\n"
        "buffer_load_dwordx4 v[72:75], v124, s[36:39], s72 offen offset:2048 sc1\n"
        "s_waitcnt vmcnt(6)\n"
        "buffer_load_dwordx4 v[76:79], v124, s[36:39], s72 offen offset:3072 sc1\n"
        "s_mov_b32 s81, 64\n"
        "buffer_load_dword v107, off, s[36:39], s81 sc1\n"
        "s_memrealtime s[26:27]\n"
        "s_waitcnt vmcnt(5)\n"
        "s_waitcnt lgkmcnt(0)\n"
        "s_barrier\n"
        "s_add_u32 s29, s29, 1024\n"
        "s_waitcnt vmcnt(0)\n"
        "L_par_1%=:\n"
        "v_readfirstlane_b32 s80, v107\n"
        "s_nop 0\n"
        "s_cmp_lt_i32 s80, 0\n"
        "s_cbranch_scc1 L_par2_1%=\n"
        "s_add_u32 s63, s63, 1\n"
        "s_cmp_gt_u32 s63, 0x40000\n"
        "s_cbranch_scc1 L_abort%=\n"
        "s_sleep 4\n"
        "buffer_load_dword v107, off, s[36:39], s81 sc1\n"
        "s_waitcnt vmcnt(0)\n"
        "s_branch L_par_1%=\n"
        "L_par2_1%=:\n"
        "s_bfe_u32 s46, s80, 0x10018\n"
        "s_lshl_b32 s46, s46, 21\n"
        "s_mov_b32 s53, 0x200000\n"
        "s_bfe_u32 s84, s80, 0x80010\n"
        "s_sub_u32 s84, 255, s84\n"
        "s_bfe_u32 s76, s80, 0x80008\n"
        "s_sub_u32 s76, s76, 32\n"
        "s_max_i32 s76, s76, 66\n"
        "s_bfe_u32 s77, s80, 0x80000\n"
        "s_sub_u32 s77, s77, 158\n"
        "s_bitset0_b32 s49, 30\n"
        "s_bitset0_b32 s50, 30\n"
        "s_bitset0_b32 s56, 30\n"
        "s_bitset0_b32 s64, 30\n"
        "v_mov_b32 v140, 0\n"
        "s_mov_b32 s28, 1000\n"
        "s_branch L_xok_1%=\n"
        "L_x2_1%=:\n"
        "s_add_u32 s69, s58, s82\n"
        "s_sub_u32 s72, s58, s82\n"
        "buffer_load_dwordx4 v[64:67], v124, s[36:39], s69 offen offset:0 sc1\n"
        "buffer_load_dwordx4 v[68:71], v124, s[36:39], s69 offen offset:1024 sc1\n"
        "buffer_load_dwordx4 v[72:75], v124, s[36:39], s72 offen offset:2048 sc1\n"
        "buffer_load_dwordx4 v[76:79], v124, s[36:39], s72 offen offset:3072 sc1\n"
        "s_waitcnt vmcnt(0)\n"
        "s_memrealtime s[26:27]\n"
        "s_waitcnt lgkmcnt(0)\n"
        "s_add_u32 s28, s28, s26\n"
        "L_xok_1%=:\n"
        "v_mfma_f32_16x16x32_bf16 v[84:87], v[64:67], a[0:3], 0\n"
        "v_mfma_f32_16x16x32_bf16 v[88:91], v[64:67], a[8:11], 0\n"
        "v_mfma_f32_16x16x32_bf16 v[56:59], a[144:147], v[64:67], 0\n"
        "s_branch L_win_1%=\n"
        "L_retry_1%=:\n"
        "s_add_u32 s63, s63, 1\n"
        "s_cmp_gt_u32 s63, 0x40000\n"
        "s_cbranch_scc1 L_abort%=\n"
        "s_sleep 2\n"
        "s_waitcnt vmcnt(0)\n"
        "s_waitcnt lgkmcnt(0)\n"
        "buffer_load_dwordx4 v[80:83], v138, s[36:39], s50 offen offset:0 sc1\n"
        "buffer_load_dwordx4 v[64:67], v138, s[36:39], s49 offen offset:0 sc1\n"
        "buffer_load_dwordx4 v[68:71], v138, s[36:39], s49 offen offset:256 sc1\n"
        "buffer_load_dwordx4 v[72:75], v138, s[36:39], s65 offen offset:0 sc1\n"
        "buffer_load_dwordx4 v[76:79], v138, s[36:39], s65 offen offset:256 sc1\n"
        "s_waitcnt vmcnt(4)\n"
        "v_bitop3_b32 v123, 32, v80, v82 bitop3:0x80\n"
        "v_add_u32_dpp v80, v81, v80 row_half_mirror row_mask:0xf bank_mask:0xf\n"
        "v_add_u32_dpp v82, v83, v82 row_half_mirror row_mask:0xf bank_mask:0xf\n"
        "v_cvt_f32_i32 v80, v80\n"
        "v_cvt_f32_i32 v82, v82\n"
        "v_cvt_pk_bf16_f32 v52, v80, v82\n"
        "ds_write_b32 v137, v52\n"
        "s_waitcnt vmcnt(3)\n"
        "v_bitop3_b32 v123, v123, v64, v66 bitop3:0x80\n"
        "v_cvt_f32_i32 v64, v64\n"
        "v_cvt_f32_i32 v65, v65\n"
        "v_cvt_f32_i32 v66, v66\n"
        "v_cvt_f32_i32 v67, v67\n"
        "v_cvt_pk_bf16_f32 v52, v64, v65\n"
        "v_cvt_pk_bf16_f32 v53, v66, v67\n"
        "ds_write_b64 v126, v[52:53] offset:0\n"
        "s_waitcnt vmcnt(2)\n"
        "v_bitop3_b32 v123, v123, v68, v70 bitop3:0x80\n"
        "v_cvt_f32_i32 v68, v68\n"
        "v_cvt_f32_i32 v69, v69\n"
        "v_cvt_f32_i32 v70, v70\n"
        "v_cvt_f32_i32 v71, v71\n"
        "v_cvt_pk_bf16_f32 v54, v68, v69\n"
        "v_cvt_pk_bf16_f32 v55, v70, v71\n"
        "ds_write_b64 v126, v[54:55] offset:512\n"
        "s_waitcnt vmcnt(1)\n"
        "v_bitop3_b32 v123, v123, v72, v74 bitop3:0x80\n"
        "v_cvt_f32_i32 v72, v72\n"
        "v_cvt_f32_i32 v73, v73\n"
        "ds_read_b128 v[48:51], v124 offset:2048\n"
        "v_cvt_f32_i32 v74, v74\n"
        "v_cvt_f32_i32 v75, v75\n"
        "v_cvt_pk_bf16_f32 v52, v72, v73\n"
        "v_cvt_pk_bf16_f32 v53, v74, v75\n"
        "ds_write_b64 v126, v[52:53] offset:1024\n"
        "s_waitcnt vmcnt(0)\n"
        "v_bitop3_b32 v123, v123, v76, v78 bitop3:0x80\n"
        "v_cmp_eq_u32 vcc, 0, v123\n"
        "v_mov_b64 v[122:123], 0\n"
        "v_cvt_f32_i32 v76, v76\n"
        "v_cvt_f32_i32 v77, v77\n"
        "v_cvt_f32_i32 v78, v78\n"
        "v_cvt_f32_i32 v79, v79\n"
        "v_cvt_pk_bf16_f32 v54, v76, v77\n"
        "v_cvt_pk_bf16_f32 v55, v78, v79\n"
        "ds_write_b64 v126, v[54:55] offset:1536\n"
        "v_mov_b64 v[92:93], 0\n"
        "v_mov_b64 v[94:95], 0\n"
        "v_mov_b64 v[96:97], 0\n"
        "v_mov_b64 v[98:99], 0\n"
        "s_waitcnt lgkmcnt(0)\n"
        "s_branch L_dec_1%=\n"
        "L_sw0_1%=:\n"
        "s_memrealtime s[26:27]\n"
        "s_waitcnt lgkmcnt(0)\n"
        "s_sub_u32 s28, s28, s26\n"
        "L_sw_1%=:\n"
        "s_add_u32 s63, s63, 1\n"
        "s_cmp_gt_u32 s63, 0x40000\n"
        "s_cbranch_scc1 L_abort%=\n"
        "s_sleep 2\n"
        "buffer_load_dword v140, v124, s[36:39], s29 offen sc1\n"
        "s_waitcnt vmcnt(0)\n"
        "v_cmp_eq_u32 vcc, 0, v140\n"
        "s_cbranch_vccnz L_sw_1%=\n"
        "s_memrealtime s[26:27]\n"
        "s_waitcnt lgkmcnt(0)\n"
        "s_add_u32 s28, s28, s26\n"
        "s_branch L_sok_1%=\n"
        ".p2align 6\n"
        "L_rot_1%=:\n"
        "v_mfma_f32_16x16x32_bf16 v[60:63], a[236:239], v[100:103], v[60:63]\n"
        "v_mfma_f32_16x16x32_bf16 v[48:51], v[196:199], v[100:103], v[48:51]\n"
        "v_mfma_f32_16x16x32_bf16 v[52:55], v[204:207], v[100:103], v[52:55]\n"
        "s_waitcnt lgkmcnt(9)\n"
        "v_mfma_f32_16x16x32_bf16 v[84:87], v[92:95], a[96:99], v[84:87]\n"
        "ds_read_b128 v[100:103], v124 offset:11264\n"
        "v_mfma_f32_16x16x32_bf16 v[88:91], v[92:95], a[104:107], v[88:91]\n"
        "v_mfma_f32_16x16x32_bf16 v[56:59], v[0:3], v[92:95], v[56:59]\n"
        "v_mfma_f32_16x16x32_bf16 v[60:63], v[8:11], v[92:95], v[60:63]\n"
        "v_mfma_f32_16x16x32_bf16 v[48:51], v[208:211], v[92:95], v[48:51]\n"
        "s_waitcnt lgkmcnt(0)\n"
        "s_barrier\n"
        "v_mfma_f32_16x16x32_bf16 v[52:55], v[216:219], v[92:95], v[52:55]\n"
        "v_mov_b64 v[116:117], 0\n"
        "v_mfma_f32_16x16x32_bf16 v[84:87], v[104:107], a[100:103], v[84:87]\n"
        "ds_read_b128 v[92:95], v124 offset:16384\n"
        "v_mov_b64 v[118:119], 0\n"
        "v_mfma_f32_16x16x32_bf16 v[88:91], v[104:107], a[108:111], v[88:91]\n"
        "v_mov_b64 v[120:121], 0\n"
        "v_mfma_f32_16x16x32_bf16 v[56:59], v[4:7], v[104:107], v[56:59]\n"
        "v_mfma_f32_16x16x32_bf16 v[60:63], v[12:15], v[104:107], v[60:63]\n"
        "v_mfma_f32_16x16x32_bf16 v[48:51], v[212:215], v[104:107], v[48:51]\n"
        "v_mfma_f32_16x16x32_bf16 v[52:55], v[220:223], v[104:107], v[52:55]\n"
        "v_mfma_f32_16x16x32_bf16 v[48:51], v[224:227], v[96:99], v[48:51]\n"
        "ds_read_b128 v[104:107], v124 offset:17408\n"
        "v_mfma_f32_16x16x32_bf16 v[52:55], v[232:235], v[96:99], v[52:55]\n"
        "v_mfma_f32_16x16x32_bf16 v[48:51], v[228:231], v[100:103], v[48:51]\n"
        "v_mfma_f32_16x16x32_bf16 v[52:55], v[236:239], v[100:103], v[52:55]\n"
        "s_waitcnt lgkmcnt(1)\n"
        "v_mfma_f32_16x16x32_bf16 v[48:51], v[240:243], v[92:95], v[48:51]\n"
        "v_mfma_f32_16x16x32_bf16 v[52:55], v[248:251], v[92:95], v[52:55]\n"
        "s_waitcnt lgkmcnt(0)\n"
        "v_mfma_f32_16x16x32_bf16 v[48:51], v[244:247], v[104:107], v[48:51]\n"
        "s_cmp_lt_i32 s44, 0\n"
        "s_cbranch_scc1 L_light_1%=\n"
        "v_mfma_f32_16x16x32_bf16 v[52:55], v[252:255], v[104:107], v[52:55]\n"
        "s_waitcnt vmcnt(4)\n"
        "v_bitop3_b32 v123, 32, v80, v82 bitop3:0x80\n"
        "s_nop 0\n"
        "v_add_u32_dpp v80, v81, v80 row_half_mirror row_mask:0xf bank_mask:0xf\n"
        "v_mfma_f32_16x16x32_bf16 v[84:87], v[96:99], a[112:115], v[84:87]\n"
        "s_nop 0\n"
        "v_add_u32_dpp v82, v83, v82 row_half_mirror row_mask:0xf bank_mask:0xf\n"
        "v_mfma_f32_16x16x32_bf16 v[88:91], v[96:99], a[120:123], v[88:91]\n"
        "v_cvt_pk_bf16_f32 v48, v48, v52\n"
        "v_cvt_pk_bf16_f32 v49, v49, v53\n"
        "s_nop 0\n"
        "v_mfma_f32_16x16x32_bf16 v[84:87], v[100:103], a[116:119], v[84:87]\n"
        "v_cvt_pk_bf16_f32 v50, v50, v54\n"
        "v_cvt_pk_bf16_f32 v51, v51, v55\n"
        "v_mfma_f32_16x16x32_bf16 v[88:91], v[100:103], a[124:127], v[88:91]\n"
        "v_cvt_f32_i32 v80, v80\n"
        "s_nop 0\n"
        "v_cvt_f32_i32 v82, v82\n"
        "v_mfma_f32_16x16x32_bf16 v[84:87], v[92:95], a[128:131], v[84:87]\n"
        "buffer_store_dwordx4 v[48:51], v124, s[36:39], s54 offen offset:1024 sc1\n"
        "v_mfma_f32_16x16x32_bf16 v[88:91], v[92:95], a[136:139], v[88:91]\n"
        "s_nop 0\n"
        "v_cvt_pk_bf16_f32 v52, v80, v82\n"
        "v_mfma_f32_16x16x32_bf16 v[84:87], v[104:107], a[132:135], v[84:87]\n"
        "s_nop 0\n"
        "v_mfma_f32_16x16x32_bf16 v[88:91], v[104:107], a[140:143], v[88:91]\n"
        "ds_write_b32 v137, v52\n"
        "s_waitcnt vmcnt(4)\n"
        "v_bitop3_b32 v123, v123, v64, v66 bitop3:0x80\n"
        "v_cvt_f32_i32 v64, v64\n"
        "v_mfma_f32_16x16x32_bf16 v[56:59], v[32:35], v[92:95], v[56:59]\n"
        "v_cvt_f32_i32 v65, v65\n"
        "v_cvt_f32_i32 v66, v66\n"
        "v_mfma_f32_16x16x32_bf16 v[60:63], v[40:43], v[92:95], v[60:63]\n"
        "v_cvt_pk_bf16_f32 v84, v84, v85\n"
        "v_cvt_pk_bf16_f32 v85, v86, v87\n"
        "v_mfma_f32_16x16x32_bf16 v[56:59], v[16:19], v[96:99], v[56:59]\n"
        "v_cvt_pk_bf16_f32 v86, v88, v89\n"
        "v_cvt_pk_bf16_f32 v87, v90, v91\n"
        "v_mfma_f32_16x16x32_bf16 v[60:63], v[24:27], v[96:99], v[60:63]\n"
        "v_cvt_f32_i32 v67, v67\n"
        "v_cvt_pk_bf16_f32 v52, v64, v65\n"
        "v_mfma_f32_16x16x32_bf16 v[56:59], v[20:23], v[100:103], v[56:59]\n"
        "buffer_store_dwordx4 v[84:87], v124, s[36:39], s54 offen offset:2048 sc1\n"
        "v_mov_b64 v[92:93], 0\n"
        "v_cvt_pk_bf16_f32 v53, v66, v67\n"
        "v_mfma_f32_16x16x32_bf16 v[60:63], v[28:31], v[100:103], v[60:63]\n"
        "v_mov_b64 v[94:95], 0\n"
        "v_mov_b64 v[96:97], 0\n"
        "ds_write_b64 v126, v[52:53] offset:0\n"
        "v_mfma_f32_16x16x32_bf16 v[56:59], v[36:39], v[104:107], v[56:59]\n"
        "v_mov_b64 v[98:99], 0\n"
        "s_waitcnt vmcnt(4)\n"
        "v_bitop3_b32 v123, v123, v68, v70 bitop3:0x80\n"
        "v_mfma_f32_16x16x32_bf16 v[60:63], v[44:47], v[104:107], v[60:63]\n"
        "v_cvt_f32_i32 v68, v68\n"
        "v_cvt_f32_i32 v69, v69\n"
        "v_cvt_f32_i32 v70, v70\n"
        "v_cvt_f32_i32 v71, v71\n"
        "v_cvt_pk_bf16_f32 v54, v68, v69\n"
        "v_cvt_pk_bf16_f32 v55, v70, v71\n"
        "ds_write_b64 v126, v[54:55] offset:512\n"
        "s_waitcnt vmcnt(3)\n"
        "v_bitop3_b32 v123, v123, v72, v74 bitop3:0x80\n"
        "v_cvt_f32_i32 v72, v72\n"
        "v_cvt_pk_bf16_f32 v100, v56, v60\n"
        "v_cvt_pk_bf16_f32 v101, v57, v61\n"
        "v_cvt_pk_bf16_f32 v102, v58, v62\n"
        "v_cvt_pk_bf16_f32 v103, v59, v63\n"
        "v_cvt_f32_i32 v73, v73\n"
        "ds_read_b128 v[48:51], v124 offset:2048\n"
        "v_cvt_f32_i32 v74, v74\n"
        "v_cvt_f32_i32 v75, v75\n"
        "v_cvt_pk_bf16_f32 v52, v72, v73\n"
        "v_cvt_pk_bf16_f32 v53, v74, v75\n"
        "ds_write_b64 v126, v[52:53] offset:1024\n"
        "s_waitcnt vmcnt(2)\n"
        "v_bitop3_b32 v123, v123, v76, v78 bitop3:0x80\n"
        "v_cmp_eq_u32 vcc, 0, v123\n"
        "v_mov_b64 v[122:123], 0\n"
        "v_cvt_f32_i32 v76, v76\n"
        "v_cvt_f32_i32 v77, v77\n"
        "v_cvt_f32_i32 v78, v78\n"
        "v_cvt_f32_i32 v79, v79\n"
        "v_cvt_pk_bf16_f32 v54, v76, v77\n"
        "v_cvt_pk_bf16_f32 v55, v78, v79\n"
        "ds_write_b64 v126, v[54:55] offset:1536\n"
        "L_dec_1%=:\n"
        "s_waitcnt lgkmcnt(2)\n"
        "v_smfmac_f32_16x16x64_bf16 v[92:95], v[48:51], a[144:151], v134\n"
        "ds_read_b128 v[52:55], v124 offset:3072\n"
        "v_max3_f32 v104, |v80|, |v82|, |v64|\n"
        "v_smfmac_f32_16x16x64_bf16 v[96:99], v[48:51], a[152:159], v134\n"
        "s_cbranch_vccnz L_retry_1%=\n"
        "v_max3_f32 v104, |v65|, |v66|, v104\n"
        "v_smfmac_f32_16x16x64_bf16 v[116:119], v[48:51], a[240:247], v134\n"
        "s_add_u32 s29, s29, 1024\n"
        "v_max3_f32 v104, |v67|, |v68|, v104\n"
        "v_smfmac_f32_16x16x64_bf16 v[120:123], v[48:51], a[248:255], v134\n"
        "s_waitcnt lgkmcnt(0)\n"
        "v_max3_f32 v104, |v69|, |v70|, v104\n"
        "s_barrier\n"
        "ds_read_b128 v[56:59], v124 offset:4096\n"
        "v_smfmac_f32_16x16x64_bf16 v[92:95], v[52:55], a[160:167], v134\n"
        "buffer_load_dwordx4 v[88:91], v124, s[36:39], s55 offen offset:3072 sc1\n"
        "ds_read_b128 v[60:63], v124 offset:7168\n"
        "v_max3_f32 v104, |v71|, |v72|, v104\n"
        "v_smfmac_f32_16x16x64_bf16 v[96:99], v[52:55], a[168:175], v134\n"
        "ds_read_b128 v[80:83], v124 offset:5120\n"
        "v_max3_f32 v104, |v73|, |v74|, v104\n"
        "v_smfmac_f32_16x16x64_bf16 v[116:119], v[52:55], v[176:183], v134\n"
        "buffer_load_dwordx4 v[84:87], v124, s[36:39], s55 offen offset:0 sc1\n"
        "ds_read_b128 v[64:67], v124 offset:8192\n"
        "v_max3_f32 v104, |v75|, |v76|, v104\n"
        "v_smfmac_f32_16x16x64_bf16 v[120:123], v[52:55], v[184:191], v134\n"
        "ds_read_b128 v[68:71], v124 offset:0\n"
        "v_max3_f32 v104, |v77|, |v78|, v104\n"
        "s_waitcnt lgkmcnt(4)\n"
        "v_smfmac_f32_16x16x64_bf16 v[92:95], v[56:59], v[0:7], v134\n"
        "ds_read_b128 v[72:75], v124 offset:1024\n"
        "v_max_f32_e64 v104, |v79|, v104\n"
        "v_smfmac_f32_16x16x64_bf16 v[96:99], v[56:59], v[8:15], v134\n"
        "ds_read_b128 v[0:3], v125 offset:12288\n"
        "v_max_f32_dpp v104, v104, v104 row_shr:1 row_mask:0xf bank_mask:0xf\n"
        "s_waitcnt lgkmcnt(3)\n"
        "v_smfmac_f32_16x16x64_bf16 v[92:95], v[60:63], a[224:231], v134\n"
        "ds_read_b128 v[76:79], v124 offset:6144\n"
        "v_max_f32_dpp v104, v104, v104 row_shr:2 row_mask:0xf bank_mask:0xf\n"
        "v_smfmac_f32_16x16x64_bf16 v[96:99], v[60:63], a[232:239], v134\n"
        "ds_read_b128 v[4:7], v125 offset:13312\n"
        "v_max_f32_dpp v104, v104, v104 row_shr:4 row_mask:0xf bank_mask:0xf\n"
        "v_smfmac_f32_16x16x64_bf16 v[92:95], v[80:83], v[16:23], v134\n"
        "ds_read_b128 v[8:11], v125 offset:14336\n"
        "v_max_f32_dpp v104, v104, v104 row_shr:8 row_mask:0xf bank_mask:0xf\n"
        "v_smfmac_f32_16x16x64_bf16 v[96:99], v[80:83], v[24:31], v134\n"
        "ds_read_b128 v[12:15], v125 offset:15360\n"
        "v_max_f32_dpp v104, v104, v104 row_bcast:15 row_mask:0xa bank_mask:0xf\n"
        "v_smfmac_f32_16x16x64_bf16 v[92:95], v[64:67], v[32:39], v134\n"
        "ds_read_b128 v[16:19], v125 offset:16384\n"
        "v_max_f32_dpp v104, v104, v104 row_bcast:31 row_mask:0xc bank_mask:0xf\n"
        "v_smfmac_f32_16x16x64_bf16 v[96:99], v[64:67], v[40:47], v134\n"
        "ds_read_b128 v[20:23], v125 offset:17408\n"
        "v_readlane_b32 s85, v104, 63\n"
        "s_waitcnt lgkmcnt(5)\n"
        "v_smfmac_f32_16x16x64_bf16 v[92:95], v[68:71], a[176:183], v134\n"
        "ds_read_b128 v[24:27], v125 offset:18432\n"
        "v_smfmac_f32_16x16x64_bf16 v[96:99], v[68:71], a[184:191], v134\n"
        "ds_read_b128 v[28:31], v125 offset:19456\n"
        "v_mov_b32 v104, s85\n"
        "v_smfmac_f32_16x16x64_bf16 v[92:95], v[72:75], a[192:199], v134\n"
        "ds_read_b128 v[32:35], v125 offset:20480\n"
        "v_smfmac_f32_16x16x64_bf16 v[96:99], v[72:75], a[200:207], v134\n"
        "ds_read_b128 v[36:39], v125 offset:21504\n"
        "s_add_u32 s69, s58, s82\n"
        "v_smfmac_f32_16x16x64_bf16 v[92:95], v[76:79], a[208:215], v134\n"
        "ds_read_b128 v[40:43], v125 offset:22528\n"
        "s_sub_u32 s72, s58, s82\n"
        "v_smfmac_f32_16x16x64_bf16 v[96:99], v[76:79], a[216:223], v134\n"
        "ds_read_b128 v[44:47], v125 offset:23552\n"
        "v_smfmac_f32_16x16x64_bf16 v[116:119], v[64:67], v[240:247], v134\n"
        "ds_write_b32 v142, v104\n"
        "v_add_f32 v92, v92, v94\n"
        "v_add_f32 v93, v93, v95\n"
        "v_smfmac_f32_16x16x64_bf16 v[120:123], v[64:67], v[248:255], v134\n"
        "v_add_f32 v96, v96, v98\n"
        "v_add_f32 v97, v97, v99\n"
        "v_cvt_pk_bf16_f32 v92, v92, v96\n"
        "v_cvt_pk_bf16_f32 v93, v93, v97\n"
        "s_waitcnt lgkmcnt(3)\n"
        "v_smfmac_f32_16x16x64_bf16 v[116:119], v[68:71], v[0:7], v134\n"
        "ds_write2_b32 v127, v92, v93 offset1:72\n"
        "v_smfmac_f32_16x16x64_bf16 v[120:123], v[68:71], v[8:15], v134\n"
        "v_mov_b64 v[98:99], 0\n"
        "v_smfmac_f32_16x16x64_bf16 v[116:119], v[72:75], v[16:23], v134\n"
        "buffer_store_dwordx4 v[100:103], v124, s[36:39], s54 offen offset:0 sc1\n"
        "v_mov_b64 v[96:97], 0\n"
        "v_smfmac_f32_16x16x64_bf16 v[120:123], v[72:75], v[24:31], v134\n"
        "s_waitcnt vmcnt(4)\n"
        "s_waitcnt lgkmcnt(0)\n"
        "v_mov_b64 v[106:107], 0\n"
        "s_barrier\n"
        "ds_read_b128 v[0:3], v128 offset:0\n"
        "v_smfmac_f32_16x16x64_bf16 v[116:119], v[76:79], v[32:39], v134\n"
        "ds_read_b128 v[24:27], v124 offset:20480\n"
        "v_mov_b64 v[104:105], 0\n"
        "v_smfmac_f32_16x16x64_bf16 v[120:123], v[76:79], v[40:47], v134\n"
        "ds_read_b128 v[4:7], v128 offset:64\n"
        "v_mov_b64 v[94:95], 0\n"
        "v_smfmac_f32_16x16x64_bf16 v[116:119], v[60:63], v[192:199], v134\n"
        "ds_read_b128 v[8:11], v128 offset:128\n"
        "v_mov_b64 v[92:93], 0\n"
        "v_smfmac_f32_16x16x64_bf16 v[120:123], v[60:63], v[200:207], v134\n"
        "ds_read_b128 v[12:15], v128 offset:192\n"
        "v_mov_b64 v[20:21], 0\n"
        "v_smfmac_f32_16x16x64_bf16 v[116:119], v[56:59], v[208:215], v134\n"
        "s_waitcnt lgkmcnt(3)\n"
        "v_max3_f32 v24, v24, v25, v26\n"
        "s_waitcnt lgkmcnt(2)\n"
        "v_smfmac_f32_16x16x64_bf16 v[104:107], v[108:111], v[0:7], v134\n"
        "v_max_f32 v24, v24, v27\n"
        "v_smfmac_f32_16x16x64_bf16 v[120:123], v[56:59], v[216:223], v134\n"
        "v_readfirstlane_b32 s84, v24\n"
        "s_waitcnt lgkmcnt(0)\n"
        "v_smfmac_f32_16x16x64_bf16 v[104:107], v[112:115], v[8:15], v134\n"
        "v_mov_b64 v[22:23], 0\n"
        "v_smfmac_f32_16x16x64_bf16 v[116:119], v[80:83], v[224:231], v134\n"
        "s_lshr_b32 s84, s84, 23\n"
        "s_add_u32 s84, s84, s101\n"
        "v_smfmac_f32_16x16x64_bf16 v[120:123], v[80:83], v[232:239], v134\n"
        "s_max_i32 s84, s84, s76\n"
        "s_min_i32 s84, s84, 186\n"
        "v_add_f32 v104, v104, v106\n"
        "v_add_f32 v105, v105, v107\n"
        "v_smfmac_f32_16x16x64_bf16 v[92:95], v[64:67], a[128:135], v134\n"
        "ds_write_b64 v131, v[104:105]\n"
        "v_add_f32 v116, v116, v118\n"
        "v_add_f32 v117, v117, v119\n"
        "v_pk_add_f32 v[120:121], v[120:121], v[122:123]\n"
        "v_cvt_pk_bf16_f32 v116, v116, v120\n"
        "v_cvt_pk_bf16_f32 v117, v117, v121\n"
        "ds_write2_b32 v129, v116, v117 offset1:8\n"
        "v_smfmac_f32_16x16x64_bf16 v[96:99], v[64:67], a[136:143], v134\n"
        "s_mul_i32 s88, s84, 0x800080\n"
        "s_mov_b32 s89, s88\n"
        "v_smfmac_f32_16x16x64_bf16 v[92:95], v[68:71], a[32:39], v134\n"
        "s_add_u32 s90, s88, s35\n"
        "s_mov_b32 s91, s90\n"
        "v_mov_b64 v[104:105], s[88:89]\n"
        "v_smfmac_f32_16x16x64_bf16 v[96:99], v[68:71], a[40:47], v134\n"
        "buffer_load_dwordx4 v[64:67], v124, s[36:39], s69 offen offset:0 sc1\n"
        "v_mov_b64 v[106:107], s[90:91]\n"
        "v_smfmac_f32_16x16x64_bf16 v[92:95], v[48:51], a[0:7], v134\n"
        "s_waitcnt lgkmcnt(0)\n"
        "v_mov_b64 v[100:101], s[88:89]\n"
        "s_barrier\n"
        "ds_read_b128 v[24:27], v130\n"
        "v_smfmac_f32_16x16x64_bf16 v[96:99], v[48:51], a[8:15], v134\n"
        "ds_read_b128 v[28:31], v130 offset:1024\n"
        "v_mov_b64 v[102:103], s[90:91]\n"
        "v_smfmac_f32_16x16x64_bf16 v[92:95], v[52:55], a[16:23], v134\n"
        "ds_read_b128 v[116:119], v124 offset:13568\n"
        "v_mov_b64 v[48:49], 0\n"
        "v_smfmac_f32_16x16x64_bf16 v[96:99], v[52:55], a[24:31], v134\n"
        "ds_read_b128 v[32:35], v141 offset:0\n"
        "v_mov_b64 v[50:51], 0\n"
        "v_smfmac_f32_16x16x64_bf16 v[92:95], v[56:59], a[96:103], v134\n"
        "ds_read_b128 v[36:39], v141 offset:1024\n"
        "v_mov_b64 v[52:53], 0\n"
        "v_smfmac_f32_16x16x64_bf16 v[96:99], v[56:59], a[104:111], v134\n"
        "ds_read_b128 v[40:43], v141 offset:2048\n"
        "v_mov_b64 v[54:55], 0\n"
        "s_waitcnt lgkmcnt(5)\n"
        "v_smfmac_f32_16x16x64_bf16 v[20:23], v[24:27], v[0:7], v134\n"
        "ds_read_b128 v[44:47], v141 offset:3072\n"
        "v_mov_b64 v[56:57], 0\n"
        "s_waitcnt lgkmcnt(4)\n"
        "v_smfmac_f32_16x16x64_bf16 v[20:23], v[28:31], v[8:15], v134\n"
        "ds_read_b128 v[0:3], v125 offset:24576\n"
        "v_max3_f32 v121, v116, v117, v118\n"
        "v_smfmac_f32_16x16x64_bf16 v[92:95], v[72:75], a[48:55], v134\n"
        "buffer_load_dwordx4 v[68:71], v124, s[36:39], s69 offen offset:1024 sc1\n"
        "v_mov_b64 v[58:59], 0\n"
        "v_smfmac_f32_16x16x64_bf16 v[96:99], v[72:75], a[56:63], v134\n"
        "ds_read_b128 v[4:7], v125 offset:25600\n"
        "v_add_f32 v20, v20, v22\n"
        "v_add_f32 v21, v21, v23\n"
        "v_cndmask_b32 v120, v20, v21, s[92:93]\n"
        "v_mul_f32 v120, s74, v120\n"
        "v_max3_f32 v121, v121, v119, v120\n"
        "v_smfmac_f32_16x16x64_bf16 v[92:95], v[76:79], a[64:71], v134\n"
        "buffer_load_dwordx4 v[72:75], v124, s[36:39], s72 offen offset:2048 sc1\n"
        "v_max_f32_dpp v121, v121, v121 quad_perm:[2,3,0,1] row_mask:0xf bank_mask:0xf\n"
        "v_mov_b32 v122, v121\n"
        "v_smfmac_f32_16x16x64_bf16 v[96:99], v[76:79], a[72:79], v134\n"
        "ds_read_b128 v[8:11], v125 offset:26624\n"
        "v_permlane16_swap_b32 v121, v122\n"
        "v_max_f32 v121, v121, v122\n"
        "v_mov_b32 v122, v121\n"
        "v_smfmac_f32_16x16x64_bf16 v[92:95], v[80:83], a[112:119], v134\n"
        "buffer_load_dwordx4 v[76:79], v124, s[36:39], s72 offen offset:3072 sc1\n"
        "v_permlane32_swap_b32 v121, v122\n"
        "v_smfmac_f32_16x16x64_bf16 v[96:99], v[80:83], a[120:127], v134\n"
        "ds_read_b128 v[12:15], v125 offset:27648\n"
        "v_max_f32 v121, v121, v122\n"
        "v_mul_f32_e64 v123, -s73, v121\n"
        "v_fma_f32 v120, v120, s73, v123\n"
        "v_fma_f32 v116, v116, s73, v123\n"
        "v_smfmac_f32_16x16x64_bf16 v[92:95], v[60:63], a[80:87], v134\n"
        "v_fma_f32 v117, v117, s73, v123\n"
        "v_fma_f32 v118, v118, s73, v123\n"
        "v_fma_f32 v119, v119, s73, v123\n"
        "v_exp_f32 v120, v120\n"
        "v_smfmac_f32_16x16x64_bf16 v[96:99], v[60:63], a[88:95], v134\n"
        "ds_read_b128 v[16:19], v125 offset:28672\n"
        "v_exp_f32 v116, v116\n"
        "v_mul_f32 v120, s74, v120\n"
        "v_exp_f32 v117, v117\n"
        "v_exp_f32 v118, v118\n"
        "v_exp_f32 v119, v119\n"
        "s_waitcnt vmcnt(5)\n"
        "v_mov_b64 v[108:109], v[86:87]\n"
        "v_mov_b64 v[112:113], v[90:91]\n"
        "v_cvt_pk_bf16_f32 v120, v120, v120\n"
        "v_cvt_pk_bf16_f32 v60, v116, v117\n"
        "v_cvt_pk_bf16_f32 v62, v118, v119\n"
        "v_and_b32 v61, v120, v135\n"
        "v_and_b32 v63, v120, v136\n"
        "v_pk_add_f32 v[92:93], v[92:93], v[94:95]\n"
        "v_pk_add_f32 v[96:97], v[96:97], v[98:99]\n"
        "v_smfmac_f32_16x16x64_bf16 v[56:59], v[60:63], v[100:107], v134\n"
        "ds_read_b128 v[20:23], v125 offset:29696\n"
        "v_cvt_pk_bf16_f32 v86, v92, v93\n"
        "v_cvt_pk_bf16_f32 v87, v92, v93\n"
        "v_mov_b64 v[90:91], v[86:87]\n"
        "v_cvt_pk_bf16_f32 v110, v96, v97\n"
        "v_cvt_pk_bf16_f32 v111, v96, v97\n"
        "v_smfmac_f32_16x16x64_bf16 v[48:51], v[60:63], v[84:91], v134\n"
        "ds_read_b128 v[24:27], v125 offset:30720\n"
        "v_mov_b64 v[114:115], v[110:111]\n"
        "v_add_f32 v56, v56, v58\n"
        "v_add_f32 v57, v57, v59\n"
        "v_smfmac_f32_16x16x64_bf16 v[52:55], v[60:63], v[108:115], v134\n"
        "ds_read_b128 v[28:31], v125 offset:31744\n"
        "s_nop 0\n"
        "v_rcp_f32 v56, v56\n"
        "v_rcp_f32 v57, v57\n"
        "v_pk_add_f32 v[48:49], v[48:49], v[50:51]\n"
        "v_pk_mul_f32 v[48:49], v[48:49], v[56:57]\n"
        "v_mov_b32 v110, v86\n"
        "v_mov_b64 v[92:93], s[70:71]\n"
        "v_pk_add_f32 v[52:53], v[52:53], v[54:55]\n"
        "v_pk_mul_f32 v[52:53], v[52:53], v[56:57]\n"
        "v_cvt_pk_bf16_f32 v48, v48, v52\n"
        "v_cvt_pk_bf16_f32 v49, v49, v53\n"
        "ds_write2_b32 v139, v48, v49 offset1:4\n"
        "v_mov_b64 v[94:95], 0\n"
        "v_mov_b64 v[96:97], 0\n"
        "v_mov_b64 v[98:99], 0\n"
        "buffer_store_dwordx2 v[110:111], v126, s[36:39], s60 offen sc1\n"
        "v_mov_b64 v[100:101], s[70:71]\n"
        "v_mov_b64 v[102:103], 0\n"
        "v_mov_b64 v[104:105], 0\n"
        "s_waitcnt lgkmcnt(0)\n"
        "v_mov_b64 v[106:107], 0\n"
        "s_barrier\n"
        "ds_read_b128 v[108:111], v124 offset:18432\n"
        "v_mov_b64 v[118:119], 0\n"
        "ds_read_b128 v[112:115], v124 offset:19456\n"
        "v_mov_b64 v[118:119], 0\n"
        "s_nop 0\n"
        "v_mov_b64 v[116:117], s[78:79]\n"
        "s_nop 0\n"
        "v_mov_b64 v[118:119], 0\n"
        "s_nop 0\n"
        "v_mov_b64 v[116:117], s[78:79]\n"
        "s_nop 0\n"
        "s_waitcnt lgkmcnt(1)\n"
        "v_smfmac_f32_16x16x64_bf16 v[116:119], v[108:111], v[32:39], v134\n"
        "s_waitcnt lgkmcnt(0)\n"
        "v_smfmac_f32_16x16x64_bf16 v[116:119], v[112:115], v[40:47], v134\n"
        "v_smfmac_f32_16x16x64_bf16 v[92:95], v[108:111], v[144:151], v134\n"
        "v_smfmac_f32_16x16x64_bf16 v[96:99], v[108:111], v[152:159], v134\n"
        "v_add_f32 v116, v116, v118\n"
        "v_add_f32 v117, v117, v119\n"
        "v_smfmac_f32_16x16x64_bf16 v[92:95], v[112:115], v[160:167], v134\n"
        "buffer_load_dword v140, v124, s[36:39], s29 offen sc1\n"
        "v_cvt_rpi_i32_f32 v118, v116\n"
        "v_bfi_b32 v118, 63, 2, v118\n"
        "v_cvt_rpi_i32_f32 v119, v117\n"
        "v_smfmac_f32_16x16x64_bf16 v[96:99], v[112:115], v[168:175], v134\n"
        "buffer_atomic_add_x2 v[118:119], v133, s[36:39], s52 offen\n"
        "v_smfmac_f32_16x16x64_bf16 v[100:103], v[108:111], v[0:7], v134\n"
        "v_add_f32 v92, v92, v94\n"
        "v_add_f32 v93, v93, v95\n"
        "v_cvt_rpi_i32_f32 v94, v92\n"
        "v_smfmac_f32_16x16x64_bf16 v[104:107], v[108:111], v[8:15], v134\n"
        "v_bfi_b32 v94, 63, 1, v94\n"
        "v_add_f32 v96, v96, v98\n"
        "v_smfmac_f32_16x16x64_bf16 v[100:103], v[112:115], v[16:23], v134\n"
        "v_cvt_rpi_i32_f32 v95, v96\n"
        "v_smfmac_f32_16x16x64_bf16 v[104:107], v[112:115], v[24:31], v134\n"
        "buffer_atomic_add_x2 v[94:95], v133, s[36:39], s51 offen\n"
        "v_add_f32 v97, v97, v99\n"
        "v_cvt_rpi_i32_f32 v98, v93\n"
        "v_bfi_b32 v98, 63, 1, v98\n"
        "v_cvt_rpi_i32_f32 v99, v97\n"
        "s_waitcnt vmcnt(7)\n"
        "v_mfma_f32_16x16x32_bf16 v[84:87], v[64:67], a[0:3], 0\n"
        "buffer_atomic_add_x2 v[98:99], v133, s[36:39], s66 offen\n"
        "v_add_f32 v100, v100, v102\n"
        "v_add_f32 v101, v101, v103\n"
        "v_add_f32 v104, v104, v106\n"
        "v_cvt_rpi_i32_f32 v102, v100\n"
        "v_bfi_b32 v102, 63, 1, v102\n"
        "v_cvt_rpi_i32_f32 v103, v104\n"
        "v_mfma_f32_16x16x32_bf16 v[88:91], v[64:67], a[8:11], 0\n"
        "buffer_atomic_add_x2 v[102:103], v133, s[36:39], s67 offen\n"
        "v_add_f32 v105, v105, v107\n"
        "v_cvt_rpi_i32_f32 v106, v101\n"
        "v_bfi_b32 v106, 63, 1, v106\n"
        "v_cvt_rpi_i32_f32 v107, v105\n"
        "v_mfma_f32_16x16x32_bf16 v[56:59], a[144:147], v[64:67], 0\n"
        "buffer_atomic_add_x2 v[106:107], v133, s[36:39], s68 offen\n"
        "L_win_1%=:\n"
        "v_mfma_f32_16x16x32_bf16 v[60:63], a[152:155], v[64:67], 0\n"
        "v_mfma_f32_16x16x32_bf16 v[48:51], a[240:243], v[64:67], 0\n"
        "v_mfma_f32_16x16x32_bf16 v[52:55], a[248:251], v[64:67], 0\n"
        "ds_write_b128 v124, v[64:67] offset:4096\n"
        "s_waitcnt vmcnt(9)\n"
        "v_mfma_f32_16x16x32_bf16 v[84:87], v[68:71], a[4:7], v[84:87]\n"
        "v_mfma_f32_16x16x32_bf16 v[88:91], v[68:71], a[12:15], v[88:91]\n"
        "ds_write_b128 v124, v[68:71] offset:5120\n"
        "v_mfma_f32_16x16x32_bf16 v[56:59], a[148:151], v[68:71], v[56:59]\n"
        "v_mfma_f32_16x16x32_bf16 v[60:63], a[156:159], v[68:71], v[60:63]\n"
        "s_waitcnt vmcnt(8)\n"
        "ds_write_b128 v124, v[72:75] offset:6144\n"
        "v_mfma_f32_16x16x32_bf16 v[48:51], a[244:247], v[68:71], v[48:51]\n"
        "v_mfma_f32_16x16x32_bf16 v[52:55], a[252:255], v[68:71], v[52:55]\n"
        "s_waitcnt vmcnt(7)\n"
        "ds_write_b128 v124, v[76:79] offset:7168\n"
        "v_mfma_f32_16x16x32_bf16 v[84:87], v[72:75], a[16:19], v[84:87]\n"
        "v_mfma_f32_16x16x32_bf16 v[88:91], v[72:75], a[24:27], v[88:91]\n"
        "v_mfma_f32_16x16x32_bf16 v[56:59], a[160:163], v[72:75], v[56:59]\n"
        "v_mfma_f32_16x16x32_bf16 v[60:63], a[168:171], v[72:75], v[60:63]\n"
        "v_mfma_f32_16x16x32_bf16 v[48:51], v[176:179], v[72:75], v[48:51]\n"
        "ds_read_b128 v[0:3], v125 offset:12288\n"
        "v_mfma_f32_16x16x32_bf16 v[52:55], v[184:187], v[72:75], v[52:55]\n"
        "ds_read_b128 v[8:11], v125 offset:14336\n"
        "v_mfma_f32_16x16x32_bf16 v[84:87], v[76:79], a[20:23], v[84:87]\n"
        "s_waitcnt lgkmcnt(2)\n"
        "s_barrier\n"
        "v_mfma_f32_16x16x32_bf16 v[88:91], v[76:79], a[28:31], v[88:91]\n"
        "ds_read_b128 v[92:95], v124 offset:0\n"
        "v_mov_b32 v116, s62\n"
        "v_mfma_f32_16x16x32_bf16 v[56:59], a[164:167], v[76:79], v[56:59]\n"
        "s_add_u32 s49, s49, 163840\n"
        "ds_read_b128 v[104:107], v124 offset:1024\n"
        "v_mfma_f32_16x16x32_bf16 v[60:63], a[172:175], v[76:79], v[60:63]\n"
        "s_add_u32 s50, s50, 163840\n"
        "ds_read_b128 v[4:7], v125 offset:13312\n"
        "v_mfma_f32_16x16x32_bf16 v[48:51], v[180:183], v[76:79], v[48:51]\n"
        "s_xor_b32 s49, s49, s86\n"
        "ds_read_b128 v[12:15], v125 offset:15360\n"
        "v_mfma_f32_16x16x32_bf16 v[52:55], v[188:191], v[76:79], v[52:55]\n"
        "s_xor_b32 s65, s49, 8192\n"
        "s_add_u32 s56, s56, 786432\n"
        "s_waitcnt lgkmcnt(3)\n"
        "v_mfma_f32_16x16x32_bf16 v[84:87], v[92:95], a[32:35], v[84:87]\n"
        "s_add_u32 s64, s64, 786432\n"
        "ds_read_b128 v[96:99], v124 offset:2048\n"
        "v_mfma_f32_16x16x32_bf16 v[88:91], v[92:95], a[40:43], v[88:91]\n"
        "ds_read_b128 v[100:103], v124 offset:3072\n"
        "v_mfma_f32_16x16x32_bf16 v[56:59], a[176:179], v[92:95], v[56:59]\n"
        "ds_read_b128 v[16:19], v125 offset:16384\n"
        "v_mfma_f32_16x16x32_bf16 v[60:63], a[184:187], v[92:95], v[60:63]\n"
        "s_add_u32 s51, s51, 163840\n"
        "ds_read_b128 v[20:23], v125 offset:17408\n"
        "v_mfma_f32_16x16x32_bf16 v[48:51], v[0:3], v[92:95], v[48:51]\n"
        "s_add_u32 s52, s52, 163840\n"
        "ds_read_b128 v[24:27], v125 offset:18432\n"
        "v_mfma_f32_16x16x32_bf16 v[52:55], v[8:11], v[92:95], v[52:55]\n"
        "ds_read_b128 v[28:31], v125 offset:19456\n"
        "s_waitcnt lgkmcnt(8)\n"
        "v_mfma_f32_16x16x32_bf16 v[84:87], v[104:107], a[36:39], v[84:87]\n"
        "s_add_u32 s83, s83, 163840\n"
        "ds_read_b128 v[92:95], v124 offset:12288\n"
        "v_mfma_f32_16x16x32_bf16 v[88:91], v[104:107], a[44:47], v[88:91]\n"
        "s_xor_b32 s86, s83, s46\n"
        "s_and_b32 s86, s86, s53\n"
        "s_lshr_b32 s86, s86, 8\n"
        "v_mfma_f32_16x16x32_bf16 v[56:59], a[180:183], v[104:107], v[56:59]\n"
        "s_waitcnt vmcnt(6)\n"
        "s_mov_b64 exec, 1\n"
        "buffer_atomic_umax v116, off, s[36:39], s61\n"
        "s_mov_b64 exec, -1\n"
        "v_mfma_f32_16x16x32_bf16 v[60:63], a[188:191], v[104:107], v[60:63]\n"
        "s_cmp_eq_u32 s44, 63\n"
        "s_cbranch_scc1 L_end%=\n"
        "s_waitcnt lgkmcnt(8)\n"
        "v_mfma_f32_16x16x32_bf16 v[48:51], v[4:7], v[104:107], v[48:51]\n"
        "s_xor_b32 s86, s86, s34\n"
        "ds_read_b128 v[32:35], v125 offset:20480\n"
        "s_waitcnt lgkmcnt(8)\n"
        "v_mfma_f32_16x16x32_bf16 v[52:55], v[12:15], v[104:107], v[52:55]\n"
        "ds_read_b128 v[36:39], v125 offset:21504\n"
        "s_waitcnt lgkmcnt(8)\n"
        "v_mfma_f32_16x16x32_bf16 v[84:87], v[96:99], a[48:51], v[84:87]\n"
        "s_xor_b32 s34, s34, s86\n"
        "ds_read_b128 v[104:107], v124 offset:13312\n"
        "v_mfma_f32_16x16x32_bf16 v[88:91], v[96:99], a[56:59], v[88:91]\n"
        "s_xor_b32 s51, s51, s86\n"
        "ds_read_b128 v[40:43], v125 offset:22528\n"
        "v_mfma_f32_16x16x32_bf16 v[56:59], a[192:195], v[96:99], v[56:59]\n"
        "s_add_u32 s55, s55, 786432\n"
        "ds_read_b128 v[44:47], v125 offset:23552\n"
        "v_mfma_f32_16x16x32_bf16 v[60:63], a[200:203], v[96:99], v[60:63]\n"
        "ds_read_b128 v[0:3], v125 offset:0\n"
        "s_waitcnt vmcnt(6)\n"
        "v_cmp_eq_u32 vcc, 0, v140\n"
        "s_waitcnt lgkmcnt(10)\n"
        "v_mfma_f32_16x16x32_bf16 v[48:51], v[16:19], v[96:99], v[48:51]\n"
        "s_cbranch_vccnz L_sw0_1%=\n"
        "L_sok_1%=:\n"
        "s_waitcnt lgkmcnt(8)\n"
        "v_mfma_f32_16x16x32_bf16 v[52:55], v[24:27], v[96:99], v[52:55]\n"
        "v_readlane_b32 s81, v140, s99\n"
        "v_max_u32_dpp v140, v140, v140 row_shr:1 row_mask:0xf bank_mask:0xf\n"
        "v_mfma_f32_16x16x32_bf16 v[84:87], v[100:103], a[52:55], v[84:87]\n"
        "ds_read_b128 v[96:99], v124 offset:14336\n"
        "v_max_u32_dpp v140, v140, v140 row_shr:2 row_mask:0xf bank_mask:0xf\n"
        "v_mfma_f32_16x16x32_bf16 v[88:91], v[100:103], a[60:63], v[88:91]\n"
        "ds_read_b128 v[4:7], v125 offset:1024\n"
        "v_max_u32_dpp v140, v140, v140 row_shr:4 row_mask:0xf bank_mask:0xf\n"
        "v_mfma_f32_16x16x32_bf16 v[56:59], a[196:199], v[100:103], v[56:59]\n"
        "buffer_load_dwordx4 v[108:111], v132, s[36:39], s56 offen offset:0 sc1\n"
        "v_max_u32_dpp v140, v140, v140 row_shr:8 row_mask:0xf bank_mask:0xf\n"
        "v_mfma_f32_16x16x32_bf16 v[60:63], a[204:207], v[100:103], v[60:63]\n"
        "ds_read_b128 v[8:11], v125 offset:2048\n"
        "v_max_u32_dpp v140, v140, v140 row_bcast:15 row_mask:0xa bank_mask:0xf\n"
        "v_mfma_f32_16x16x32_bf16 v[48:51], v[20:23], v[100:103], v[48:51]\n"
        "buffer_load_dwordx4 v[112:115], v132, s[36:39], s64 offen offset:0 sc1\n"
        "v_max_u32_dpp v140, v140, v140 row_bcast:31 row_mask:0xc bank_mask:0xf\n"
        "s_waitcnt lgkmcnt(10)\n"
        "v_mfma_f32_16x16x32_bf16 v[52:55], v[28:31], v[100:103], v[52:55]\n"
        "v_readlane_b32 s33, v140, 63\n"
        "s_lshl1_add_u32 s28, s28, s81\n"
        "s_waitcnt lgkmcnt(9)\n"
        "v_mfma_f32_16x16x32_bf16 v[84:87], v[92:95], a[64:67], v[84:87]\n"
        "ds_read_b128 v[100:103], v124 offset:15360\n"
        "s_add_u32 s28, s28, 36\n"
        "s_sub_i32 s33, s33, s28\n"
        "v_mfma_f32_16x16x32_bf16 v[88:91], v[92:95], a[72:75], v[88:91]\n"
        "s_min_i32 s33, s33, 500\n"
        "s_mov_b32 s28, 0\n"
        "L_dl_1%=:\n"
        "s_sub_i32 s33, s33, 3\n"
        "s_cmp_gt_i32 s33, 0\n"
        "s_cbranch_scc1 L_dl_1%=\n"
        "v_mfma_f32_16x16x32_bf16 v[56:59], a[208:211], v[92:95], v[56:59]\n"
        "s_add_u32 s54, s54, 786432\n"
        "ds_read_b128 v[12:15], v125 offset:3072\n"
        "v_mfma_f32_16x16x32_bf16 v[60:63], a[216:219], v[92:95], v[60:63]\n"
        "s_add_u32 s60, s60, 131072\n"
        "buffer_load_dwordx4 v[80:83], v138, s[36:39], s50 offen offset:0 sc1\n"
        "s_waitcnt lgkmcnt(10)\n"
        "v_mfma_f32_16x16x32_bf16 v[48:51], v[32:35], v[92:95], v[48:51]\n"
        "s_add_u32 s58, s58, 36864\n"
        "ds_read_b128 v[16:19], v125 offset:4096\n"
        "s_waitcnt lgkmcnt(8)\n"
        "v_mfma_f32_16x16x32_bf16 v[52:55], v[40:43], v[92:95], v[52:55]\n"
        "buffer_load_dwordx4 v[64:67], v138, s[36:39], s49 offen offset:0 sc1\n"
        "v_mfma_f32_16x16x32_bf16 v[84:87], v[104:107], a[68:71], v[84:87]\n"
        "s_xor_b32 s66, s51, 1024\n"
        "ds_read_b128 v[92:95], v124 offset:8192\n"
        "v_mfma_f32_16x16x32_bf16 v[88:91], v[104:107], a[76:79], v[88:91]\n"
        "s_xor_b32 s67, s51, 8192\n"
        "buffer_load_dwordx4 v[68:71], v138, s[36:39], s49 offen offset:256 sc1\n"
        "v_mfma_f32_16x16x32_bf16 v[56:59], a[212:215], v[104:107], v[56:59]\n"
        "s_xor_b32 s68, s51, 9216\n"
        "ds_read_b128 v[20:23], v125 offset:5120\n"
        "v_mfma_f32_16x16x32_bf16 v[60:63], a[220:223], v[104:107], v[60:63]\n"
        "s_sub_u32 s75, 127, s84\n"
        "buffer_load_dwordx4 v[72:75], v138, s[36:39], s65 offen offset:0 sc1\n"
        "v_mfma_f32_16x16x32_bf16 v[48:51], v[36:39], v[104:107], v[48:51]\n"
        "s_sub_u32 s101, s77, s75\n"
        "ds_read_b128 v[24:27], v125 offset:6144\n"
        "s_waitcnt lgkmcnt(10)\n"
        "v_mfma_f32_16x16x32_bf16 v[52:55], v[44:47], v[104:107], v[52:55]\n"
        "buffer_load_dwordx4 v[76:79], v138, s[36:39], s65 offen offset:256 sc1\n"
        "s_waitcnt lgkmcnt(8)\n"
        "v_mfma_f32_16x16x32_bf16 v[84:87], v[96:99], a[80:83], v[84:87]\n"
        "s_mul_i32 s35, s75, 0x800080\n"
        "ds_read_b128 v[104:107], v124 offset:9216\n"
        "v_mfma_f32_16x16x32_bf16 v[88:91], v[96:99], a[88:91], v[88:91]\n"
        "s_lshl_b32 s80, s75, 23\n"
        "ds_read_b128 v[28:31], v125 offset:7168\n"
        "v_mfma_f32_16x16x32_bf16 v[56:59], a[224:227], v[96:99], v[56:59]\n"
        "s_sub_u32 s73, 0x3e0293ee, s80\n"
        "ds_read_b128 v[32:35], v125 offset:8192\n"
        "v_mfma_f32_16x16x32_bf16 v[60:63], a[232:235], v[96:99], v[60:63]\n"
        "s_sub_u32 s74, 0x3f800000, s80\n"
        "ds_read_b128 v[36:39], v125 offset:9216\n"
        "v_mfma_f32_16x16x32_bf16 v[48:51], v[192:195], v[96:99], v[48:51]\n"
        "s_add_u32 s80, s75, 128\n"
        "ds_read_b128 v[40:43], v125 offset:10240\n"
        "v_mfma_f32_16x16x32_bf16 v[52:55], v[200:203], v[96:99], v[52:55]\n"
        "ds_read_b128 v[44:47], v125 offset:11264\n"
        "s_waitcnt lgkmcnt(11)\n"
        "v_mfma_f32_16x16x32_bf16 v[84:87], v[100:103], a[84:87], v[84:87]\n"
        "s_lshl_b32 s80, s80, 8\n"
        "ds_read_b128 v[96:99], v124 offset:10240\n"
        "v_mfma_f32_16x16x32_bf16 v[88:91], v[100:103], a[92:95], v[88:91]\n"
        "s_add_u32 s44, s44, 1\n"
        "s_add_u32 s61, s61, 32\n"
        "s_add_u32 s62, s80, 1\n"
        "v_mfma_f32_16x16x32_bf16 v[56:59], a[228:231], v[100:103], v[56:59]\n"
        "s_branch L_rot_1%=\n"
        "L_light_2%=:\n"
        "v_mfma_f32_16x16x32_bf16 v[52:55], v[252:255], v[104:107], v[52:55]\n"
        "v_mfma_f32_16x16x32_bf16 v[84:87], v[96:99], a[112:115], v[84:87]\n"
        "v_mfma_f32_16x16x32_bf16 v[88:91], v[96:99], a[120:123], v[88:91]\n"
        "v_cvt_pk_bf16_f32 v48, v48, v52\n"
        "v_cvt_pk_bf16_f32 v49, v49, v53\n"
        "v_mfma_f32_16x16x32_bf16 v[84:87], v[100:103], a[116:119], v[84:87]\n"
        "v_cvt_pk_bf16_f32 v50, v50, v54\n"
        "v_cvt_pk_bf16_f32 v51, v51, v55\n"
        "v_mfma_f32_16x16x32_bf16 v[88:91], v[100:103], a[124:127], v[88:91]\n"
        "v_mfma_f32_16x16x32_bf16 v[84:87], v[92:95], a[128:131], v[84:87]\n"
        "v_mfma_f32_16x16x32_bf16 v[88:91], v[92:95], a[136:139], v[88:91]\n"
        "buffer_store_dwordx4 v[48:51], v124, s[36:39], s54 offen offset:1024 sc1\n"
        "v_mfma_f32_16x16x32_bf16 v[84:87], v[104:107], a[132:135], v[84:87]\n"
        "v_mfma_f32_16x16x32_bf16 v[88:91], v[104:107], a[140:143], v[88:91]\n"
        "v_mfma_f32_16x16x32_bf16 v[56:59], v[32:35], v[92:95], v[56:59]\n"
        "v_mfma_f32_16x16x32_bf16 v[60:63], v[40:43], v[92:95], v[60:63]\n"
        "v_cvt_pk_bf16_f32 v84, v84, v85\n"
        "v_cvt_pk_bf16_f32 v85, v86, v87\n"
        "v_mfma_f32_16x16x32_bf16 v[56:59], v[16:19], v[96:99], v[56:59]\n"
        "v_cvt_pk_bf16_f32 v86, v88, v89\n"
        "v_cvt_pk_bf16_f32 v87, v90, v91\n"
        "v_mfma_f32_16x16x32_bf16 v[60:63], v[24:27], v[96:99], v[60:63]\n"
        "v_mfma_f32_16x16x32_bf16 v[56:59], v[20:23], v[100:103], v[56:59]\n"
        "v_mov_b64 v[92:93], 0\n"
        "v_mov_b64 v[94:95], 0\n"
        "v_mfma_f32_16x16x32_bf16 v[60:63], v[28:31], v[100:103], v[60:63]\n"
        "buffer_store_dwordx4 v[84:87], v124, s[36:39], s54 offen offset:2048 sc1\n"
        "v_mov_b64 v[96:97], 0\n"
        "v_mfma_f32_16x16x32_bf16 v[56:59], v[36:39], v[104:107], v[56:59]\n"
        "v_mov_b64 v[98:99], 0\n"
        "v_mfma_f32_16x16x32_bf16 v[60:63], v[44:47], v[104:107], v[60:63]\n"
        "s_nop 7\n"
        "v_cvt_pk_bf16_f32 v100, v56, v60\n"
        "v_cvt_pk_bf16_f32 v101, v57, v61\n"
        "v_cvt_pk_bf16_f32 v102, v58, v62\n"
        "v_cvt_pk_bf16_f32 v103, v59, v63\n"
        "buffer_store_dwordx4 v[100:103], v124, s[36:39], s54 offen offset:0 sc1\n"
        "s_barrier\n"
        "s_add_u32 s69, s58, s82\n"
        "s_sub_u32 s72, s58, s82\n"
        "s_add_u32 s87, s58, 8192\n"
        "s_waitcnt vmcnt(6)\n"
        "buffer_load_dwordx4 v[64:67], v124, s[36:39], s69 offen offset:0 sc1\n"
        "s_waitcnt vmcnt(6)\n"
        "buffer_load_dwordx4 v[68:71], v124, s[36:39], s69 offen offset:1024 sc1\n"
        "s_waitcnt vmcnt(6)\n"
        "buffer_load_dwordx4 v[72:75], v124, s[36:39], s72 offen offset:2048 sc1\n"
        "s_waitcnt vmcnt(6)\n"
        "buffer_load_dwordx4 v[76:79], v124, s[36:39], s72 offen offset:3072 sc1\n"
        "buffer_load_dwordx4 v[80:83], v124, s[36:39], s87 offen offset:0 sc1\n"
        "s_mov_b32 s81, 64\n"
        "buffer_load_dword v107, off, s[36:39], s81 sc1\n"
        "s_memrealtime s[26:27]\n"
        "s_waitcnt vmcnt(6)\n"
        "s_waitcnt lgkmcnt(0)\n"
        "s_barrier\n"
        "s_add_u32 s29, s29, 1024\n"
        "s_waitcnt vmcnt(0)\n"
        "L_par_2%=:\n"
        "v_readfirstlane_b32 s80, v107\n"
        "s_nop 0\n"
        "s_cmp_lt_i32 s80, 0\n"
        "s_cbranch_scc1 L_par2_2%=\n"
        "s_add_u32 s63, s63, 1\n"
        "s_cmp_gt_u32 s63, 0x40000\n"
        "s_cbranch_scc1 L_abort%=\n"
        "s_sleep 4\n"
        "buffer_load_dword v107, off, s[36:39], s81 sc1\n"
        "s_waitcnt vmcnt(0)\n"
        "s_branch L_par_2%=\n"
        "L_par2_2%=:\n"
        "s_bfe_u32 s46, s80, 0x10018\n"
        "s_lshl_b32 s46, s46, 21\n"
        "s_mov_b32 s53, 0x200000\n"
        "s_bfe_u32 s84, s80, 0x80010\n"
        "s_sub_u32 s84, 255, s84\n"
        "s_bfe_u32 s76, s80, 0x80008\n"
        "s_sub_u32 s76, s76, 32\n"
        "s_max_i32 s76, s76, 66\n"
        "s_bfe_u32 s77, s80, 0x80000\n"
        "s_sub_u32 s77, s77, 158\n"
        "s_bitset0_b32 s49, 30\n"
        "s_bitset0_b32 s50, 30\n"
        "s_bitset0_b32 s56, 30\n"
        "s_bitset0_b32 s64, 30\n"
        "v_mov_b32 v140, 0\n"
        "s_mov_b32 s28, 1000\n"
        "s_branch L_xok_2%=\n"
        "L_x2_2%=:\n"
        "s_add_u32 s69, s58, s82\n"
        "s_sub_u32 s72, s58, s82\n"
        "s_add_u32 s87, s58, 8192\n"
        "buffer_load_dwordx4 v[64:67], v124, s[36:39], s69 offen offset:0 sc1\n"
        "buffer_load_dwordx4 v[68:71], v124, s[36:39], s69 offen offset:1024 sc1\n"
        "buffer_load_dwordx4 v[72:75], v124, s[36:39], s72 offen offset:2048 sc1\n"
        "buffer_load_dwordx4 v[76:79], v124, s[36:39], s72 offen offset:3072 sc1\n"
        "buffer_load_dwordx4 v[80:83], v124, s[36:39], s87 offen offset:0 sc1\n"
        "s_waitcnt vmcnt(0)\n"
        "s_memrealtime s[26:27]\n"
        "s_waitcnt lgkmcnt(0)\n"
        "s_add_u32 s28, s28, s26\n"
        "L_xok_2%=:\n"
        "v_mfma_f32_16x16x32_bf16 v[84:87], v[64:67], a[0:3], 0\n"
        "v_mfma_f32_16x16x32_bf16 v[88:91], v[64:67], a[8:11], 0\n"
        "v_mfma_f32_16x16x32_bf16 v[56:59], a[144:147], v[64:67], 0\n"
        "s_branch L_win_2%=\n"
        "L_retry_2%=:\n"
        "s_add_u32 s63, s63, 1\n"
        "s_cmp_gt_u32 s63, 0x40000\n"
        "s_cbranch_scc1 L_abort%=\n"
        "s_sleep 2\n"
        "s_waitcnt vmcnt(0)\n"
        "s_waitcnt lgkmcnt(0)\n"
        "buffer_load_dwordx4 v[80:83], v138, s[36:39], s50 offen offset:0 sc1\n"
        "buffer_load_dwordx4 v[64:67], v138, s[36:39], s49 offen offset:0 sc1\n"
        "buffer_load_dwordx4 v[68:71], v138, s[36:39], s49 offen offset:256 sc1\n"
        "buffer_load_dwordx4 v[72:75], v138, s[36:39], s65 offen offset:0 sc1\n"
        "buffer_load_dwordx4 v[76:79], v138, s[36:39], s65 offen offset:256 sc1\n"
        "s_waitcnt vmcnt(4)\n"
        "v_bitop3_b32 v123, 32, v80, v82 bitop3:0x80\n"
        "v_add_u32_dpp v80, v81, v80 row_half_mirror row_mask:0xf bank_mask:0xf\n"
        "v_add_u32_dpp v82, v83, v82 row_half_mirror row_mask:0xf bank_mask:0xf\n"
        "v_cvt_f32_i32 v80, v80\n"
        "v_cvt_f32_i32 v82, v82\n"
        "v_cvt_pk_bf16_f32 v52, v80, v82\n"
        "ds_write_b32 v137, v52\n"
        "s_waitcnt vmcnt(3)\n"
        "v_bitop3_b32 v123, v123, v64, v66 bitop3:0x80\n"
        "v_cvt_f32_i32 v64, v64\n"
        "v_cvt_f32_i32 v65, v65\n"
        "v_cvt_f32_i32 v66, v66\n"
        "v_cvt_f32_i32 v67, v67\n"
        "v_cvt_pk_bf16_f32 v52, v64, v65\n"
        "v_cvt_pk_bf16_f32 v53, v66, v67\n"
        "ds_write_b64 v126, v[52:53] offset:0\n"
        "s_waitcnt vmcnt(2)\n"
        "v_bitop3_b32 v123, v123, v68, v70 bitop3:0x80\n"
        "v_cvt_f32_i32 v68, v68\n"
        "v_cvt_f32_i32 v69, v69\n"
        "v_cvt_f32_i32 v70, v70\n"
        "v_cvt_f32_i32 v71, v71\n"
        "v_cvt_pk_bf16_f32 v54, v68, v69\n"
        "v_cvt_pk_bf16_f32 v55, v70, v71\n"
        "ds_write_b64 v126, v[54:55] offset:512\n"
        "s_waitcnt vmcnt(1)\n"
        "v_bitop3_b32 v123, v123, v72, v74 bitop3:0x80\n"
        "v_cvt_f32_i32 v72, v72\n"
        "v_cvt_f32_i32 v73, v73\n"
        "ds_read_b128 v[48:51], v124 offset:4096\n"
        "v_cvt_f32_i32 v74, v74\n"
        "v_cvt_f32_i32 v75, v75\n"
        "v_cvt_pk_bf16_f32 v52, v72, v73\n"
        "v_cvt_pk_bf16_f32 v53, v74, v75\n"
        "ds_write_b64 v126, v[52:53] offset:1024\n"
        "s_waitcnt vmcnt(0)\n"
        "v_bitop3_b32 v123, v123, v76, v78 bitop3:0x80\n"
        "v_cmp_eq_u32 vcc, 0, v123\n"
        "v_mov_b64 v[122:123], 0\n"
        "v_cvt_f32_i32 v76, v76\n"
        "v_cvt_f32_i32 v77, v77\n"
        "v_cvt_f32_i32 v78, v78\n"
        "v_cvt_f32_i32 v79, v79\n"
        "v_cvt_pk_bf16_f32 v54, v76, v77\n"
        "v_cvt_pk_bf16_f32 v55, v78, v79\n"
        "ds_write_b64 v126, v[54:55] offset:1536\n"
        "v_mov_b64 v[92:93], 0\n"
        "v_mov_b64 v[94:95], 0\n"
        "v_mov_b64 v[96:97], 0\n"
        "v_mov_b64 v[98:99], 0\n"
        "s_waitcnt lgkmcnt(0)\n"
        "s_branch L_dec_2%=\n"
        "L_sw0_2%=:\n"
        "s_memrealtime s[26:27]\n"
        "s_waitcnt lgkmcnt(0)\n"
        "s_sub_u32 s28, s28, s26\n"
        "L_sw_2%=:\n"
        "s_add_u32 s63, s63, 1\n"
        "s_cmp_gt_u32 s63, 0x40000\n"
        "s_cbranch_scc1 L_abort%=\n"
        "s_sleep 2\n"
        "buffer_load_dword v140, v124, s[36:39], s29 offen sc1\n"
        "s_waitcnt vmcnt(0)\n"
        "v_cmp_eq_u32 vcc, 0, v140\n"
        "s_cbranch_vccnz L_sw_2%=\n"
        "s_memrealtime s[26:27]\n"
        "s_waitcnt lgkmcnt(0)\n"
        "s_add_u32 s28, s28, s26\n"
        "s_branch L_sok_2%=\n"
        ".p2align 6\n"
        "L_rot_2%=:\n"
        "v_mfma_f32_16x16x32_bf16 v[60:63], a[236:239], v[100:103], v[60:63]\n"
        "v_mfma_f32_16x16x32_bf16 v[48:51], v[196:199], v[100:103], v[48:51]\n"
        "v_mfma_f32_16x16x32_bf16 v[52:55], v[204:207], v[100:103], v[52:55]\n"
        "s_waitcnt lgkmcnt(10)\n"
        "v_mfma_f32_16x16x32_bf16 v[84:87], v[92:95], a[96:99], v[84:87]\n"
        "ds_read_b128 v[100:103], v124 offset:7168\n"
        "v_mfma_f32_16x16x32_bf16 v[88:91], v[92:95], a[104:107], v[88:91]\n"
        "v_mfma_f32_16x16x32_bf16 v[56:59], v[0:3], v[92:95], v[56:59]\n"
        "v_mfma_f32_16x16x32_bf16 v[60:63], v[8:11], v[92:95], v[60:63]\n"
        "v_mfma_f32_16x16x32_bf16 v[48:51], v[208:211], v[92:95], v[48:51]\n"
        "s_waitcnt lgkmcnt(0)\n"
        "s_barrier\n"
        "v_mfma_f32_16x16x32_bf16 v[52:55], v[216:219], v[92:95], v[52:55]\n"
        "v_mov_b64 v[116:117], 0\n"
        "v_mfma_f32_16x16x32_bf16 v[84:87], v[104:107], a[100:103], v[84:87]\n"
        "ds_read_b128 v[92:95], v124 offset:16384\n"
        "v_mov_b64 v[118:119], 0\n"
        "v_mfma_f32_16x16x32_bf16 v[88:91], v[104:107], a[108:111], v[88:91]\n"
        "v_mov_b64 v[120:121], 0\n"
        "v_mfma_f32_16x16x32_bf16 v[56:59], v[4:7], v[104:107], v[56:59]\n"
        "v_mfma_f32_16x16x32_bf16 v[60:63], v[12:15], v[104:107], v[60:63]\n"
        "v_mfma_f32_16x16x32_bf16 v[48:51], v[212:215], v[104:107], v[48:51]\n"
        "v_mfma_f32_16x16x32_bf16 v[52:55], v[220:223], v[104:107], v[52:55]\n"
        "v_mfma_f32_16x16x32_bf16 v[48:51], v[224:227], v[96:99], v[48:51]\n"
        "ds_read_b128 v[104:107], v124 offset:17408\n"
        "v_mfma_f32_16x16x32_bf16 v[52:55], v[232:235], v[96:99], v[52:55]\n"
        "v_mfma_f32_16x16x32_bf16 v[48:51], v[228:231], v[100:103], v[48:51]\n"
        "v_mfma_f32_16x16x32_bf16 v[52:55], v[236:239], v[100:103], v[52:55]\n"
        "s_waitcnt lgkmcnt(1)\n"
        "v_mfma_f32_16x16x32_bf16 v[48:51], v[240:243], v[92:95], v[48:51]\n"
        "v_mfma_f32_16x16x32_bf16 v[52:55], v[248:251], v[92:95], v[52:55]\n"
        "s_waitcnt lgkmcnt(0)\n"
        "v_mfma_f32_16x16x32_bf16 v[48:51], v[244:247], v[104:107], v[48:51]\n"
        "s_cmp_lt_i32 s44, 0\n"
        "s_cbranch_scc1 L_light_2%=\n"
        "v_mfma_f32_16x16x32_bf16 v[52:55], v[252:255], v[104:107], v[52:55]\n"
        "s_waitcnt vmcnt(4)\n"
        "v_bitop3_b32 v123, 32, v80, v82 bitop3:0x80\n"
        "s_nop 0\n"
        "v_add_u32_dpp v80, v81, v80 row_half_mirror row_mask:0xf bank_mask:0xf\n"
        "v_mfma_f32_16x16x32_bf16 v[84:87], v[96:99], a[112:115], v[84:87]\n"
        "s_nop 0\n"
        "v_add_u32_dpp v82, v83, v82 row_half_mirror row_mask:0xf bank_mask:0xf\n"
        "v_mfma_f32_16x16x32_bf16 v[88:91], v[96:99], a[120:123], v[88:91]\n"
        "v_cvt_pk_bf16_f32 v48, v48, v52\n"
        "v_cvt_pk_bf16_f32 v49, v49, v53\n"
        "s_nop 0\n"
        "v_mfma_f32_16x16x32_bf16 v[84:87], v[100:103], a[116:119], v[84:87]\n"
        "v_cvt_pk_bf16_f32 v50, v50, v54\n"
        "v_cvt_pk_bf16_f32 v51, v51, v55\n"
        "v_mfma_f32_16x16x32_bf16 v[88:91], v[100:103], a[124:127], v[88:91]\n"
        "v_cvt_f32_i32 v80, v80\n"
        "s_nop 0\n"
        "v_cvt_f32_i32 v82, v82\n"
        "v_mfma_f32_16x16x32_bf16 v[84:87], v[92:95], a[128:131], v[84:87]\n"
        "s_nop 0\n"
        "v_cvt_pk_bf16_f32 v52, v80, v82\n"
        "v_mfma_f32_16x16x32_bf16 v[88:91], v[92:95], a[136:139], v[88:91]\n"
        "buffer_store_dwordx4 v[48:51], v124, s[36:39], s54 offen offset:1024 sc1\n"
        "v_mfma_f32_16x16x32_bf16 v[84:87], v[104:107], a[132:135], v[84:87]\n"
        "s_nop 0\n"
        "v_mfma_f32_16x16x32_bf16 v[88:91], v[104:107], a[140:143], v[88:91]\n"
        "ds_write_b32 v137, v52\n"
        "s_waitcnt vmcnt(4)\n"
        "v_bitop3_b32 v123, v123, v64, v66 bitop3:0x80\n"
        "v_cvt_f32_i32 v64, v64\n"
        "v_mfma_f32_16x16x32_bf16 v[56:59], v[32:35], v[92:95], v[56:59]\n"
        "v_cvt_f32_i32 v65, v65\n"
        "v_cvt_f32_i32 v66, v66\n"
        "v_mfma_f32_16x16x32_bf16 v[60:63], v[40:43], v[92:95], v[60:63]\n"
        "v_cvt_pk_bf16_f32 v84, v84, v85\n"
        "v_cvt_pk_bf16_f32 v85, v86, v87\n"
        "v_mfma_f32_16x16x32_bf16 v[56:59], v[16:19], v[96:99], v[56:59]\n"
        "v_cvt_pk_bf16_f32 v86, v88, v89\n"
        "v_cvt_pk_bf16_f32 v87, v90, v91\n"
        "v_mfma_f32_16x16x32_bf16 v[60:63], v[24:27], v[96:99], v[60:63]\n"
        "v_cvt_f32_i32 v67, v67\n"
        "v_cvt_pk_bf16_f32 v52, v64, v65\n"
        "v_mfma_f32_16x16x32_bf16 v[56:59], v[20:23], v[100:103], v[56:59]\n"
        "v_mov_b64 v[92:93], 0\n"
        "v_mov_b64 v[94:95], 0\n"
        "v_mfma_f32_16x16x32_bf16 v[60:63], v[28:31], v[100:103], v[60:63]\n"
        "buffer_store_dwordx4 v[84:87], v124, s[36:39], s54 offen offset:2048 sc1\n"
        "v_mov_b64 v[96:97], 0\n"
        "v_cvt_pk_bf16_f32 v53, v66, v67\n"
        "v_mfma_f32_16x16x32_bf16 v[56:59], v[36:39], v[104:107], v[56:59]\n"
        "v_mov_b64 v[98:99], 0\n"
        "ds_write_b64 v126, v[52:53] offset:0\n"
        "s_waitcnt vmcnt(4)\n"
        "v_bitop3_b32 v123, v123, v68, v70 bitop3:0x80\n"
        "v_mfma_f32_16x16x32_bf16 v[60:63], v[44:47], v[104:107], v[60:63]\n"
        "v_cvt_f32_i32 v68, v68\n"
        "v_cvt_f32_i32 v69, v69\n"
        "v_cvt_f32_i32 v70, v70\n"
        "v_cvt_f32_i32 v71, v71\n"
        "v_cvt_pk_bf16_f32 v54, v68, v69\n"
        "v_cvt_pk_bf16_f32 v55, v70, v71\n"
        "ds_write_b64 v126, v[54:55] offset:512\n"
        "s_waitcnt vmcnt(3)\n"
        "v_bitop3_b32 v123, v123, v72, v74 bitop3:0x80\n"
        "v_cvt_f32_i32 v72, v72\n"
        "v_cvt_pk_bf16_f32 v100, v56, v60\n"
        "v_cvt_pk_bf16_f32 v101, v57, v61\n"
        "v_cvt_pk_bf16_f32 v102, v58, v62\n"
        "v_cvt_pk_bf16_f32 v103, v59, v63\n"
        "v_cvt_f32_i32 v73, v73\n"
        "ds_read_b128 v[48:51], v124 offset:4096\n"
        "v_cvt_f32_i32 v74, v74\n"
        "v_cvt_f32_i32 v75, v75\n"
        "v_cvt_pk_bf16_f32 v52, v72, v73\n"
        "v_cvt_pk_bf16_f32 v53, v74, v75\n"
        "ds_write_b64 v126, v[52:53] offset:1024\n"
        "s_waitcnt vmcnt(2)\n"
        "v_bitop3_b32 v123, v123, v76, v78 bitop3:0x80\n"
        "v_cmp_eq_u32 vcc, 0, v123\n"
        "v_mov_b64 v[122:123], 0\n"
        "v_cvt_f32_i32 v76, v76\n"
        "v_cvt_f32_i32 v77, v77\n"
        "v_cvt_f32_i32 v78, v78\n"
        "v_cvt_f32_i32 v79, v79\n"
        "v_cvt_pk_bf16_f32 v54, v76, v77\n"
        "v_cvt_pk_bf16_f32 v55, v78, v79\n"
        "ds_write_b64 v126, v[54:55] offset:1536\n"
        "L_dec_2%=:\n"
        "s_waitcnt lgkmcnt(2)\n"
        "v_smfmac_f32_16x16x64_bf16 v[92:95], v[48:51], a[144:151], v134\n"
        "ds_read_b128 v[52:55], v124 offset:5120\n"
        "v_max3_f32 v104, |v80|, |v82|, |v64|\n"
        "v_smfmac_f32_16x16x64_bf16 v[96:99], v[48:51], a[152:159], v134\n"
        "s_cbranch_vccnz L_retry_2%=\n"
        "v_max3_f32 v104, |v65|, |v66|, v104\n"
        "v_smfmac_f32_16x16x64_bf16 v[116:119], v[48:51], a[240:247], v134\n"
        "s_add_u32 s29, s29, 1024\n"
        "v_max3_f32 v104, |v67|, |v68|, v104\n"
        "v_smfmac_f32_16x16x64_bf16 v[120:123], v[48:51], a[248:255], v134\n"
        "s_waitcnt lgkmcnt(0)\n"
        "v_max3_f32 v104, |v69|, |v70|, v104\n"
        "s_barrier\n"
        "ds_read_b128 v[56:59], v124 offset:2048\n"
        "v_smfmac_f32_16x16x64_bf16 v[92:95], v[52:55], a[160:167], v134\n"
        "ds_read_b128 v[60:63], v124 offset:1024\n"
        "v_max3_f32 v104, |v71|, |v72|, v104\n"
        "v_smfmac_f32_16x16x64_bf16 v[96:99], v[52:55], a[168:175], v134\n"
        "buffer_load_dwordx4 v[88:91], v124, s[36:39], s55 offen offset:3072 sc1\n"
        "ds_read_b128 v[80:83], v124 offset:3072\n"
        "v_max3_f32 v104, |v73|, |v74|, v104\n"
        "v_smfmac_f32_16x16x64_bf16 v[116:119], v[52:55], v[176:183], v134\n"
        "ds_read_b128 v[64:67], v124 offset:8192\n"
        "v_max3_f32 v104, |v75|, |v76|, v104\n"
        "v_smfmac_f32_16x16x64_bf16 v[120:123], v[52:55], v[184:191], v134\n"
        "buffer_load_dwordx4 v[84:87], v124, s[36:39], s55 offen offset:0 sc1\n"
        "ds_read_b128 v[68:71], v124 offset:6144\n"
        "v_max3_f32 v104, |v77|, |v78|, v104\n"
        "s_waitcnt lgkmcnt(4)\n"
        "v_smfmac_f32_16x16x64_bf16 v[92:95], v[56:59], v[0:7], v134\n"
        "ds_read_b128 v[72:75], v124 offset:7168\n"
        "v_max_f32_e64 v104, |v79|, v104\n"
        "v_smfmac_f32_16x16x64_bf16 v[96:99], v[56:59], v[8:15], v134\n"
        "ds_read_b128 v[0:3], v125 offset:12288\n"
        "v_max_f32_dpp v104, v104, v104 row_shr:1 row_mask:0xf bank_mask:0xf\n"
        "s_waitcnt lgkmcnt(3)\n"
        "v_smfmac_f32_16x16x64_bf16 v[92:95], v[60:63], a[224:231], v134\n"
        "ds_read_b128 v[76:79], v124 offset:0\n"
        "v_max_f32_dpp v104, v104, v104 row_shr:2 row_mask:0xf bank_mask:0xf\n"
        "v_smfmac_f32_16x16x64_bf16 v[96:99], v[60:63], a[232:239], v134\n"
        "ds_read_b128 v[4:7], v125 offset:13312\n"
        "v_max_f32_dpp v104, v104, v104 row_shr:4 row_mask:0xf bank_mask:0xf\n"
        "v_smfmac_f32_16x16x64_bf16 v[92:95], v[80:83], v[16:23], v134\n"
        "ds_read_b128 v[8:11], v125 offset:14336\n"
        "v_max_f32_dpp v104, v104, v104 row_shr:8 row_mask:0xf bank_mask:0xf\n"
        "v_smfmac_f32_16x16x64_bf16 v[96:99], v[80:83], v[24:31], v134\n"
        "ds_read_b128 v[12:15], v125 offset:15360\n"
        "v_max_f32_dpp v104, v104, v104 row_bcast:15 row_mask:0xa bank_mask:0xf\n"
        "v_smfmac_f32_16x16x64_bf16 v[92:95], v[64:67], v[32:39], v134\n"
        "ds_read_b128 v[16:19], v125 offset:16384\n"
        "v_max_f32_dpp v104, v104, v104 row_bcast:31 row_mask:0xc bank_mask:0xf\n"
        "v_smfmac_f32_16x16x64_bf16 v[96:99], v[64:67], v[40:47], v134\n"
        "ds_read_b128 v[20:23], v125 offset:17408\n"
        "v_readlane_b32 s85, v104, 63\n"
        "s_waitcnt lgkmcnt(5)\n"
        "v_smfmac_f32_16x16x64_bf16 v[92:95], v[68:71], a[176:183], v134\n"
        "ds_read_b128 v[24:27], v125 offset:18432\n"
        "v_smfmac_f32_16x16x64_bf16 v[96:99], v[68:71], a[184:191], v134\n"
        "ds_read_b128 v[28:31], v125 offset:19456\n"
        "v_mov_b32 v104, s85\n"
        "v_smfmac_f32_16x16x64_bf16 v[92:95], v[72:75], a[192:199], v134\n"
        "ds_read_b128 v[32:35], v125 offset:20480\n"
        "v_smfmac_f32_16x16x64_bf16 v[96:99], v[72:75], a[200:207], v134\n"
        "ds_read_b128 v[36:39], v125 offset:21504\n"
        "s_add_u32 s69, s58, s82\n"
        "v_smfmac_f32_16x16x64_bf16 v[92:95], v[76:79], a[208:215], v134\n"
        "ds_read_b128 v[40:43], v125 offset:22528\n"
        "s_sub_u32 s72, s58, s82\n"
        "v_smfmac_f32_16x16x64_bf16 v[96:99], v[76:79], a[216:223], v134\n"
        "ds_read_b128 v[44:47], v125 offset:23552\n"
        "s_add_u32 s87, s58, 8192\n"
        "v_smfmac_f32_16x16x64_bf16 v[116:119], v[64:67], v[240:247], v134\n"
        "ds_write_b32 v142, v104\n"
        "v_add_f32 v92, v92, v94\n"
        "v_add_f32 v93, v93, v95\n"
        "v_smfmac_f32_16x16x64_bf16 v[120:123], v[64:67], v[248:255], v134\n"
        "v_add_f32 v96, v96, v98\n"
        "v_add_f32 v97, v97, v99\n"
        "v_cvt_pk_bf16_f32 v92, v92, v96\n"
        "v_cvt_pk_bf16_f32 v93, v93, v97\n"
        "s_waitcnt lgkmcnt(3)\n"
        "v_smfmac_f32_16x16x64_bf16 v[116:119], v[68:71], v[0:7], v134\n"
        "ds_write2_b32 v127, v92, v93 offset1:72\n"
        "v_smfmac_f32_16x16x64_bf16 v[120:123], v[68:71], v[8:15], v134\n"
        "v_mov_b64 v[98:99], 0\n"
        "v_smfmac_f32_16x16x64_bf16 v[116:119], v[72:75], v[16:23], v134\n"
        "v_mov_b64 v[96:97], 0\n"
        "v_smfmac_f32_16x16x64_bf16 v[120:123], v[72:75], v[24:31], v134\n"
        "s_waitcnt vmcnt(3)\n"
        "s_waitcnt lgkmcnt(0)\n"
        "v_mov_b64 v[106:107], 0\n"
        "s_barrier\n"
        "ds_read_b128 v[0:3], v128 offset:0\n"
        "v_smfmac_f32_16x16x64_bf16 v[116:119], v[76:79], v[32:39], v134\n"
        "ds_read_b128 v[24:27], v124 offset:20480\n"
        "v_mov_b64 v[104:105], 0\n"
        "v_smfmac_f32_16x16x64_bf16 v[120:123], v[76:79], v[40:47], v134\n"
        "ds_read_b128 v[4:7], v128 offset:64\n"
        "v_mov_b64 v[94:95], 0\n"
        "v_smfmac_f32_16x16x64_bf16 v[116:119], v[60:63], v[192:199], v134\n"
        "ds_read_b128 v[8:11], v128 offset:128\n"
        "v_mov_b64 v[92:93], 0\n"
        "v_smfmac_f32_16x16x64_bf16 v[120:123], v[60:63], v[200:207], v134\n"
        "ds_read_b128 v[12:15], v128 offset:192\n"
        "v_mov_b64 v[20:21], 0\n"
        "v_smfmac_f32_16x16x64_bf16 v[116:119], v[56:59], v[208:215], v134\n"
        "s_waitcnt lgkmcnt(3)\n"
        "v_max3_f32 v24, v24, v25, v26\n"
        "s_waitcnt lgkmcnt(2)\n"
        "v_smfmac_f32_16x16x64_bf16 v[104:107], v[108:111], v[0:7], v134\n"
        "v_max_f32 v24, v24, v27\n"
        "v_smfmac_f32_16x16x64_bf16 v[120:123], v[56:59], v[216:223], v134\n"
        "v_readfirstlane_b32 s84, v24\n"
        "s_waitcnt lgkmcnt(0)\n"
        "v_smfmac_f32_16x16x64_bf16 v[104:107], v[112:115], v[8:15], v134\n"
        "v_mov_b64 v[22:23], 0\n"
        "v_smfmac_f32_16x16x64_bf16 v[116:119], v[80:83], v[224:231], v134\n"
        "s_lshr_b32 s84, s84, 23\n"
        "s_add_u32 s84, s84, s101\n"
        "v_smfmac_f32_16x16x64_bf16 v[120:123], v[80:83], v[232:239], v134\n"
        "s_max_i32 s84, s84, s76\n"
        "s_min_i32 s84, s84, 186\n"
        "v_add_f32 v104, v104, v106\n"
        "v_add_f32 v105, v105, v107\n"
        "v_smfmac_f32_16x16x64_bf16 v[92:95], v[64:67], a[128:135], v134\n"
        "ds_write_b64 v131, v[104:105]\n"
        "v_add_f32 v116, v116, v118\n"
        "v_add_f32 v117, v117, v119\n"
        "v_pk_add_f32 v[120:121], v[120:121], v[122:123]\n"
        "v_cvt_pk_bf16_f32 v116, v116, v120\n"
        "v_cvt_pk_bf16_f32 v117, v117, v121\n"
        "ds_write2_b32 v129, v116, v117 offset1:8\n"
        "v_smfmac_f32_16x16x64_bf16 v[96:99], v[64:67], a[136:143], v134\n"
        "s_mul_i32 s88, s84, 0x800080\n"
        "s_mov_b32 s89, s88\n"
        "v_smfmac_f32_16x16x64_bf16 v[92:95], v[68:71], a[32:39], v134\n"
        "buffer_store_dwordx4 v[100:103], v124, s[36:39], s54 offen offset:0 sc1\n"
        "v_mov_b64 v[104:105], s[88:89]\n"
        "v_smfmac_f32_16x16x64_bf16 v[96:99], v[68:71], a[40:47], v134\n"
        "s_add_u32 s90, s88, s35\n"
        "s_mov_b32 s91, s90\n"
        "v_mov_b64 v[106:107], s[90:91]\n"
        "v_smfmac_f32_16x16x64_bf16 v[92:95], v[48:51], a[0:7], v134\n"
        "s_waitcnt lgkmcnt(0)\n"
        "v_mov_b64 v[100:101], s[88:89]\n"
        "s_barrier\n"
        "ds_read_b128 v[24:27], v130\n"
        "v_smfmac_f32_16x16x64_bf16 v[96:99], v[48:51], a[8:15], v134\n"
        "ds_read_b128 v[28:31], v130 offset:1024\n"
        "v_mov_b64 v[102:103], s[90:91]\n"
        "v_smfmac_f32_16x16x64_bf16 v[92:95], v[52:55], a[16:23], v134\n"
        "ds_read_b128 v[116:119], v124 offset:13568\n"
        "v_mov_b64 v[48:49], 0\n"
        "v_smfmac_f32_16x16x64_bf16 v[96:99], v[52:55], a[24:31], v134\n"
        "buffer_load_dwordx4 v[64:67], v124, s[36:39], s69 offen offset:0 sc1\n"
        "v_mov_b64 v[50:51], 0\n"
        "v_smfmac_f32_16x16x64_bf16 v[92:95], v[56:59], a[96:103], v134\n"
        "v_mov_b64 v[52:53], 0\n"
        "v_smfmac_f32_16x16x64_bf16 v[96:99], v[56:59], a[104:111], v134\n"
        "buffer_load_dwordx4 v[68:71], v124, s[36:39], s69 offen offset:1024 sc1\n"
        "v_mov_b64 v[54:55], 0\n"
        "s_waitcnt lgkmcnt(2)\n"
        "v_smfmac_f32_16x16x64_bf16 v[20:23], v[24:27], v[0:7], v134\n"
        "v_mov_b64 v[56:57], 0\n"
        "s_waitcnt lgkmcnt(0)\n"
        "v_smfmac_f32_16x16x64_bf16 v[20:23], v[28:31], v[8:15], v134\n"
        "v_max3_f32 v121, v116, v117, v118\n"
        "v_smfmac_f32_16x16x64_bf16 v[92:95], v[72:75], a[48:55], v134\n"
        "ds_read_b128 v[0:3], v125 offset:24576\n"
        "v_mov_b64 v[58:59], 0\n"
        "v_smfmac_f32_16x16x64_bf16 v[96:99], v[72:75], a[56:63], v134\n"
        "buffer_load_dwordx4 v[72:75], v124, s[36:39], s72 offen offset:2048 sc1\n"
        "v_add_f32 v20, v20, v22\n"
        "v_add_f32 v21, v21, v23\n"
        "v_cndmask_b32 v120, v20, v21, s[92:93]\n"
        "v_mul_f32 v120, s74, v120\n"
        "v_max3_f32 v121, v121, v119, v120\n"
        "v_smfmac_f32_16x16x64_bf16 v[92:95], v[76:79], a[64:71], v134\n"
        "ds_read_b128 v[4:7], v125 offset:25600\n"
        "v_max_f32_dpp v121, v121, v121 quad_perm:[2,3,0,1] row_mask:0xf bank_mask:0xf\n"
        "v_mov_b32 v122, v121\n"
        "v_smfmac_f32_16x16x64_bf16 v[96:99], v[76:79], a[72:79], v134\n"
        "buffer_load_dwordx4 v[76:79], v124, s[36:39], s72 offen offset:3072 sc1\n"
        "v_permlane16_swap_b32 v121, v122\n"
        "v_max_f32 v121, v121, v122\n"
        "v_mov_b32 v122, v121\n"
        "v_smfmac_f32_16x16x64_bf16 v[92:95], v[80:83], a[112:119], v134\n"
        "ds_read_b128 v[8:11], v125 offset:26624\n"
        "v_permlane32_swap_b32 v121, v122\n"
        "v_smfmac_f32_16x16x64_bf16 v[96:99], v[80:83], a[120:127], v134\n"
        "buffer_load_dwordx4 v[80:83], v124, s[36:39], s87 offen offset:0 sc1\n"
        "v_max_f32 v121, v121, v122\n"
        "v_mul_f32_e64 v123, -s73, v121\n"
        "v_fma_f32 v120, v120, s73, v123\n"
        "v_fma_f32 v116, v116, s73, v123\n"
        "v_smfmac_f32_16x16x64_bf16 v[92:95], v[60:63], a[80:87], v134\n"
        "ds_read_b128 v[12:15], v125 offset:27648\n"
        "v_fma_f32 v117, v117, s73, v123\n"
        "v_fma_f32 v118, v118, s73, v123\n"
        "v_fma_f32 v119, v119, s73, v123\n"
        "v_exp_f32 v120, v120\n"
        "v_smfmac_f32_16x16x64_bf16 v[96:99], v[60:63], a[88:95], v134\n"
        "ds_read_b128 v[16:19], v125 offset:28672\n"
        "v_exp_f32 v116, v116\n"
        "v_mul_f32 v120, s74, v120\n"
        "v_exp_f32 v117, v117\n"
        "v_exp_f32 v118, v118\n"
        "v_exp_f32 v119, v119\n"
        "s_waitcnt vmcnt(6)\n"
        "v_mov_b64 v[108:109], v[86:87]\n"
        "v_mov_b64 v[112:113], v[90:91]\n"
        "v_cvt_pk_bf16_f32 v120, v120, v120\n"
        "v_cvt_pk_bf16_f32 v60, v116, v117\n"
        "v_cvt_pk_bf16_f32 v62, v118, v119\n"
        "v_and_b32 v61, v120, v135\n"
        "v_and_b32 v63, v120, v136\n"
        "v_pk_add_f32 v[92:93], v[92:93], v[94:95]\n"
        "v_pk_add_f32 v[96:97], v[96:97], v[98:99]\n"
        "v_smfmac_f32_16x16x64_bf16 v[56:59], v[60:63], v[100:107], v134\n"
        "ds_read_b128 v[20:23], v125 offset:29696\n"
        "v_cvt_pk_bf16_f32 v86, v92, v93\n"
        "v_cvt_pk_bf16_f32 v87, v92, v93\n"
        "v_mov_b64 v[90:91], v[86:87]\n"
        "v_cvt_pk_bf16_f32 v110, v96, v97\n"
        "v_cvt_pk_bf16_f32 v111, v96, v97\n"
        "v_smfmac_f32_16x16x64_bf16 v[48:51], v[60:63], v[84:91], v134\n"
        "ds_read_b128 v[28:31], v125 offset:31744\n"
        "v_mov_b64 v[114:115], v[110:111]\n"
        "v_add_f32 v56, v56, v58\n"
        "v_add_f32 v57, v57, v59\n"
        "v_smfmac_f32_16x16x64_bf16 v[52:55], v[60:63], v[108:115], v134\n"
        "s_nop 1\n"
        "v_rcp_f32 v56, v56\n"
        "v_rcp_f32 v57, v57\n"
        "v_pk_add_f32 v[48:49], v[48:49], v[50:51]\n"
        "v_pk_mul_f32 v[48:49], v[48:49], v[56:57]\n"
        "v_mov_b32 v110, v86\n"
        "v_mov_b64 v[92:93], s[70:71]\n"
        "v_pk_add_f32 v[52:53], v[52:53], v[54:55]\n"
        "v_pk_mul_f32 v[52:53], v[52:53], v[56:57]\n"
        "v_cvt_pk_bf16_f32 v48, v48, v52\n"
        "v_cvt_pk_bf16_f32 v49, v49, v53\n"
        "ds_write2_b32 v139, v48, v49 offset1:4\n"
        "v_mov_b64 v[94:95], 0\n"
        "v_mov_b64 v[96:97], 0\n"
        "v_mov_b64 v[98:99], 0\n"
        "v_mov_b64 v[100:101], s[70:71]\n"
        "v_mov_b64 v[102:103], 0\n"
        "buffer_store_dwordx2 v[110:111], v126, s[36:39], s60 offen sc1\n"
        "v_mov_b64 v[104:105], 0\n"
        "s_waitcnt lgkmcnt(0)\n"
        "v_mov_b64 v[106:107], 0\n"
        "s_barrier\n"
        "ds_read_b128 v[108:111], v124 offset:18432\n"
        "v_mov_b64 v[106:107], 0\n"
        "ds_read_b128 v[112:115], v124 offset:19456\n"
        "v_mov_b64 v[106:107], 0\n"
        "s_bitcmp1_b32 s59, 30\n"
        "s_cbranch_scc1 L_s1_2%=\n"
        "buffer_store_dwordx4 v[24:27], v124, s[36:39], s59 offen offset:0 sc1\n"
        "L_s1_2%=:\n"
        "v_mov_b64 v[106:107], 0\n"
        "v_mov_b64 v[106:107], 0\n"
        "ds_read_b128 v[24:27], v125 offset:30720\n"
        "v_mov_b64 v[106:107], 0\n"
        "s_waitcnt lgkmcnt(2)\n"
        "v_smfmac_f32_16x16x64_bf16 v[92:95], v[108:111], v[144:151], v134\n"
        "v_smfmac_f32_16x16x64_bf16 v[96:99], v[108:111], v[152:159], v134\n"
        "s_waitcnt lgkmcnt(1)\n"
        "v_smfmac_f32_16x16x64_bf16 v[92:95], v[112:115], v[160:167], v134\n"
        "v_smfmac_f32_16x16x64_bf16 v[96:99], v[112:115], v[168:175], v134\n"
        "v_smfmac_f32_16x16x64_bf16 v[100:103], v[108:111], v[0:7], v134\n"
        "v_add_f32 v92, v92, v94\n"
        "v_add_f32 v93, v93, v95\n"
        "v_cvt_rpi_i32_f32 v94, v92\n"
        "v_smfmac_f32_16x16x64_bf16 v[104:107], v[108:111], v[8:15], v134\n"
        "buffer_load_dword v140, v124, s[36:39], s29 offen sc1\n"
        "v_bfi_b32 v94, 63, 1, v94\n"
        "v_add_f32 v96, v96, v98\n"
        "v_smfmac_f32_16x16x64_bf16 v[100:103], v[112:115], v[16:23], v134\n"
        "v_cvt_rpi_i32_f32 v95, v96\n"
        "s_waitcnt lgkmcnt(0)\n"
        "v_smfmac_f32_16x16x64_bf16 v[104:107], v[112:115], v[24:31], v134\n"
        "buffer_atomic_add_x2 v[94:95], v133, s[36:39], s51 offen\n"
        "v_add_f32 v97, v97, v99\n"
        "v_cvt_rpi_i32_f32 v98, v93\n"
        "v_bfi_b32 v98, 63, 1, v98\n"
        "v_cvt_rpi_i32_f32 v99, v97\n"
        "s_waitcnt vmcnt(7)\n"
        "v_mfma_f32_16x16x32_bf16 v[84:87], v[64:67], a[0:3], 0\n"
        "buffer_atomic_add_x2 v[98:99], v133, s[36:39], s66 offen\n"
        "v_add_f32 v100, v100, v102\n"
        "v_add_f32 v101, v101, v103\n"
        "v_add_f32 v104, v104, v106\n"
        "v_cvt_rpi_i32_f32 v102, v100\n"
        "v_bfi_b32 v102, 63, 1, v102\n"
        "v_cvt_rpi_i32_f32 v103, v104\n"
        "v_mfma_f32_16x16x32_bf16 v[88:91], v[64:67], a[8:11], 0\n"
        "buffer_atomic_add_x2 v[102:103], v133, s[36:39], s67 offen\n"
        "v_add_f32 v105, v105, v107\n"
        "v_cvt_rpi_i32_f32 v106, v101\n"
        "v_bfi_b32 v106, 63, 1, v106\n"
        "v_cvt_rpi_i32_f32 v107, v105\n"
        "v_mfma_f32_16x16x32_bf16 v[56:59], a[144:147], v[64:67], 0\n"
        "buffer_atomic_add_x2 v[106:107], v133, s[36:39], s68 offen\n"
        "L_win_2%=:\n"
        "v_mfma_f32_16x16x32_bf16 v[60:63], a[152:155], v[64:67], 0\n"
        "v_mfma_f32_16x16x32_bf16 v[48:51], a[240:243], v[64:67], 0\n"
        "ds_write_b128 v124, v[64:67] offset:8192\n"
        "v_mfma_f32_16x16x32_bf16 v[52:55], a[248:251], v[64:67], 0\n"
        "s_waitcnt vmcnt(9)\n"
        "v_mfma_f32_16x16x32_bf16 v[84:87], v[68:71], a[4:7], v[84:87]\n"
        "ds_write_b128 v124, v[68:71] offset:9216\n"
        "v_mfma_f32_16x16x32_bf16 v[88:91], v[68:71], a[12:15], v[88:91]\n"
        "v_mfma_f32_16x16x32_bf16 v[56:59], a[148:151], v[68:71], v[56:59]\n"
        "s_waitcnt vmcnt(8)\n"
        "ds_write_b128 v124, v[72:75] offset:10240\n"
        "v_mfma_f32_16x16x32_bf16 v[60:63], a[156:159], v[68:71], v[60:63]\n"
        "v_mfma_f32_16x16x32_bf16 v[48:51], a[244:247], v[68:71], v[48:51]\n"
        "s_waitcnt vmcnt(7)\n"
        "ds_write_b128 v124, v[76:79] offset:11264\n"
        "v_mfma_f32_16x16x32_bf16 v[52:55], a[252:255], v[68:71], v[52:55]\n"
        "v_mfma_f32_16x16x32_bf16 v[84:87], v[72:75], a[16:19], v[84:87]\n"
        "s_waitcnt vmcnt(6)\n"
        "ds_write_b128 v124, v[80:83] offset:16384\n"
        "v_mfma_f32_16x16x32_bf16 v[88:91], v[72:75], a[24:27], v[88:91]\n"
        "v_mfma_f32_16x16x32_bf16 v[56:59], a[160:163], v[72:75], v[56:59]\n"
        "v_mfma_f32_16x16x32_bf16 v[60:63], a[168:171], v[72:75], v[60:63]\n"
        "v_mfma_f32_16x16x32_bf16 v[48:51], v[176:179], v[72:75], v[48:51]\n"
        "ds_read_b128 v[0:3], v125 offset:12288\n"
        "v_mfma_f32_16x16x32_bf16 v[52:55], v[184:187], v[72:75], v[52:55]\n"
        "ds_read_b128 v[8:11], v125 offset:14336\n"
        "v_mfma_f32_16x16x32_bf16 v[84:87], v[76:79], a[20:23], v[84:87]\n"
        "s_waitcnt lgkmcnt(2)\n"
        "s_barrier\n"
        "v_mfma_f32_16x16x32_bf16 v[88:91], v[76:79], a[28:31], v[88:91]\n"
        "ds_read_b128 v[92:95], v124 offset:12288\n"
        "v_mov_b32 v116, s62\n"
        "v_mfma_f32_16x16x32_bf16 v[56:59], a[164:167], v[76:79], v[56:59]\n"
        "s_add_u32 s49, s49, 163840\n"
        "ds_read_b128 v[104:107], v124 offset:13312\n"
        "v_mfma_f32_16x16x32_bf16 v[60:63], a[172:175], v[76:79], v[60:63]\n"
        "s_add_u32 s50, s50, 163840\n"
        "ds_read_b128 v[4:7], v125 offset:13312\n"
        "v_mfma_f32_16x16x32_bf16 v[48:51], v[180:183], v[76:79], v[48:51]\n"
        "s_xor_b32 s49, s49, s86\n"
        "ds_read_b128 v[12:15], v125 offset:15360\n"
        "v_mfma_f32_16x16x32_bf16 v[52:55], v[188:191], v[76:79], v[52:55]\n"
        "s_xor_b32 s65, s49, 8192\n"
        "s_add_u32 s56, s56, 786432\n"
        "s_waitcnt lgkmcnt(3)\n"
        "v_mfma_f32_16x16x32_bf16 v[84:87], v[92:95], a[32:35], v[84:87]\n"
        "s_add_u32 s64, s64, 786432\n"
        "ds_read_b128 v[96:99], v124 offset:14336\n"
        "v_mfma_f32_16x16x32_bf16 v[88:91], v[92:95], a[40:43], v[88:91]\n"
        "ds_read_b128 v[100:103], v124 offset:15360\n"
        "v_mfma_f32_16x16x32_bf16 v[56:59], a[176:179], v[92:95], v[56:59]\n"
        "ds_read_b128 v[16:19], v125 offset:16384\n"
        "v_mfma_f32_16x16x32_bf16 v[60:63], a[184:187], v[92:95], v[60:63]\n"
        "s_add_u32 s51, s51, 163840\n"
        "ds_read_b128 v[20:23], v125 offset:17408\n"
        "v_mfma_f32_16x16x32_bf16 v[48:51], v[0:3], v[92:95], v[48:51]\n"
        "s_add_u32 s83, s83, 163840\n"
        "ds_read_b128 v[24:27], v125 offset:18432\n"
        "v_mfma_f32_16x16x32_bf16 v[52:55], v[8:11], v[92:95], v[52:55]\n"
        "ds_read_b128 v[28:31], v125 offset:19456\n"
        "s_waitcnt lgkmcnt(8)\n"
        "v_mfma_f32_16x16x32_bf16 v[84:87], v[104:107], a[36:39], v[84:87]\n"
        "s_xor_b32 s86, s83, s46\n"
        "ds_read_b128 v[92:95], v124 offset:0\n"
        "v_mfma_f32_16x16x32_bf16 v[88:91], v[104:107], a[44:47], v[88:91]\n"
        "s_and_b32 s86, s86, s53\n"
        "s_lshr_b32 s86, s86, 8\n"
        "s_xor_b32 s86, s86, s34\n"
        "v_mfma_f32_16x16x32_bf16 v[56:59], a[180:183], v[104:107], v[56:59]\n"
        "s_xor_b32 s34, s34, s86\n"
        "s_xor_b32 s51, s51, s86\n"
        "s_add_u32 s55, s55, 786432\n"
        "v_mfma_f32_16x16x32_bf16 v[60:63], a[188:191], v[104:107], v[60:63]\n"
        "s_waitcnt vmcnt(5)\n"
        "s_mov_b64 exec, 1\n"
        "buffer_atomic_umax v116, off, s[36:39], s61\n"
        "s_mov_b64 exec, -1\n"
        "s_waitcnt lgkmcnt(8)\n"
        "v_mfma_f32_16x16x32_bf16 v[48:51], v[4:7], v[104:107], v[48:51]\n"
        "s_cmp_eq_u32 s44, 63\n"
        "s_cbranch_scc1 L_end%=\n"
        "s_waitcnt lgkmcnt(7)\n"
        "v_mfma_f32_16x16x32_bf16 v[52:55], v[12:15], v[104:107], v[52:55]\n"
        "ds_read_b128 v[32:35], v125 offset:20480\n"
        "s_waitcnt lgkmcnt(7)\n"
        "v_mfma_f32_16x16x32_bf16 v[84:87], v[96:99], a[48:51], v[84:87]\n"
        "s_add_u32 s54, s54, 786432\n"
        "ds_read_b128 v[104:107], v124 offset:1024\n"
        "v_mfma_f32_16x16x32_bf16 v[88:91], v[96:99], a[56:59], v[88:91]\n"
        "s_add_u32 s59, s59, 131072\n"
        "ds_read_b128 v[36:39], v125 offset:21504\n"
        "v_mfma_f32_16x16x32_bf16 v[56:59], a[192:195], v[96:99], v[56:59]\n"
        "s_add_u32 s60, s60, 131072\n"
        "ds_read_b128 v[40:43], v125 offset:22528\n"
        "v_mfma_f32_16x16x32_bf16 v[60:63], a[200:203], v[96:99], v[60:63]\n"
        "ds_read_b128 v[44:47], v125 offset:23552\n"
        "s_waitcnt vmcnt(5)\n"
        "v_cmp_eq_u32 vcc, 0, v140\n"
        "s_waitcnt lgkmcnt(9)\n"
        "v_mfma_f32_16x16x32_bf16 v[48:51], v[16:19], v[96:99], v[48:51]\n"
        "s_cbranch_vccnz L_sw0_2%=\n"
        "L_sok_2%=:\n"
        "s_waitcnt lgkmcnt(7)\n"
        "v_mfma_f32_16x16x32_bf16 v[52:55], v[24:27], v[96:99], v[52:55]\n"
        "v_readlane_b32 s81, v140, s99\n"
        "v_max_u32_dpp v140, v140, v140 row_shr:1 row_mask:0xf bank_mask:0xf\n"
        "v_mfma_f32_16x16x32_bf16 v[84:87], v[100:103], a[52:55], v[84:87]\n"
        "ds_read_b128 v[96:99], v124 offset:2048\n"
        "v_max_u32_dpp v140, v140, v140 row_shr:2 row_mask:0xf bank_mask:0xf\n"
        "v_mfma_f32_16x16x32_bf16 v[88:91], v[100:103], a[60:63], v[88:91]\n"
        "buffer_load_dwordx4 v[108:111], v132, s[36:39], s56 offen offset:0 sc1\n"
        "v_max_u32_dpp v140, v140, v140 row_shr:4 row_mask:0xf bank_mask:0xf\n"
        "v_mfma_f32_16x16x32_bf16 v[56:59], a[196:199], v[100:103], v[56:59]\n"
        "ds_read_b128 v[0:3], v125 offset:0\n"
        "v_max_u32_dpp v140, v140, v140 row_shr:8 row_mask:0xf bank_mask:0xf\n"
        "v_mfma_f32_16x16x32_bf16 v[60:63], a[204:207], v[100:103], v[60:63]\n"
        "buffer_load_dwordx4 v[112:115], v132, s[36:39], s64 offen offset:0 sc1\n"
        "v_max_u32_dpp v140, v140, v140 row_bcast:15 row_mask:0xa bank_mask:0xf\n"
        "v_mfma_f32_16x16x32_bf16 v[48:51], v[20:23], v[100:103], v[48:51]\n"
        "ds_read_b128 v[4:7], v125 offset:1024\n"
        "v_max_u32_dpp v140, v140, v140 row_bcast:31 row_mask:0xc bank_mask:0xf\n"
        "s_waitcnt lgkmcnt(9)\n"
        "v_mfma_f32_16x16x32_bf16 v[52:55], v[28:31], v[100:103], v[52:55]\n"
        "v_readlane_b32 s33, v140, 63\n"
        "s_lshl1_add_u32 s28, s28, s81\n"
        "s_waitcnt lgkmcnt(8)\n"
        "v_mfma_f32_16x16x32_bf16 v[84:87], v[92:95], a[64:67], v[84:87]\n"
        "ds_read_b128 v[100:103], v124 offset:3072\n"
        "s_add_u32 s28, s28, 36\n"
        "s_sub_i32 s33, s33, s28\n"
        "v_mfma_f32_16x16x32_bf16 v[88:91], v[92:95], a[72:75], v[88:91]\n"
        "s_min_i32 s33, s33, 500\n"
        "s_mov_b32 s28, 0\n"
        "L_dl_2%=:\n"
        "s_sub_i32 s33, s33, 3\n"
        "s_cmp_gt_i32 s33, 0\n"
        "s_cbranch_scc1 L_dl_2%=\n"
        "v_mfma_f32_16x16x32_bf16 v[56:59], a[208:211], v[92:95], v[56:59]\n"
        "s_add_u32 s58, s58, 36864\n"
        "buffer_load_dwordx4 v[80:83], v138, s[36:39], s50 offen offset:0 sc1\n"
        "v_mfma_f32_16x16x32_bf16 v[60:63], a[216:219], v[92:95], v[60:63]\n"
        "s_xor_b32 s66, s51, 1024\n"
        "ds_read_b128 v[8:11], v125 offset:2048\n"
        "s_waitcnt lgkmcnt(9)\n"
        "v_mfma_f32_16x16x32_bf16 v[48:51], v[32:35], v[92:95], v[48:51]\n"
        "s_xor_b32 s67, s51, 8192\n"
        "buffer_load_dwordx4 v[64:67], v138, s[36:39], s49 offen offset:0 sc1\n"
        "s_waitcnt lgkmcnt(6)\n"
        "v_mfma_f32_16x16x32_bf16 v[52:55], v[40:43], v[92:95], v[52:55]\n"
        "ds_read_b128 v[12:15], v125 offset:3072\n"
        "v_mfma_f32_16x16x32_bf16 v[84:87], v[104:107], a[68:71], v[84:87]\n"
        "s_xor_b32 s68, s51, 9216\n"
        "ds_read_b128 v[92:95], v124 offset:4096\n"
        "v_mfma_f32_16x16x32_bf16 v[88:91], v[104:107], a[76:79], v[88:91]\n"
        "s_sub_u32 s75, 127, s84\n"
        "buffer_load_dwordx4 v[68:71], v138, s[36:39], s49 offen offset:256 sc1\n"
        "v_mfma_f32_16x16x32_bf16 v[56:59], a[212:215], v[104:107], v[56:59]\n"
        "s_sub_u32 s101, s77, s75\n"
        "ds_read_b128 v[16:19], v125 offset:4096\n"
        "v_mfma_f32_16x16x32_bf16 v[60:63], a[220:223], v[104:107], v[60:63]\n"
        "s_mul_i32 s35, s75, 0x800080\n"
        "buffer_load_dwordx4 v[72:75], v138, s[36:39], s65 offen offset:0 sc1\n"
        "v_mfma_f32_16x16x32_bf16 v[48:51], v[36:39], v[104:107], v[48:51]\n"
        "s_lshl_b32 s80, s75, 23\n"
        "ds_read_b128 v[20:23], v125 offset:5120\n"
        "s_waitcnt lgkmcnt(9)\n"
        "v_mfma_f32_16x16x32_bf16 v[52:55], v[44:47], v[104:107], v[52:55]\n"
        "buffer_load_dwordx4 v[76:79], v138, s[36:39], s65 offen offset:256 sc1\n"
        "s_waitcnt lgkmcnt(8)\n"
        "v_mfma_f32_16x16x32_bf16 v[84:87], v[96:99], a[80:83], v[84:87]\n"
        "s_sub_u32 s73, 0x3e0293ee, s80\n"
        "ds_read_b128 v[104:107], v124 offset:5120\n"
        "v_mfma_f32_16x16x32_bf16 v[88:91], v[96:99], a[88:91], v[88:91]\n"
        "s_sub_u32 s74, 0x3f800000, s80\n"
        "ds_read_b128 v[24:27], v125 offset:6144\n"
        "v_mfma_f32_16x16x32_bf16 v[56:59], a[224:227], v[96:99], v[56:59]\n"
        "s_add_u32 s80, s75, 128\n"
        "ds_read_b128 v[28:31], v125 offset:7168\n"
        "v_mfma_f32_16x16x32_bf16 v[60:63], a[232:235], v[96:99], v[60:63]\n"
        "s_lshl_b32 s80, s80, 8\n"
        "ds_read_b128 v[32:35], v125 offset:8192\n"
        "v_mfma_f32_16x16x32_bf16 v[48:51], v[192:195], v[96:99], v[48:51]\n"
        "s_add_u32 s62, s80, 1\n"
        "ds_read_b128 v[36:39], v125 offset:9216\n"
        "v_mfma_f32_16x16x32_bf16 v[52:55], v[200:203], v[96:99], v[52:55]\n"
        "ds_read_b128 v[40:43], v125 offset:10240\n"
        "s_waitcnt lgkmcnt(11)\n"
        "v_mfma_f32_16x16x32_bf16 v[84:87], v[100:103], a[84:87], v[84:87]\n"
        "s_add_u32 s44, s44, 1\n"
        "ds_read_b128 v[96:99], v124 offset:6144\n"
        "v_mfma_f32_16x16x32_bf16 v[88:91], v[100:103], a[92:95], v[88:91]\n"
        "s_add_u32 s61, s61, 32\n"
        "ds_read_b128 v[44:47], v125 offset:11264\n"
        "v_mfma_f32_16x16x32_bf16 v[56:59], a[228:231], v[100:103], v[56:59]\n"
        "s_branch L_rot_2%=\n"
        "L_light_3%=:\n"
        "v_mfma_f32_16x16x32_bf16 v[52:55], v[252:255], v[104:107], v[52:55]\n"
        "v_mfma_f32_16x16x32_bf16 v[84:87], v[96:99], a[112:115], v[84:87]\n"
        "v_mfma_f32_16x16x32_bf16 v[88:91], v[96:99], a[120:123], v[88:91]\n"
        "v_cvt_pk_bf16_f32 v48, v48, v52\n"
        "v_cvt_pk_bf16_f32 v49, v49, v53\n"
        "v_mfma_f32_16x16x32_bf16 v[84:87], v[100:103], a[116:119], v[84:87]\n"
        "v_cvt_pk_bf16_f32 v50, v50, v54\n"
        "v_cvt_pk_bf16_f32 v51, v51, v55\n"
        "v_mfma_f32_16x16x32_bf16 v[88:91], v[100:103], a[124:127], v[88:91]\n"
        "v_mfma_f32_16x16x32_bf16 v[84:87], v[92:95], a[128:131], v[84:87]\n"
        "v_mfma_f32_16x16x32_bf16 v[88:91], v[92:95], a[136:139], v[88:91]\n"
        "v_mfma_f32_16x16x32_bf16 v[84:87], v[104:107], a[132:135], v[84:87]\n"
        "buffer_store_dwordx4 v[48:51], v124, s[36:39], s54 offen offset:1024 sc1\n"
        "v_mfma_f32_16x16x32_bf16 v[88:91], v[104:107], a[140:143], v[88:91]\n"
        "v_mfma_f32_16x16x32_bf16 v[56:59], v[32:35], v[92:95], v[56:59]\n"
        "v_mfma_f32_16x16x32_bf16 v[60:63], v[40:43], v[92:95], v[60:63]\n"
        "v_cvt_pk_bf16_f32 v84, v84, v85\n"
        "v_cvt_pk_bf16_f32 v85, v86, v87\n"
        "v_mfma_f32_16x16x32_bf16 v[56:59], v[16:19], v[96:99], v[56:59]\n"
        "v_cvt_pk_bf16_f32 v86, v88, v89\n"
        "v_cvt_pk_bf16_f32 v87, v90, v91\n"
        "v_mfma_f32_16x16x32_bf16 v[60:63], v[24:27], v[96:99], v[60:63]\n"
        "v_mfma_f32_16x16x32_bf16 v[56:59], v[20:23], v[100:103], v[56:59]\n"
        "v_mov_b64 v[92:93], 0\n"
        "v_mov_b64 v[94:95], 0\n"
        "v_mfma_f32_16x16x32_bf16 v[60:63], v[28:31], v[100:103], v[60:63]\n"
        "v_mov_b64 v[96:97], 0\n"
        "v_mov_b64 v[98:99], 0\n"
        "v_mfma_f32_16x16x32_bf16 v[56:59], v[36:39], v[104:107], v[56:59]\n"
        "buffer_store_dwordx4 v[84:87], v124, s[36:39], s54 offen offset:2048 sc1\n"
        "v_mfma_f32_16x16x32_bf16 v[60:63], v[44:47], v[104:107], v[60:63]\n"
        "s_nop 7\n"
        "v_cvt_pk_bf16_f32 v100, v56, v60\n"
        "v_cvt_pk_bf16_f32 v101, v57, v61\n"
        "v_cvt_pk_bf16_f32 v102, v58, v62\n"
        "v_cvt_pk_bf16_f32 v103, v59, v63\n"
        "buffer_store_dwordx4 v[100:103], v124, s[36:39], s54 offen offset:0 sc1\n"
        "s_waitcnt vmcnt(3)\n"
        "v_cmp_ge_u32 vcc, s31, v140\n"
        "s_and_b32 vcc_lo, vcc_lo, 0x1ff\n"
        "s_cbranch_scc0 L_xl_3%=\n"
        "s_branch L_xl2_3%=\n"
        "L_xl1_3%=:\n"
        "s_add_u32 s63, s63, 1\n"
        "s_cmp_gt_u32 s63, 0x40000\n"
        "s_cbranch_scc1 L_abort%=\n"
        "s_sleep 4\n"
        "L_xl2_3%=:\n"
        "v_mov_b32 v140, 0\n"
        "buffer_atomic_add v140, v143, s[36:39], s57 offen sc0\n"
        "s_waitcnt vmcnt(0)\n"
        "v_cmp_ge_u32 vcc, s31, v140\n"
        "s_and_b32 vcc_lo, vcc_lo, 0x1ff\n"
        "s_cbranch_scc1 L_xl1_3%=\n"
        "L_xl_3%=:\n"
        "s_barrier\n"
        "s_add_u32 s69, s58, s82\n"
        "s_sub_u32 s72, s58, s82\n"
        "s_add_u32 s87, s58, 5120\n"
        "buffer_load_dwordx4 v[64:67], v124, s[36:39], s69 offen offset:0 sc1\n"
        "buffer_load_dwordx4 v[68:71], v124, s[36:39], s69 offen offset:1024 sc1\n"
        "buffer_load_dwordx4 v[72:75], v124, s[36:39], s72 offen offset:2048 sc1\n"
        "buffer_load_dwordx4 v[76:79], v124, s[36:39], s72 offen offset:3072 sc1\n"
        "buffer_load_dwordx4 v[80:83], v124, s[36:39], s87 offen offset:0 sc1\n"
        "s_mov_b32 s81, 64\n"
        "buffer_load_dword v107, off, s[36:39], s81 sc1\n"
        "s_memrealtime s[26:27]\n"
        "s_waitcnt vmcnt(6)\n"
        "s_waitcnt lgkmcnt(0)\n"
        "s_barrier\n"
        "v_lshl_or_b32 v20, s26, 1, 1\n"
        "s_add_u32 s29, s29, 1024\n"
        "s_add_u32 s80, s29, s30\n"
        "s_mov_b64 exec, 1\n"
        "buffer_atomic_umax v20, off, s[36:39], s80\n"
        "s_mov_b64 exec, -1\n"
        "s_waitcnt vmcnt(1)\n"
        "L_par_3%=:\n"
        "v_readfirstlane_b32 s80, v107\n"
        "s_nop 0\n"
        "s_cmp_lt_i32 s80, 0\n"
        "s_cbranch_scc1 L_par2_3%=\n"
        "s_add_u32 s63, s63, 1\n"
        "s_cmp_gt_u32 s63, 0x40000\n"
        "s_cbranch_scc1 L_abort%=\n"
        "s_sleep 4\n"
        "buffer_load_dword v107, off, s[36:39], s81 sc1\n"
        "s_waitcnt vmcnt(0)\n"
        "s_branch L_par_3%=\n"
        "L_par2_3%=:\n"
        "s_bfe_u32 s46, s80, 0x10018\n"
        "s_lshl_b32 s46, s46, 21\n"
        "s_mov_b32 s53, 0x200000\n"
        "s_bfe_u32 s84, s80, 0x80010\n"
        "s_sub_u32 s84, 255, s84\n"
        "s_bfe_u32 s76, s80, 0x80008\n"
        "s_sub_u32 s76, s76, 32\n"
        "s_max_i32 s76, s76, 66\n"
        "s_bfe_u32 s77, s80, 0x80000\n"
        "s_sub_u32 s77, s77, 158\n"
        "s_bitset0_b32 s49, 30\n"
        "s_bitset0_b32 s50, 30\n"
        "s_bitset0_b32 s56, 30\n"
        "s_bitset0_b32 s64, 30\n"
        "v_mov_b32 v140, 0\n"
        "s_mov_b32 s28, 1000\n"
        "s_branch L_xok_3%=\n"
        "L_x2_3%=:\n"
        "s_add_u32 s69, s58, s82\n"
        "s_sub_u32 s72, s58, s82\n"
        "s_add_u32 s87, s58, 5120\n"
        "buffer_load_dwordx4 v[64:67], v124, s[36:39], s69 offen offset:0 sc1\n"
        "buffer_load_dwordx4 v[68:71], v124, s[36:39], s69 offen offset:1024 sc1\n"
        "buffer_load_dwordx4 v[72:75], v124, s[36:39], s72 offen offset:2048 sc1\n"
        "buffer_load_dwordx4 v[76:79], v124, s[36:39], s72 offen offset:3072 sc1\n"
        "buffer_load_dwordx4 v[80:83], v124, s[36:39], s87 offen offset:0 sc1\n"
        "s_waitcnt vmcnt(0)\n"
        "s_memrealtime s[26:27]\n"
        "s_waitcnt lgkmcnt(0)\n"
        "s_add_u32 s28, s28, s26\n"
        "L_xok_3%=:\n"
        "v_mfma_f32_16x16x32_bf16 v[84:87], v[64:67], a[0:3], 0\n"
        "v_mfma_f32_16x16x32_bf16 v[88:91], v[64:67], a[8:11], 0\n"
        "v_mfma_f32_16x16x32_bf16 v[56:59], a[144:147], v[64:67], 0\n"
        "s_branch L_win_3%=\n"
        "L_retry_3%=:\n"
        "s_add_u32 s63, s63, 1\n"
        "s_cmp_gt_u32 s63, 0x40000\n"
        "s_cbranch_scc1 L_abort%=\n"
        "s_sleep 2\n"
        "s_waitcnt vmcnt(0)\n"
        "s_waitcnt lgkmcnt(0)\n"
        "buffer_load_dwordx4 v[80:83], v138, s[36:39], s50 offen offset:0 sc1\n"
        "buffer_load_dwordx4 v[64:67], v138, s[36:39], s49 offen offset:0 sc1\n"
        "buffer_load_dwordx4 v[68:71], v138, s[36:39], s49 offen offset:256 sc1\n"
        "buffer_load_dwordx4 v[72:75], v138, s[36:39], s65 offen offset:0 sc1\n"
        "buffer_load_dwordx4 v[76:79], v138, s[36:39], s65 offen offset:256 sc1\n"
        "s_waitcnt vmcnt(4)\n"
        "v_bitop3_b32 v123, 32, v80, v82 bitop3:0x80\n"
        "v_add_u32_dpp v80, v81, v80 row_half_mirror row_mask:0xf bank_mask:0xf\n"
        "v_add_u32_dpp v82, v83, v82 row_half_mirror row_mask:0xf bank_mask:0xf\n"
        "v_cvt_f32_i32 v80, v80\n"
        "v_cvt_f32_i32 v82, v82\n"
        "v_cvt_pk_bf16_f32 v52, v80, v82\n"
        "ds_write_b32 v137, v52\n"
        "s_waitcnt vmcnt(3)\n"
        "v_bitop3_b32 v123, v123, v64, v66 bitop3:0x80\n"
        "v_cvt_f32_i32 v64, v64\n"
        "v_cvt_f32_i32 v65, v65\n"
        "v_cvt_f32_i32 v66, v66\n"
        "v_cvt_f32_i32 v67, v67\n"
        "v_cvt_pk_bf16_f32 v52, v64, v65\n"
        "v_cvt_pk_bf16_f32 v53, v66, v67\n"
        "ds_write_b64 v126, v[52:53] offset:0\n"
        "s_waitcnt vmcnt(2)\n"
        "v_bitop3_b32 v123, v123, v68, v70 bitop3:0x80\n"
        "v_cvt_f32_i32 v68, v68\n"
        "v_cvt_f32_i32 v69, v69\n"
        "v_cvt_f32_i32 v70, v70\n"
        "v_cvt_f32_i32 v71, v71\n"
        "v_cvt_pk_bf16_f32 v54, v68, v69\n"
        "v_cvt_pk_bf16_f32 v55, v70, v71\n"
        "ds_write_b64 v126, v[54:55] offset:512\n"
        "s_waitcnt vmcnt(1)\n"
        "v_bitop3_b32 v123, v123, v72, v74 bitop3:0x80\n"
        "v_cvt_f32_i32 v72, v72\n"
        "v_cvt_f32_i32 v73, v73\n"
        "ds_read_b128 v[48:51], v124 offset:6144\n"
        "v_cvt_f32_i32 v74, v74\n"
        "v_cvt_f32_i32 v75, v75\n"
        "v_cvt_pk_bf16_f32 v52, v72, v73\n"
        "v_cvt_pk_bf16_f32 v53, v74, v75\n"
        "ds_write_b64 v126, v[52:53] offset:1024\n"
        "s_waitcnt vmcnt(0)\n"
        "v_bitop3_b32 v123, v123, v76, v78 bitop3:0x80\n"
        "v_cmp_eq_u32 vcc, 0, v123\n"
        "v_mov_b64 v[122:123], 0\n"
        "v_cvt_f32_i32 v76, v76\n"
        "v_cvt_f32_i32 v77, v77\n"
        "v_cvt_f32_i32 v78, v78\n"
        "v_cvt_f32_i32 v79, v79\n"
        "v_cvt_pk_bf16_f32 v54, v76, v77\n"
        "v_cvt_pk_bf16_f32 v55, v78, v79\n"
        "ds_write_b64 v126, v[54:55] offset:1536\n"
        "v_mov_b64 v[92:93], 0\n"
        "v_mov_b64 v[94:95], 0\n"
        "v_mov_b64 v[96:97], 0\n"
        "v_mov_b64 v[98:99], 0\n"
        "s_waitcnt lgkmcnt(0)\n"
        "s_branch L_dec_3%=\n"
        "L_sw0_3%=:\n"
        "s_memrealtime s[26:27]\n"
        "s_waitcnt lgkmcnt(0)\n"
        "s_sub_u32 s28, s28, s26\n"
        "L_sw_3%=:\n"
        "s_add_u32 s63, s63, 1\n"
        "s_cmp_gt_u32 s63, 0x40000\n"
        "s_cbranch_scc1 L_abort%=\n"
        "s_sleep 2\n"
        "buffer_load_dword v140, v124, s[36:39], s29 offen sc1\n"
        "s_waitcnt vmcnt(0)\n"
        "v_cmp_eq_u32 vcc, 0, v140\n"
        "s_cbranch_vccnz L_sw_3%=\n"
        "s_memrealtime s[26:27]\n"
        "s_waitcnt lgkmcnt(0)\n"
        "s_add_u32 s28, s28, s26\n"
        "s_branch L_sok_3%=\n"
        "L_xs_3%=:\n"
        "s_branch L_xs2_3%=\n"
        "L_xs1_3%=:\n"
        "s_add_u32 s63, s63, 1\n"
        "s_cmp_gt_u32 s63, 0x40000\n"
        "s_cbranch_scc1 L_abort%=\n"
        "s_sleep 4\n"
        "L_xs2_3%=:\n"
        "v_mov_b32 v140, 0\n"
        "buffer_atomic_add v140, v143, s[36:39], s57 offen sc0\n"
        "s_waitcnt vmcnt(0)\n"
        "v_cmp_ge_u32 vcc, s31, v140\n"
        "s_and_b32 vcc_lo, vcc_lo, 0x1ff\n"
        "s_cbranch_scc1 L_xs1_3%=\n"
        "s_branch L_xr_3%=\n"
        ".p2align 6\n"
        "L_rot_3%=:\n"
        "v_mfma_f32_16x16x32_bf16 v[48:51], v[196:199], v[100:103], v[48:51]\n"
        "v_mfma_f32_16x16x32_bf16 v[52:55], v[204:207], v[100:103], v[52:55]\n"
        "s_waitcnt lgkmcnt(10)\n"
        "v_mfma_f32_16x16x32_bf16 v[84:87], v[92:95], a[96:99], v[84:87]\n"
        "ds_read_b128 v[100:103], v124 offset:3072\n"
        "v_mfma_f32_16x16x32_bf16 v[88:91], v[92:95], a[104:107], v[88:91]\n"
        "v_mfma_f32_16x16x32_bf16 v[56:59], v[0:3], v[92:95], v[56:59]\n"
        "v_mfma_f32_16x16x32_bf16 v[60:63], v[8:11], v[92:95], v[60:63]\n"
        "v_mfma_f32_16x16x32_bf16 v[48:51], v[208:211], v[92:95], v[48:51]\n"
        "s_waitcnt lgkmcnt(0)\n"
        "s_barrier\n"
        "v_mfma_f32_16x16x32_bf16 v[52:55], v[216:219], v[92:95], v[52:55]\n"
        "v_mov_b64 v[116:117], 0\n"
        "v_mfma_f32_16x16x32_bf16 v[84:87], v[104:107], a[100:103], v[84:87]\n"
        "ds_read_b128 v[92:95], v124 offset:16384\n"
        "v_mov_b32 v140, 0\n"
        "v_mfma_f32_16x16x32_bf16 v[88:91], v[104:107], a[108:111], v[88:91]\n"
        "buffer_atomic_add v140, v143, s[36:39], s57 offen sc0\n"
        "v_mov_b64 v[118:119], 0\n"
        "v_mfma_f32_16x16x32_bf16 v[56:59], v[4:7], v[104:107], v[56:59]\n"
        "v_mov_b64 v[120:121], 0\n"
        "v_mfma_f32_16x16x32_bf16 v[60:63], v[12:15], v[104:107], v[60:63]\n"
        "v_mfma_f32_16x16x32_bf16 v[48:51], v[212:215], v[104:107], v[48:51]\n"
        "v_mfma_f32_16x16x32_bf16 v[52:55], v[220:223], v[104:107], v[52:55]\n"
        "v_mfma_f32_16x16x32_bf16 v[48:51], v[224:227], v[96:99], v[48:51]\n"
        "ds_read_b128 v[104:107], v124 offset:17408\n"
        "v_mfma_f32_16x16x32_bf16 v[52:55], v[232:235], v[96:99], v[52:55]\n"
        "v_mfma_f32_16x16x32_bf16 v[48:51], v[228:231], v[100:103], v[48:51]\n"
        "v_mfma_f32_16x16x32_bf16 v[52:55], v[236:239], v[100:103], v[52:55]\n"
        "s_waitcnt lgkmcnt(1)\n"
        "v_mfma_f32_16x16x32_bf16 v[48:51], v[240:243], v[92:95], v[48:51]\n"
        "v_mfma_f32_16x16x32_bf16 v[52:55], v[248:251], v[92:95], v[52:55]\n"
        "s_waitcnt lgkmcnt(0)\n"
        "v_mfma_f32_16x16x32_bf16 v[48:51], v[244:247], v[104:107], v[48:51]\n"
        "s_cmp_lt_i32 s44, 0\n"
        "s_cbranch_scc1 L_light_3%=\n"
        "v_mfma_f32_16x16x32_bf16 v[52:55], v[252:255], v[104:107], v[52:55]\n"
        "s_waitcnt vmcnt(5)\n"
        "v_bitop3_b32 v123, 32, v80, v82 bitop3:0x80\n"
        "s_nop 0\n"
        "v_add_u32_dpp v80, v81, v80 row_half_mirror row_mask:0xf bank_mask:0xf\n"
        "v_mfma_f32_16x16x32_bf16 v[84:87], v[96:99], a[112:115], v[84:87]\n"
        "s_nop 0\n"
        "v_add_u32_dpp v82, v83, v82 row_half_mirror row_mask:0xf bank_mask:0xf\n"
        "v_mfma_f32_16x16x32_bf16 v[88:91], v[96:99], a[120:123], v[88:91]\n"
        "v_cvt_pk_bf16_f32 v48, v48, v52\n"
        "v_cvt_pk_bf16_f32 v49, v49, v53\n"
        "s_nop 0\n"
        "v_mfma_f32_16x16x32_bf16 v[84:87], v[100:103], a[116:119], v[84:87]\n"
        "v_cvt_pk_bf16_f32 v50, v50, v54\n"
        "v_cvt_pk_bf16_f32 v51, v51, v55\n"
        "v_mfma_f32_16x16x32_bf16 v[88:91], v[100:103], a[124:127], v[88:91]\n"
        "v_cvt_f32_i32 v80, v80\n"
        "s_nop 0\n"
        "v_cvt_f32_i32 v82, v82\n"
        "v_mfma_f32_16x16x32_bf16 v[84:87], v[92:95], a[128:131], v[84:87]\n"
        "s_nop 0\n"
        "v_cvt_pk_bf16_f32 v52, v80, v82\n"
        "v_mfma_f32_16x16x32_bf16 v[88:91], v[92:95], a[136:139], v[88:91]\n"
        "s_nop 0\n"
        "v_mfma_f32_16x16x32_bf16 v[84:87], v[104:107], a[132:135], v[84:87]\n"
        "buffer_store_dwordx4 v[48:51], v124, s[36:39], s54 offen offset:1024 sc1\n"
        "v_mfma_f32_16x16x32_bf16 v[88:91], v[104:107], a[140:143], v[88:91]\n"
        "ds_write_b32 v137, v52\n"
        "s_waitcnt vmcnt(5)\n"
        "v_bitop3_b32 v123, v123, v64, v66 bitop3:0x80\n"
        "v_cvt_f32_i32 v64, v64\n"
        "v_mfma_f32_16x16x32_bf16 v[56:59], v[32:35], v[92:95], v[56:59]\n"
        "v_cvt_f32_i32 v65, v65\n"
        "v_cvt_f32_i32 v66, v66\n"
        "v_mfma_f32_16x16x32_bf16 v[60:63], v[40:43], v[92:95], v[60:63]\n"
        "v_cvt_pk_bf16_f32 v84, v84, v85\n"
        "v_cvt_pk_bf16_f32 v85, v86, v87\n"
        "v_mfma_f32_16x16x32_bf16 v[56:59], v[16:19], v[96:99], v[56:59]\n"
        "v_cvt_pk_bf16_f32 v86, v88, v89\n"
        "v_cvt_pk_bf16_f32 v87, v90, v91\n"
        "v_mfma_f32_16x16x32_bf16 v[60:63], v[24:27], v[96:99], v[60:63]\n"
        "v_cvt_f32_i32 v67, v67\n"
        "v_cvt_pk_bf16_f32 v52, v64, v65\n"
        "v_mfma_f32_16x16x32_bf16 v[56:59], v[20:23], v[100:103], v[56:59]\n"
        "v_mov_b64 v[92:93], 0\n"
        "v_mov_b64 v[94:95], 0\n"
        "v_mfma_f32_16x16x32_bf16 v[60:63], v[28:31], v[100:103], v[60:63]\n"
        "v_mov_b64 v[96:97], 0\n"
        "v_mov_b64 v[98:99], 0\n"
        "v_mfma_f32_16x16x32_bf16 v[56:59], v[36:39], v[104:107], v[56:59]\n"
        "buffer_store_dwordx4 v[84:87], v124, s[36:39], s54 offen offset:2048 sc1\n"
        "v_cvt_pk_bf16_f32 v53, v66, v67\n"
        "v_mfma_f32_16x16x32_bf16 v[60:63], v[44:47], v[104:107], v[60:63]\n"
        "ds_write_b64 v126, v[52:53] offset:0\n"
        "s_waitcnt vmcnt(5)\n"
        "v_bitop3_b32 v123, v123, v68, v70 bitop3:0x80\n"
        "v_cvt_f32_i32 v68, v68\n"
        "v_cvt_f32_i32 v69, v69\n"
        "v_cvt_f32_i32 v70, v70\n"
        "v_cvt_f32_i32 v71, v71\n"
        "v_cvt_pk_bf16_f32 v54, v68, v69\n"
        "v_cvt_pk_bf16_f32 v55, v70, v71\n"
        "ds_write_b64 v126, v[54:55] offset:512\n"
        "s_waitcnt vmcnt(4)\n"
        "v_bitop3_b32 v123, v123, v72, v74 bitop3:0x80\n"
        "v_cvt_pk_bf16_f32 v100, v56, v60\n"
        "v_cvt_pk_bf16_f32 v101, v57, v61\n"
        "v_cvt_pk_bf16_f32 v102, v58, v62\n"
        "v_cvt_pk_bf16_f32 v103, v59, v63\n"
        "v_cvt_f32_i32 v72, v72\n"
        "v_cvt_f32_i32 v73, v73\n"
        "ds_read_b128 v[48:51], v124 offset:6144\n"
        "v_cvt_f32_i32 v74, v74\n"
        "v_cvt_f32_i32 v75, v75\n"
        "v_cvt_pk_bf16_f32 v52, v72, v73\n"
        "v_cvt_pk_bf16_f32 v53, v74, v75\n"
        "ds_write_b64 v126, v[52:53] offset:1024\n"
        "s_waitcnt vmcnt(3)\n"
        "v_bitop3_b32 v123, v123, v76, v78 bitop3:0x80\n"
        "v_cmp_eq_u32 vcc, 0, v123\n"
        "v_mov_b64 v[122:123], 0\n"
        "v_cvt_f32_i32 v76, v76\n"
        "v_cvt_f32_i32 v77, v77\n"
        "v_cvt_f32_i32 v78, v78\n"
        "v_cvt_f32_i32 v79, v79\n"
        "v_cvt_pk_bf16_f32 v54, v76, v77\n"
        "v_cvt_pk_bf16_f32 v55, v78, v79\n"
        "ds_write_b64 v126, v[54:55] offset:1536\n"
        "L_dec_3%=:\n"
        "s_waitcnt lgkmcnt(2)\n"
        "v_smfmac_f32_16x16x64_bf16 v[92:95], v[48:51], a[144:151], v134\n"
        "ds_read_b128 v[52:55], v124 offset:7168\n"
        "v_max3_f32 v104, |v80|, |v82|, |v64|\n"
        "v_smfmac_f32_16x16x64_bf16 v[96:99], v[48:51], a[152:159], v134\n"
        "s_cbranch_vccnz L_retry_3%=\n"
        "v_max3_f32 v104, |v65|, |v66|, v104\n"
        "v_smfmac_f32_16x16x64_bf16 v[116:119], v[48:51], a[240:247], v134\n"
        "s_add_u32 s29, s29, 1024\n"
        "s_add_u32 s80, s29, s30\n"
        "v_max3_f32 v104, |v67|, |v68|, v104\n"
        "v_smfmac_f32_16x16x64_bf16 v[120:123], v[48:51], a[248:255], v134\n"
        "s_waitcnt lgkmcnt(0)\n"
        "v_max3_f32 v104, |v69|, |v70|, v104\n"
        "s_barrier\n"
        "ds_read_b128 v[56:59], v124 offset:0\n"
        "v_smfmac_f32_16x16x64_bf16 v[92:95], v[52:55], a[160:167], v134\n"
        "ds_read_b128 v[60:63], v124 offset:3072\n"
        "v_max3_f32 v104, |v71|, |v72|, v104\n"
        "v_smfmac_f32_16x16x64_bf16 v[96:99], v[52:55], a[168:175], v134\n"
        "ds_read_b128 v[80:83], v124 offset:1024\n"
        "v_max3_f32 v104, |v73|, |v74|, v104\n"
        "v_smfmac_f32_16x16x64_bf16 v[116:119], v[52:55], v[176:183], v134\n"
        "buffer_load_dwordx4 v[88:91], v124, s[36:39], s55 offen offset:3072 sc1\n"
        "ds_read_b128 v[64:67], v124 offset:8192\n"
        "v_max3_f32 v104, |v75|, |v76|, v104\n"
        "v_smfmac_f32_16x16x64_bf16 v[120:123], v[52:55], v[184:191], v134\n"
        "ds_read_b128 v[68:71], v124 offset:4096\n"
        "v_max3_f32 v104, |v77|, |v78|, v104\n"
        "s_waitcnt lgkmcnt(4)\n"
        "v_smfmac_f32_16x16x64_bf16 v[92:95], v[56:59], v[0:7], v134\n"
        "buffer_load_dwordx4 v[84:87], v124, s[36:39], s55 offen offset:0 sc1\n"
        "ds_read_b128 v[72:75], v124 offset:5120\n"
        "v_max_f32_e64 v104, |v79|, v104\n"
        "v_smfmac_f32_16x16x64_bf16 v[96:99], v[56:59], v[8:15], v134\n"
        "ds_read_b128 v[0:3], v125 offset:12288\n"
        "v_max_f32_dpp v104, v104, v104 row_shr:1 row_mask:0xf bank_mask:0xf\n"
        "s_waitcnt lgkmcnt(3)\n"
        "v_smfmac_f32_16x16x64_bf16 v[92:95], v[60:63], a[224:231], v134\n"
        "ds_read_b128 v[76:79], v124 offset:2048\n"
        "v_max_f32_dpp v104, v104, v104 row_shr:2 row_mask:0xf bank_mask:0xf\n"
        "v_smfmac_f32_16x16x64_bf16 v[96:99], v[60:63], a[232:239], v134\n"
        "ds_read_b128 v[4:7], v125 offset:13312\n"
        "v_max_f32_dpp v104, v104, v104 row_shr:4 row_mask:0xf bank_mask:0xf\n"
        "v_smfmac_f32_16x16x64_bf16 v[92:95], v[80:83], v[16:23], v134\n"
        "ds_read_b128 v[8:11], v125 offset:14336\n"
        "v_max_f32_dpp v104, v104, v104 row_shr:8 row_mask:0xf bank_mask:0xf\n"
        "v_smfmac_f32_16x16x64_bf16 v[96:99], v[80:83], v[24:31], v134\n"
        "ds_read_b128 v[12:15], v125 offset:15360\n"
        "v_max_f32_dpp v104, v104, v104 row_bcast:15 row_mask:0xa bank_mask:0xf\n"
        "v_smfmac_f32_16x16x64_bf16 v[92:95], v[64:67], v[32:39], v134\n"
        "ds_read_b128 v[16:19], v125 offset:16384\n"
        "v_max_f32_dpp v104, v104, v104 row_bcast:31 row_mask:0xc bank_mask:0xf\n"
        "v_smfmac_f32_16x16x64_bf16 v[96:99], v[64:67], v[40:47], v134\n"
        "ds_read_b128 v[20:23], v125 offset:17408\n"
        "v_readlane_b32 s85, v104, 63\n"
        "s_waitcnt lgkmcnt(5)\n"
        "v_smfmac_f32_16x16x64_bf16 v[92:95], v[68:71], a[176:183], v134\n"
        "ds_read_b128 v[24:27], v125 offset:18432\n"
        "v_smfmac_f32_16x16x64_bf16 v[96:99], v[68:71], a[184:191], v134\n"
        "ds_read_b128 v[28:31], v125 offset:19456\n"
        "v_mov_b32 v104, s85\n"
        "v_smfmac_f32_16x16x64_bf16 v[92:95], v[72:75], a[192:199], v134\n"
        "ds_read_b128 v[32:35], v125 offset:20480\n"
        "v_smfmac_f32_16x16x64_bf16 v[96:99], v[72:75], a[200:207], v134\n"
        "ds_read_b128 v[36:39], v125 offset:21504\n"
        "s_add_u32 s69, s58, s82\n"
        "v_smfmac_f32_16x16x64_bf16 v[92:95], v[76:79], a[208:215], v134\n"
        "ds_read_b128 v[40:43], v125 offset:22528\n"
        "s_sub_u32 s72, s58, s82\n"
        "v_smfmac_f32_16x16x64_bf16 v[96:99], v[76:79], a[216:223], v134\n"
        "ds_read_b128 v[44:47], v125 offset:23552\n"
        "s_add_u32 s87, s58, 5120\n"
        "v_smfmac_f32_16x16x64_bf16 v[116:119], v[64:67], v[240:247], v134\n"
        "ds_write_b32 v142, v104\n"
        "v_add_f32 v92, v92, v94\n"
        "v_add_f32 v93, v93, v95\n"
        "v_smfmac_f32_16x16x64_bf16 v[120:123], v[64:67], v[248:255], v134\n"
        "v_add_f32 v96, v96, v98\n"
        "v_add_f32 v97, v97, v99\n"
        "v_cvt_pk_bf16_f32 v92, v92, v96\n"
        "v_cvt_pk_bf16_f32 v93, v93, v97\n"
        "s_waitcnt lgkmcnt(3)\n"
        "v_smfmac_f32_16x16x64_bf16 v[116:119], v[68:71], v[0:7], v134\n"
        "ds_write2_b32 v127, v92, v93 offset1:72\n"
        "s_waitcnt vmcnt(4)\n"
        "v_cmp_ge_u32 vcc, s31, v140\n"
        "v_smfmac_f32_16x16x64_bf16 v[120:123], v[68:71], v[8:15], v134\n"
        "s_and_b32 vcc_lo, vcc_lo, 0x1ff\n"
        "s_cbranch_scc1 L_xs_3%=\n"
        "L_xr_3%=:\n"
        "s_memrealtime s[26:27]\n"
        "v_smfmac_f32_16x16x64_bf16 v[116:119], v[72:75], v[16:23], v134\n"
        "v_mov_b64 v[96:97], 0\n"
        "v_mov_b64 v[106:107], 0\n"
        "v_smfmac_f32_16x16x64_bf16 v[120:123], v[72:75], v[24:31], v134\n"
        "s_waitcnt vmcnt(3)\n"
        "s_waitcnt lgkmcnt(0)\n"
        "v_lshl_or_b32 v20, s26, 1, 1\n"
        "s_barrier\n"
        "ds_read_b128 v[0:3], v128 offset:0\n"
        "v_smfmac_f32_16x16x64_bf16 v[116:119], v[76:79], v[32:39], v134\n"
        "s_mov_b64 exec, 1\n"
        "buffer_atomic_umax v20, off, s[36:39], s80\n"
        "s_mov_b64 exec, -1\n"
        "ds_read_b128 v[24:27], v124 offset:20480\n"
        "v_mov_b64 v[104:105], 0\n"
        "v_smfmac_f32_16x16x64_bf16 v[120:123], v[76:79], v[40:47], v134\n"
        "ds_read_b128 v[4:7], v128 offset:64\n"
        "v_mov_b64 v[94:95], 0\n"
        "v_smfmac_f32_16x16x64_bf16 v[116:119], v[60:63], v[192:199], v134\n"
        "ds_read_b128 v[8:11], v128 offset:128\n"
        "v_mov_b64 v[92:93], 0\n"
        "v_smfmac_f32_16x16x64_bf16 v[120:123], v[60:63], v[200:207], v134\n"
        "ds_read_b128 v[12:15], v128 offset:192\n"
        "v_mov_b64 v[20:21], 0\n"
        "v_smfmac_f32_16x16x64_bf16 v[116:119], v[56:59], v[208:215], v134\n"
        "s_waitcnt lgkmcnt(3)\n"
        "v_max3_f32 v24, v24, v25, v26\n"
        "v_mov_b64 v[98:99], 0\n"
        "s_waitcnt lgkmcnt(2)\n"
        "v_smfmac_f32_16x16x64_bf16 v[104:107], v[108:111], v[0:7], v134\n"
        "v_max_f32 v24, v24, v27\n"
        "v_smfmac_f32_16x16x64_bf16 v[120:123], v[56:59], v[216:223], v134\n"
        "v_readfirstlane_b32 s84, v24\n"
        "s_waitcnt lgkmcnt(0)\n"
        "v_smfmac_f32_16x16x64_bf16 v[104:107], v[112:115], v[8:15], v134\n"
        "v_mov_b64 v[22:23], 0\n"
        "v_smfmac_f32_16x16x64_bf16 v[116:119], v[80:83], v[224:231], v134\n"
        "s_lshr_b32 s84, s84, 23\n"
        "s_add_u32 s84, s84, s101\n"
        "v_smfmac_f32_16x16x64_bf16 v[120:123], v[80:83], v[232:239], v134\n"
        "s_max_i32 s84, s84, s76\n"
        "s_min_i32 s84, s84, 186\n"
        "v_add_f32 v104, v104, v106\n"
        "v_add_f32 v105, v105, v107\n"
        "v_smfmac_f32_16x16x64_bf16 v[92:95], v[64:67], a[128:135], v134\n"
        "ds_write_b64 v131, v[104:105]\n"
        "v_add_f32 v116, v116, v118\n"
        "v_add_f32 v117, v117, v119\n"
        "v_pk_add_f32 v[120:121], v[120:121], v[122:123]\n"
        "v_cvt_pk_bf16_f32 v116, v116, v120\n"
        "v_cvt_pk_bf16_f32 v117, v117, v121\n"
        "ds_write2_b32 v129, v116, v117 offset1:8\n"
        "v_smfmac_f32_16x16x64_bf16 v[96:99], v[64:67], a[136:143], v134\n"
        "s_mul_i32 s88, s84, 0x800080\n"
        "s_mov_b32 s89, s88\n"
        "v_smfmac_f32_16x16x64_bf16 v[92:95], v[68:71], a[32:39], v134\n"
        "s_add_u32 s90, s88, s35\n"
        "s_mov_b32 s91, s90\n"
        "v_mov_b64 v[104:105], s[88:89]\n"
        "v_smfmac_f32_16x16x64_bf16 v[96:99], v[68:71], a[40:47], v134\n"
        "buffer_store_dwordx4 v[100:103], v124, s[36:39], s54 offen offset:0 sc1\n"
        "v_mov_b64 v[106:107], s[90:91]\n"
        "v_smfmac_f32_16x16x64_bf16 v[92:95], v[48:51], a[0:7], v134\n"
        "s_waitcnt lgkmcnt(0)\n"
        "v_mov_b64 v[100:101], s[88:89]\n"
        "s_barrier\n"
        "ds_read_b128 v[24:27], v130\n"
        "v_smfmac_f32_16x16x64_bf16 v[96:99], v[48:51], a[8:15], v134\n"
        "ds_read_b128 v[28:31], v130 offset:1024\n"
        "v_mov_b64 v[102:103], s[90:91]\n"
        "v_smfmac_f32_16x16x64_bf16 v[92:95], v[52:55], a[16:23], v134\n"
        "ds_read_b128 v[116:119], v124 offset:13568\n"
        "v_mov_b64 v[48:49], 0\n"
        "v_smfmac_f32_16x16x64_bf16 v[96:99], v[52:55], a[24:31], v134\n"
        "v_mov_b64 v[50:51], 0\n"
        "v_smfmac_f32_16x16x64_bf16 v[92:95], v[56:59], a[96:103], v134\n"
        "buffer_load_dwordx4 v[64:67], v124, s[36:39], s69 offen offset:0 sc1\n"
        "v_mov_b64 v[52:53], 0\n"
        "v_smfmac_f32_16x16x64_bf16 v[96:99], v[56:59], a[104:111], v134\n"
        "v_mov_b64 v[54:55], 0\n"
        "s_waitcnt lgkmcnt(2)\n"
        "v_smfmac_f32_16x16x64_bf16 v[20:23], v[24:27], v[0:7], v134\n"
        "buffer_load_dwordx4 v[68:71], v124, s[36:39], s69 offen offset:1024 sc1\n"
        "v_mov_b64 v[56:57], 0\n"
        "s_waitcnt lgkmcnt(0)\n"
        "v_smfmac_f32_16x16x64_bf16 v[20:23], v[28:31], v[8:15], v134\n"
        "ds_read_b128 v[0:3], v125 offset:24576\n"
        "v_max3_f32 v121, v116, v117, v118\n"
        "v_smfmac_f32_16x16x64_bf16 v[92:95], v[72:75], a[48:55], v134\n"
        "v_mov_b64 v[58:59], 0\n"
        "v_smfmac_f32_16x16x64_bf16 v[96:99], v[72:75], a[56:63], v134\n"
        "ds_read_b128 v[4:7], v125 offset:25600\n"
        "v_add_f32 v20, v20, v22\n"
        "v_add_f32 v21, v21, v23\n"
        "v_cndmask_b32 v120, v20, v21, s[92:93]\n"
        "v_mul_f32 v120, s74, v120\n"
        "v_max3_f32 v121, v121, v119, v120\n"
        "v_smfmac_f32_16x16x64_bf16 v[92:95], v[76:79], a[64:71], v134\n"
        "buffer_load_dwordx4 v[72:75], v124, s[36:39], s72 offen offset:2048 sc1\n"
        "v_max_f32_dpp v121, v121, v121 quad_perm:[2,3,0,1] row_mask:0xf bank_mask:0xf\n"
        "v_mov_b32 v122, v121\n"
        "v_smfmac_f32_16x16x64_bf16 v[96:99], v[76:79], a[72:79], v134\n"
        "ds_read_b128 v[8:11], v125 offset:26624\n"
        "v_permlane16_swap_b32 v121, v122\n"
        "v_max_f32 v121, v121, v122\n"
        "v_mov_b32 v122, v121\n"
        "v_smfmac_f32_16x16x64_bf16 v[92:95], v[80:83], a[112:119], v134\n"
        "buffer_load_dwordx4 v[76:79], v124, s[36:39], s72 offen offset:3072 sc1\n"
        "v_permlane32_swap_b32 v121, v122\n"
        "v_smfmac_f32_16x16x64_bf16 v[96:99], v[80:83], a[120:127], v134\n"
        "ds_read_b128 v[12:15], v125 offset:27648\n"
        "v_max_f32 v121, v121, v122\n"
        "v_mul_f32_e64 v123, -s73, v121\n"
        "v_fma_f32 v120, v120, s73, v123\n"
        "v_fma_f32 v116, v116, s73, v123\n"
        "v_smfmac_f32_16x16x64_bf16 v[92:95], v[60:63], a[80:87], v134\n"
        "buffer_load_dwordx4 v[80:83], v124, s[36:39], s87 offen offset:0 sc1\n"
        "v_fma_f32 v117, v117, s73, v123\n"
        "v_fma_f32 v118, v118, s73, v123\n"
        "v_fma_f32 v119, v119, s73, v123\n"
        "v_exp_f32 v120, v120\n"
        "v_smfmac_f32_16x16x64_bf16 v[96:99], v[60:63], a[88:95], v134\n"
        "ds_read_b128 v[16:19], v125 offset:28672\n"
        "v_exp_f32 v116, v116\n"
        "v_mul_f32 v120, s74, v120\n"
        "v_exp_f32 v117, v117\n"
        "v_exp_f32 v118, v118\n"
        "v_exp_f32 v119, v119\n"
        "s_waitcnt vmcnt(7)\n"
        "v_mov_b64 v[108:109], v[86:87]\n"
        "v_mov_b64 v[112:113], v[90:91]\n"
        "v_cvt_pk_bf16_f32 v120, v120, v120\n"
        "v_cvt_pk_bf16_f32 v60, v116, v117\n"
        "v_cvt_pk_bf16_f32 v62, v118, v119\n"
        "v_and_b32 v61, v120, v135\n"
        "v_and_b32 v63, v120, v136\n"
        "v_pk_add_f32 v[92:93], v[92:93], v[94:95]\n"
        "v_pk_add_f32 v[96:97], v[96:97], v[98:99]\n"
        "v_smfmac_f32_16x16x64_bf16 v[56:59], v[60:63], v[100:107], v134\n"
        "ds_read_b128 v[20:23], v125 offset:29696\n"
        "v_cvt_pk_bf16_f32 v86, v92, v93\n"
        "v_cvt_pk_bf16_f32 v87, v92, v93\n"
        "v_mov_b64 v[90:91], v[86:87]\n"
        "v_cvt_pk_bf16_f32 v110, v96, v97\n"
        "v_cvt_pk_bf16_f32 v111, v96, v97\n"
        "v_smfmac_f32_16x16x64_bf16 v[48:51], v[60:63], v[84:91], v134\n"
        "ds_read_b128 v[24:27], v125 offset:30720\n"
        "v_mov_b64 v[114:115], v[110:111]\n"
        "v_add_f32 v56, v56, v58\n"
        "v_add_f32 v57, v57, v59\n"
        "v_smfmac_f32_16x16x64_bf16 v[52:55], v[60:63], v[108:115], v134\n"
        "s_nop 1\n"
        "v_rcp_f32 v56, v56\n"
        "v_rcp_f32 v57, v57\n"
        "v_pk_add_f32 v[48:49], v[48:49], v[50:51]\n"
        "v_pk_mul_f32 v[48:49], v[48:49], v[56:57]\n"
        "v_mov_b32 v110, v86\n"
        "v_mov_b64 v[92:93], s[70:71]\n"
        "v_pk_add_f32 v[52:53], v[52:53], v[54:55]\n"
        "v_pk_mul_f32 v[52:53], v[52:53], v[56:57]\n"
        "v_cvt_pk_bf16_f32 v48, v48, v52\n"
        "v_cvt_pk_bf16_f32 v49, v49, v53\n"
        "ds_write2_b32 v139, v48, v49 offset1:4\n"
        "v_mov_b64 v[94:95], 0\n"
        "v_mov_b64 v[96:97], 0\n"
        "v_mov_b64 v[98:99], 0\n"
        "v_mov_b64 v[100:101], s[70:71]\n"
        "v_mov_b64 v[102:103], 0\n"
        "v_mov_b64 v[104:105], 0\n"
        "buffer_store_dwordx2 v[110:111], v126, s[36:39], s60 offen sc1\n"
        "s_waitcnt lgkmcnt(0)\n"
        "v_mov_b64 v[106:107], 0\n"
        "s_barrier\n"
        "ds_read_b128 v[108:111], v124 offset:18432\n"
        "v_mov_b64 v[106:107], 0\n"
        "ds_read_b128 v[112:115], v124 offset:19456\n"
        "v_mov_b64 v[106:107], 0\n"
        "s_bitcmp1_b32 s59, 30\n"
        "s_cbranch_scc1 L_s1_3%=\n"
        "buffer_store_dwordx4 v[28:31], v124, s[36:39], s59 offen offset:1024 sc1\n"
        "L_s1_3%=:\n"
        "v_mov_b64 v[106:107], 0\n"
        "v_mov_b64 v[106:107], 0\n"
        "ds_read_b128 v[28:31], v125 offset:31744\n"
        "v_mov_b64 v[106:107], 0\n"
        "s_waitcnt lgkmcnt(2)\n"
        "v_smfmac_f32_16x16x64_bf16 v[92:95], v[108:111], v[144:151], v134\n"
        "v_smfmac_f32_16x16x64_bf16 v[96:99], v[108:111], v[152:159], v134\n"
        "s_waitcnt lgkmcnt(1)\n"
        "v_smfmac_f32_16x16x64_bf16 v[92:95], v[112:115], v[160:167], v134\n"
        "v_smfmac_f32_16x16x64_bf16 v[96:99], v[112:115], v[168:175], v134\n"
        "v_smfmac_f32_16x16x64_bf16 v[100:103], v[108:111], v[0:7], v134\n"
        "v_add_f32 v92, v92, v94\n"
        "v_add_f32 v93, v93, v95\n"
        "v_cvt_rpi_i32_f32 v94, v92\n"
        "v_smfmac_f32_16x16x64_bf16 v[104:107], v[108:111], v[8:15], v134\n"
        "v_bfi_b32 v94, 63, 1, v94\n"
        "v_add_f32 v96, v96, v98\n"
        "v_smfmac_f32_16x16x64_bf16 v[100:103], v[112:115], v[16:23], v134\n"
        "buffer_load_dword v140, v124, s[36:39], s29 offen sc1\n"
        "v_cvt_rpi_i32_f32 v95, v96\n"
        "s_waitcnt lgkmcnt(0)\n"
        "v_smfmac_f32_16x16x64_bf16 v[104:107], v[112:115], v[24:31], v134\n"
        "buffer_atomic_add_x2 v[94:95], v133, s[36:39], s51 offen\n"
        "v_add_f32 v97, v97, v99\n"
        "v_cvt_rpi_i32_f32 v98, v93\n"
        "v_bfi_b32 v98, 63, 1, v98\n"
        "v_cvt_rpi_i32_f32 v99, v97\n"
        "s_waitcnt vmcnt(7)\n"
        "v_mfma_f32_16x16x32_bf16 v[84:87], v[64:67], a[0:3], 0\n"
        "buffer_atomic_add_x2 v[98:99], v133, s[36:39], s66 offen\n"
        "v_add_f32 v100, v100, v102\n"
        "v_add_f32 v101, v101, v103\n"
        "v_add_f32 v104, v104, v106\n"
        "v_cvt_rpi_i32_f32 v102, v100\n"
        "v_bfi_b32 v102, 63, 1, v102\n"
        "v_cvt_rpi_i32_f32 v103, v104\n"
        "v_mfma_f32_16x16x32_bf16 v[88:91], v[64:67], a[8:11], 0\n"
        "buffer_atomic_add_x2 v[102:103], v133, s[36:39], s67 offen\n"
        "v_add_f32 v105, v105, v107\n"
        "v_cvt_rpi_i32_f32 v106, v101\n"
        "v_bfi_b32 v106, 63, 1, v106\n"
        "v_cvt_rpi_i32_f32 v107, v105\n"
        "v_mfma_f32_16x16x32_bf16 v[56:59], a[144:147], v[64:67], 0\n"
        "buffer_atomic_add_x2 v[106:107], v133, s[36:39], s68 offen\n"
        "L_win_3%=:\n"
        "v_mfma_f32_16x16x32_bf16 v[60:63], a[152:155], v[64:67], 0\n"
        "v_mfma_f32_16x16x32_bf16 v[48:51], a[240:243], v[64:67], 0\n"
        "v_mfma_f32_16x16x32_bf16 v[52:55], a[248:251], v[64:67], 0\n"
        "ds_write_b128 v124, v[64:67] offset:12288\n"
        "s_waitcnt vmcnt(9)\n"
        "v_mfma_f32_16x16x32_bf16 v[84:87], v[68:71], a[4:7], v[84:87]\n"
        "v_mfma_f32_16x16x32_bf16 v[88:91], v[68:71], a[12:15], v[88:91]\n"
        "ds_write_b128 v124, v[68:71] offset:13312\n"
        "v_mfma_f32_16x16x32_bf16 v[56:59], a[148:151], v[68:71], v[56:59]\n"
        "v_mfma_f32_16x16x32_bf16 v[60:63], a[156:159], v[68:71], v[60:63]\n"
        "s_waitcnt vmcnt(8)\n"
        "ds_write_b128 v124, v[72:75] offset:14336\n"
        "v_mfma_f32_16x16x32_bf16 v[48:51], a[244:247], v[68:71], v[48:51]\n"
        "v_mfma_f32_16x16x32_bf16 v[52:55], a[252:255], v[68:71], v[52:55]\n"
        "s_waitcnt vmcnt(7)\n"
        "ds_write_b128 v124, v[76:79] offset:15360\n"
        "v_mfma_f32_16x16x32_bf16 v[84:87], v[72:75], a[16:19], v[84:87]\n"
        "v_mfma_f32_16x16x32_bf16 v[88:91], v[72:75], a[24:27], v[88:91]\n"
        "s_waitcnt vmcnt(6)\n"
        "ds_write_b128 v124, v[80:83] offset:17408\n"
        "v_mfma_f32_16x16x32_bf16 v[56:59], a[160:163], v[72:75], v[56:59]\n"
        "v_mfma_f32_16x16x32_bf16 v[60:63], a[168:171], v[72:75], v[60:63]\n"
        "v_mfma_f32_16x16x32_bf16 v[48:51], v[176:179], v[72:75], v[48:51]\n"
        "ds_read_b128 v[0:3], v125 offset:12288\n"
        "v_mfma_f32_16x16x32_bf16 v[52:55], v[184:187], v[72:75], v[52:55]\n"
        "ds_read_b128 v[8:11], v125 offset:14336\n"
        "v_mfma_f32_16x16x32_bf16 v[84:87], v[76:79], a[20:23], v[84:87]\n"
        "s_waitcnt lgkmcnt(2)\n"
        "s_barrier\n"
        "v_mfma_f32_16x16x32_bf16 v[88:91], v[76:79], a[28:31], v[88:91]\n"
        "ds_read_b128 v[92:95], v124 offset:8192\n"
        "v_mov_b32 v116, s62\n"
        "v_mfma_f32_16x16x32_bf16 v[56:59], a[164:167], v[76:79], v[56:59]\n"
        "s_add_u32 s49, s49, 163840\n"
        "ds_read_b128 v[104:107], v124 offset:9216\n"
        "v_mfma_f32_16x16x32_bf16 v[60:63], a[172:175], v[76:79], v[60:63]\n"
        "s_add_u32 s50, s50, 163840\n"
        "ds_read_b128 v[4:7], v125 offset:13312\n"
        "v_mfma_f32_16x16x32_bf16 v[48:51], v[180:183], v[76:79], v[48:51]\n"
        "s_xor_b32 s49, s49, s86\n"
        "ds_read_b128 v[12:15], v125 offset:15360\n"
        "v_mfma_f32_16x16x32_bf16 v[52:55], v[188:191], v[76:79], v[52:55]\n"
        "s_xor_b32 s65, s49, 8192\n"
        "s_add_u32 s56, s56, 786432\n"
        "s_waitcnt lgkmcnt(3)\n"
        "v_mfma_f32_16x16x32_bf16 v[84:87], v[92:95], a[32:35], v[84:87]\n"
        "s_add_u32 s64, s64, 786432\n"
        "ds_read_b128 v[96:99], v124 offset:10240\n"
        "v_mfma_f32_16x16x32_bf16 v[88:91], v[92:95], a[40:43], v[88:91]\n"
        "ds_read_b128 v[100:103], v124 offset:11264\n"
        "v_mfma_f32_16x16x32_bf16 v[56:59], a[176:179], v[92:95], v[56:59]\n"
        "ds_read_b128 v[16:19], v125 offset:16384\n"
        "v_mfma_f32_16x16x32_bf16 v[60:63], a[184:187], v[92:95], v[60:63]\n"
        "s_add_u32 s51, s51, 163840\n"
        "ds_read_b128 v[20:23], v125 offset:17408\n"
        "v_mfma_f32_16x16x32_bf16 v[48:51], v[0:3], v[92:95], v[48:51]\n"
        "s_add_u32 s83, s83, 163840\n"
        "ds_read_b128 v[24:27], v125 offset:18432\n"
        "v_mfma_f32_16x16x32_bf16 v[52:55], v[8:11], v[92:95], v[52:55]\n"
        "ds_read_b128 v[28:31], v125 offset:19456\n"
        "s_waitcnt lgkmcnt(8)\n"
        "v_mfma_f32_16x16x32_bf16 v[84:87], v[104:107], a[36:39], v[84:87]\n"
        "s_xor_b32 s86, s83, s46\n"
        "ds_read_b128 v[92:95], v124 offset:4096\n"
        "v_mfma_f32_16x16x32_bf16 v[88:91], v[104:107], a[44:47], v[88:91]\n"
        "s_and_b32 s86, s86, s53\n"
        "s_lshr_b32 s86, s86, 8\n"
        "s_xor_b32 s86, s86, s34\n"
        "v_mfma_f32_16x16x32_bf16 v[56:59], a[180:183], v[104:107], v[56:59]\n"
        "s_xor_b32 s34, s34, s86\n"
        "s_xor_b32 s51, s51, s86\n"
        "s_add_u32 s55, s55, 786432\n"
        "v_mfma_f32_16x16x32_bf16 v[60:63], a[188:191], v[104:107], v[60:63]\n"
        "s_add_u32 s54, s54, 786432\n"
        "ds_read_b128 v[32:35], v125 offset:20480\n"
        "s_waitcnt lgkmcnt(9)\n"
        "v_mfma_f32_16x16x32_bf16 v[48:51], v[4:7], v[104:107], v[48:51]\n"
        "s_waitcnt vmcnt(5)\n"
        "s_mov_b64 exec, 1\n"
        "buffer_atomic_umax v116, off, s[36:39], s61\n"
        "s_mov_b64 exec, -1\n"
        "s_waitcnt lgkmcnt(8)\n"
        "v_mfma_f32_16x16x32_bf16 v[52:55], v[12:15], v[104:107], v[52:55]\n"
        "ds_read_b128 v[36:39], v125 offset:21504\n"
        "s_waitcnt lgkmcnt(8)\n"
        "v_mfma_f32_16x16x32_bf16 v[84:87], v[96:99], a[48:51], v[84:87]\n"
        "s_add_u32 s59, s59, 131072\n"
        "ds_read_b128 v[104:107], v124 offset:5120\n"
        "v_mfma_f32_16x16x32_bf16 v[88:91], v[96:99], a[56:59], v[88:91]\n"
        "s_cmp_eq_u32 s44, 63\n"
        "s_cbranch_scc1 L_end%=\n"
        "v_mfma_f32_16x16x32_bf16 v[56:59], a[192:195], v[96:99], v[56:59]\n"
        "s_add_u32 s60, s60, 131072\n"
        "ds_read_b128 v[40:43], v125 offset:22528\n"
        "v_mfma_f32_16x16x32_bf16 v[60:63], a[200:203], v[96:99], v[60:63]\n"
        "ds_read_b128 v[44:47], v125 offset:23552\n"
        "s_waitcnt vmcnt(5)\n"
        "v_cmp_eq_u32 vcc, 0, v140\n"
        "s_waitcnt lgkmcnt(9)\n"
        "v_mfma_f32_16x16x32_bf16 v[48:51], v[16:19], v[96:99], v[48:51]\n"
        "s_cbranch_vccnz L_sw0_3%=\n"
        "L_sok_3%=:\n"
        "s_waitcnt lgkmcnt(7)\n"
        "v_mfma_f32_16x16x32_bf16 v[52:55], v[24:27], v[96:99], v[52:55]\n"
        "v_readlane_b32 s81, v140, s99\n"
        "v_max_u32_dpp v140, v140, v140 row_shr:1 row_mask:0xf bank_mask:0xf\n"
        "v_mfma_f32_16x16x32_bf16 v[84:87], v[100:103], a[52:55], v[84:87]\n"
        "ds_read_b128 v[96:99], v124 offset:6144\n"
        "v_max_u32_dpp v140, v140, v140 row_shr:2 row_mask:0xf bank_mask:0xf\n"
        "v_mfma_f32_16x16x32_bf16 v[88:91], v[100:103], a[60:63], v[88:91]\n"
        "ds_read_b128 v[0:3], v125 offset:0\n"
        "v_max_u32_dpp v140, v140, v140 row_shr:4 row_mask:0xf bank_mask:0xf\n"
        "v_mfma_f32_16x16x32_bf16 v[56:59], a[196:199], v[100:103], v[56:59]\n"
        "buffer_load_dwordx4 v[108:111], v132, s[36:39], s56 offen offset:0 sc1\n"
        "v_max_u32_dpp v140, v140, v140 row_shr:8 row_mask:0xf bank_mask:0xf\n"
        "v_mfma_f32_16x16x32_bf16 v[60:63], a[204:207], v[100:103], v[60:63]\n"
        "ds_read_b128 v[4:7], v125 offset:1024\n"
        "v_max_u32_dpp v140, v140, v140 row_bcast:15 row_mask:0xa bank_mask:0xf\n"
        "v_mfma_f32_16x16x32_bf16 v[48:51], v[20:23], v[100:103], v[48:51]\n"
        "buffer_load_dwordx4 v[112:115], v132, s[36:39], s64 offen offset:0 sc1\n"
        "v_max_u32_dpp v140, v140, v140 row_bcast:31 row_mask:0xc bank_mask:0xf\n"
        "s_waitcnt lgkmcnt(9)\n"
        "v_mfma_f32_16x16x32_bf16 v[52:55], v[28:31], v[100:103], v[52:55]\n"
        "v_readlane_b32 s33, v140, 63\n"
        "s_lshl1_add_u32 s28, s28, s81\n"
        "s_waitcnt lgkmcnt(8)\n"
        "v_mfma_f32_16x16x32_bf16 v[84:87], v[92:95], a[64:67], v[84:87]\n"
        "ds_read_b128 v[100:103], v124 offset:7168\n"
        "s_add_u32 s28, s28, 36\n"
        "s_sub_i32 s33, s33, s28\n"
        "v_mfma_f32_16x16x32_bf16 v[88:91], v[92:95], a[72:75], v[88:91]\n"
        "s_min_i32 s33, s33, 500\n"
        "s_mov_b32 s28, 0\n"
        "L_dl_3%=:\n"
        "s_sub_i32 s33, s33, 3\n"
        "s_cmp_gt_i32 s33, 0\n"
        "s_cbranch_scc1 L_dl_3%=\n"
        "v_mfma_f32_16x16x32_bf16 v[56:59], a[208:211], v[92:95], v[56:59]\n"
        "s_add_u32 s58, s58, 36864\n"
        "ds_read_b128 v[8:11], v125 offset:2048\n"
        "v_mfma_f32_16x16x32_bf16 v[60:63], a[216:219], v[92:95], v[60:63]\n"
        "s_xor_b32 s66, s51, 1024\n"
        "buffer_load_dwordx4 v[80:83], v138, s[36:39], s50 offen offset:0 sc1\n"
        "s_waitcnt lgkmcnt(9)\n"
        "v_mfma_f32_16x16x32_bf16 v[48:51], v[32:35], v[92:95], v[48:51]\n"
        "s_xor_b32 s67, s51, 8192\n"
        "ds_read_b128 v[12:15], v125 offset:3072\n"
        "s_waitcnt lgkmcnt(7)\n"
        "v_mfma_f32_16x16x32_bf16 v[52:55], v[40:43], v[92:95], v[52:55]\n"
        "buffer_load_dwordx4 v[64:67], v138, s[36:39], s49 offen offset:0 sc1\n"
        "v_mfma_f32_16x16x32_bf16 v[84:87], v[104:107], a[68:71], v[84:87]\n"
        "s_xor_b32 s68, s51, 9216\n"
        "ds_read_b128 v[92:95], v124 offset:0\n"
        "v_mfma_f32_16x16x32_bf16 v[88:91], v[104:107], a[76:79], v[88:91]\n"
        "s_sub_u32 s75, 127, s84\n"
        "buffer_load_dwordx4 v[68:71], v138, s[36:39], s49 offen offset:256 sc1\n"
        "v_mfma_f32_16x16x32_bf16 v[56:59], a[212:215], v[104:107], v[56:59]\n"
        "s_sub_u32 s101, s77, s75\n"
        "ds_read_b128 v[16:19], v125 offset:4096\n"
        "v_mfma_f32_16x16x32_bf16 v[60:63], a[220:223], v[104:107], v[60:63]\n"
        "s_mul_i32 s35, s75, 0x800080\n"
        "buffer_load_dwordx4 v[72:75], v138, s[36:39], s65 offen offset:0 sc1\n"
        "v_mfma_f32_16x16x32_bf16 v[48:51], v[36:39], v[104:107], v[48:51]\n"
        "s_lshl_b32 s80, s75, 23\n"
        "ds_read_b128 v[20:23], v125 offset:5120\n"
        "s_waitcnt lgkmcnt(9)\n"
        "v_mfma_f32_16x16x32_bf16 v[52:55], v[44:47], v[104:107], v[52:55]\n"
        "buffer_load_dwordx4 v[76:79], v138, s[36:39], s65 offen offset:256 sc1\n"
        "s_waitcnt lgkmcnt(8)\n"
        "v_mfma_f32_16x16x32_bf16 v[84:87], v[96:99], a[80:83], v[84:87]\n"
        "s_sub_u32 s73, 0x3e0293ee, s80\n"
        "ds_read_b128 v[104:107], v124 offset:1024\n"
        "v_mfma_f32_16x16x32_bf16 v[88:91], v[96:99], a[88:91], v[88:91]\n"
        "s_sub_u32 s74, 0x3f800000, s80\n"
        "ds_read_b128 v[24:27], v125 offset:6144\n"
        "v_mfma_f32_16x16x32_bf16 v[56:59], a[224:227], v[96:99], v[56:59]\n"
        "s_add_u32 s80, s75, 128\n"
        "ds_read_b128 v[28:31], v125 offset:7168\n"
        "v_mfma_f32_16x16x32_bf16 v[60:63], a[232:235], v[96:99], v[60:63]\n"
        "s_lshl_b32 s80, s80, 8\n"
        "ds_read_b128 v[32:35], v125 offset:8192\n"
        "v_mfma_f32_16x16x32_bf16 v[48:51], v[192:195], v[96:99], v[48:51]\n"
        "s_add_u32 s62, s80, 1\n"
        "ds_read_b128 v[36:39], v125 offset:9216\n"
        "v_mfma_f32_16x16x32_bf16 v[52:55], v[200:203], v[96:99], v[52:55]\n"
        "ds_read_b128 v[40:43], v125 offset:10240\n"
        "s_waitcnt lgkmcnt(11)\n"
        "v_mfma_f32_16x16x32_bf16 v[84:87], v[100:103], a[84:87], v[84:87]\n"
        "s_add_u32 s44, s44, 1\n"
        "ds_read_b128 v[96:99], v124 offset:2048\n"
        "v_mfma_f32_16x16x32_bf16 v[88:91], v[100:103], a[92:95], v[88:91]\n"
        "s_add_u32 s61, s61, 32\n"
        "ds_read_b128 v[44:47], v125 offset:11264\n"
        "v_mfma_f32_16x16x32_bf16 v[56:59], a[228:231], v[100:103], v[56:59]\n"
        "s_xor_b32 s57, s57, 4096\n"
        "s_add_u32 s31, s31, 1\n"
        "s_and_b32 s31, s31, 63\n"
        "v_mfma_f32_16x16x32_bf16 v[60:63], a[236:239], v[100:103], v[60:63]\n"
        "s_branch L_rot_3%=\n"
        "L_abort%=:\n"
        "v_mov_b32 v0, 1\n"
        "s_mov_b32 s80, 0\n"
        "buffer_store_dword v0, off, s[36:39], s80 sc1\n"
        "L_end%=:\n"
        "s_waitcnt vmcnt(0)\n"
        "s_waitcnt lgkmcnt(0)\n"
        :: "s"(ws), "s"(Wq), "s"(Wk), "s"(Wv), "s"(Wo), "s"(id), "s"(ack)
        : "v0", "v1", "v2", "v3", "v4", "v5", "v6", "v7", "v8", "v9", "v10", "v11", "v12", "v13", "v14", "v15", "v16", "v17", "v18", "v19", "v20", "v21", "v22", "v23",
          "v24", "v25", "v26", "v27", "v28", "v29", "v30", "v31", "v32", "v33", "v34", "v35", "v36", "v37", "v38", "v39", "v40", "v41", "v42", "v43", "v44", "v45", "v46", "v47",
          "v48", "v49", "v50", "v51", "v52", "v53", "v54", "v55", "v56", "v57", "v58", "v59", "v60", "v61", "v62", "v63", "v64", "v65", "v66", "v67", "v68", "v69", "v70", "v71",
          "v72", "v73", "v74", "v75", "v76", "v77", "v78", "v79", "v80", "v81", "v82", "v83", "v84", "v85", "v86", "v87", "v88", "v89", "v90", "v91", "v92", "v93", "v94", "v95",
          "v96", "v97", "v98", "v99", "v100", "v101", "v102", "v103", "v104", "v105", "v106", "v107", "v108", "v109", "v110", "v111", "v112", "v113", "v114", "v115", "v116", "v117", "v118", "v119",
          "v120", "v121", "v122", "v123", "v124", "v125", "v126", "v127", "v128", "v129", "v130", "v131", "v132", "v133", "v134", "v135", "v136", "v137", "v138", "v139", "v140", "v141", "v142", "v143",
          "v144", "v145", "v146", "v147", "v148", "v149", "v150", "v151", "v152", "v153", "v154", "v155", "v156", "v157", "v158", "v159", "v160", "v161", "v162", "v163", "v164", "v165", "v166", "v167",
          "v168", "v169", "v170", "v171", "v172", "v173", "v174", "v175", "v176", "v177", "v178", "v179", "v180", "v181", "v182", "v183", "v184", "v185", "v186", "v187", "v188", "v189", "v190", "v191",
          "v192", "v193", "v194", "v195", "v196", "v197", "v198", "v199", "v200", "v201", "v202", "v203", "v204", "v205", "v206", "v207", "v208", "v209", "v210", "v211", "v212", "v213", "v214", "v215",
          "v216", "v217", "v218", "v219", "v220", "v221", "v222", "v223", "v224", "v225", "v226", "v227", "v228", "v229", "v230", "v231", "v232", "v233", "v234", "v235", "v236", "v237", "v238", "v239",
          "v240", "v241", "v242", "v243", "v244", "v245", "v246", "v247", "v248", "v249", "v250", "v251", "v252", "v253", "v254", "v255", "a0", "a1", "a2", "a3", "a4", "a5", "a6", "a7",
          "a8", "a9", "a10", "a11", "a12", "a13", "a14", "a15", "a16", "a17", "a18", "a19", "a20", "a21", "a22", "a23", "a24", "a25", "a26", "a27", "a28", "a29", "a30", "a31",
          "a32", "a33", "a34", "a35", "a36", "a37", "a38", "a39", "a40", "a41", "a42", "a43", "a44", "a45", "a46", "a47", "a48", "a49", "a50", "a51", "a52", "a53", "a54", "a55",
          "a56", "a57", "a58", "a59", "a60", "a61", "a62", "a63", "a64", "a65", "a66", "a67", "a68", "a69", "a70", "a71", "a72", "a73", "a74", "a75", "a76", "a77", "a78", "a79",
          "a80", "a81", "a82", "a83", "a84", "a85", "a86", "a87", "a88", "a89", "a90", "a91", "a92", "a93", "a94", "a95", "a96", "a97", "a98", "a99", "a100", "a101", "a102", "a103",
          "a104", "a105", "a106", "a107", "a108", "a109", "a110", "a111", "a112", "a113", "a114", "a115", "a116", "a117", "a118", "a119", "a120", "a121", "a122", "a123", "a124", "a125", "a126", "a127",
          "a128", "a129", "a130", "a131", "a132", "a133", "a134", "a135", "a136", "a137", "a138", "a139", "a140", "a141", "a142", "a143", "a144", "a145", "a146", "a147", "a148", "a149", "a150", "a151",
          "a152", "a153", "a154", "a155", "a156", "a157", "a158", "a159", "a160", "a161", "a162", "a163", "a164", "a165", "a166", "a167", "a168", "a169", "a170", "a171", "a172", "a173", "a174", "a175",
          "a176", "a177", "a178", "a179", "a180", "a181", "a182", "a183", "a184", "a185", "a186", "a187", "a188", "a189", "a190", "a191", "a192", "a193", "a194", "a195", "a196", "a197", "a198", "a199",
          "a200", "a201", "a202", "a203", "a204", "a205", "a206", "a207", "a208", "a209", "a210", "a211", "a212", "a213", "a214", "a215", "a216", "a217", "a218", "a219", "a220", "a221", "a222", "a223",
          "a224", "a225", "a226", "a227", "a228", "a229", "a230", "a231", "a232", "a233", "a234", "a235", "a236", "a237", "a238", "a239", "a240", "a241", "a242", "a243", "a244", "a245", "a246", "a247",
          "a248", "a249", "a250", "a251", "a252", "a253", "a254", "a255", "s20", "s21", "s22", "s23", "s24", "s25", "s26", "s27", "s28", "s29", "s30", "s31", "s32", "s33", "s34", "s35",
          "s36", "s37", "s38", "s39", "s40", "s41", "s42", "s43", "s44", "s45", "s46", "s47", "s48", "s49", "s50", "s51", "s52", "s53", "s54", "s55", "s56", "s57", "s58", "s59",
          "s60", "s61", "s62", "s63", "s64", "s65", "s66", "s67", "s68", "s69", "s70", "s71", "s72", "s73", "s74", "s75", "s76", "s77", "s78", "s79", "s80", "s81", "s82", "s83",
          "s84", "s85", "s86", "s87", "s88", "s89", "s90", "s91", "s92", "s93", "s94", "s95", "s96", "s97", "s98", "s99", "s100", "s101", "vcc", "scc", "m0", "memory");
}
constexpr u32 OOB = 0x40000000u;
#define BID (blockIdx.x ^ 248u)

constexpr u32 XNB_W = OFF_XNB, FX_W = 0;
__device__ __forceinline__ float bf2f(u16 v) { return __uint_as_float(((u32)v) << 16); }
__device__ __forceinline__ float bflo(u32 v) { return __uint_as_float(v << 16); }
__device__ __forceinline__ float bfhi(u32 v) { return __uint_as_float(v & 0xffff0000u); }
__device__ __forceinline__ u32 pack2(float a, float b) { bfv2 v = {(__bf16)a, (__bf16)b}; return __builtin_bit_cast(u32, v); }
__device__ __forceinline__ float rbf(float f) { return (float)(__bf16)f; }
__device__ __forceinline__ v4u ld16(rsrc_t r, u32 off) { return __builtin_amdgcn_raw_buffer_load_b128(r, off, 0, 16); }
__device__ __forceinline__ v2u ld8(rsrc_t r, u32 off) { return __builtin_amdgcn_raw_buffer_load_b64(r, off, 0, 16); }
__device__ __forceinline__ void st16(rsrc_t r, u32 off, v4u v) { __builtin_amdgcn_raw_buffer_store_b128(v, r, off, 0, 16); }
__device__ __forceinline__ void st8(rsrc_t r, u32 off, v2u v) { __builtin_amdgcn_raw_buffer_store_b64(v, r, off, 0, 16); }
__device__ __forceinline__ void st4(rsrc_t r, u32 off, u32 v) { __builtin_amdgcn_raw_buffer_store_b32(v, r, off, 0, 16); }
#define DRAIN() asm volatile("s_waitcnt vmcnt(0)" ::: "memory")
#define CBAR() asm volatile("" ::: "memory")
#define SCHED() __builtin_amdgcn_sched_barrier(0)
#define VMW(n) asm volatile("s_waitcnt vmcnt(%0)" :: "n"(n) : "memory")
#define LBAR() asm volatile("s_waitcnt lgkmcnt(0)\n\ts_barrier" ::: "memory")
__device__ __forceinline__ u32 ldw(char* ws, u32 off) { return __hip_atomic_load((gu32*)(ws + off), RLX_AGENT); }
constexpr u32 SPIN_CAP = 1u << 20;
#define SLP_A 2
#define SLP_S 6
__device__ __forceinline__ bool spin_fail(char* ws, u32& spins) {
    ++spins;
    if ((spins & 63u) == 0u) {
        if (ldw(ws, OFF_ABORT)) return true;
        if (spins > SPIN_CAP) { __hip_atomic_store((gu32*)(ws + OFF_ABORT), 1u, RLX_AGENT); return true; }
    }
    return false;
}
#define NAP_A 2
#define NAP_W 2
#define NAP_C 1
__device__ __forceinline__ void nap(int n) { for (int i = 0; i < n; ++i) __builtin_amdgcn_s_sleep(127); }
__device__ __forceinline__ f32x4 mfma32(bf16x8 a, bf16x8 b, f32x4 c) { return __builtin_amdgcn_mfma_f32_16x16x32_bf16(a, b, c, 0, 0, 0); }
__device__ __forceinline__ f32x4 mfma16(bf16x4 a, bf16x4 b, f32x4 c) { return __builtin_amdgcn_mfma_f32_16x16x16bf16_1k(a, b, c, 0, 0, 0); }
template <bool AG, bool SW, bool FIRST>
__device__ __forceinline__ void mmaw(f32x4& acc, const bf16x8& w, const bf16x8& x) {
    if constexpr (FIRST) {
        if constexpr (AG && !SW) asm volatile("v_mfma_f32_16x16x32_bf16 %0, %1, %2, 0" : "=&v"(acc) : "a"(w), "v"(x));
        else if constexpr (AG && SW) asm volatile("v_mfma_f32_16x16x32_bf16 %0, %2, %1, 0" : "=&v"(acc) : "a"(w), "v"(x));
        else if constexpr (!SW) asm volatile("v_mfma_f32_16x16x32_bf16 %0, %1, %2, 0" : "=&v"(acc) : "v"(w), "v"(x));
        else asm volatile("v_mfma_f32_16x16x32_bf16 %0, %2, %1, 0" : "=&v"(acc) : "v"(w), "v"(x));
    } else {
        if constexpr (AG && !SW) asm volatile("v_mfma_f32_16x16x32_bf16 %0, %1, %2, %0" : "+v"(acc) : "a"(w), "v"(x));
        else if constexpr (AG && SW) asm volatile("v_mfma_f32_16x16x32_bf16 %0, %2, %1, %0" : "+v"(acc) : "a"(w), "v"(x));
        else if constexpr (!SW) asm volatile("v_mfma_f32_16x16x32_bf16 %0, %1, %2, %0" : "+v"(acc) : "v"(w), "v"(x));
        else asm volatile("v_mfma_f32_16x16x32_bf16 %0, %2, %1, %0" : "+v"(acc) : "v"(w), "v"(x));
    }
}
#define NOP2() asm volatile("s_nop 1")
#define NOP8() asm volatile("s_nop 7")
#define PIN(x) asm volatile("" : "+v"(x))
#define PINA(x) asm volatile("" : "+a"(x))
template <bool PERM = false>
__device__ __forceinline__ void load_pair(const u16* W, int ld, int krow, int ncol, bf16x8& f0, bf16x8& f1) {
    u32 d[8];
#pragma unroll
    for (int j = 0; j < 8; ++j) d[j] = *(const u32*)(W + (size_t)(PERM ? krow + 16 * (j & 1) + (j >> 1) : krow + j) * ld + ncol);
    v4u a, b;
#pragma unroll
    for (int q = 0; q < 4; ++q) {
        a[q] = __builtin_amdgcn_perm(d[2 * q + 1], d[2 * q], 0x05040100u);
        b[q] = __builtin_amdgcn_perm(d[2 * q + 1], d[2 * q], 0x07060302u);
    }
    f0 = __builtin_bit_cast(bf16x8, a);
    f1 = __builtin_bit_cast(bf16x8, b);
}
template <bool PERM = false>
__device__ __forceinline__ void load_quad(const u16* W, int ld, int krow, int ncol, bf16x8& f0, bf16x8& f1, bf16x8& f2, bf16x8& f3) {
    v2u d[8];
#pragma unroll
    for (int j = 0; j < 8; ++j) d[j] = *(const v2u*)(W + (size_t)(PERM ? krow + 16 * (j & 1) + (j >> 1) : krow + j) * ld + ncol);
    v4u a, b, c, e;
#pragma unroll
    for (int q = 0; q < 4; ++q) {
        a[q] = __builtin_amdgcn_perm(d[2 * q + 1][0], d[2 * q][0], 0x05040100u);
        b[q] = __builtin_amdgcn_perm(d[2 * q + 1][0], d[2 * q][0], 0x07060302u);
        c[q] = __builtin_amdgcn_perm(d[2 * q + 1][1], d[2 * q][1], 0x05040100u);
        e[q] = __builtin_amdgcn_perm(d[2 * q + 1][1], d[2 * q][1], 0x07060302u);
    }
    f0 = __builtin_bit_cast(bf16x8, a); f1 = __builtin_bit_cast(bf16x8, b);
    f2 = __builtin_bit_cast(bf16x8, c); f3 = __builtin_bit_cast(bf16x8, e);
}
__device__ __forceinline__ v4u pack_quad(f32x4 t0, f32x4 t1, f32x4 t2, f32x4 t3, bool hf) {
    return v4u{pack2(hf ? t0[2] : t0[0], hf ? t1[2] : t1[0]), pack2(hf ? t2[2] : t2[0], hf ? t3[2] : t3[0]),
               pack2(hf ? t0[3] : t0[1], hf ? t1[3] : t1[1]), pack2(hf ? t2[3] : t2[1], hf ? t3[3] : t3[1])};
}
__device__ __forceinline__ v4u pack_pair(f32x4 t0, f32x4 t1) {
    v4u o;
#pragma unroll
    for (int r = 0; r < 4; ++r) o[r] = pack2(t0[r], t1[r]);
    return o;
}
__device__ __forceinline__ float vmax(float a, float b) { float r; asm("v_max_f32 %0, %1, %2" : "=v"(r) : "v"(a), "v"(b)); return r; }
__device__ __forceinline__ float xmax(float v) {
    auto a = __builtin_amdgcn_permlane16_swap(__float_as_uint(v), __float_as_uint(v), false, false);
    v = vmax(__uint_as_float(a[0]), __uint_as_float(a[1]));
    auto b = __builtin_amdgcn_permlane32_swap(__float_as_uint(v), __float_as_uint(v), false, false);
    return vmax(__uint_as_float(b[0]), __uint_as_float(b[1]));
}
__device__ __forceinline__ float xsum(float v) {
    auto a = __builtin_amdgcn_permlane16_swap(__float_as_uint(v), __float_as_uint(v), false, false);
    v = __uint_as_float(a[0]) + __uint_as_float(a[1]);
    auto b = __builtin_amdgcn_permlane32_swap(__float_as_uint(v), __float_as_uint(v), false, false);
    return __uint_as_float(b[0]) + __uint_as_float(b[1]);
}
template <int CTRL> __device__ __forceinline__ float dpp(float v) {
    return __uint_as_float(__builtin_amdgcn_update_dpp(0u, __float_as_uint(v), CTRL, 0xf, 0xf, false));
}
__device__ __forceinline__ float wave_sum(float v) {
    v += dpp<0xB1>(v);
    v += dpp<0x4E>(v);
    v += dpp<0x141>(v);
    v += dpp<0x140>(v);
    return xsum(v);
}
__device__ __forceinline__ f32x2 un2(u32 w) { return f32x2{bflo(w), bfhi(w)}; }
__device__ __forceinline__ u32 pack2(f32x2 v) { return pack2(v[0], v[1]); }
__device__ __forceinline__ void ln_par(float s, float q, f32x2& a, f32x2& b) {
    const float mean = s * (1.f / D_), inv = __builtin_amdgcn_rcpf(rbf(__builtin_amdgcn_sqrtf(rbf(rbf(fmaxf(q * (1.f / D_) - mean * mean, 0.f)) + 1e-5f))));
    a = f32x2{inv, inv};
    b = -rbf(mean) * a;
}
__device__ __forceinline__ void ln_row(const f32x2 (&v)[6], v2u (&y)[3]) {
    f32x2 s = v[0], q = v[0] * v[0], a, b;
#pragma unroll
    for (int i = 1; i < 6; ++i) { s += v[i]; q += v[i] * v[i]; }
    ln_par(wave_sum(s[0] + s[1]), wave_sum(q[0] + q[1]), a, b);
#pragma unroll
    for (int i = 0; i < 6; ++i) y[i >> 1][i & 1] = pack2(v[i] * a + b);
}

__device__ __forceinline__ u32 prog(u32 k) { return OFF_PROG + (k & 7) * 4096 + (k >> 3) * 256; }
#define CR 2
#define CA 1
#define CP 1
#define CB 1
constexpr u32 FOA = 8 * CX, FR = FOA + 8 * CA, FPB = FR + 2 * CR, FOB = FPB + 16 * CP, NPROG = FOB + 8 * CB;
static_assert(NPROG <= 128);
__device__ __forceinline__ u32 rdw(rsrc_t rs, u32 off) { return (u32)__builtin_amdgcn_raw_ptr_buffer_atomic_add_i32(0, rs, off, 0, 0); }
__device__ int buf_umax(int, rsrc_t, int, int, int) __asm("llvm.amdgcn.raw.ptr.buffer.atomic.umax.i32");
__device__ int buf_swap(int, rsrc_t, int, int, int) __asm("llvm.amdgcn.raw.ptr.buffer.atomic.swap.i32");
__device__ __forceinline__ void put(rsrc_t rs, u32 off, u32 v) { buf_swap(v, rs, off, 0, 0); }
__device__ __forceinline__ void tell(rsrc_t rs, u32 mine, u32 done) { buf_umax(done, rs, mine, 0, 0); }
template <int N> __device__ __forceinline__ u32 words(u32 off) { const u32 lane = threadIdx.x & 63; return lane < N ? off + lane * 4 : OOB; }
template <int N> __device__ __forceinline__ bool reached(u32 f, u32 need) { return ((u32)__ballot(f < need) & (u32)((1ull << N) - 1)) == 0u; }
template <int N> __device__ __forceinline__ u32 wait_prog(char* ws, rsrc_t rs, u32 off, u32 need) {
    u32 spins = 0, f;
    while (!reached<N>(f = rdw(rs, off), need)) {
        if (spin_fail(ws, spins)) break;
        __builtin_amdgcn_s_sleep(SLP_A);
    }
    CBAR();
    return __builtin_amdgcn_readfirstlane(f);
}
#define PS 8
#define AS 0
#define BS 2
template <int N, bool ANN, int P> __device__ __forceinline__ u32 watch(char* ws, rsrc_t rs, u32 mine, u32 done, u32 off, u32 need) {
    u32 f = rdw(rs, off), spins = 0;
    if (ANN) { VMW(1); tell(rs, mine, done); PIN(f); }
    if (!reached<N>(f, need))
        do {
            if (P) __builtin_amdgcn_s_sleep(P);
            f = rdw(rs, off);
        } while (!reached<N>(f, need) && !spin_fail(ws, spins));
    CBAR();
    return __builtin_amdgcn_readfirstlane(f);
}
template <int N> __device__ __forceinline__ void meet(char* ws, rsrc_t rs, bool me, u32 mine, u32 done, u32 off, u32 need) {
    if (me) watch<N, true, BS>(ws, rs, mine, done, off, need);
    else { DRAIN(); tell(rs, mine, done); }
    asm volatile("s_barrier" ::: "memory");
}
__device__ __forceinline__ bool told(const char* p) { return __builtin_amdgcn_readfirstlane(*(const u32*)p); }
__device__ __forceinline__ u32 my_word() { return words<1>(OFF_ACK + (BID * 4 + (threadIdx.x >> 6)) * 4); }
__device__ __forceinline__ u32 ask(rsrc_t rs) { DRAIN(); return rdw(rs, my_word()); }
__device__ __forceinline__ void go(rsrc_t rs, u32 a) {
    for (u32 spins = 0; !reached<1>(a, 1u) && spins < SPIN_CAP; ++spins) { __builtin_amdgcn_s_sleep(SLP_A); a = rdw(rs, my_word()); }
    CBAR();
    put(rs, my_word(), 2u);
}
#define CLR 16
template <int E = 1> __device__ __forceinline__ void clear(rsrc_t rs, u32 off, int n) {
#pragma unroll 5
    for (int i = 0; i < n; ++i) __builtin_amdgcn_raw_buffer_store_b128(v4u{0u, 0u, 0u, 0u}, rs, off + (i * 32 + (threadIdx.x >> 3)) * (128 * E) + (threadIdx.x & 7) * 16, 0, CLR);
}
__device__ __forceinline__ void role_lead(char* ws, rsrc_t rs) {
    const u32 tid = threadIdx.x;
    clear(rs, 0, 3);
#pragma unroll
    for (u32 i = FX_W ? CX / 4 : 0; i < (NPROG + 31) / 32; ++i) st16(rs, prog(i * 32 + (tid >> 3)) + (tid & 7) * 16, v4u{0u, 0u, 0u, 0u});
    DRAIN();
    __syncthreads();
#pragma unroll
    for (int k = 0; k < 4; ++k) put(rs, OFF_ACK + (k * 256 + tid) * 4, 1u);
    DRAIN();
    u32 pend = tid < 248 || tid >= 252 ? 15u : 7u, spins = 0;
    bool z = tid < 128;
    while (__any(pend)) {
        u32 old[4];
#pragma unroll
        for (int k = 0; k < 4; ++k) old[k] = __hip_atomic_fetch_max((gu32*)(ws + OFF_ACK) + k * 256 + tid, 1u, RLX_AGENT);
#pragma unroll
        for (int k = 0; k < 4; ++k) if (old[k] == 2u || (old[k] == 0u && !(k == 2 && tid < 128))) pend &= ~(1u << k);
        if (z && !__any(pend & 4u)) { tell(rs, words<1>(OFF_Z + (tid >> 6) * 4), 1u); z = false; }
        if (spin_fail(ws, spins)) break;
        __builtin_amdgcn_s_sleep(SLP_A);
    }
}
typedef short s16x4 __attribute__((ext_vector_type(4)));
__device__ __forceinline__ v2u tr_read(const char* p) {
    return __builtin_bit_cast(v2u, __builtin_amdgcn_ds_read_tr16_b64_v4i16((__attribute__((address_space(3))) s16x4*)p));
}
__device__ __forceinline__ bf16x8 asf(v4u v) { return __builtin_bit_cast(bf16x8, v); }

constexpr u32 TILE = 18432;
__device__ __forceinline__ u32 quad(int lane) { return ((lane >> 2) & 3) * 4608 + (lane >> 4) * 64 + (lane & 3) * 16; }
__device__ __forceinline__ v4u ldt(rsrc_t r, u32 vo, u32 so) { return __builtin_amdgcn_raw_buffer_load_b128(r, vo, so, 16); }
__device__ __forceinline__ void stt(rsrc_t r, u32 vo, u32 so, v4u v) {
    __builtin_amdgcn_raw_buffer_store_b128(v, r, vo, so, 16);
    asm volatile("s_nop 1" :: "v"(v) : "memory");
}

__device__ __forceinline__ void ld_tile(int tile, const u16* x0, v4u (&t)[18]) {
    const int lane = threadIdx.x & 63;
#pragma unroll
    for (int ks = 0; ks < 18; ++ks) t[ks] = *(const v4u*)(x0 + (size_t)(tile * 16 + (lane & 15)) * D_ + ks * 32 + (lane >> 4) * 8);
}
__device__ __forceinline__ void ln_tile(v4u (&t)[18]) {
    f32x2 s = {0.f, 0.f}, q = {0.f, 0.f}, a, b;
#pragma unroll
    for (int i = 0; i < 72; ++i) { const f32x2 v = un2(t[i >> 2][i & 3]); s += v; q += v * v; }
    ln_par(xsum(s[0] + s[1]), xsum(q[0] + q[1]), a, b);
#pragma unroll
    for (int i = 0; i < 72; ++i) t[i >> 2][i & 3] = pack2(un2(t[i >> 2][i & 3]) * a + b);
}
__device__ __forceinline__ void role_t0(int tile, const u16* x0, char* ws, rsrc_t rs) {
    v4u t[18];
    ld_tile(tile, x0, t);
    const u32 a = ask(rs);
    ln_tile(t);
    go(rs, a);
#pragma unroll
    for (int ks = 0; ks < 18; ++ks) stt(rs, quad(threadIdx.x & 63) + (ks & 15) * 256, OFF_XNA + tile * TILE + (ks >> 4) * 4096, t[ks]);
    DRAIN();
    tell(rs, words<16>(OFF_CXNA + tile * 64), NSEG);
}

constexpr int SL = 56;
template <int NR, bool FIN, int S0 = 0, int S1 = NSEG>
__device__ __forceinline__ void role_r(int idx, const u16* x0, u16* out, char* ws, rsrc_t rs, char* smem) {
    const int tid = threadIdx.x, lane = tid & 63, wave = __builtin_amdgcn_readfirstlane(tid >> 6);
    const bool act = lane < 48;
    const int lc = act ? lane : 0;
    const int rl = (idx * 4 + wave) * NR;
    const u32 po = act ? lane * 16 : OOB, xo = act ? (lane >> 1) * 64 + (lane & 1) * 8 : OOB;
    const u32 fp = words<24>(prog(FPB + idx % CP * 16 + (FIN ? 4 : 0) + (rl >> 3) + (lane < 12 ? 0 : 8)) + 16), fo = lane < CR ? prog(FR + lane * 2 + (rl >> 4)) + (rl & 15) * 4 : OOB;
    if (!FIN) {
        v2u xr[2][3];
#pragma unroll
        for (int i = 0; i < 6; ++i) xr[i / 3][i % 3] = *(const v2u*)(x0 + ((i / 3) * SEG + rl) * D_ + ((i % 3) * 48 + lc) * 4);
        const u32 a = ask(rs);
#pragma unroll
        for (int u = 0; u < 2; ++u) {
            f32x2 v[6];
#pragma unroll
            for (int i = 0; i < 6; ++i) v[i] = un2(act ? xr[u][i >> 1][i & 1] : 0u);
            v2u y[3];
            ln_row(v, y);
#pragma unroll
            for (int j = 0; j < 3; ++j) if (act) *(v2u*)(smem + 9216 + u * 4608 + (j * 24 + (lc >> 1)) * 64 + wave * 16 + (lc & 1) * 8) = y[j];
        }
        LBAR();
#pragma unroll
        for (int i = 0; i < 3; ++i) {
            const int c = tid + 256 * i;
            if (c < 576) st16(rs, OFF_XNA + (idx >> 2) * TILE + (idx & 3) * 4608 + (c < 288 ? c * 16 : 2 * TILE - 4608 + c * 16), *(const v4u*)(smem + 9216 + c * 16));
        }
        DRAIN();
        go(rs, a);
        tell(rs, lane < 2 ? OFF_CXNA + lane * 128 + rl * 4 : OOB, NSEG);
    }
    bool owe = false;
    const u32 f1 = wave ? OOB : fp;
    meet<24>(ws, rs, !wave, OOB, 0, fp, S0 + 1);
    const rsrc_t ro = __builtin_amdgcn_make_buffer_rsrc(out, 0, XB, 0x00020000);
    v2u xn[NR][3];
    auto ldx = [&](int s) {
#pragma unroll
        for (int i = 0; i < 3 * NR; ++i)
            xn[i / 3][i % 3] = __builtin_amdgcn_raw_buffer_load_b64(rs, xo + (i % 3) * 1536, OFF_X + (FIN ? 2 : 0) * XB + (s * 2 + (rl >> 4)) * TILE + ((rl >> 2) & 3) * 4608 + ((rl + i / 3) & 3) * 16, 16);
    };
    ldx(S0);
#pragma unroll 1
    for (int s = S0; s < S1; ++s) {
        v2u xr[NR][3];
        v4u pr[NR][4][3];
#pragma unroll
        for (int u = 0; u < NR; ++u) {
#pragma unroll
            for (int p = 0; p < 4; ++p)
#pragma unroll
                for (int j = 0; j < 3; ++j) pr[u][p][j] = ldt(rs, po + j * 768, OFF_PB + (((FIN ? 64 + s : s) * 4 + p) * SEG + rl + u) * D_ * 4);
        }
        SCHED();
#pragma unroll
        for (int i = 0; i < 3 * NR; ++i) xr[i / 3][i % 3] = xn[i / 3][i % 3];
        ldx(min(s + 1, NSEG - 1));
        SCHED();
        if (!FIN && owe) { VMW(15 * NR); tell(rs, fo, s); }
        u32 fn;
        if (FIN) { CBAR(); fn = rdw(rs, f1); }
        char* sm = smem + (s & 1) * 9216;
#pragma unroll
        for (int u = 0; u < NR; ++u) {
            f32x2 v[6];
#pragma unroll
            for (int i = 0; i < 6; ++i) {
                auto P = [&](int p) { return f32x2{__uint_as_float(pr[u][p][i >> 1][2 * (i & 1)]), __uint_as_float(pr[u][p][i >> 1][2 * (i & 1) + 1])}; };
                v[i] = ((P(0) + P(1)) + (P(2) + P(3))) + un2(xr[u][i >> 1][i & 1]);
            }
            v2u y[3];
            if (!FIN) { PIN(v[5]); CBAR(); fn = rdw(rs, f1); }
            if (!FIN) ln_row(v, y);
            if (FIN) {
#pragma unroll
                for (int j = 0; j < 3; ++j) __builtin_amdgcn_raw_buffer_store_b64(v2u{pack2(v[2 * j]), pack2(v[2 * j + 1])}, ro, (po >> 1) + j * 384, (s * SEG + rl + u) * (D_ * 2), 0);
            } else if (act) {
#pragma unroll
                for (int j = 0; j < 3; ++j) {
                    const v2u xw = {pack2(v[2 * j]), pack2(v[2 * j + 1])};
                    {
                        *(v2u*)(sm + (j * 24 + (lc >> 1)) * 64 + wave * 16 + (lc & 1) * 8) = y[j];
                        *(v2u*)(sm + 4608 + (j * 24 + (lc >> 1)) * 64 + wave * 16 + (lc & 1) * 8) = xw;
                    }
                }
            }
        }
        SCHED();
        PIN(fn);
        if (!wave) *(u32*)(smem + 32768 + (s & 1) * 4) = reached<24>(fn, s + 2);
        LBAR();
        owe = told(smem + 32768 + (s & 1) * 4);
        if (!FIN) {
#pragma unroll
            for (int i = 0; i < 3; ++i) {
                const int c = tid + 256 * i, c2 = c < 288 ? c : c - 288;
                if (c < 576) st16(rs, (c < 288 ? OFF_XNA : OFF_X) + XB + (s * 2 + (idx >> 2)) * TILE + (idx & 3) * 4608 + c2 * 16, *(const v4u*)(sm + c * 16));
            }
        }
        CBAR();
        if (!owe) {
            if (!FIN) meet<24>(ws, rs, !wave, fo, s + 1, fp, min(s + 2, NSEG));
            else if (s + 1 < S1) { if (!wave) watch<24, false, BS>(ws, rs, 0, 0, fp, s + 2); asm volatile("s_barrier" ::: "memory"); }
        }
    }
}

__device__ __forceinline__ void role_a1(int layer, int hp, const u16* wqkv, char* ws, rsrc_t rs, char* smem) {
    const int tid = threadIdx.x, lane = tid & 63, l15 = lane & 15, g = lane >> 4;
    const int wave = __builtin_amdgcn_readfirstlane(tid >> 6), hd = wave >> 1, m = wave & 1;
    bf16x8 w[18][4];
    v4u x[18];
    u32 a0;
    const u32 fx = wave < 3 ? OOB : words<32>(layer ? prog(FR + hp % CR * 2 + (g & 1)) - (g & 1) * 64 : OFF_CXNA), fs = layer ? 0 : 128, xb = OFF_XNA + layer * XB + m * TILE;
    const u32 fo = lane < CA ? prog(FOA + lane * 8 + (layer * 2 + m) * 2) + hp * 4 : OOB, ql = quad(lane);
    {
        const rsrc_t wr = __builtin_amdgcn_make_buffer_rsrc((void*)wqkv, 0, D_ * NQA * 2, 0x00020000);
        const int c = l15 ^ (2 * (g | hd << 2));
        const u32 vo = c < 15 ? (4 * wave + g) * (NQA * 2) + (c / 5) * (D_ * 2) + ((hp * 72) & ~15) + (c % 5) * 16 : OOB;
#pragma unroll
        for (int i = 0; i < 36; ++i) __builtin_amdgcn_raw_ptr_buffer_load_lds(wr, (__attribute__((address_space(3))) void*)(smem + (4 * i + wave) * 1024), 16, vo, i * 16 * NQA * 2, 0, 0);
        const char* rd[4];
#pragma unroll
        for (int t = 0; t < 4; ++t) {
            const int by = (hp & 1) * 8 + (t == 2 ? 32 + 80 * min(l15 & 3, 2) : hd * 40 + (l15 & 3) * 8 + 80 * min(t, 2));
            rd[t] = smem + (8 * g + (l15 >> 2)) * 256 + (by ^ (32 * ((l15 >> 2) | (g & 1) << 2)));
        }
        a0 = ask(rs);
        LBAR();
#pragma unroll
        for (int ks = 0; ks < 18; ++ks)
#pragma unroll
            for (int t = 0; t < 4; ++t) {
                const v2u a = tr_read(rd[t] + ks * 8192), b = tr_read(rd[t] + ks * 8192 + 1024);
                w[ks][t] = asf(v4u{a[0], a[1], b[0], b[1]});
            }
#pragma unroll
        for (int i = 0; i < 72; ++i) if (i < 60) PINA(w[i >> 2][i & 3]); else PIN(w[i >> 2][i & 3]);
        LBAR();
    }
    char* sK = smem + hd * 8192;
    char* sV = smem + 16384 + hd * 8192;
#pragma unroll
    for (int i = 0; i < 8; ++i) *(v4u*)(smem + (i * 256 + tid) * 16) = v4u{0u, 0u, 0u, 0u};
    LBAR();
    const bool odd = l15 & 1;
    const u32 pm = odd ? 0xffff0000u : 0xffffu;
    bool ok[4];
#pragma unroll
    for (int i = 0; i < 4; ++i) { const int d = 16 + l15 - (16 * (i >> 1) + 4 * g + 2 * (i & 1) + (l15 & 1)); ok[i] = d >= 0 && d <= 14; }
    const u32 vb = (4 * g + (l15 >> 2)) * 48 + (l15 & 3) * 8, vb2 = (4 * g + (l15 >> 2)) * 48 + ((l15 & 3) == 0 ? 32 : 40);
    char* sO = smem + 32768 + m * 1024;
    bool owe = false;
    auto body = [&](int s, auto own) {
        auto ldx = [&](int ks) { if constexpr (!own) x[ks] = ldt(rs, ql + (ks & 15) * 256, xb + s * 2 * TILE + (ks >> 4) * 4096); SCHED(); };
#pragma unroll
        for (int ks = 0; ks < 12; ++ks) ldx(ks);
        if (owe) { VMW(12); tell(rs, fo + (~s & 1) * 4096, s); }
        f32x4 acc[4];
#pragma unroll
        for (int ks = 0; ks < 18; ++ks) {
#pragma unroll
            for (int t = 0; t < 4; ++t) {
                if (ks == 0) mmaw<true, false, true>(acc[t], w[ks][t], asf(x[ks]));
                else if (ks < 15) mmaw<true, false, false>(acc[t], w[ks][t], asf(x[ks]));
                else mmaw<false, false, false>(acc[t], w[ks][t], asf(x[ks]));
            }
            SCHED();
            if (ks < 6) ldx(ks + 12);
        }
        CBAR();
        NOP8();
#pragma unroll
        for (int t = 0; t < 4; ++t) PIN(acc[t]);
        const int T = (2 * s + m) & 7, Tp = (T + 7) & 7;
        const u32 e01 = pack2(hd ? acc[2][2] : acc[2][0], hd ? acc[2][3] : acc[2][1]);
        const v4u qf = {pack2(acc[0][0], acc[0][1]), pack2(acc[0][2], acc[0][3]), g ? 0u : e01, 0u};
        const v4u kf = {pack2(acc[1][0], acc[1][1]), pack2(acc[1][2], acc[1][3]), __builtin_amdgcn_permlane16_swap(e01, e01, false, false)[1], 0u};
        *(v4u*)(sK + T * 1024 + lane * 16) = kf;
        *(v2u*)(sV + T * 1024 + l15 * 48 + g * 8) = v2u{pack2(acc[3][0], acc[3][1]), pack2(acc[3][2], acc[3][3])};
        if (g == 2) *(u32*)(sV + T * 1024 + l15 * 48 + 32) = e01;
        const f32x4 s1 = mfma32(asf(kf), asf(qf), f32x4{0.f, 0.f, 0.f, 0.f});
        LBAR();
        const u32 fn = rdw(rs, fx + (s + 1 < NSEG ? s + 1 : s) * fs);
        CBAR();
        const v4u kp = *(const v4u*)(sK + Tp * 1024 + lane * 16);
        const v2u va0 = tr_read(sV + Tp * 1024 + vb), va1 = tr_read(sV + T * 1024 + vb), vc0 = tr_read(sV + Tp * 1024 + vb2), vc1 = tr_read(sV + T * 1024 + vb2);
        const f32x4 s0 = mfma32(asf(kp), asf(qf), f32x4{0.f, 0.f, 0.f, 0.f});
        float l[4] = {odd ? s0[1] : s0[0], odd ? s0[3] : s0[2], odd ? s1[1] : s1[0], odd ? s1[3] : s1[2]};
        constexpr float SC = 0.23570226039551584f * 1.4426950408889634f;
#pragma unroll
        for (int i = 0; i < 4; ++i) l[i] = ok[i] && (i > 1 || s + m) ? l[i] : -3.0e38f;
        const float mx = -SC * xmax(fmaxf(fmaxf(l[0], l[1]), fmaxf(l[2], l[3])));
        v4u p;
#pragma unroll
        for (int i = 0; i < 4; ++i) { l[i] = __builtin_amdgcn_exp2f(l[i] * SC + mx); p[i] = pack2(l[i], l[i]) & pm; }
        const f32x4 oa = mfma32(asf(v4u{va0[0], va0[1], va1[0], va1[1]}), asf(p), f32x4{0.f, 0.f, 0.f, 0.f});
        const f32x4 ob = mfma32(asf(v4u{vc0[0], vc0[1], vc1[0], vc1[1]}), asf(p), f32x4{0.f, 0.f, 0.f, 0.f});
        const float inv = __builtin_amdgcn_rcpf(xsum((l[0] + l[1]) + (l[2] + l[3])));
        const v2u o = {pack2(oa[0] * inv, oa[1] * inv), pack2(oa[2] * inv, oa[3] * inv)};
        const u32 oe = pack2(ob[0] * inv, ob[1] * inv);
        if (hd) { *(v2u*)(sO + lane * 8) = o; if (g == 0) *(u32*)(sO + 512 + lane * 4) = oe; }
        LBAR();
        if (!hd) {
            const v2u o2 = *(const v2u*)(sO + lane * 8);
            const u32 oe2 = *(const u32*)(sO + 512 + l15 * 4);
            const u32 dst = OFF_OA + layer * XB + (s * 2 + m) * TILE;
            stt(rs, lane * 16, dst + hp * 1024, v4u{o[0], o[1], o2[0], o2[1]});
            if (g == 0) st8(rs, dst + 16384 + hp * 128 + lane * 8, v2u{oe, oe2});
        }
        CBAR();
        if (wave == 3) *(u32*)(smem + 36864) = reached<32>(fn, s + 2);
        LBAR();
        const bool have = told(smem + 36864);
        owe = have && !hd;
        if (!have) meet<32>(ws, rs, wave == 3, hd ? OOB : fo + (s & 1) * 4096, s + 1, fx + (s + 1 < NSEG ? s + 1 : s) * fs, min(s + 2, NSEG));
    };
    go(rs, a0);
    meet<32>(ws, rs, wave == 3, OOB, 0, fx, 1);
    for (int s = 0; s < NSEG; ++s) body(s, std::false_type());
}

__device__ __forceinline__ void role_a2(int layer, int nb, const u16* x0, const u16* woA, char* ws, rsrc_t rs, char* smem) {
    const int tid = threadIdx.x, lane = tid & 63, l15 = lane & 15, g = lane >> 4;
    const int wave = __builtin_amdgcn_readfirstlane(tid >> 6), m = wave & 1;
    bf16x8 w[18][4];
    {
        v4u t[18];
#pragma unroll
        for (int i = 0; i < 18; ++i) t[i] = *(const v4u*)(woA + (size_t)(i * 32 + (tid >> 3)) * D_ + nb * 64 + (tid & 7) * 8);
#pragma unroll
        for (int i = 0; i < 18; ++i) *(v4u*)(smem + (i * 32 + (tid >> 3)) * 144 + (tid & 7) * 16) = t[i];
        __syncthreads();
        const int q = l15 >> 2;
        const char* ra = smem + (4 * g + q) * 144 + (l15 & 3) * 16;
        const char* rb = smem + (36 * g + 16 + q) * 144 + (l15 & 3) * 16;
#pragma unroll
        for (int ks = 0; ks < 18; ++ks)
#pragma unroll
            for (int T = 0; T < 4; ++T) {
                const int co = (T >> 1) * 64 + (T & 1) * 8;
                v2u a, b;
                if (ks < 16) { a = tr_read(ra + ks * 36 * 144 + co); b = tr_read(ra + (ks * 36 + 20) * 144 + co); }
                else { a = tr_read(rb + (ks - 16) * 288 * 144 + co); b = tr_read(rb + ((ks - 16) * 288 + 144) * 144 + co); }
                w[ks][T] = asf(v4u{a[0], a[1], b[0], b[1]});
            }
#pragma unroll
        for (int i = 0; i < 72; ++i) if (i < 60) PINA(w[i >> 2][i & 3]); else PIN(w[i >> 2][i & 3]);
    }
    go(rs, ask(rs));
    wait_prog<2>(ws, rs, words<2>(OFF_Z), 1);
    const u32 fi = words<16>(prog(FOA + nb % CA * 8 + (layer * 2 + m) * 2 + (wave >> 1))), fo = lane < CX ? FX_W + prog(lane * 8 + (layer * 2 + m) * 2 + (wave >> 1)) + nb * 4 : OOB;
    v4u xn[2];
    auto ldr = [&](int s) {
#pragma unroll
        for (int P = 0; P < 2; ++P) xn[P] = *(const v4u*)(x0 + (size_t)(s * SEG + m * 16 + l15) * D_ + nb * 64 + P * 32 + g * 8);
    };
    if (layer == 0) ldr(wave >> 1);
    watch<16, false, PS>(ws, rs, 0, 0, fi, (wave >> 1) + 1);
    for (int s = wave >> 1; s < NSEG; s += 2) {
        const u32 task = (layer * 64 + s) * 2 + m;
        const u32 ro = (s * 2 + m) * TILE + nb * 2048, rq = (s * 2 + m) * TILE + nb * 512;
        v4u x[18], xr[2];
#pragma unroll
        for (int ks = 0; ks < 16; ++ks) x[ks] = ldt(rs, lane * 16 + (ks & 3) * 1024, OFF_OA + layer * XB + (s * 2 + m) * TILE + (ks >> 2) * 4096);
#pragma unroll
        for (int e = 0; e < 2; ++e) {
            const v2u a = __builtin_amdgcn_raw_buffer_load_b64(rs, lane * 8 + e * 1024, OFF_OA + layer * XB + (s * 2 + m) * TILE + 16384, 16);
            const v2u b = __builtin_amdgcn_raw_buffer_load_b64(rs, lane * 8 + e * 1024 + 512, OFF_OA + layer * XB + (s * 2 + m) * TILE + 16384, 16);
            x[16 + e] = v4u{a[0], a[1], b[0], b[1]};
        }
        if (layer != 0) {
#pragma unroll
            for (int P = 0; P < 2; ++P) xr[P] = ldt(rs, quad(lane) + P * 256, OFF_X + XB + rq);
        } else {
            xr[0] = xn[0]; xr[1] = xn[1];
            ldr(min(s + 2, NSEG - 1));
        }
        f32x4 acc[4];
        f32x2 xf[8];
#pragma unroll
        for (int ks = 0; ks < 18; ++ks)
#pragma unroll
            for (int t = 0; t < 4; ++t) {
                if (ks == 0) mmaw<true, false, true>(acc[t], w[ks][t], asf(x[ks]));
                else if (ks < 15) mmaw<true, false, false>(acc[t], w[ks][t], asf(x[ks]));
                else mmaw<false, false, false>(acc[t], w[ks][t], asf(x[ks]));
                if (ks >= 16) { u32 xw = xr[ks & 1][t]; PIN(xw); xf[(ks & 1) * 4 + t] = un2(xw); PIN(xf[(ks & 1) * 4 + t]); }
            }
        NOP8();
        f32x2 v[8];
#pragma unroll
        for (int i = 0; i < 8; ++i) { PIN(acc[i >> 1]); v[i] = f32x2{acc[i >> 1][2 * (i & 1)], acc[i >> 1][2 * (i & 1) + 1]} + xf[i]; }
        f32x2 su = v[0], sq = v[0] * v[0], a, b;
#pragma unroll
        for (int i = 1; i < 8; ++i) { su += v[i]; sq += v[i] * v[i]; }
        const u32 so = OFF_STAT + task * 2304 + l15 * 8;
        asm volatile("buffer_atomic_or_x2 %0, %1, %2, 0 offen" :: "v"(v2u{__float_as_uint(xsum(su[0] + su[1])), __float_as_uint(xsum(sq[0] + sq[1])) | 1u}),
                     "v"(g ? OOB : so + nb * 256), "s"(rs) : "memory");
        u64 st[3] = {0, 0, g ? 1ull << 32 : 0};
        u32 spins = 0;
        __builtin_amdgcn_s_sleep(SLP_S);
        for (;;) {
            CBAR();
            u64 z = 0;
            PIN(z);
#pragma unroll
            for (int i = 0; i < 3; ++i) if ((st[i] >> 32) == 0) st[i] = __hip_atomic_fetch_add((GAS u64*)(ws + (so + (g + 4 * i) * 256)), z, RLX_AGENT);
            if (__all((st[0] >> 32) != 0 && (st[1] >> 32) != 0 && (st[2] >> 32) != 0)) break;
            if (spin_fail(ws, spins)) break;
        }
        ln_par(xsum(__uint_as_float((u32)st[0]) + __uint_as_float((u32)st[1]) + __uint_as_float((u32)st[2])),
               xsum(__uint_as_float(st[0] >> 32) + __uint_as_float(st[1] >> 32) + __uint_as_float(st[2] >> 32)), a, b);
#pragma unroll
        for (int P = 0; P < 2; ++P) stt(rs, lane * 16 + P * 1024, XNB_W + layer * XB + ro, v4u{pack2(v[4 * P] * a + b), pack2(v[4 * P + 1] * a + b), pack2(v[4 * P + 2] * a + b), pack2(v[4 * P + 3] * a + b)});
#pragma unroll
        for (int P = 0; P < 2; ++P) stt(rs, quad(lane) + P * 256, OFF_X + 2 * layer * XB + rq, v4u{pack2(v[4 * P]), pack2(v[4 * P + 1]), pack2(v[4 * P + 2]), pack2(v[4 * P + 3])});
        if ((s & ~1) == 8) wait_prog<1>(ws, rs, words<1>(OFF_Z2), 112);
        watch<16, true, PS>(ws, rs, fo, s + 1, fi, s + 2 < NSEG ? s + 3 : s + 1);
    }
}

__device__ __forceinline__ void role_w(int layer, int kb, int nb, const u16* Wo, char* ws, rsrc_t rs, char* smem) {
    const int tid = threadIdx.x, lane = tid & 63, l15 = lane & 15, g = lane >> 4;
    const int wave = __builtin_amdgcn_readfirstlane(tid >> 6);
    float* sR = (float*)smem;
    bf16x8 w[3][8][2];
#pragma unroll
    for (int i = 0; i < 3; ++i)
#pragma unroll
        for (int ks = 0; ks < 8; ++ks) load_pair<true>(Wo, D_, kb * 1024 + wave * 256 + ks * 32 + 4 * g, nb * 96 + i * 32 + 2 * l15, w[i][ks][0], w[i][ks][1]);
#pragma unroll
    for (int i = 0; i < 48; ++i) PINA(w[i >> 4][(i >> 1) & 7][i & 1]);
    const u32 fo = wave ? OOB : words<8>(prog(FOB + nb % CB * 8 + layer * 4 + kb)), ob = OFF_OB + (layer * 2048 + kb * 8 + wave * 2) * 8192;
    const u32 fp = lane < CP ? prog(FPB + lane * 16 + layer * 4 + wave + (kb >> 1) * 8) + 16 + (kb * 6 + nb) * 4 : OOB;
    bool owe = false;
    go(rs, ask(rs));
    meet<8>(ws, rs, !wave, OOB, 0, fo, 1);
    for (int s = 0; s < NSEG; ++s) {
        v4u tb[16];
        auto ldb = [&](int i) { tb[i] = ldt(rs, lane * 16 + (i & 3) * 1024, ob + s * 262144 + (i >> 2) * 4096); SCHED(); };
#pragma unroll
        for (int i = 0; i < 10; ++i) ldb(i);
        if (owe) { VMW(10); tell(rs, fp, s); }
        f32x4 acc[3][2][2];
        u32 fn;
#pragma unroll
        for (int ks = 0; ks < 8; ++ks) {
#pragma unroll
            for (int m = 0; m < 2; ++m) {
#pragma unroll
                for (int i = 0; i < 3; ++i)
#pragma unroll
                    for (int t = 0; t < 2; ++t) {
                        if (ks == 0) mmaw<true, false, true>(acc[i][t][m], w[i][ks][t], asf(tb[2 * ks + m]));
                        else mmaw<true, false, false>(acc[i][t][m], w[i][ks][t], asf(tb[2 * ks + m]));
                    }
                SCHED();
                if (ks < 3) ldb(2 * ks + m + 10);
            }
        }
        NOP8();
        float* sB = sR + (s & 1) * 12800;
#pragma unroll
        for (int m = 0; m < 2; ++m)
#pragma unroll
            for (int i = 0; i < 3; ++i) {
                if (m && i == 0) { CBAR(); fn = rdw(rs, fo); CBAR(); }
                PIN(acc[i][0][m]); PIN(acc[i][1][m]);
                const f32x4 a0 = acc[i][0][m], a1 = acc[i][1][m];
                float* dst = sB + (wave * 32 + m * 16 + l15) * 100 + i * 32 + 8 * g;
                *(f32x4*)dst = f32x4{a0[0], a1[0], a0[1], a1[1]};
                *(f32x4*)(dst + 4) = f32x4{a0[2], a1[2], a0[3], a1[3]};
            }
        LBAR();
#pragma unroll
        for (int i = 0; i < 3; ++i) {
            const int c = wave * 192 + lane + 64 * i, tok = c / 24, c4 = (c % 24) * 4;
            f32x4 a = *(const f32x4*)(sB + tok * 100 + c4);
#pragma unroll
            for (int wv = 1; wv < 4; ++wv) a += *(const f32x4*)(sB + (wv * 32 + tok) * 100 + c4);
            st16(rs, OFF_PB + ((((layer * 64 + s) * 4 + kb) * SEG + tok) * D_ + nb * 96 + c4) * 4, __builtin_bit_cast(v4u, a));
        }
        CBAR();
        if (!wave) *(u32*)(smem + 102400) = reached<8>(fn, s + 2);
        LBAR();
        owe = told(smem + 102400);
        if (!owe) meet<8>(ws, rs, !wave, fp, s + 1, fo, min(s + 2, NSEG));
    }
}

__device__ __forceinline__ int exp_above(float x) { return (int)((__float_as_uint(x) >> 23) & 255u) - 126; }
__device__ __forceinline__ float sq2(u32 w, float c) { return __builtin_amdgcn_fdot2_f32_bf16(__builtin_bit_cast(bfv2, w), __builtin_bit_cast(bfv2, w), c, false); }
__device__ __forceinline__ float wave_max(float v) {
    v = vmax(v, dpp<0xB1>(v)); v = vmax(v, dpp<0x4E>(v)); v = vmax(v, dpp<0x141>(v)); v = vmax(v, dpp<0x140>(v));
    return xmax(v);
}
__device__ __forceinline__ void par_done(char* ws, rsrc_t rs) {
    const int tid = threadIdx.x;
    DRAIN();
    __syncthreads();
    if (tid < 64) {
        u32 n = 0;
        if (tid == 0) n = __hip_atomic_fetch_add((gu32*)(ws + OFF_CC), 1u, RLX_AGENT);
        if (__builtin_amdgcn_readfirstlane(n) == 63) {
            const v2u fm = ld8(rs, OFF_CH + (tid & 31) * 8);
            const u32 b0 = ldw(ws, OFF_CH + 256);
            u32 ph, spins = 0;
            while ((ph = ldw(ws, OFF_CH + 384)) == 0u && !spin_fail(ws, spins)) __builtin_amdgcn_s_sleep(2);
            const float c = sqrtf(__uint_as_float(fm[0])) * sqrtf(__uint_as_float(fm[1]));
            const float cs = wave_sum(c) * 0.5f;
            const int e35 = min(max(exp_above(43.75f * cs), -100), 100), ek = min(max(exp_above(30.f * cs), -100), 100);
            if (tid == 0) st4(rs, OFF_PAR, 0x80000000u | (ph & 1u) << 24 | b0 << 16 | (u32)(e35 + 128) << 8 | (u32)(ek + 128));
        }
    }
}
__device__ __forceinline__ void touch(int j, int xc, const u16* wqkv, const u16* Wq, const u16* Wk, const u16* Wv, const u16* Wo, rsrc_t rs) {
    const u32 L = j * 256 + threadIdx.x, o = (L >> 3) * NB + xc * 512 + (L & 7) * 64;
    auto get = [](const u16* p) { asm volatile("" :: "v"(*(const u32*)p) : "memory"); };
    get(wqkv + min(L >> 1, 1727u) * D_ + xc * 72 + (L & 1) * 70);
    get(Wv + o);
    if ((j & ~3) == 8) go(rs, ask(rs));
    get(Wo + (xc * 4608 + L) * 64);
    get(Wq + o);
    get(Wk + o);
}
__device__ __forceinline__ void role_par(int h, int p, const u16* mem0, const u16* Wv, const u16* Wo, char* ws, rsrc_t rs, char* smem) {
    const int tid = threadIdx.x;
    float* sR = (float*)smem;
    float* sW = sR + 1024;
    const u32 a0 = ask(rs);
    if (p == 0) {
        v4u w[36];
#pragma unroll
        for (int i = 0; i < 36; ++i) w[i] = *(const v4u*)(Wv + (size_t)(i * 16 + (tid >> 4)) * NB + h * 128 + (tid & 15) * 8);
        float f[4] = {0.f, 0.f, 0.f, 0.f};
#pragma unroll
        for (int i = 0; i < 144; ++i) f[i & 3] = sq2(w[i >> 2][i & 3], f[i & 3]);
        const float t = wave_sum((f[0] + f[1]) + (f[2] + f[3]));
        if ((tid & 63) == 0) sW[tid >> 6] = t;
        __syncthreads();
        go(rs, a0);
        if (tid == 0) st4(rs, OFF_CH + h * 8, __float_as_uint((sW[0] + sW[1]) + (sW[2] + sW[3])));
    } else {
        u32 me[9];
        v4u w[43];
        const int q = h * 72 + min(tid, 71);
        int i0, i1;
        if (q < 2048) { i0 = ((q >> 3) & 7) * D_ + (q >> 8) * 64 + ((q >> 6) & 3) * 16 + (q & 3) * 4 + ((q >> 2) & 1) * 2; i1 = i0 + 1; }
        else { i0 = ((q >> 4) & 3) * 2 * D_ + 512 + ((q >> 6) & 3) * 16 + (q & 15); i1 = i0 + D_; }
        const u16 m0 = mem0[i0], m1 = mem0[i1];
#pragma unroll
        for (int i = 0; i < 9; ++i) me[i] = ((const u32*)mem0)[i * 256 + tid];
#pragma unroll
        for (int i = 0; i < 43; ++i) w[i] = *(const v4u*)(Wo + (size_t)(h * 128 + min(3 * i + tid / 72, 127)) * D_ + (tid % 72) * 8);
        float mm = 0.f;
#pragma unroll
        for (int i = 0; i < 9; ++i) mm = fmaxf(mm, fmaxf(fabsf(bflo(me[i])), fabsf(bfhi(me[i]))));
        mm = wave_max(mm);
        if ((tid & 63) == 0) sW[tid >> 6] = mm;
        LBAR();
        const int b0 = 28 - min(max(exp_above(fmaxf(fmaxf(sW[0], sW[1]), fmaxf(sW[2], sW[3]))), -30), 90);
        f32x4 c = {0.f, 0.f, 0.f, 0.f};
#pragma unroll
        for (int i = 0; i < 172; ++i) c[i & 3] = sq2(w[i >> 2][i & 3], c[i & 3]);
        *(f32x4*)(sR + tid * 4) = c;
        if (tid < 72) {
            const v2u z = {(u32)(int)rintf(ldexpf(bf2f(m0), b0 - 3)) << 6 | 32u, (u32)(int)rintf(ldexpf(bf2f(m1), b0 + 3))};
            const u32 a = (q < 2048 ? (q >> 9) * 16384 + ((q >> 8) & 1) * 8192 + ((q >> 3) & 7) * 1024 + ((q >> 6) & 1) * 512 + ((q >> 7) & 1) * 256
                                    : 65536 + ((q >> 3) & 1) * 512 + ((q >> 4) & 3) * 2048 + ((q >> 6) & 1) * 256 + ((q >> 7) & 1) * 8192) + (q & 7) * 8;
            for (int i = 0; i < 4; ++i) {
                st8(rs, OFF_ACC + (2 + (i >> 1)) * (ACC_SLOT / 2) + (i & 1) * 64 + a, z);
                st8(rs, q < 2048 ? OOB : OFF_ACC + (2 + (i >> 1)) * (ACC_SLOT / 2) + 1024 + (i & 1) * 64 + a, v2u{32u, 0u});
            }
        }
        LBAR();
        go(rs, a0);
        if (tid < 64) {
            float m = 0.f;
#pragma unroll
            for (int i = 0; i < 5; ++i) { const int d = min(tid + 64 * i, 287); m = fmaxf(m, sR[d] + sR[288 + d] + sR[576 + d]); }
            m = wave_max(m);
            if (tid < 2) st4(rs, tid ? OFF_CH + 256 : OFF_CH + h * 8 + 4, tid ? (u32)(b0 + 3 + 128) : __float_as_uint(m));
        }
    }
    par_done(ws, rs);
}
__device__ __forceinline__ void role_side(char* ws, rsrc_t rs, char* smem) {
    const int lane = threadIdx.x & 63, wave = threadIdx.x >> 6;
    go(rs, ask(rs));
    const u32 base = ((OFF_JUNK + 16383u) & ~16383u) + wave * 32768;
    st16(rs, lane < 32 ? base + (lane >> 3) * 8192 + (lane & 7) * 16 : OOB, v4u{0u, 0u, 0u, 0u});
    int vote = 0;
    DRAIN();
#pragma unroll 1
    for (int r = 0; r < 2; ++r) {
        u32 t[2];
        __builtin_amdgcn_raw_ptr_buffer_atomic_add_i32(1, rs, lane < 2 ? base + r * 16384 + lane * 8192 : OOB, 0, 0);
        DRAIN();
#pragma unroll
        for (int e = 0; e < 2; ++e) {
            const u32 a = base + r * 16384 + e * 8192;
            v4u q;
            u64 t0, t1;
            asm volatile("s_memtime %0\n\ts_waitcnt lgkmcnt(0)\n\tbuffer_load_dwordx4 %2, %3, %4, 0 offen sc1\n\ts_waitcnt vmcnt(0)\n\ts_memtime %1\n\ts_waitcnt lgkmcnt(0)"
                         : "=&s"(t0), "=&s"(t1), "=&v"(q) : "v"(lane < 4 ? a + lane * 16 : OOB), "s"(rs) : "memory");
            t[e] = (u32)(t1 - t0);
        }
        const u32 near = t[1] < t[0], b21 = ((u32)(size_t)ws + base + r * 16384) >> 21 & 1;
        vote += (near ^ (BID >> 2 & 1) ^ b21) ? 1 : -1;
    }
    if (lane == 0) *(int*)(smem + wave * 4) = vote;
    __syncthreads();
    if (threadIdx.x == 0) { const int* v = (const int*)smem; st4(rs, OFF_CH + 384, v[0] + v[1] + v[2] + v[3] > 0 ? 3u : 2u); }
}
__device__ __forceinline__ void clear_late(int i, rsrc_t rs) {
    nap(NAP_A);
    clear<2>(rs, OFF_ACC + (10 + 2 * i) * ACC_SLOT, 40);
    go(rs, ask(rs));
    __builtin_amdgcn_raw_ptr_buffer_atomic_add_i32(1, rs, words<1>(OFF_Z2), 0, 0);
}

using v8u = __attribute__((ext_vector_type(8))) unsigned;
template <bool FIRST> __device__ __forceinline__ void mm16(f32x4& acc, v2u a, v2u b) {
    if constexpr (FIRST) asm volatile("v_mfma_f32_16x16x16_bf16 %0, %1, %2, 0" : "=&v"(acc) : "v"(a), "v"(b));
    else asm volatile("v_mfma_f32_16x16x16_bf16 %0, %1, %2, %0" : "+v"(acc) : "v"(a), "v"(b));
}
__device__ __forceinline__ v4u half(const v8u& v, int i) { return i ? v4u{v[4], v[5], v[6], v[7]} : v4u{v[0], v[1], v[2], v[3]}; }
__device__ __forceinline__ void role_att(int layer, int h, char* ws, rsrc_t rs) {
    const int lane = threadIdx.x & 63, l15 = lane & 15, g = lane >> 4;
    const u32 fl = words<6>(OFF_FKVM + ((layer * 32 + h) * 68 + 2) * 32);
    const u32 kvo = ((g >> 1) * 16 + ((l15 >> 3) | ((l15 >> 1) & 2)) * 4 + (l15 & 1) + (g & 1) * 2) * 16, vmo = (lane & 57) * 8 + (lane & 2) * 16 + (lane & 4) * 4;
    constexpr float SC = 0.08838834764831845f * 1.4426950408889634f;
    const u32 fo = lane < CB ? prog(FOB + lane * 8 + layer * 4 + (h >> 3)) + (h & 7) * 4 : OOB;
    v4u k[2][4], v[2][4];
    auto issue = [&](int s, v8u (&q)[2][2]) {
        const u32 blk = OFF_QKV + ((layer * 66 + s) * 32 + h) * 24576;
        v4u t[2][4];
#pragma unroll
        for (int w = 0; w < 4; ++w)
#pragma unroll
            for (int p = 0; p < 2; ++p) { t[p][w] = ldt(rs, lane * 16, blk + w * 6144 + p * 3072); k[p][w] = ldt(rs, lane * 16 + 1024, blk + w * 6144 + p * 3072); }
#pragma unroll
        for (int p = 0; p < 2; ++p)
#pragma unroll
            for (int i = 0; i < 2; ++i) q[p][i] = __builtin_shufflevector(t[p][2 * i], t[p][2 * i + 1], 0, 1, 2, 3, 4, 5, 6, 7);
    };
    auto issuev = [&](int s) {
        const u32 blk = OFF_QKV + ((layer * 66 + s) * 32 + h) * 24576;
#pragma unroll
        for (int p = 0; p < 2; ++p)
#pragma unroll
            for (int w = 0; w < 4; ++w) v[p][w] = ldt(rs, lane * 16 + 2048, blk + w * 6144 + p * 3072);
    };
    auto body = [&](int s, v8u (&q)[2][2], v8u (&qn)[2][2]) {
        const u32 mb = OFF_KVM + ((layer * 64 + s) * 32 + h) * 4096;
        f32x4 sc[3];
#pragma unroll
        for (int w = 0; w < 4; ++w)
#pragma unroll
            for (int u = 0; u < 3; ++u) {
                if (w == 0) mmaw<false, false, true>(sc[u], asf(k[u >> 1][w]), asf(half(q[u > 0][w >> 1], w & 1)));
                else mmaw<false, false, false>(sc[u], asf(k[u >> 1][w]), asf(half(q[u > 0][w >> 1], w & 1)));
            }
        NOP8();
        float mx[2] = {-3.0e38f, -3.0e38f}, sum[2] = {0.f, 0.f};
#pragma unroll
        for (int u = 0; u < 3; ++u) {
            PIN(sc[u]);
#pragma unroll
            for (int r = 0; r < 4; ++r) {
                if (u != 1) sc[u][r] = 4 * g + r <= l15 ? sc[u][r] : -3.0e38f;
                mx[u > 0] = fmaxf(mx[u > 0], sc[u][r]);
            }
        }
        SCHED();
        u32 f = rdw(rs, fl + s * 32);
        SCHED();
        v2u pt[3];
#pragma unroll
        for (int m = 0; m < 2; ++m) mx[m] = xmax(mx[m]) * SC;
#pragma unroll
        for (int u = 0; u < 3; ++u) {
#pragma unroll
            for (int r = 0; r < 4; ++r) { sc[u][r] = __builtin_amdgcn_exp2f(sc[u][r] * SC - mx[u > 0]); sum[u > 0] += sc[u][r]; }
            pt[u] = v2u{pack2(sc[u][0], sc[u][1]), pack2(sc[u][2], sc[u][3])};
            PIN(pt[u]);
        }
        VMW(9);
        tell(rs, fo, s);
        NOP2();
        f32x4 o[2][4][2], sm[2];
#pragma unroll
        for (int u = 0; u < 3; ++u)
#pragma unroll
            for (int w = 0; w < 4; ++w)
#pragma unroll
                for (int t = 0; t < 2; ++t) {
                    const v2u a = {v[u >> 1][w][2 * t], v[u >> 1][w][2 * t + 1]};
                    if (u < 2) mm16<true>(o[u][w][t], a, pt[u]); else mm16<false>(o[1][w][t], a, pt[u]);
                }
#pragma unroll
        for (int m = 0; m < 2; ++m) { sum[m] = xsum(sum[m]); PIN(sum[m]); PIN(mx[m]); }
        NOP8();
        SCHED();
        PIN(f);
        f = reached<6>(f, 1u) ? __builtin_amdgcn_readfirstlane(f) : watch<6, false, AS>(ws, rs, 0, 0, fl + s * 32, 1u);
        CBAR();
        v4u km[4];
        v2u vm[4];
#pragma unroll
        for (int w = 0; w < 4; ++w) km[w] = ldt(rs, kvo + w * 512, mb);
#pragma unroll
        for (int w = 0; w < 4; ++w) vm[w] = __builtin_amdgcn_raw_buffer_load_b64(rs, vmo, mb + 2048 + w * 512, 16);
        SCHED();
        issue(s + 1 < NSEG ? s + 1 : s, qn);
        SCHED();
        const float si = __uint_as_float((255u - ((f >> 8) & 255u)) << 23);
#pragma unroll
        for (int w = 0; w < 4; ++w)
#pragma unroll
            for (int m = 0; m < 2; ++m) {
                if (w == 0) mmaw<false, false, true>(sm[m], asf(km[w]), asf(half(q[m][w >> 1], w & 1)));
                else mmaw<false, false, false>(sm[m], asf(km[w]), asf(half(q[m][w >> 1], w & 1)));
            }
        NOP8();
        v2u pm[2][2];
        float a[2];
#pragma unroll
        for (int m = 0; m < 2; ++m) {
            PIN(sm[m]);
            float m0 = sm[m][0] * (si * SC), m1 = sm[m][1] * (si * SC);
            const float mm = fmaxf(xmax(fmaxf(m0, m1)), mx[m]);
            m0 = __builtin_amdgcn_exp2f(m0 - mm); m1 = __builtin_amdgcn_exp2f(m1 - mm);
            a[m] = __builtin_amdgcn_exp2f(mx[m] - mm);
            const float inv = __builtin_amdgcn_rcpf(xsum(m0 + m1) + a[m] * sum[m]);
            a[m] *= inv;
            const u32 pp = pack2(m0 * inv * si, m1 * inv * si);
            pm[m][0] = v2u{pp, 0u}; pm[m][1] = v2u{0u, pp};
            PIN(pm[m][0]); PIN(pm[m][1]);
        }
        auto scale = [&](int w) {
#pragma unroll
            for (int m = 0; m < 2; ++m)
#pragma unroll
                for (int t = 0; t < 2; ++t) { o[m][w][t] *= a[m]; PIN(o[m][w][t]); }
        };
        auto put = [&](int w) {
#pragma unroll
            for (int m = 0; m < 2; ++m) { PIN(o[m][w][0]); PIN(o[m][w][1]); stt(rs, lane * 16 + m * 1024, OFF_OB + (((layer * 64 + s) * 32 + h) * 4 + w) * 2048, pack_pair(o[m][w][0], o[m][w][1])); }
        };
        scale(0);
#pragma unroll
        for (int w = 0; w < 4; ++w) {
            if (w < 3) scale(w + 1); else NOP2();
#pragma unroll
            for (int m = 0; m < 2; ++m)
#pragma unroll
                for (int t = 0; t < 2; ++t) mm16<false>(o[m][w][t], vm[w], pm[m][t]);
            if (w) put(w - 1);
        }
        NOP8();
        put(3);
        issuev(s + 1 < NSEG ? s + 1 : s);
    };
    v8u A[2][2], B[2][2];
    go(rs, ask(rs));
    wait_prog<2>(ws, rs, words<2>(OFF_Z), 1);
    watch<6, false, PS>(ws, rs, 0, 0, fl - 32, 1u);
    issue(0, A);
    issuev(0);
    for (int s = 0; s < NSEG; s += 2) { body(s, A, B); body(s + 1, B, A); }
    VMW(8);
    tell(rs, fo, NSEG);
}

__global__ __launch_bounds__(256, 1) void mega_kernel(const u16* __restrict__ x0, const u16* __restrict__ wqkv, const u16* __restrict__ woA,
                                                     const u16* __restrict__ mem0, const u16* __restrict__ Wq, const u16* __restrict__ Wk,
                                                     const u16* __restrict__ Wv, const u16* __restrict__ Wo, u16* out, char* ws) {
    __shared__ __attribute__((aligned(16))) char smem[163840];
    const rsrc_t rs = __builtin_amdgcn_make_buffer_rsrc(ws, 0, WS_BYTES, 0x00020000);
    const int b = BID;
    const int xc = b & 7, ix = b >> 3;
    const int wave = __builtin_amdgcn_readfirstlane(threadIdx.x >> 6);
    put(rs, my_word(), 0u);
    if ((ix & ~3) == 16) {
#pragma unroll
        for (int i = 0; i < 5; ++i) {
            const int u = (b - 128) * 5 + i;
            if (u < 68) clear(rs, OFF_FKVM + u * 4096, 1); else clear<2>(rs, b < 156 ? OFF_STAT + (u - 68) * 8192 : OOB, 1);
        }
        clear<2>(rs, OFF_ACC + 2 * ACC_SLOT + (b - 128) * 40960, 5);
    }
    const u32 late = 0x4b3fccccu;
    if (late >> ix & 1) touch(__popc(late << (31 - ix)) - 1, xc, wqkv, Wq, Wk, Wv, Wo, rs);
    if (ix == 24 || ix == 25 || ix == 30 || (b >= 250 && b < 254)) clear_late(ix < 26 ? b - 192 : ix == 30 ? b - 224 : b - 226, rs);
#define ROLE(m)
    if (ix < 16) {
        if (ix & 2) { role_par(4 * xc + (ix >> 2), ix & 1, mem0, Wv, Wo, ws, rs, smem); nap(NAP_C); }
        role_chain(ws, Wq, Wk, Wv, Wo, (u32)(((ix >> 1) & 1) << 8 | (4 * xc + (ix >> 2)) << 3 | (ix & 1) << 2 | wave), OFF_ACK + (b * 4 + wave) * 4);
    }
    else if (ix < 22) {
        if (xc & 1) {
            nap(NAP_W);
            ROLE(1) if (ix >= 20) for (int i = 0; i < 2; ++i) role_t0(64 + ((ix - 20) * 8 + xc - i) * 4 + wave, x0, ws, rs);
        }
        ROLE(4) role_w(xc & 1, xc >> 1, ix - 16, Wo, ws, rs, smem);
    }
    else if (ix < 26) { ROLE(ix < 24 ? 1 : 16) role_a1((ix - 22) >> 1, 2 * xc + (ix & 1), wqkv, ws, rs, smem);
        ROLE(8) if (ix == 22) { DRAIN(); role_r<1, true, SL, NSEG>(xc, x0, out, ws, rs, smem); }
    }
    else if (ix < 28) {
        if (ix == 27) nap(NAP_A);
        ROLE(1) if (b != 208) role_t0(((ix - 26) * 8 + xc) * 4 + wave, x0, ws, rs);
        ROLE(2) role_att(ix - 26, 4 * xc + wave, ws, rs);
    }
    else if (ix == 28) { ROLE(8) role_r<1, false>(xc, x0, out, ws, rs, smem); }
    else if (b < 250) { ROLE(b & 8 && b != 249 ? 64 : 32) role_a2(ix < 31 ? ix - 29 : xc, ix < 31 ? xc : 8, x0, woA, ws, rs, smem); }
    else if (b < 254) { ROLE(8) role_r<2, true, 0, SL>(xc - 2, x0, out, ws, rs, smem); }
    else if (b == 254) role_lead(ws, rs);
    else role_side(ws, rs, smem);
}

extern "C" void kernel_launch(void* const* d_in, const int* in_sizes, int n_in, void* d_out, int out_size, void* d_ws,
                              size_t ws_size, hipStream_t stream) {
    mega_kernel<<<256, 256, 0, stream>>>((const u16*)d_in[0], (const u16*)d_in[1], (const u16*)d_in[2], (const u16*)d_in[4],
                                         (const u16*)d_in[5], (const u16*)d_in[6], (const u16*)d_in[7], (const u16*)d_in[8],
                                         (u16*)d_out, (char*)d_ws);
}
```

```cpp
#include <hip/hip_runtime.h>
#include <hip/hip_bf16.h>
#include <stdint.h>
#include <type_traits>

typedef unsigned short u16;
typedef unsigned u32;
typedef unsigned long long u64;
using bf16x8 = __attribute__((ext_vector_type(8))) short;
using bf16x4 = __attribute__((ext_vector_type(4))) short;
using f32x4 = __attribute__((ext_vector_type(4))) float;
using f32x2 = __attribute__((ext_vector_type(2))) float;
using v4u = __attribute__((ext_vector_type(4))) unsigned;
using v2u = __attribute__((ext_vector_type(2))) unsigned;
typedef __bf16 bfv2 __attribute__((ext_vector_type(2)));
#define GAS __attribute__((address_space(1)))
typedef GAS unsigned gu32;
#define RLX_AGENT __ATOMIC_RELAXED, __HIP_MEMORY_SCOPE_AGENT
typedef __amdgpu_buffer_rsrc_t rsrc_t;

constexpr int S_ = 2048, D_ = 576, NSEG = 64, SEG = 32, NB = 4096, NQA = 1728;

constexpr u32 XB = S_ * D_ * 2;
constexpr u32 OFF_ABORT = 0u;
constexpr u32 OFF_PAR = 64u;
constexpr u32 OFF_CC = 128u;
constexpr u32 OFF_Z = 256u;
constexpr u32 OFF_Z2 = 384u;
constexpr u32 OFF_CH = 512u;
constexpr u32 OFF_CXNA = 1024u;
constexpr u32 OFF_FKVM = 12288u;
constexpr u32 OFF_SCT = 151552u;
constexpr u32 OFF_STAT = 290816u;
constexpr u32 SYNC_BYTES = 880640u;
constexpr u32 CX = 4u;
constexpr u32 OFF_PROG = 884736u;
constexpr u32 OFF_ACK = 1015808u;
constexpr u32 OFF_ACC = 1048576u;
constexpr u32 OFF_XNA = 11862016u;
constexpr u32 OFF_XNB = 16580608u;
constexpr u32 OFF_X = 21299200u;
constexpr u32 OFF_OA = 28377088u;
constexpr u32 OFF_PB = 33095680u;
constexpr u32 OFF_OB = 70844416u;
constexpr u32 OFF_QKV = 104398848u;
constexpr u32 OFF_KVM = 208207872u;
constexpr u32 OFF_DBG = 224985088u;
constexpr u32 OFF_JUNK = 225574912u;
constexpr u32 WS_BYTES = 230326272u;
constexpr u32 ACC_SLOT = 163840;
__device__ __forceinline__ void role_chain(char* ws, const u16* Wq, const u16* Wk, const u16* Wv, const u16* Wo, u32 id, u32 ack) {
    asm volatile(
        "s_mov_b64 s[36:37], %0\n"
        "s_mov_b32 s38, 0xdba8000\n"
        "s_mov_b32 s39, 0x00020000\n"
        "s_mov_b32 s42, 0x480000\n"
        "s_mov_b32 s43, 0x00020000\n"
        "s_and_b32 s45, %5, 3\n"
        "s_bfe_u32 s46, %5, 0x10002\n"
        "s_bfe_u32 s47, %5, 0x50003\n"
        "s_bfe_u32 s48, %5, 0x10008\n"
        "s_mov_b32 s63, 0\n"
        "v_mbcnt_lo_u32_b32 v128, -1, 0\n"
        "v_mbcnt_hi_u32_b32 v128, -1, v128\n"
        "v_and_b32 v129, 15, v128\n"
        "v_lshrrev_b32 v130, 4, v128\n"
        "v_lshlrev_b32 v131, 1, v130\n"
        "v_xor_b32 v131, v131, v129\n"
        "v_lshlrev_b32 v131, 4, v131\n"
        "v_lshl_add_u32 v134, v130, 13, v131\n"
        "v_xor_b32 v135, 0x80, v134\n"
        "v_and_b32 v131, 0xc0, v131\n"
        "v_lshl_or_b32 v140, v131, 14, v134\n"
        "v_xor_b32 v141, 0x200080, v140\n"
        "v_lshrrev_b32 v131, 2, v129\n"
        "v_lshl_add_u32 v132, v130, 3, v131\n"
        "v_lshlrev_b32 v136, 8, v132\n"
        "v_and_b32 v132, 1, v130\n"
        "v_lshl_or_b32 v131, v132, 2, v131\n"
        "v_lshlrev_b32 v131, 1, v131\n"
        "v_bfe_u32 v132, v129, 1, 1\n"
        "s_lshl_b32 s80, s45, 2\n"
        "v_or_b32 v132, s80, v132\n"
        "v_xor_b32 v131, v131, v132\n"
        "v_lshl_add_u32 v136, v131, 4, v136\n"
        "v_and_b32 v132, 1, v129\n"
        "v_lshl_add_u32 v136, v132, 3, v136\n"
        "v_xor_b32 v137, 32, v136\n"
        "s_lshl_b32 s49, s45, 17\n"
        "s_lshl_b32 s80, s47, 8\n"
        "s_add_u32 s49, s49, s80\n"
        "s_lshl_b32 s50, s45, 12\n"
        "s_bfe_u32 s80, s47, 0x10004\n"
        "s_xor_b32 s83, s80, 1\n"
        "s_lshl_b32 s67, s83, 19\n"
        "s_lshl_b32 s68, s80, 19\n"
        "s_add_u32 s67, s67, s49\n"
        "s_add_u32 s68, s68, s49\n"
        "s_mov_b64 s[40:41], %3\n"
        "v_xor_b32 v142, 0x0, v140\n"
        "v_xor_b32 v143, 0x0, v141\n"
        "s_add_u32 m0, s50, 0\n"
        "s_add_u32 s80, s67, 0\n"
        "buffer_load_dwordx4 v142, s[40:43], s80 offen lds\n"
        "s_add_u32 m0, s50, 1024\n"
        "s_add_u32 s80, s67, 32768\n"
        "buffer_load_dwordx4 v142, s[40:43], s80 offen lds\n"
        "s_add_u32 m0, s50, 2048\n"
        "s_add_u32 s80, s67, 65536\n"
        "buffer_load_dwordx4 v143, s[40:43], s80 offen lds\n"
        "s_add_u32 m0, s50, 3072\n"
        "s_add_u32 s80, s67, 98304\n"
        "buffer_load_dwordx4 v143, s[40:43], s80 offen lds\n"
        "v_xor_b32 v142, 0x0, v140\n"
        "v_xor_b32 v143, 0x0, v141\n"
        "s_add_u32 m0, s50, 16384\n"
        "s_add_u32 s80, s68, 0\n"
        "buffer_load_dwordx4 v142, s[40:43], s80 offen lds\n"
        "s_add_u32 m0, s50, 17408\n"
        "s_add_u32 s80, s68, 32768\n"
        "buffer_load_dwordx4 v142, s[40:43], s80 offen lds\n"
        "s_add_u32 m0, s50, 18432\n"
        "s_add_u32 s80, s68, 65536\n"
        "buffer_load_dwordx4 v143, s[40:43], s80 offen lds\n"
        "s_add_u32 m0, s50, 19456\n"
        "s_add_u32 s80, s68, 98304\n"
        "buffer_load_dwordx4 v143, s[40:43], s80 offen lds\n"
        "v_xor_b32 v142, 0x100000, v140\n"
        "v_xor_b32 v143, 0x100000, v141\n"
        "s_add_u32 m0, s50, 32768\n"
        "s_add_u32 s80, s67, 0\n"
        "buffer_load_dwordx4 v142, s[40:43], s80 offen lds\n"
        "s_add_u32 m0, s50, 33792\n"
        "s_add_u32 s80, s67, 32768\n"
        "buffer_load_dwordx4 v142, s[40:43], s80 offen lds\n"
        "s_add_u32 m0, s50, 34816\n"
        "s_add_u32 s80, s67, 65536\n"
        "buffer_load_dwordx4 v143, s[40:43], s80 offen lds\n"
        "s_add_u32 m0, s50, 35840\n"
        "s_add_u32 s80, s67, 98304\n"
        "buffer_load_dwordx4 v143, s[40:43], s80 offen lds\n"
        "s_mov_b64 s[40:41], %3\n"
        "v_xor_b32 v142, 0x100000, v140\n"
        "v_xor_b32 v143, 0x100000, v141\n"
        "s_add_u32 m0, s50, 49152\n"
        "s_add_u32 s80, s68, 0\n"
        "buffer_load_dwordx4 v142, s[40:43], s80 offen lds\n"
        "s_add_u32 m0, s50, 50176\n"
        "s_add_u32 s80, s68, 32768\n"
        "buffer_load_dwordx4 v142, s[40:43], s80 offen lds\n"
        "s_add_u32 m0, s50, 51200\n"
        "s_add_u32 s80, s68, 65536\n"
        "buffer_load_dwordx4 v143, s[40:43], s80 offen lds\n"
        "s_add_u32 m0, s50, 52224\n"
        "s_add_u32 s80, s68, 98304\n"
        "buffer_load_dwordx4 v143, s[40:43], s80 offen lds\n"
        "v_xor_b32 v142, 0x200000, v140\n"
        "v_xor_b32 v143, 0x200000, v141\n"
        "s_add_u32 m0, s50, 65536\n"
        "s_add_u32 s80, s67, 0\n"
        "buffer_load_dwordx4 v142, s[40:43], s80 offen lds\n"
        "s_add_u32 m0, s50, 66560\n"
        "s_add_u32 s80, s67, 32768\n"
        "buffer_load_dwordx4 v142, s[40:43], s80 offen lds\n"
        "s_add_u32 m0, s50, 67584\n"
        "s_add_u32 s80, s67, 65536\n"
        "buffer_load_dwordx4 v143, s[40:43], s80 offen lds\n"
        "s_add_u32 m0, s50, 68608\n"
        "s_add_u32 s80, s67, 98304\n"
        "buffer_load_dwordx4 v143, s[40:43], s80 offen lds\n"
        "v_xor_b32 v142, 0x200000, v140\n"
        "v_xor_b32 v143, 0x200000, v141\n"
        "s_add_u32 m0, s50, 81920\n"
        "s_add_u32 s80, s68, 0\n"
        "buffer_load_dwordx4 v142, s[40:43], s80 offen lds\n"
        "s_add_u32 m0, s50, 82944\n"
        "s_add_u32 s80, s68, 32768\n"
        "buffer_load_dwordx4 v142, s[40:43], s80 offen lds\n"
        "s_add_u32 m0, s50, 83968\n"
        "s_add_u32 s80, s68, 65536\n"
        "buffer_load_dwordx4 v143, s[40:43], s80 offen lds\n"
        "s_add_u32 m0, s50, 84992\n"
        "s_add_u32 s80, s68, 98304\n"
        "buffer_load_dwordx4 v143, s[40:43], s80 offen lds\n"
        "s_mov_b64 s[40:41], %3\n"
        "v_xor_b32 v142, 0x300000, v140\n"
        "v_xor_b32 v143, 0x300000, v141\n"
        "s_add_u32 m0, s50, 98304\n"
        "s_add_u32 s80, s67, 0\n"
        "buffer_load_dwordx4 v142, s[40:43], s80 offen lds\n"
        "s_add_u32 m0, s50, 99328\n"
        "s_add_u32 s80, s67, 32768\n"
        "buffer_load_dwordx4 v142, s[40:43], s80 offen lds\n"
        "s_add_u32 m0, s50, 100352\n"
        "s_add_u32 s80, s67, 65536\n"
        "buffer_load_dwordx4 v143, s[40:43], s80 offen lds\n"
        "s_add_u32 m0, s50, 101376\n"
        "s_add_u32 s80, s67, 98304\n"
        "buffer_load_dwordx4 v143, s[40:43], s80 offen lds\n"
        "v_xor_b32 v142, 0x300000, v140\n"
        "v_xor_b32 v143, 0x300000, v141\n"
        "s_add_u32 m0, s50, 114688\n"
        "s_add_u32 s80, s68, 0\n"
        "buffer_load_dwordx4 v142, s[40:43], s80 offen lds\n"
        "s_add_u32 m0, s50, 115712\n"
        "s_add_u32 s80, s68, 32768\n"
        "buffer_load_dwordx4 v142, s[40:43], s80 offen lds\n"
        "s_add_u32 m0, s50, 116736\n"
        "s_add_u32 s80, s68, 65536\n"
        "buffer_load_dwordx4 v143, s[40:43], s80 offen lds\n"
        "s_add_u32 m0, s50, 117760\n"
        "s_add_u32 s80, s68, 98304\n"
        "buffer_load_dwordx4 v143, s[40:43], s80 offen lds\n"
        "s_add_u32 m0, s50, 131072\n"
        "s_add_u32 s80, s49, 4194304\n"
        "buffer_load_dwordx4 v134, s[40:43], s80 offen lds\n"
        "s_add_u32 m0, s50, 132096\n"
        "s_add_u32 s80, s49, 4227072\n"
        "buffer_load_dwordx4 v134, s[40:43], s80 offen lds\n"
        "s_add_u32 m0, s50, 133120\n"
        "s_add_u32 s80, s49, 4259840\n"
        "buffer_load_dwordx4 v135, s[40:43], s80 offen lds\n"
        "s_add_u32 m0, s50, 134144\n"
        "s_add_u32 s80, s49, 4292608\n"
        "buffer_load_dwordx4 v135, s[40:43], s80 offen lds\n"
        "v_lshrrev_b32 v131, 3, v129\n"
        "v_lshlrev_b32 v131, 5, v131\n"
        "v_and_b32 v132, 3, v129\n"
        "v_lshl_add_u32 v131, v132, 3, v131\n"
        "v_and_b32 v132, 4, v129\n"
        "v_add_u32 v131, v131, v132\n"
        "s_movk_i32 s80, 4608\n"
        "v_mad_u32_u24 v138, v130, s80, v131\n"
        "s_and_b32 s81, s47, 1\n"
        "s_mul_i32 s81, s81, 6\n"
        "v_xor_b32 v131, s81, v129\n"
        "v_and_b32 v131, 14, v131\n"
        "v_lshlrev_b32 v131, 1, v131\n"
        "v_mad_u32_u24 v127, v130, s80, v131\n"
        "s_mul_i32 s51, s47, 147456\n"
        "s_lshl2_add_u32 s80, s46, s45\n"
        "s_lshl_b32 s80, s80, 7\n"
        "s_add_u32 s52, s51, s80\n"
        "s_and_b32 s80, s45, 1\n"
        "s_lshl_b32 s81, s80, 6\n"
        "s_sub_u32 s52, s52, s81\n"
        "s_lshl1_add_u32 s80, s46, s80\n"
        "s_lshl_b32 s80, s80, 5\n"
        "s_add_u32 s51, s51, s80\n"
        "s_add_u32 s51, s51, 1024\n"
        "s_and_b32 s80, s47, 2\n"
        "s_lshl_b32 s80, s80, 6\n"
        "s_add_u32 s52, s52, s80\n"
        "s_xor_b32 s66, s52, 128\n"
        "s_mov_b32 s53, 0x05040100\n"
        "s_mov_b32 s54, 0x07060302\n"
        "v_and_b32 v131, 1, v129\n"
        "s_mov_b32 s80, 0x02020202\n"
        "v_mul_lo_u32 v131, v131, s80\n"
        "v_add_u32 v139, s53, v131\n"
        "v_lshlrev_b32 v131, 4, v128\n"
        "s_and_b32 s80, s45, 1\n"
        "s_lshl_b32 s80, s80, 12\n"
        "s_add_u32 s84, s80, 152576\n"
        "s_mov_b64 s[40:41], %4\n"
        "s_add_u32 s80, s51, 0\n"
        "buffer_load_dword v64, v127, s[40:43], s80 offen offset:0\n"
        "buffer_load_dword v66, v127, s[40:43], s80 offen offset:1152\n"
        "buffer_load_dword v68, v127, s[40:43], s80 offen offset:2304\n"
        "buffer_load_dword v70, v127, s[40:43], s80 offen offset:3456\n"
        "s_add_u32 s80, s51, 18432\n"
        "buffer_load_dword v65, v127, s[40:43], s80 offen offset:0\n"
        "buffer_load_dword v67, v127, s[40:43], s80 offen offset:1152\n"
        "buffer_load_dword v69, v127, s[40:43], s80 offen offset:2304\n"
        "buffer_load_dword v71, v127, s[40:43], s80 offen offset:3456\n"
        "s_add_u32 s80, s51, 36864\n"
        "buffer_load_dword v72, v127, s[40:43], s80 offen offset:0\n"
        "buffer_load_dword v74, v127, s[40:43], s80 offen offset:1152\n"
        "buffer_load_dword v76, v127, s[40:43], s80 offen offset:2304\n"
        "buffer_load_dword v78, v127, s[40:43], s80 offen offset:3456\n"
        "s_add_u32 s80, s51, 55296\n"
        "buffer_load_dword v73, v127, s[40:43], s80 offen offset:0\n"
        "buffer_load_dword v75, v127, s[40:43], s80 offen offset:1152\n"
        "buffer_load_dword v77, v127, s[40:43], s80 offen offset:2304\n"
        "buffer_load_dword v79, v127, s[40:43], s80 offen offset:3456\n"
        "s_add_u32 s80, s51, 73728\n"
        "buffer_load_dword v80, v127, s[40:43], s80 offen offset:0\n"
        "buffer_load_dword v82, v127, s[40:43], s80 offen offset:1152\n"
        "buffer_load_dword v84, v127, s[40:43], s80 offen offset:2304\n"
        "buffer_load_dword v86, v127, s[40:43], s80 offen offset:3456\n"
        "s_add_u32 s80, s51, 92160\n"
        "buffer_load_dword v81, v127, s[40:43], s80 offen offset:0\n"
        "buffer_load_dword v83, v127, s[40:43], s80 offen offset:1152\n"
        "buffer_load_dword v85, v127, s[40:43], s80 offen offset:2304\n"
        "buffer_load_dword v87, v127, s[40:43], s80 offen offset:3456\n"
        "s_add_u32 s80, s51, 110592\n"
        "buffer_load_dword v88, v127, s[40:43], s80 offen offset:0\n"
        "buffer_load_dword v90, v127, s[40:43], s80 offen offset:1152\n"
        "buffer_load_dword v92, v127, s[40:43], s80 offen offset:2304\n"
        "buffer_load_dword v94, v127, s[40:43], s80 offen offset:3456\n"
        "s_add_u32 s80, s51, 129024\n"
        "buffer_load_dword v89, v127, s[40:43], s80 offen offset:0\n"
        "buffer_load_dword v91, v127, s[40:43], s80 offen offset:1152\n"
        "buffer_load_dword v93, v127, s[40:43], s80 offen offset:2304\n"
        "buffer_load_dword v95, v127, s[40:43], s80 offen offset:3456\n"
        "v_add_u32 v132, 0, v136\n"
        "v_add_u32 v133, 0, v137\n"
        "s_waitcnt vmcnt(56)\n"
        "s_barrier\n"
        "ds_read_b64_tr_b16 a[0:1], v132 offset:0\n"
        "ds_read_b64_tr_b16 a[2:3], v132 offset:1024\n"
        "ds_read_b64_tr_b16 a[4:5], v132 offset:8192\n"
        "ds_read_b64_tr_b16 a[6:7], v132 offset:9216\n"
        "ds_read_b64_tr_b16 a[8:9], v133 offset:0\n"
        "ds_read_b64_tr_b16 a[10:11], v133 offset:1024\n"
        "ds_read_b64_tr_b16 a[12:13], v133 offset:8192\n"
        "ds_read_b64_tr_b16 a[14:15], v133 offset:9216\n"
        "ds_read_b64_tr_b16 a[16:17], v132 offset:16384\n"
        "ds_read_b64_tr_b16 a[18:19], v132 offset:17408\n"
        "ds_read_b64_tr_b16 a[20:21], v132 offset:24576\n"
        "ds_read_b64_tr_b16 a[22:23], v132 offset:25600\n"
        "ds_read_b64_tr_b16 a[24:25], v133 offset:16384\n"
        "ds_read_b64_tr_b16 a[26:27], v133 offset:17408\n"
        "ds_read_b64_tr_b16 a[28:29], v133 offset:24576\n"
        "ds_read_b64_tr_b16 a[30:31], v133 offset:25600\n"
        "ds_read_b64_tr_b16 a[32:33], v132 offset:32768\n"
        "ds_read_b64_tr_b16 a[34:35], v132 offset:33792\n"
        "ds_read_b64_tr_b16 a[36:37], v132 offset:40960\n"
        "ds_read_b64_tr_b16 a[38:39], v132 offset:41984\n"
        "ds_read_b64_tr_b16 a[40:41], v133 offset:32768\n"
        "ds_read_b64_tr_b16 a[42:43], v133 offset:33792\n"
        "ds_read_b64_tr_b16 a[44:45], v133 offset:40960\n"
        "ds_read_b64_tr_b16 a[46:47], v133 offset:41984\n"
        "s_mov_b64 s[40:41], %4\n"
        "s_add_u32 s80, s52, 0\n"
        "buffer_load_dword v0, v138, s[40:43], s80 offen offset:0\n"
        "buffer_load_dword v2, v138, s[40:43], s80 offen offset:1152\n"
        "buffer_load_dword v4, v138, s[40:43], s80 offen offset:2304\n"
        "buffer_load_dword v6, v138, s[40:43], s80 offen offset:3456\n"
        "s_add_u32 s80, s52, 18432\n"
        "buffer_load_dword v1, v138, s[40:43], s80 offen offset:0\n"
        "buffer_load_dword v3, v138, s[40:43], s80 offen offset:1152\n"
        "buffer_load_dword v5, v138, s[40:43], s80 offen offset:2304\n"
        "buffer_load_dword v7, v138, s[40:43], s80 offen offset:3456\n"
        "s_add_u32 s80, s52, 36864\n"
        "buffer_load_dword v8, v138, s[40:43], s80 offen offset:0\n"
        "buffer_load_dword v10, v138, s[40:43], s80 offen offset:1152\n"
        "buffer_load_dword v12, v138, s[40:43], s80 offen offset:2304\n"
        "buffer_load_dword v14, v138, s[40:43], s80 offen offset:3456\n"
        "s_add_u32 s80, s52, 55296\n"
        "buffer_load_dword v9, v138, s[40:43], s80 offen offset:0\n"
        "buffer_load_dword v11, v138, s[40:43], s80 offen offset:1152\n"
        "buffer_load_dword v13, v138, s[40:43], s80 offen offset:2304\n"
        "buffer_load_dword v15, v138, s[40:43], s80 offen offset:3456\n"
        "s_add_u32 s80, s52, 73728\n"
        "buffer_load_dword v16, v138, s[40:43], s80 offen offset:0\n"
        "buffer_load_dword v18, v138, s[40:43], s80 offen offset:1152\n"
        "buffer_load_dword v20, v138, s[40:43], s80 offen offset:2304\n"
        "buffer_load_dword v22, v138, s[40:43], s80 offen offset:3456\n"
        "s_add_u32 s80, s52, 92160\n"
        "buffer_load_dword v17, v138, s[40:43], s80 offen offset:0\n"
        "buffer_load_dword v19, v138, s[40:43], s80 offen offset:1152\n"
        "buffer_load_dword v21, v138, s[40:43], s80 offen offset:2304\n"
        "buffer_load_dword v23, v138, s[40:43], s80 offen offset:3456\n"
        "s_add_u32 s80, s52, 110592\n"
        "buffer_load_dword v24, v138, s[40:43], s80 offen offset:0\n"
        "buffer_load_dword v26, v138, s[40:43], s80 offen offset:1152\n"
        "buffer_load_dword v28, v138, s[40:43], s80 offen offset:2304\n"
        "buffer_load_dword v30, v138, s[40:43], s80 offen offset:3456\n"
        "s_add_u32 s80, s52, 129024\n"
        "buffer_load_dword v25, v138, s[40:43], s80 offen offset:0\n"
        "buffer_load_dword v27, v138, s[40:43], s80 offen offset:1152\n"
        "buffer_load_dword v29, v138, s[40:43], s80 offen offset:2304\n"
        "buffer_load_dword v31, v138, s[40:43], s80 offen offset:3456\n"
        "v_add_u32 v132, 49152, v136\n"
        "v_add_u32 v133, 49152, v137\n"
        "s_waitcnt vmcnt(60)\n"
        "s_barrier\n"
        "ds_read_b64_tr_b16 a[48:49], v132 offset:0\n"
        "ds_read_b64_tr_b16 a[50:51], v132 offset:1024\n"
        "ds_read_b64_tr_b16 a[52:53], v132 offset:8192\n"
        "ds_read_b64_tr_b16 a[54:55], v132 offset:9216\n"
        "ds_read_b64_tr_b16 a[56:57], v133 offset:0\n"
        "ds_read_b64_tr_b16 a[58:59], v133 offset:1024\n"
        "ds_read_b64_tr_b16 a[60:61], v133 offset:8192\n"
        "ds_read_b64_tr_b16 a[62:63], v133 offset:9216\n"
        "ds_read_b64_tr_b16 a[64:65], v132 offset:16384\n"
        "ds_read_b64_tr_b16 a[66:67], v132 offset:17408\n"
        "ds_read_b64_tr_b16 a[68:69], v132 offset:24576\n"
        "ds_read_b64_tr_b16 a[70:71], v132 offset:25600\n"
        "ds_read_b64_tr_b16 a[72:73], v133 offset:16384\n"
        "ds_read_b64_tr_b16 a[74:75], v133 offset:17408\n"
        "ds_read_b64_tr_b16 a[76:77], v133 offset:24576\n"
        "ds_read_b64_tr_b16 a[78:79], v133 offset:25600\n"
        "ds_read_b64_tr_b16 a[80:81], v132 offset:32768\n"
        "ds_read_b64_tr_b16 a[82:83], v132 offset:33792\n"
        "ds_read_b64_tr_b16 a[84:85], v132 offset:40960\n"
        "ds_read_b64_tr_b16 a[86:87], v132 offset:41984\n"
        "ds_read_b64_tr_b16 a[88:89], v133 offset:32768\n"
        "ds_read_b64_tr_b16 a[90:91], v133 offset:33792\n"
        "ds_read_b64_tr_b16 a[92:93], v133 offset:40960\n"
        "ds_read_b64_tr_b16 a[94:95], v133 offset:41984\n"
        "s_mov_b64 s[40:41], %4\n"
        "s_add_u32 s80, s66, 0\n"
        "buffer_load_dword v32, v138, s[40:43], s80 offen offset:0\n"
        "buffer_load_dword v34, v138, s[40:43], s80 offen offset:1152\n"
        "buffer_load_dword v36, v138, s[40:43], s80 offen offset:2304\n"
        "buffer_load_dword v38, v138, s[40:43], s80 offen offset:3456\n"
        "s_add_u32 s80, s66, 18432\n"
        "buffer_load_dword v33, v138, s[40:43], s80 offen offset:0\n"
        "buffer_load_dword v35, v138, s[40:43], s80 offen offset:1152\n"
        "buffer_load_dword v37, v138, s[40:43], s80 offen offset:2304\n"
        "buffer_load_dword v39, v138, s[40:43], s80 offen offset:3456\n"
        "s_add_u32 s80, s66, 36864\n"
        "buffer_load_dword v40, v138, s[40:43], s80 offen offset:0\n"
        "buffer_load_dword v42, v138, s[40:43], s80 offen offset:1152\n"
        "buffer_load_dword v44, v138, s[40:43], s80 offen offset:2304\n"
        "buffer_load_dword v46, v138, s[40:43], s80 offen offset:3456\n"
        "s_add_u32 s80, s66, 55296\n"
        "buffer_load_dword v41, v138, s[40:43], s80 offen offset:0\n"
        "buffer_load_dword v43, v138, s[40:43], s80 offen offset:1152\n"
        "buffer_load_dword v45, v138, s[40:43], s80 offen offset:2304\n"
        "buffer_load_dword v47, v138, s[40:43], s80 offen offset:3456\n"
        "s_add_u32 s80, s66, 73728\n"
        "buffer_load_dword v48, v138, s[40:43], s80 offen offset:0\n"
        "buffer_load_dword v50, v138, s[40:43], s80 offen offset:1152\n"
        "buffer_load_dword v52, v138, s[40:43], s80 offen offset:2304\n"
        "buffer_load_dword v54, v138, s[40:43], s80 offen offset:3456\n"
        "s_add_u32 s80, s66, 92160\n"
        "buffer_load_dword v49, v138, s[40:43], s80 offen offset:0\n"
        "buffer_load_dword v51, v138, s[40:43], s80 offen offset:1152\n"
        "buffer_load_dword v53, v138, s[40:43], s80 offen offset:2304\n"
        "buffer_load_dword v55, v138, s[40:43], s80 offen offset:3456\n"
        "s_add_u32 s80, s66, 110592\n"
        "buffer_load_dword v56, v138, s[40:43], s80 offen offset:0\n"
        "buffer_load_dword v58, v138, s[40:43], s80 offen offset:1152\n"
        "buffer_load_dword v60, v138, s[40:43], s80 offen offset:2304\n"
        "buffer_load_dword v62, v138, s[40:43], s80 offen offset:3456\n"
        "s_add_u32 s80, s66, 129024\n"
        "buffer_load_dword v57, v138, s[40:43], s80 offen offset:0\n"
        "buffer_load_dword v59, v138, s[40:43], s80 offen offset:1152\n"
        "buffer_load_dword v61, v138, s[40:43], s80 offen offset:2304\n"
        "buffer_load_dword v63, v138, s[40:43], s80 offen offset:3456\n"
        "v_add_u32 v132, 98304, v136\n"
        "v_add_u32 v133, 98304, v137\n"
        "s_waitcnt vmcnt(60)\n"
        "s_barrier\n"
        "ds_read_b64_tr_b16 a[96:97], v132 offset:0\n"
        "ds_read_b64_tr_b16 a[98:99], v132 offset:1024\n"
        "ds_read_b64_tr_b16 a[100:101], v132 offset:8192\n"
        "ds_read_b64_tr_b16 a[102:103], v132 offset:9216\n"
        "ds_read_b64_tr_b16 a[104:105], v133 offset:0\n"
        "ds_read_b64_tr_b16 a[106:107], v133 offset:1024\n"
        "ds_read_b64_tr_b16 a[108:109], v133 offset:8192\n"
        "ds_read_b64_tr_b16 a[110:111], v133 offset:9216\n"
        "ds_read_b64_tr_b16 a[112:113], v132 offset:16384\n"
        "ds_read_b64_tr_b16 a[114:115], v132 offset:17408\n"
        "ds_read_b64_tr_b16 a[116:117], v132 offset:24576\n"
        "ds_read_b64_tr_b16 a[118:119], v132 offset:25600\n"
        "ds_read_b64_tr_b16 a[120:121], v133 offset:16384\n"
        "ds_read_b64_tr_b16 a[122:123], v133 offset:17408\n"
        "ds_read_b64_tr_b16 a[124:125], v133 offset:24576\n"
        "ds_read_b64_tr_b16 a[126:127], v133 offset:25600\n"
        "ds_read_b64_tr_b16 a[128:129], v132 offset:32768\n"
        "ds_read_b64_tr_b16 a[130:131], v132 offset:33792\n"
        "ds_read_b64_tr_b16 a[132:133], v132 offset:40960\n"
        "ds_read_b64_tr_b16 a[134:135], v132 offset:41984\n"
        "ds_read_b64_tr_b16 a[136:137], v133 offset:32768\n"
        "ds_read_b64_tr_b16 a[138:139], v133 offset:33792\n"
        "ds_read_b64_tr_b16 a[140:141], v133 offset:40960\n"
        "ds_read_b64_tr_b16 a[142:143], v133 offset:41984\n"
        "s_waitcnt lgkmcnt(0)\n"
        "s_barrier\n"
        "s_mov_b64 s[40:41], %1\n"
        "v_xor_b32 v142, 0x0, v140\n"
        "v_xor_b32 v143, 0x0, v141\n"
        "s_add_u32 m0, s50, 0\n"
        "s_add_u32 s80, s67, 0\n"
        "buffer_load_dwordx4 v142, s[40:43], s80 offen lds\n"
        "s_add_u32 m0, s50, 1024\n"
        "s_add_u32 s80, s67, 32768\n"
        "buffer_load_dwordx4 v142, s[40:43], s80 offen lds\n"
        "s_add_u32 m0, s50, 2048\n"
        "s_add_u32 s80, s67, 65536\n"
        "buffer_load_dwordx4 v143, s[40:43], s80 offen lds\n"
        "s_add_u32 m0, s50, 3072\n"
        "s_add_u32 s80, s67, 98304\n"
        "buffer_load_dwordx4 v143, s[40:43], s80 offen lds\n"
        "v_xor_b32 v142, 0x0, v140\n"
        "v_xor_b32 v143, 0x0, v141\n"
        "s_add_u32 m0, s50, 16384\n"
        "s_add_u32 s80, s68, 0\n"
        "buffer_load_dwordx4 v142, s[40:43], s80 offen lds\n"
        "s_add_u32 m0, s50, 17408\n"
        "s_add_u32 s80, s68, 32768\n"
        "buffer_load_dwordx4 v142, s[40:43], s80 offen lds\n"
        "s_add_u32 m0, s50, 18432\n"
        "s_add_u32 s80, s68, 65536\n"
        "buffer_load_dwordx4 v143, s[40:43], s80 offen lds\n"
        "s_add_u32 m0, s50, 19456\n"
        "s_add_u32 s80, s68, 98304\n"
        "buffer_load_dwordx4 v143, s[40:43], s80 offen lds\n"
        "v_xor_b32 v142, 0x100000, v140\n"
        "v_xor_b32 v143, 0x100000, v141\n"
        "s_add_u32 m0, s50, 32768\n"
        "s_add_u32 s80, s67, 0\n"
        "buffer_load_dwordx4 v142, s[40:43], s80 offen lds\n"
        "s_add_u32 m0, s50, 33792\n"
        "s_add_u32 s80, s67, 32768\n"
        "buffer_load_dwordx4 v142, s[40:43], s80 offen lds\n"
        "s_add_u32 m0, s50, 34816\n"
        "s_add_u32 s80, s67, 65536\n"
        "buffer_load_dwordx4 v143, s[40:43], s80 offen lds\n"
        "s_add_u32 m0, s50, 35840\n"
        "s_add_u32 s80, s67, 98304\n"
        "buffer_load_dwordx4 v143, s[40:43], s80 offen lds\n"
        "s_mov_b64 s[40:41], %1\n"
        "v_xor_b32 v142, 0x100000, v140\n"
        "v_xor_b32 v143, 0x100000, v141\n"
        "s_add_u32 m0, s50, 49152\n"
        "s_add_u32 s80, s68, 0\n"
        "buffer_load_dwordx4 v142, s[40:43], s80 offen lds\n"
        "s_add_u32 m0, s50, 50176\n"
        "s_add_u32 s80, s68, 32768\n"
        "buffer_load_dwordx4 v142, s[40:43], s80 offen lds\n"
        "s_add_u32 m0, s50, 51200\n"
        "s_add_u32 s80, s68, 65536\n"
        "buffer_load_dwordx4 v143, s[40:43], s80 offen lds\n"
        "s_add_u32 m0, s50, 52224\n"
        "s_add_u32 s80, s68, 98304\n"
        "buffer_load_dwordx4 v143, s[40:43], s80 offen lds\n"
        "v_xor_b32 v142, 0x200000, v140\n"
        "v_xor_b32 v143, 0x200000, v141\n"
        "s_add_u32 m0, s50, 65536\n"
        "s_add_u32 s80, s67, 0\n"
        "buffer_load_dwordx4 v142, s[40:43], s80 offen lds\n"
        "s_add_u32 m0, s50, 66560\n"
        "s_add_u32 s80, s67, 32768\n"
        "buffer_load_dwordx4 v142, s[40:43], s80 offen lds\n"
        "s_add_u32 m0, s50, 67584\n"
        "s_add_u32 s80, s67, 65536\n"
        "buffer_load_dwordx4 v143, s[40:43], s80 offen lds\n"
        "s_add_u32 m0, s50, 68608\n"
        "s_add_u32 s80, s67, 98304\n"
        "buffer_load_dwordx4 v143, s[40:43], s80 offen lds\n"
        "v_xor_b32 v142, 0x200000, v140\n"
        "v_xor_b32 v143, 0x200000, v141\n"
        "s_add_u32 m0, s50, 81920\n"
        "s_add_u32 s80, s68, 0\n"
        "buffer_load_dwordx4 v142, s[40:43], s80 offen lds\n"
        "s_add_u32 m0, s50, 82944\n"
        "s_add_u32 s80, s68, 32768\n"
        "buffer_load_dwordx4 v142, s[40:43], s80 offen lds\n"
        "s_add_u32 m0, s50, 83968\n"
        "s_add_u32 s80, s68, 65536\n"
        "buffer_load_dwordx4 v143, s[40:43], s80 offen lds\n"
        "s_add_u32 m0, s50, 84992\n"
        "s_add_u32 s80, s68, 98304\n"
        "buffer_load_dwordx4 v143, s[40:43], s80 offen lds\n"
        "s_mov_b64 s[40:41], %2\n"
        "v_xor_b32 v142, 0x0, v140\n"
        "v_xor_b32 v143, 0x0, v141\n"
        "s_add_u32 m0, s50, 98304\n"
        "s_add_u32 s80, s67, 0\n"
        "buffer_load_dwordx4 v142, s[40:43], s80 offen lds\n"
        "s_add_u32 m0, s50, 99328\n"
        "s_add_u32 s80, s67, 32768\n"
        "buffer_load_dwordx4 v142, s[40:43], s80 offen lds\n"
        "s_add_u32 m0, s50, 100352\n"
        "s_add_u32 s80, s67, 65536\n"
        "buffer_load_dwordx4 v143, s[40:43], s80 offen lds\n"
        "s_add_u32 m0, s50, 101376\n"
        "s_add_u32 s80, s67, 98304\n"
        "buffer_load_dwordx4 v143, s[40:43], s80 offen lds\n"
        "v_xor_b32 v142, 0x0, v140\n"
        "v_xor_b32 v143, 0x0, v141\n"
        "s_add_u32 m0, s50, 114688\n"
        "s_add_u32 s80, s68, 0\n"
        "buffer_load_dwordx4 v142, s[40:43], s80 offen lds\n"
        "s_add_u32 m0, s50, 115712\n"
        "s_add_u32 s80, s68, 32768\n"
        "buffer_load_dwordx4 v142, s[40:43], s80 offen lds\n"
        "s_add_u32 m0, s50, 116736\n"
        "s_add_u32 s80, s68, 65536\n"
        "buffer_load_dwordx4 v143, s[40:43], s80 offen lds\n"
        "s_add_u32 m0, s50, 117760\n"
        "s_add_u32 s80, s68, 98304\n"
        "buffer_load_dwordx4 v143, s[40:43], s80 offen lds\n"
        "v_xor_b32 v142, 0x200000, v140\n"
        "v_xor_b32 v143, 0x200000, v141\n"
        "s_add_u32 m0, s50, 131072\n"
        "s_add_u32 s80, s68, 0\n"
        "buffer_load_dwordx4 v142, s[40:43], s80 offen lds\n"
        "s_add_u32 m0, s50, 132096\n"
        "s_add_u32 s80, s68, 32768\n"
        "buffer_load_dwordx4 v142, s[40:43], s80 offen lds\n"
        "s_add_u32 m0, s50, 133120\n"
        "s_add_u32 s80, s68, 65536\n"
        "buffer_load_dwordx4 v143, s[40:43], s80 offen lds\n"
        "s_add_u32 m0, s50, 134144\n"
        "s_add_u32 s80, s68, 98304\n"
        "buffer_load_dwordx4 v143, s[40:43], s80 offen lds\n"
        "s_waitcnt vmcnt(63)\n"
        "v_perm_b32 v96, v65, v64, v139\n"
        "s_waitcnt vmcnt(63)\n"
        "v_perm_b32 v97, v67, v66, v139\n"
        "s_waitcnt vmcnt(63)\n"
        "v_perm_b32 v98, v69, v68, v139\n"
        "s_waitcnt vmcnt(63)\n"
        "v_perm_b32 v99, v71, v70, v139\n"
        "s_waitcnt vmcnt(63)\n"
        "v_perm_b32 v100, v73, v72, v139\n"
        "s_waitcnt vmcnt(63)\n"
        "v_perm_b32 v101, v75, v74, v139\n"
        "s_waitcnt vmcnt(63)\n"
        "v_perm_b32 v102, v77, v76, v139\n"
        "s_waitcnt vmcnt(63)\n"
        "v_perm_b32 v103, v79, v78, v139\n"
        "s_waitcnt vmcnt(63)\n"
        "v_perm_b32 v104, v81, v80, v139\n"
        "s_waitcnt vmcnt(63)\n"
        "v_perm_b32 v105, v83, v82, v139\n"
        "s_waitcnt vmcnt(63)\n"
        "v_perm_b32 v106, v85, v84, v139\n"
        "s_waitcnt vmcnt(63)\n"
        "v_perm_b32 v107, v87, v86, v139\n"
        "s_waitcnt vmcnt(63)\n"
        "v_perm_b32 v108, v89, v88, v139\n"
        "s_waitcnt vmcnt(63)\n"
        "v_perm_b32 v109, v91, v90, v139\n"
        "s_waitcnt vmcnt(63)\n"
        "v_perm_b32 v110, v93, v92, v139\n"
        "s_waitcnt vmcnt(63)\n"
        "v_perm_b32 v111, v95, v94, v139\n"
        "v_add_u32 v127, s84, v131\n"
        "ds_write_b128 v127, v[96:99] offset:0\n"
        "ds_write_b128 v127, v[100:103] offset:1024\n"
        "ds_write_b128 v127, v[104:107] offset:2048\n"
        "ds_write_b128 v127, v[108:111] offset:3072\n"
        "s_waitcnt vmcnt(63)\n"
        "v_perm_b32 v144, v1, v0, s53\n"
        "v_perm_b32 v152, v1, v0, s54\n"
        "s_waitcnt vmcnt(63)\n"
        "v_perm_b32 v145, v3, v2, s53\n"
        "v_perm_b32 v153, v3, v2, s54\n"
        "s_waitcnt vmcnt(63)\n"
        "v_perm_b32 v146, v5, v4, s53\n"
        "v_perm_b32 v154, v5, v4, s54\n"
        "s_waitcnt vmcnt(63)\n"
        "v_perm_b32 v147, v7, v6, s53\n"
        "v_perm_b32 v155, v7, v6, s54\n"
        "s_waitcnt vmcnt(63)\n"
        "v_perm_b32 v148, v9, v8, s53\n"
        "v_perm_b32 v156, v9, v8, s54\n"
        "s_waitcnt vmcnt(63)\n"
        "v_perm_b32 v149, v11, v10, s53\n"
        "v_perm_b32 v157, v11, v10, s54\n"
        "s_waitcnt vmcnt(63)\n"
        "v_perm_b32 v150, v13, v12, s53\n"
        "v_perm_b32 v158, v13, v12, s54\n"
        "s_waitcnt vmcnt(63)\n"
        "v_perm_b32 v151, v15, v14, s53\n"
        "v_perm_b32 v159, v15, v14, s54\n"
        "s_waitcnt vmcnt(63)\n"
        "v_perm_b32 v160, v17, v16, s53\n"
        "v_perm_b32 v168, v17, v16, s54\n"
        "s_waitcnt vmcnt(63)\n"
        "v_perm_b32 v161, v19, v18, s53\n"
        "v_perm_b32 v169, v19, v18, s54\n"
        "s_waitcnt vmcnt(63)\n"
        "v_perm_b32 v162, v21, v20, s53\n"
        "v_perm_b32 v170, v21, v20, s54\n"
        "s_waitcnt vmcnt(63)\n"
        "v_perm_b32 v163, v23, v22, s53\n"
        "v_perm_b32 v171, v23, v22, s54\n"
        "s_waitcnt vmcnt(63)\n"
        "v_perm_b32 v164, v25, v24, s53\n"
        "v_perm_b32 v172, v25, v24, s54\n"
        "s_waitcnt vmcnt(63)\n"
        "v_perm_b32 v165, v27, v26, s53\n"
        "v_perm_b32 v173, v27, v26, s54\n"
        "s_waitcnt vmcnt(63)\n"
        "v_perm_b32 v166, v29, v28, s53\n"
        "v_perm_b32 v174, v29, v28, s54\n"
        "s_waitcnt vmcnt(63)\n"
        "v_perm_b32 v167, v31, v30, s53\n"
        "v_perm_b32 v175, v31, v30, s54\n"
        "s_waitcnt lgkmcnt(0)\n"
        "s_waitcnt vmcnt(63)\n"
        "v_perm_b32 v96, v33, v32, s53\n"
        "v_perm_b32 v104, v33, v32, s54\n"
        "s_waitcnt vmcnt(62)\n"
        "v_perm_b32 v97, v35, v34, s53\n"
        "v_perm_b32 v105, v35, v34, s54\n"
        "s_waitcnt vmcnt(61)\n"
        "v_perm_b32 v98, v37, v36, s53\n"
        "v_perm_b32 v106, v37, v36, s54\n"
        "s_waitcnt vmcnt(60)\n"
        "v_perm_b32 v99, v39, v38, s53\n"
        "v_perm_b32 v107, v39, v38, s54\n"
        "s_waitcnt vmcnt(55)\n"
        "v_perm_b32 v100, v41, v40, s53\n"
        "v_perm_b32 v108, v41, v40, s54\n"
        "s_waitcnt vmcnt(54)\n"
        "v_perm_b32 v101, v43, v42, s53\n"
        "v_perm_b32 v109, v43, v42, s54\n"
        "s_waitcnt vmcnt(53)\n"
        "v_perm_b32 v102, v45, v44, s53\n"
        "v_perm_b32 v110, v45, v44, s54\n"
        "s_waitcnt vmcnt(52)\n"
        "v_perm_b32 v103, v47, v46, s53\n"
        "v_perm_b32 v111, v47, v46, s54\n"
        "s_waitcnt vmcnt(47)\n"
        "v_perm_b32 v112, v49, v48, s53\n"
        "v_perm_b32 v120, v49, v48, s54\n"
        "s_waitcnt vmcnt(46)\n"
        "v_perm_b32 v113, v51, v50, s53\n"
        "v_perm_b32 v121, v51, v50, s54\n"
        "s_waitcnt vmcnt(45)\n"
        "v_perm_b32 v114, v53, v52, s53\n"
        "v_perm_b32 v122, v53, v52, s54\n"
        "s_waitcnt vmcnt(44)\n"
        "v_perm_b32 v115, v55, v54, s53\n"
        "v_perm_b32 v123, v55, v54, s54\n"
        "s_waitcnt vmcnt(39)\n"
        "v_perm_b32 v116, v57, v56, s53\n"
        "v_perm_b32 v124, v57, v56, s54\n"
        "s_waitcnt vmcnt(38)\n"
        "v_perm_b32 v117, v59, v58, s53\n"
        "v_perm_b32 v125, v59, v58, s54\n"
        "s_waitcnt vmcnt(37)\n"
        "v_perm_b32 v118, v61, v60, s53\n"
        "v_perm_b32 v126, v61, v60, s54\n"
        "s_waitcnt vmcnt(36)\n"
        "v_perm_b32 v119, v63, v62, s53\n"
        "v_perm_b32 v127, v63, v62, s54\n"
        "v_add_u32 v132, 0, v136\n"
        "v_add_u32 v133, 0, v137\n"
        "s_waitcnt vmcnt(24)\n"
        "s_barrier\n"
        "ds_read_b64_tr_b16 a[144:145], v132 offset:0\n"
        "ds_read_b64_tr_b16 a[146:147], v132 offset:1024\n"
        "ds_read_b64_tr_b16 a[148:149], v132 offset:8192\n"
        "ds_read_b64_tr_b16 a[150:151], v132 offset:9216\n"
        "ds_read_b64_tr_b16 a[152:153], v133 offset:0\n"
        "ds_read_b64_tr_b16 a[154:155], v133 offset:1024\n"
        "ds_read_b64_tr_b16 a[156:157], v133 offset:8192\n"
        "ds_read_b64_tr_b16 a[158:159], v133 offset:9216\n"
        "ds_read_b64_tr_b16 a[160:161], v132 offset:16384\n"
        "ds_read_b64_tr_b16 a[162:163], v132 offset:17408\n"
        "ds_read_b64_tr_b16 a[164:165], v132 offset:24576\n"
        "ds_read_b64_tr_b16 a[166:167], v132 offset:25600\n"
        "ds_read_b64_tr_b16 a[168:169], v133 offset:16384\n"
        "ds_read_b64_tr_b16 a[170:171], v133 offset:17408\n"
        "ds_read_b64_tr_b16 a[172:173], v133 offset:24576\n"
        "ds_read_b64_tr_b16 a[174:175], v133 offset:25600\n"
        "ds_read_b64_tr_b16 a[176:177], v132 offset:32768\n"
        "ds_read_b64_tr_b16 a[178:179], v132 offset:33792\n"
        "ds_read_b64_tr_b16 a[180:181], v132 offset:40960\n"
        "ds_read_b64_tr_b16 a[182:183], v132 offset:41984\n"
        "ds_read_b64_tr_b16 a[184:185], v133 offset:32768\n"
        "ds_read_b64_tr_b16 a[186:187], v133 offset:33792\n"
        "ds_read_b64_tr_b16 a[188:189], v133 offset:40960\n"
        "ds_read_b64_tr_b16 a[190:191], v133 offset:41984\n"
        "s_waitcnt lgkmcnt(0)\n"
        "s_barrier\n"
        "s_mov_b64 s[40:41], %2\n"
        "v_xor_b32 v142, 0x300000, v140\n"
        "v_xor_b32 v143, 0x300000, v141\n"
        "s_add_u32 m0, s50, 0\n"
        "s_add_u32 s80, s67, 0\n"
        "buffer_load_dwordx4 v142, s[40:43], s80 offen lds\n"
        "s_add_u32 m0, s50, 1024\n"
        "s_add_u32 s80, s67, 32768\n"
        "buffer_load_dwordx4 v142, s[40:43], s80 offen lds\n"
        "s_add_u32 m0, s50, 2048\n"
        "s_add_u32 s80, s67, 65536\n"
        "buffer_load_dwordx4 v143, s[40:43], s80 offen lds\n"
        "s_add_u32 m0, s50, 3072\n"
        "s_add_u32 s80, s67, 98304\n"
        "buffer_load_dwordx4 v143, s[40:43], s80 offen lds\n"
        "v_xor_b32 v142, 0x300000, v140\n"
        "v_xor_b32 v143, 0x300000, v141\n"
        "s_add_u32 m0, s50, 16384\n"
        "s_add_u32 s80, s68, 0\n"
        "buffer_load_dwordx4 v142, s[40:43], s80 offen lds\n"
        "s_add_u32 m0, s50, 17408\n"
        "s_add_u32 s80, s68, 32768\n"
        "buffer_load_dwordx4 v142, s[40:43], s80 offen lds\n"
        "s_add_u32 m0, s50, 18432\n"
        "s_add_u32 s80, s68, 65536\n"
        "buffer_load_dwordx4 v143, s[40:43], s80 offen lds\n"
        "s_add_u32 m0, s50, 19456\n"
        "s_add_u32 s80, s68, 98304\n"
        "buffer_load_dwordx4 v143, s[40:43], s80 offen lds\n"
        "s_add_u32 m0, s50, 32768\n"
        "s_add_u32 s80, s49, 4194304\n"
        "buffer_load_dwordx4 v134, s[40:43], s80 offen lds\n"
        "s_add_u32 m0, s50, 33792\n"
        "s_add_u32 s80, s49, 4227072\n"
        "buffer_load_dwordx4 v134, s[40:43], s80 offen lds\n"
        "s_add_u32 m0, s50, 34816\n"
        "s_add_u32 s80, s49, 4259840\n"
        "buffer_load_dwordx4 v135, s[40:43], s80 offen lds\n"
        "s_add_u32 m0, s50, 35840\n"
        "s_add_u32 s80, s49, 4292608\n"
        "buffer_load_dwordx4 v135, s[40:43], s80 offen lds\n"
        "v_add_u32 v132, 49152, v136\n"
        "v_add_u32 v133, 49152, v137\n"
        "s_waitcnt vmcnt(24)\n"
        "s_barrier\n"
        "ds_read_b64_tr_b16 a[192:193], v132 offset:0\n"
        "ds_read_b64_tr_b16 a[194:195], v132 offset:1024\n"
        "ds_read_b64_tr_b16 a[196:197], v132 offset:8192\n"
        "ds_read_b64_tr_b16 a[198:199], v132 offset:9216\n"
        "ds_read_b64_tr_b16 a[200:201], v133 offset:0\n"
        "ds_read_b64_tr_b16 a[202:203], v133 offset:1024\n"
        "ds_read_b64_tr_b16 a[204:205], v133 offset:8192\n"
        "ds_read_b64_tr_b16 a[206:207], v133 offset:9216\n"
        "ds_read_b64_tr_b16 a[208:209], v132 offset:16384\n"
        "ds_read_b64_tr_b16 a[210:211], v132 offset:17408\n"
        "ds_read_b64_tr_b16 a[212:213], v132 offset:24576\n"
        "ds_read_b64_tr_b16 a[214:215], v132 offset:25600\n"
        "ds_read_b64_tr_b16 a[216:217], v133 offset:16384\n"
        "ds_read_b64_tr_b16 a[218:219], v133 offset:17408\n"
        "ds_read_b64_tr_b16 a[220:221], v133 offset:24576\n"
        "ds_read_b64_tr_b16 a[222:223], v133 offset:25600\n"
        "ds_read_b64_tr_b16 a[224:225], v132 offset:32768\n"
        "ds_read_b64_tr_b16 a[226:227], v132 offset:33792\n"
        "ds_read_b64_tr_b16 a[228:229], v132 offset:40960\n"
        "ds_read_b64_tr_b16 a[230:231], v132 offset:41984\n"
        "ds_read_b64_tr_b16 a[232:233], v133 offset:32768\n"
        "ds_read_b64_tr_b16 a[234:235], v133 offset:33792\n"
        "ds_read_b64_tr_b16 a[236:237], v133 offset:40960\n"
        "ds_read_b64_tr_b16 a[238:239], v133 offset:41984\n"
        "s_waitcnt lgkmcnt(0)\n"
        "s_barrier\n"
        "s_mov_b64 s[40:41], %1\n"
        "v_xor_b32 v142, 0x300000, v140\n"
        "v_xor_b32 v143, 0x300000, v141\n"
        "s_add_u32 m0, s50, 49152\n"
        "s_add_u32 s80, s67, 0\n"
        "buffer_load_dwordx4 v142, s[40:43], s80 offen lds\n"
        "s_add_u32 m0, s50, 50176\n"
        "s_add_u32 s80, s67, 32768\n"
        "buffer_load_dwordx4 v142, s[40:43], s80 offen lds\n"
        "s_add_u32 m0, s50, 51200\n"
        "s_add_u32 s80, s67, 65536\n"
        "buffer_load_dwordx4 v143, s[40:43], s80 offen lds\n"
        "s_add_u32 m0, s50, 52224\n"
        "s_add_u32 s80, s67, 98304\n"
        "buffer_load_dwordx4 v143, s[40:43], s80 offen lds\n"
        "v_xor_b32 v142, 0x300000, v140\n"
        "v_xor_b32 v143, 0x300000, v141\n"
        "s_add_u32 m0, s50, 65536\n"
        "s_add_u32 s80, s68, 0\n"
        "buffer_load_dwordx4 v142, s[40:43], s80 offen lds\n"
        "s_add_u32 m0, s50, 66560\n"
        "s_add_u32 s80, s68, 32768\n"
        "buffer_load_dwordx4 v142, s[40:43], s80 offen lds\n"
        "s_add_u32 m0, s50, 67584\n"
        "s_add_u32 s80, s68, 65536\n"
        "buffer_load_dwordx4 v143, s[40:43], s80 offen lds\n"
        "s_add_u32 m0, s50, 68608\n"
        "s_add_u32 s80, s68, 98304\n"
        "buffer_load_dwordx4 v143, s[40:43], s80 offen lds\n"
        "s_add_u32 m0, s50, 81920\n"
        "s_add_u32 s80, s49, 4194304\n"
        "buffer_load_dwordx4 v134, s[40:43], s80 offen lds\n"
        "s_add_u32 m0, s50, 82944\n"
        "s_add_u32 s80, s49, 4227072\n"
        "buffer_load_dwordx4 v134, s[40:43], s80 offen lds\n"
        "s_add_u32 m0, s50, 83968\n"
        "s_add_u32 s80, s49, 4259840\n"
        "buffer_load_dwordx4 v135, s[40:43], s80 offen lds\n"
        "s_add_u32 m0, s50, 84992\n"
        "s_add_u32 s80, s49, 4292608\n"
        "buffer_load_dwordx4 v135, s[40:43], s80 offen lds\n"
        "v_add_u32 v132, 98304, v136\n"
        "v_add_u32 v133, 98304, v137\n"
        "s_waitcnt vmcnt(24)\n"
        "s_barrier\n"
        "ds_read_b64_tr_b16 a[240:241], v132 offset:0\n"
        "ds_read_b64_tr_b16 a[242:243], v132 offset:1024\n"
        "ds_read_b64_tr_b16 a[244:245], v132 offset:8192\n"
        "ds_read_b64_tr_b16 a[246:247], v132 offset:9216\n"
        "ds_read_b64_tr_b16 a[248:249], v133 offset:0\n"
        "ds_read_b64_tr_b16 a[250:251], v133 offset:1024\n"
        "ds_read_b64_tr_b16 a[252:253], v133 offset:8192\n"
        "ds_read_b64_tr_b16 a[254:255], v133 offset:9216\n"
        "ds_read_b64_tr_b16 v[176:177], v132 offset:16384\n"
        "ds_read_b64_tr_b16 v[178:179], v132 offset:17408\n"
        "ds_read_b64_tr_b16 v[180:181], v132 offset:24576\n"
        "ds_read_b64_tr_b16 v[182:183], v132 offset:25600\n"
        "ds_read_b64_tr_b16 v[184:185], v133 offset:16384\n"
        "ds_read_b64_tr_b16 v[186:187], v133 offset:17408\n"
        "ds_read_b64_tr_b16 v[188:189], v133 offset:24576\n"
        "ds_read_b64_tr_b16 v[190:191], v133 offset:25600\n"
        "ds_read_b64_tr_b16 v[192:193], v132 offset:32768\n"
        "ds_read_b64_tr_b16 v[194:195], v132 offset:33792\n"
        "ds_read_b64_tr_b16 v[196:197], v132 offset:40960\n"
        "ds_read_b64_tr_b16 v[198:199], v132 offset:41984\n"
        "ds_read_b64_tr_b16 v[200:201], v133 offset:32768\n"
        "ds_read_b64_tr_b16 v[202:203], v133 offset:33792\n"
        "ds_read_b64_tr_b16 v[204:205], v133 offset:40960\n"
        "ds_read_b64_tr_b16 v[206:207], v133 offset:41984\n"
        "s_waitcnt lgkmcnt(0)\n"
        "s_barrier\n"
        "s_mov_b64 s[40:41], %2\n"
        "v_xor_b32 v142, 0x100000, v140\n"
        "v_xor_b32 v143, 0x100000, v141\n"
        "s_add_u32 m0, s50, 98304\n"
        "s_add_u32 s80, s67, 0\n"
        "buffer_load_dwordx4 v142, s[40:43], s80 offen lds\n"
        "s_add_u32 m0, s50, 99328\n"
        "s_add_u32 s80, s67, 32768\n"
        "buffer_load_dwordx4 v142, s[40:43], s80 offen lds\n"
        "s_add_u32 m0, s50, 100352\n"
        "s_add_u32 s80, s67, 65536\n"
        "buffer_load_dwordx4 v143, s[40:43], s80 offen lds\n"
        "s_add_u32 m0, s50, 101376\n"
        "s_add_u32 s80, s67, 98304\n"
        "buffer_load_dwordx4 v143, s[40:43], s80 offen lds\n"
        "v_xor_b32 v142, 0x100000, v140\n"
        "v_xor_b32 v143, 0x100000, v141\n"
        "s_add_u32 m0, s50, 114688\n"
        "s_add_u32 s80, s68, 0\n"
        "buffer_load_dwordx4 v142, s[40:43], s80 offen lds\n"
        "s_add_u32 m0, s50, 115712\n"
        "s_add_u32 s80, s68, 32768\n"
        "buffer_load_dwordx4 v142, s[40:43], s80 offen lds\n"
        "s_add_u32 m0, s50, 116736\n"
        "s_add_u32 s80, s68, 65536\n"
        "buffer_load_dwordx4 v143, s[40:43], s80 offen lds\n"
        "s_add_u32 m0, s50, 117760\n"
        "s_add_u32 s80, s68, 98304\n"
        "buffer_load_dwordx4 v143, s[40:43], s80 offen lds\n"
        "v_xor_b32 v142, 0x200000, v140\n"
        "v_xor_b32 v143, 0x200000, v141\n"
        "s_add_u32 m0, s50, 131072\n"
        "s_add_u32 s80, s67, 0\n"
        "buffer_load_dwordx4 v142, s[40:43], s80 offen lds\n"
        "s_add_u32 m0, s50, 132096\n"
        "s_add_u32 s80, s67, 32768\n"
        "buffer_load_dwordx4 v142, s[40:43], s80 offen lds\n"
        "s_add_u32 m0, s50, 133120\n"
        "s_add_u32 s80, s67, 65536\n"
        "buffer_load_dwordx4 v143, s[40:43], s80 offen lds\n"
        "s_add_u32 m0, s50, 134144\n"
        "s_add_u32 s80, s67, 98304\n"
        "buffer_load_dwordx4 v143, s[40:43], s80 offen lds\n"
        "v_add_u32 v132, 0, v136\n"
        "v_add_u32 v133, 0, v137\n"
        "s_waitcnt vmcnt(24)\n"
        "s_barrier\n"
        "ds_read_b64_tr_b16 v[208:209], v132 offset:0\n"
        "ds_read_b64_tr_b16 v[210:211], v132 offset:1024\n"
        "ds_read_b64_tr_b16 v[212:213], v132 offset:8192\n"
        "ds_read_b64_tr_b16 v[214:215], v132 offset:9216\n"
        "ds_read_b64_tr_b16 v[216:217], v133 offset:0\n"
        "ds_read_b64_tr_b16 v[218:219], v133 offset:1024\n"
        "ds_read_b64_tr_b16 v[220:221], v133 offset:8192\n"
        "ds_read_b64_tr_b16 v[222:223], v133 offset:9216\n"
        "ds_read_b64_tr_b16 v[224:225], v132 offset:16384\n"
        "ds_read_b64_tr_b16 v[226:227], v132 offset:17408\n"
        "ds_read_b64_tr_b16 v[228:229], v132 offset:24576\n"
        "ds_read_b64_tr_b16 v[230:231], v132 offset:25600\n"
        "ds_read_b64_tr_b16 v[232:233], v133 offset:16384\n"
        "ds_read_b64_tr_b16 v[234:235], v133 offset:17408\n"
        "ds_read_b64_tr_b16 v[236:237], v133 offset:24576\n"
        "ds_read_b64_tr_b16 v[238:239], v133 offset:25600\n"
        "ds_read_b64_tr_b16 v[240:241], v132 offset:32768\n"
        "ds_read_b64_tr_b16 v[242:243], v132 offset:33792\n"
        "ds_read_b64_tr_b16 v[244:245], v132 offset:40960\n"
        "ds_read_b64_tr_b16 v[246:247], v132 offset:41984\n"
        "ds_read_b64_tr_b16 v[248:249], v133 offset:32768\n"
        "ds_read_b64_tr_b16 v[250:251], v133 offset:33792\n"
        "ds_read_b64_tr_b16 v[252:253], v133 offset:40960\n"
        "ds_read_b64_tr_b16 v[254:255], v133 offset:41984\n"
        "v_add_u32 v132, 49152, v136\n"
        "v_add_u32 v133, 49152, v137\n"
        "s_waitcnt vmcnt(12)\n"
        "s_barrier\n"
        "ds_read_b64_tr_b16 v[0:1], v132 offset:0\n"
        "ds_read_b64_tr_b16 v[2:3], v132 offset:1024\n"
        "ds_read_b64_tr_b16 v[4:5], v132 offset:8192\n"
        "ds_read_b64_tr_b16 v[6:7], v132 offset:9216\n"
        "ds_read_b64_tr_b16 v[8:9], v133 offset:0\n"
        "ds_read_b64_tr_b16 v[10:11], v133 offset:1024\n"
        "ds_read_b64_tr_b16 v[12:13], v133 offset:8192\n"
        "ds_read_b64_tr_b16 v[14:15], v133 offset:9216\n"
        "ds_read_b64_tr_b16 v[16:17], v132 offset:16384\n"
        "ds_read_b64_tr_b16 v[18:19], v132 offset:17408\n"
        "ds_read_b64_tr_b16 v[20:21], v132 offset:24576\n"
        "ds_read_b64_tr_b16 v[22:23], v132 offset:25600\n"
        "ds_read_b64_tr_b16 v[24:25], v133 offset:16384\n"
        "ds_read_b64_tr_b16 v[26:27], v133 offset:17408\n"
        "ds_read_b64_tr_b16 v[28:29], v133 offset:24576\n"
        "ds_read_b64_tr_b16 v[30:31], v133 offset:25600\n"
        "ds_read_b64_tr_b16 v[32:33], v132 offset:32768\n"
        "ds_read_b64_tr_b16 v[34:35], v132 offset:33792\n"
        "ds_read_b64_tr_b16 v[36:37], v132 offset:40960\n"
        "ds_read_b64_tr_b16 v[38:39], v132 offset:41984\n"
        "ds_read_b64_tr_b16 v[40:41], v133 offset:32768\n"
        "ds_read_b64_tr_b16 v[42:43], v133 offset:33792\n"
        "ds_read_b64_tr_b16 v[44:45], v133 offset:40960\n"
        "ds_read_b64_tr_b16 v[46:47], v133 offset:41984\n"
        "v_add_u32 v132, 98304, v136\n"
        "v_add_u32 v133, 98304, v137\n"
        "s_waitcnt vmcnt(0)\n"
        "s_barrier\n"
        "ds_read_b64_tr_b16 v[48:49], v132 offset:0\n"
        "ds_read_b64_tr_b16 v[50:51], v132 offset:1024\n"
        "ds_read_b64_tr_b16 v[52:53], v132 offset:8192\n"
        "ds_read_b64_tr_b16 v[54:55], v132 offset:9216\n"
        "ds_read_b64_tr_b16 v[56:57], v133 offset:0\n"
        "ds_read_b64_tr_b16 v[58:59], v133 offset:1024\n"
        "ds_read_b64_tr_b16 v[60:61], v133 offset:8192\n"
        "ds_read_b64_tr_b16 v[62:63], v133 offset:9216\n"
        "ds_read_b64_tr_b16 v[64:65], v132 offset:16384\n"
        "ds_read_b64_tr_b16 v[66:67], v132 offset:17408\n"
        "ds_read_b64_tr_b16 v[68:69], v132 offset:24576\n"
        "ds_read_b64_tr_b16 v[70:71], v132 offset:25600\n"
        "ds_read_b64_tr_b16 v[72:73], v133 offset:16384\n"
        "ds_read_b64_tr_b16 v[74:75], v133 offset:17408\n"
        "ds_read_b64_tr_b16 v[76:77], v133 offset:24576\n"
        "ds_read_b64_tr_b16 v[78:79], v133 offset:25600\n"
        "ds_read_b64_tr_b16 v[80:81], v132 offset:32768\n"
        "ds_read_b64_tr_b16 v[82:83], v132 offset:33792\n"
        "ds_read_b64_tr_b16 v[84:85], v132 offset:40960\n"
        "ds_read_b64_tr_b16 v[86:87], v132 offset:41984\n"
        "ds_read_b64_tr_b16 v[88:89], v133 offset:32768\n"
        "ds_read_b64_tr_b16 v[90:91], v133 offset:33792\n"
        "ds_read_b64_tr_b16 v[92:93], v133 offset:40960\n"
        "ds_read_b64_tr_b16 v[94:95], v133 offset:41984\n"
        "s_waitcnt lgkmcnt(0)\n"
        "s_barrier\n"
        "s_lshl_b32 s80, s45, 15\n"
        "s_add_u32 s80, s80, 21504\n"
        "v_add_u32 v132, s80, v131\n"
        "v_mov_b32 v134, 0\n"
        "s_mov_b64 exec, 1\n"
        "buffer_atomic_add v134, off, s[36:39], %6 sc0\n"
        "s_mov_b64 exec, -1\n"
        "ds_write_b128 v132, v[0:3] offset:0\n"
        "ds_write_b128 v132, v[4:7] offset:1024\n"
        "ds_write_b128 v132, v[8:11] offset:2048\n"
        "ds_write_b128 v132, v[12:15] offset:3072\n"
        "ds_write_b128 v132, v[16:19] offset:4096\n"
        "ds_write_b128 v132, v[20:23] offset:5120\n"
        "ds_write_b128 v132, v[24:27] offset:6144\n"
        "ds_write_b128 v132, v[28:31] offset:7168\n"
        "ds_write_b128 v132, v[32:35] offset:8192\n"
        "ds_write_b128 v132, v[36:39] offset:9216\n"
        "ds_write_b128 v132, v[40:43] offset:10240\n"
        "ds_write_b128 v132, v[44:47] offset:11264\n"
        "ds_write_b128 v132, v[48:51] offset:12288\n"
        "ds_write_b128 v132, v[52:55] offset:13312\n"
        "ds_write_b128 v132, v[56:59] offset:14336\n"
        "ds_write_b128 v132, v[60:63] offset:15360\n"
        "ds_write_b128 v132, v[64:67] offset:16384\n"
        "ds_write_b128 v132, v[68:71] offset:17408\n"
        "ds_write_b128 v132, v[72:75] offset:18432\n"
        "ds_write_b128 v132, v[76:79] offset:19456\n"
        "ds_write_b128 v132, v[80:83] offset:20480\n"
        "ds_write_b128 v132, v[84:87] offset:21504\n"
        "ds_write_b128 v132, v[88:91] offset:22528\n"
        "ds_write_b128 v132, v[92:95] offset:23552\n"
        "ds_write_b128 v132, v[96:99] offset:24576\n"
        "ds_write_b128 v132, v[100:103] offset:25600\n"
        "ds_write_b128 v132, v[104:107] offset:26624\n"
        "ds_write_b128 v132, v[108:111] offset:27648\n"
        "ds_write_b128 v132, v[112:115] offset:28672\n"
        "ds_write_b128 v132, v[116:119] offset:29696\n"
        "ds_write_b128 v132, v[120:123] offset:30720\n"
        "ds_write_b128 v132, v[124:127] offset:31744\n"
        "s_waitcnt lgkmcnt(0)\n"
        "s_waitcnt lgkmcnt(0)\n"
        "L_hs%=:\n"
        "s_waitcnt vmcnt(0)\n"
        "v_readfirstlane_b32 s81, v134\n"
        "s_nop 0\n"
        "s_cmp_lg_u32 s81, 0\n"
        "s_cbranch_scc1 L_hs2%=\n"
        "s_add_u32 s63, s63, 1\n"
        "s_cmp_gt_u32 s63, 0x40000\n"
        "s_cbranch_scc1 L_abort%=\n"
        "s_sleep 4\n"
        "v_mov_b32 v134, 0\n"
        "s_mov_b64 exec, 1\n"
        "buffer_atomic_add v134, off, s[36:39], %6 sc0\n"
        "s_mov_b64 exec, -1\n"
        "s_branch L_hs%=\n"
        "L_hs2%=:\n"
        "v_mov_b32 v134, 2\n"
        "s_mov_b64 exec, 1\n"
        "buffer_atomic_swap v134, off, s[36:39], %6\n"
        "s_mov_b64 exec, -1\n"
        "v_mbcnt_lo_u32_b32 v0, -1, 0\n"
        "v_mbcnt_hi_u32_b32 v0, -1, v0\n"
        "v_and_b32 v1, 15, v0\n"
        "v_lshrrev_b32 v2, 4, v0\n"
        "v_lshlrev_b32 v124, 4, v0\n"
        "v_add_u32 v125, s80, v124\n"
        "s_mul_i32 s80, s45, 0\n"
        "s_add_u32 s80, s80, 0\n"
        "v_mov_b32 v138, s80\n"
        "v_bfe_u32 v3, v0, 0, 2\n"
        "s_mov_b32 s80, 16\n"
        "v_mad_u32_u24 v138, v3, s80, v138\n"
        "v_bfe_u32 v3, v0, 2, 3\n"
        "s_mov_b32 s80, 1024\n"
        "v_mad_u32_u24 v138, v3, s80, v138\n"
        "v_bfe_u32 v3, v0, 5, 1\n"
        "s_mov_b32 s80, 512\n"
        "v_mad_u32_u24 v138, v3, s80, v138\n"
        "s_mul_i32 s80, s45, 64\n"
        "s_add_u32 s80, s80, 9216\n"
        "v_mov_b32 v127, s80\n"
        "v_bfe_u32 v3, v0, 0, 2\n"
        "s_mov_b32 s80, 8\n"
        "v_mad_u32_u24 v127, v3, s80, v127\n"
        "v_bfe_u32 v3, v0, 2, 1\n"
        "s_mov_b32 s80, 4\n"
        "v_mad_u32_u24 v127, v3, s80, v127\n"
        "v_bfe_u32 v3, v0, 3, 1\n"
        "s_mov_b32 s80, 32\n"
        "v_mad_u32_u24 v127, v3, s80, v127\n"
        "v_bfe_u32 v3, v0, 4, 2\n"
        "s_mov_b32 s80, 576\n"
        "v_mad_u32_u24 v127, v3, s80, v127\n"
        "s_mul_i32 s80, s45, 2048\n"
        "s_add_u32 s80, s80, 0\n"
        "v_mov_b32 v126, s80\n"
        "v_bfe_u32 v3, v0, 0, 1\n"
        "s_mov_b32 s80, 8\n"
        "v_mad_u32_u24 v126, v3, s80, v126\n"
        "v_bfe_u32 v3, v0, 1, 1\n"
        "s_mov_b32 s80, 32\n"
        "v_mad_u32_u24 v126, v3, s80, v126\n"
        "v_bfe_u32 v3, v0, 2, 1\n"
        "s_mov_b32 s80, 16\n"
        "v_mad_u32_u24 v126, v3, s80, v126\n"
        "v_bfe_u32 v3, v0, 3, 3\n"
        "s_mov_b32 s80, 64\n"
        "v_mad_u32_u24 v126, v3, s80, v126\n"
        "s_mul_i32 s80, s45, 0\n"
        "s_add_u32 s80, s80, 9216\n"
        "v_mov_b32 v128, s80\n"
        "v_bfe_u32 v3, v0, 0, 1\n"
        "s_mov_b32 s80, 288\n"
        "v_mad_u32_u24 v128, v3, s80, v128\n"
        "v_bfe_u32 v3, v0, 2, 2\n"
        "s_mov_b32 s80, 576\n"
        "v_mad_u32_u24 v128, v3, s80, v128\n"
        "v_bfe_u32 v3, v0, 4, 2\n"
        "s_mov_b32 s80, 16\n"
        "v_mad_u32_u24 v128, v3, s80, v128\n"
        "s_mul_i32 s80, s45, 512\n"
        "s_add_u32 s80, s80, 11520\n"
        "v_mov_b32 v129, s80\n"
        "v_bfe_u32 v3, v0, 0, 2\n"
        "s_mov_b32 s80, 4\n"
        "v_mad_u32_u24 v129, v3, s80, v129\n"
        "v_bfe_u32 v3, v0, 2, 1\n"
        "s_mov_b32 s80, 16\n"
        "v_mad_u32_u24 v129, v3, s80, v129\n"
        "v_bfe_u32 v3, v0, 3, 1\n"
        "s_mov_b32 s80, 256\n"
        "v_mad_u32_u24 v129, v3, s80, v129\n"
        "v_bfe_u32 v3, v0, 4, 2\n"
        "s_mov_b32 s80, 64\n"
        "v_mad_u32_u24 v129, v3, s80, v129\n"
        "s_mul_i32 s80, s45, 512\n"
        "s_add_u32 s80, s80, 18432\n"
        "v_mov_b32 v139, s80\n"
        "v_bfe_u32 v3, v0, 0, 1\n"
        "s_mov_b32 s80, 32\n"
        "v_mad_u32_u24 v139, v3, s80, v139\n"
        "v_bfe_u32 v3, v0, 1, 2\n"
        "s_mov_b32 s80, 4\n"
        "v_mad_u32_u24 v139, v3, s80, v139\n"
        "v_bfe_u32 v3, v0, 3, 1\n"
        "s_mov_b32 s80, 256\n"
        "v_mad_u32_u24 v139, v3, s80, v139\n"
        "v_bfe_u32 v3, v0, 4, 2\n"
        "s_mov_b32 s80, 64\n"
        "v_mad_u32_u24 v139, v3, s80, v139\n"
        "s_mul_i32 s80, s45, 0\n"
        "s_add_u32 s80, s80, 11520\n"
        "v_mov_b32 v130, s80\n"
        "v_bfe_u32 v3, v0, 0, 1\n"
        "s_mov_b32 s80, 32\n"
        "v_mad_u32_u24 v130, v3, s80, v130\n"
        "v_bfe_u32 v3, v0, 1, 1\n"
        "s_mov_b32 s80, 16\n"
        "v_mad_u32_u24 v130, v3, s80, v130\n"
        "v_bfe_u32 v3, v0, 2, 1\n"
        "s_mov_b32 s80, 128\n"
        "v_mad_u32_u24 v130, v3, s80, v130\n"
        "v_bfe_u32 v3, v0, 3, 1\n"
        "s_mov_b32 s80, 64\n"
        "v_mad_u32_u24 v130, v3, s80, v130\n"
        "v_bfe_u32 v3, v0, 4, 2\n"
        "s_mov_b32 s80, 256\n"
        "v_mad_u32_u24 v130, v3, s80, v130\n"
        "s_mul_i32 s80, s45, 256\n"
        "s_add_u32 s80, s80, 13568\n"
        "v_mov_b32 v131, s80\n"
        "v_bfe_u32 v3, v0, 0, 1\n"
        "s_mov_b32 s80, 16\n"
        "v_mad_u32_u24 v131, v3, s80, v131\n"
        "v_bfe_u32 v3, v0, 2, 2\n"
        "s_mov_b32 s80, 64\n"
        "v_mad_u32_u24 v131, v3, s80, v131\n"
        "v_bfe_u32 v3, v0, 4, 1\n"
        "s_mov_b32 s80, 32\n"
        "v_mad_u32_u24 v131, v3, s80, v131\n"
        "v_bfe_u32 v3, v0, 5, 1\n"
        "s_mov_b32 s80, 8\n"
        "v_mad_u32_u24 v131, v3, s80, v131\n"
        "s_mul_i32 s80, s45, 0\n"
        "s_add_u32 s80, s80, 0\n"
        "v_mov_b32 v132, s80\n"
        "v_bfe_u32 v3, v0, 0, 1\n"
        "s_mov_b32 s80, 16\n"
        "v_mad_u32_u24 v132, v3, s80, v132\n"
        "v_bfe_u32 v3, v0, 1, 1\n"
        "s_mov_b32 s80, 256\n"
        "v_mad_u32_u24 v132, v3, s80, v132\n"
        "v_bfe_u32 v3, v0, 2, 2\n"
        "s_mov_b32 s80, 32\n"
        "v_mad_u32_u24 v132, v3, s80, v132\n"
        "v_bfe_u32 v3, v0, 4, 1\n"
        "s_mov_b32 s80, 512\n"
        "v_mad_u32_u24 v132, v3, s80, v132\n"
        "v_bfe_u32 v3, v0, 5, 1\n"
        "s_mov_b32 s80, 6144\n"
        "v_mad_u32_u24 v132, v3, s80, v132\n"
        "s_mul_i32 s80, s45, 0\n"
        "s_add_u32 s80, s80, 0\n"
        "v_mov_b32 v133, s80\n"
        "v_bfe_u32 v3, v0, 0, 3\n"
        "s_mov_b32 s80, 8\n"
        "v_mad_u32_u24 v133, v3, s80, v133\n"
        "v_bfe_u32 v3, v0, 3, 1\n"
        "s_mov_b32 s80, 512\n"
        "v_mad_u32_u24 v133, v3, s80, v133\n"
        "v_bfe_u32 v3, v0, 4, 2\n"
        "s_mov_b32 s80, 2048\n"
        "v_mad_u32_u24 v133, v3, s80, v133\n"
        "s_mul_i32 s80, s45, 256\n"
        "s_add_u32 s80, s80, 8192\n"
        "v_mov_b32 v137, s80\n"
        "v_bfe_u32 v3, v0, 0, 1\n"
        "s_mov_b32 s80, 32\n"
        "v_mad_u32_u24 v137, v3, s80, v137\n"
        "v_bfe_u32 v3, v0, 1, 1\n"
        "s_mov_b32 s80, 4\n"
        "v_mad_u32_u24 v137, v3, s80, v137\n"
        "v_bfe_u32 v3, v0, 2, 1\n"
        "s_mov_b32 s80, 16\n"
        "v_mad_u32_u24 v137, v3, s80, v137\n"
        "v_bfe_u32 v3, v0, 3, 2\n"
        "s_mov_b32 s80, 64\n"
        "v_mad_u32_u24 v137, v3, s80, v137\n"
        "v_bfe_u32 v3, v0, 5, 1\n"
        "s_mov_b32 s80, 8\n"
        "v_mad_u32_u24 v137, v3, s80, v137\n"
        "s_and_b32 s80, s47, 1\n"
        "v_bfe_u32 v3, v0, 2, 1\n"
        "v_xor_b32 v3, s80, v3\n"
        "v_mul_u32_u24 v3, 36, v3\n"
        "v_xor_b32 v137, v3, v137\n"
        "s_lshl_b32 s66, s80, 10\n"
        "v_xor_b32 v138, s66, v138\n"
        "s_mul_i32 s80, s45, 4\n"
        "s_add_u32 s80, s80, 20480\n"
        "v_mov_b32 v142, s80\n"
        "s_mul_i32 s80, s45, 0\n"
        "s_add_u32 s80, s80, 0\n"
        "v_mov_b32 v143, s80\n"
        "v_bfe_u32 v3, v0, 0, 6\n"
        "s_mov_b32 s80, 4\n"
        "v_mad_u32_u24 v143, v3, s80, v143\n"
        "v_cmp_gt_u32 vcc, 36, v143\n"
        "s_nop 1\n"
        "v_cndmask_b32 v143, 2.0, v143, vcc\n"
        "v_add_u32 v141, s84, v124\n"
        "s_mov_b32 s92, 0xcccccccc\n"
        "s_mov_b32 s93, 0xcccccccc\n"
        "s_lshl_b32 s82, s45, 12\n"
        "s_and_b32 s80, s45, 1\n"
        "s_mul_i32 s80, s80, -1408\n"
        "s_mul_i32 s100, s45, -4608\n"
        "s_add_u32 s100, s100, s80\n"
        "s_add_u32 s100, s100, 1024\n"
        "v_mov_b32 v3, 0x4444\n"
        "v_mov_b32 v4, 0xeeee\n"
        "v_cndmask_b32 v134, v3, v4, s[92:93]\n"
        "v_mov_b32 v3, 0xffff\n"
        "v_mov_b32 v4, 0xffff0000\n"
        "v_cndmask_b32 v3, v3, v4, s[92:93]\n"
        "v_cmp_gt_u32 vcc, 32, v0\n"
        "s_nop 1\n"
        "v_cndmask_b32 v135, 0, v3, vcc\n"
        "v_xor_b32 v136, v135, v3\n"
        "s_mul_i32 s80, s48, 81920\n"
        "s_add_u32 s80, s80, 884736\n"
        "s_lshl_b32 s81, s45, 14\n"
        "s_add_u32 s49, s80, s81\n"
        "s_lshl_b32 s81, s83, 13\n"
        "s_add_u32 s49, s49, s81\n"
        "s_and_b32 s81, s45, 1\n"
        "s_lshl_b32 s81, s81, 8\n"
        "s_add_u32 s50, s80, s81\n"
        "s_lshr_b32 s81, s45, 1\n"
        "s_lshl_b32 s81, s81, 13\n"
        "s_add_u32 s50, s50, s81\n"
        "s_add_u32 s50, s50, 65536\n"
        "s_add_u32 s80, s80, 163840\n"
        "s_lshl_b32 s69, s83, 11\n"
        "s_add_u32 s83, s80, s36\n"
        "s_mov_b32 s86, 0\n"
        "s_mov_b32 s34, 0\n"
        "s_mov_b32 s53, 0\n"
        "s_add_u32 s80, s80, s66\n"
        "s_lshr_b32 s81, s45, 1\n"
        "s_lshl1_add_u32 s81, s46, s81\n"
        "s_lshl_b32 s81, s81, 14\n"
        "s_add_u32 s51, s80, s81\n"
        "s_and_b32 s81, s45, 1\n"
        "s_lshl_b32 s81, s81, 8\n"
        "s_add_u32 s51, s51, s81\n"
        "s_and_b32 s81, s47, 2\n"
        "s_lshl_b32 s81, s81, 12\n"
        "s_add_u32 s51, s51, s81\n"
        "s_lshl_b32 s81, s46, 5\n"
        "s_add_u32 s81, s81, s45\n"
        "s_lshl_b32 s81, s81, 8\n"
        "s_add_u32 s81, s81, s80\n"
        "s_add_u32 s81, s81, 65536\n"
        "s_cmp_lt_u32 s45, 2\n"
        "s_cselect_b32 s52, s81, 0x40000000\n"
        "s_lshl1_add_u32 s81, s47, s46\n"
        "s_mov_b32 s99, s81\n"
        "s_mov_b32 s28, 0\n"
        "s_lshl_b32 s30, s81, 4\n"
        "s_mul_i32 s29, s48, 69632\n"
        "s_add_u32 s29, s29, 150528\n"
        "s_mul_i32 s80, s48, 2112\n"
        "s_add_u32 s80, s80, s47\n"
        "s_mul_i32 s81, s80, 24576\n"
        "s_mul_i32 s85, s45, 6144\n"
        "s_add_u32 s81, s81, s85\n"
        "s_add_u32 s81, s81, 102825984\n"
        "s_add_u32 s55, s81, 2048\n"
        "s_add_u32 s56, s81, s100\n"
        "s_add_u32 s64, s56, 12288\n"
        "s_mul_i32 s85, s46, 3072\n"
        "s_add_u32 s81, s81, s85\n"
        "s_add_u32 s54, s81, 786432\n"
        "s_lshl_b32 s80, s48, 11\n"
        "s_add_u32 s80, s80, s47\n"
        "s_lshl_b32 s80, s80, 12\n"
        "s_add_u32 s80, s80, 207945728\n"
        "s_bfe_u32 s81, %5, 0x20001\n"
        "s_cmp_eq_u32 s81, 1\n"
        "s_cselect_b32 s59, s80, 0x40000000\n"
        "s_add_u32 s80, s80, 2048\n"
        "s_mul_i32 s85, s45, 1536\n"
        "s_sub_u32 s80, s80, s85\n"
        "s_cmp_eq_u32 s46, 1\n"
        "s_cselect_b32 s60, s80, 0x40000000\n"
        "s_lshl_b32 s80, s48, 5\n"
        "s_add_u32 s80, s80, s47\n"
        "s_mul_i32 s80, s80, 544\n"
        "s_xor_b32 s81, s46, 1\n"
        "s_lshl1_add_u32 s80, s81, s80\n"
        "s_add_u32 s80, s80, s45\n"
        "s_lshl_b32 s80, s80, 2\n"
        "s_add_u32 s80, s80, 12288\n"
        "s_and_b32 s81, s59, s60\n"
        "s_bitcmp1_b32 s81, 30\n"
        "s_cselect_b32 s61, 0x40000000, s80\n"
        "s_lshl1_add_u32 s80, s48, s46\n"
        "s_lshl_b32 s57, s80, 13\n"
        "s_and_b32 s80, s47, 3\n"
        "s_lshl_b32 s80, s80, 8\n"
        "s_add_u32 s57, s57, s80\n"
        "s_add_u32 s57, s57, 884736\n"
        "s_mov_b32 s31, 0\n"
        "s_mul_i32 s80, s48, 2359296\n"
        "s_mul_i32 s81, s46, 18432\n"
        "s_add_u32 s81, s81, s82\n"
        "s_add_u32 s80, s80, s81\n"
        "s_add_u32 s58, s80, 16580608\n"
        "s_mov_b32 s82, s69\n"
        "s_mov_b32 s70, 0x41f40000\n"
        "s_mov_b32 s71, 0x41f40000\n"
        "s_mov_b32 s78, 0x41f40000\n"
        "s_mov_b32 s79, 0\n"
        "s_mov_b32 s44, -2\n"
        "s_waitcnt lgkmcnt(0)\n"
        "s_cmp_lg_u32 s45, 0\n"
        "s_cbranch_scc1 L_x1%=\n"
        "s_branch L_xf2%=\n"
        "L_xf1%=:\n"
        "s_add_u32 s63, s63, 1\n"
        "s_cmp_gt_u32 s63, 0x40000\n"
        "s_cbranch_scc1 L_abort%=\n"
        "s_sleep 2\n"
        "L_xf2%=:\n"
        "v_mov_b32 v140, 0\n"
        "buffer_atomic_add v140, v143, s[36:39], s57 offen sc0\n"
        "s_waitcnt vmcnt(0)\n"
        "v_cmp_ge_u32 vcc, s31, v140\n"
        "s_and_b32 vcc_lo, vcc_lo, 0x1ff\n"
        "s_cbranch_scc1 L_xf1%=\n"
        "L_x1%=:\n"
        "s_bitset1_b32 s49, 30\n"
        "s_bitset1_b32 s50, 30\n"
        "s_bitset1_b32 s56, 30\n"
        "s_bitset1_b32 s64, 30\n"
        "s_add_u32 s29, s29, 1024\n"
        "s_add_u32 s80, s29, s30\n"
        "v_mov_b32 v140, 1\n"
        "s_barrier\n"
        "s_memrealtime s[26:27]\n"
        "s_waitcnt lgkmcnt(0)\n"
        "s_sub_u32 s28, s28, s26\n"
        "s_cmp_eq_u32 s45, 0\n"
        "s_cbranch_scc1 L_x2_0%=\n"
        "s_cmp_eq_u32 s45, 1\n"
        "s_cbranch_scc1 L_x2_1%=\n"
        "s_cmp_eq_u32 s45, 2\n"
        "s_cbranch_scc1 L_x2_2%=\n"
        "s_cmp_eq_u32 s45, 3\n"
        "s_cbranch_scc1 L_x2_3%=\n"
        "L_light_0%=:\n"
        "v_mfma_f32_16x16x32_bf16 v[52:55], v[252:255], v[104:107], v[52:55]\n"
        "v_mfma_f32_16x16x32_bf16 v[84:87], v[96:99], a[112:115], v[84:87]\n"
        "v_mfma_f32_16x16x32_bf16 v[88:91], v[96:99], a[120:123], v[88:91]\n"
        "v_cvt_pk_bf16_f32 v48, v48, v52\n"
        "v_cvt_pk_bf16_f32 v49, v49, v53\n"
        "v_mfma_f32_16x16x32_bf16 v[84:87], v[100:103], a[116:119], v[84:87]\n"
        "v_cvt_pk_bf16_f32 v50, v50, v54\n"
        "v_cvt_pk_bf16_f32 v51, v51, v55\n"
        "v_mfma_f32_16x16x32_bf16 v[88:91], v[100:103], a[124:127], v[88:91]\n"
        "buffer_store_dwordx4 v[48:51], v124, s[36:39], s54 offen offset:1024 sc1\n"
        "v_mfma_f32_16x16x32_bf16 v[84:87], v[92:95], a[128:131], v[84:87]\n"
        "v_mfma_f32_16x16x32_bf16 v[88:91], v[92:95], a[136:139], v[88:91]\n"
        "v_mfma_f32_16x16x32_bf16 v[84:87], v[104:107], a[132:135], v[84:87]\n"
        "v_mfma_f32_16x16x32_bf16 v[88:91], v[104:107], a[140:143], v[88:91]\n"
        "v_mfma_f32_16x16x32_bf16 v[56:59], v[32:35], v[92:95], v[56:59]\n"
        "v_mfma_f32_16x16x32_bf16 v[60:63], v[40:43], v[92:95], v[60:63]\n"
        "v_cvt_pk_bf16_f32 v84, v84, v85\n"
        "v_cvt_pk_bf16_f32 v85, v86, v87\n"
        "v_mfma_f32_16x16x32_bf16 v[56:59], v[16:19], v[96:99], v[56:59]\n"
        "v_cvt_pk_bf16_f32 v86, v88, v89\n"
        "v_cvt_pk_bf16_f32 v87, v90, v91\n"
        "v_mfma_f32_16x16x32_bf16 v[60:63], v[24:27], v[96:99], v[60:63]\n"
        "buffer_store_dwordx4 v[84:87], v124, s[36:39], s54 offen offset:2048 sc1\n"
        "v_mfma_f32_16x16x32_bf16 v[56:59], v[20:23], v[100:103], v[56:59]\n"
        "v_mov_b64 v[92:93], 0\n"
        "v_mov_b64 v[94:95], 0\n"
        "v_mfma_f32_16x16x32_bf16 v[60:63], v[28:31], v[100:103], v[60:63]\n"
        "v_mov_b64 v[96:97], 0\n"
        "v_mov_b64 v[98:99], 0\n"
        "v_mfma_f32_16x16x32_bf16 v[56:59], v[36:39], v[104:107], v[56:59]\n"
        "v_mfma_f32_16x16x32_bf16 v[60:63], v[44:47], v[104:107], v[60:63]\n"
        "s_nop 7\n"
        "v_cvt_pk_bf16_f32 v100, v56, v60\n"
        "v_cvt_pk_bf16_f32 v101, v57, v61\n"
        "v_cvt_pk_bf16_f32 v102, v58, v62\n"
        "v_cvt_pk_bf16_f32 v103, v59, v63\n"
        "buffer_store_dwordx4 v[100:103], v124, s[36:39], s54 offen offset:0 sc1\n"
        "s_barrier\n"
        "s_add_u32 s69, s58, s82\n"
        "s_sub_u32 s72, s58, s82\n"
        "s_waitcnt vmcnt(6)\n"
        "buffer_load_dwordx4 v[64:67], v124, s[36:39], s69 offen offset:0 sc1\n"
        "s_waitcnt vmcnt(6)\n"
        "buffer_load_dwordx4 v[68:71], v124, s[36:39], s69 offen offset:1024 sc1\n"
        "s_waitcnt vmcnt(6)\n"
        "buffer_load_dwordx4 v[72:75], v124, s[36:39], s72 offen offset:2048 sc1\n"
        "s_waitcnt vmcnt(6)\n"
        "buffer_load_dwordx4 v[76:79], v124, s[36:39], s72 offen offset:3072 sc1\n"
        "s_mov_b32 s81, 64\n"
        "buffer_load_dword v107, off, s[36:39], s81 sc1\n"
        "s_memrealtime s[26:27]\n"
        "s_waitcnt vmcnt(5)\n"
        "s_waitcnt lgkmcnt(0)\n"
        "s_barrier\n"
        "s_add_u32 s29, s29, 1024\n"
        "s_waitcnt vmcnt(0)\n"
        "L_par_0%=:\n"
        "v_readfirstlane_b32 s80, v107\n"
        "s_nop 0\n"
        "s_cmp_lt_i32 s80, 0\n"
        "s_cbranch_scc1 L_par2_0%=\n"
        "s_add_u32 s63, s63, 1\n"
        "s_cmp_gt_u32 s63, 0x40000\n"
        "s_cbranch_scc1 L_abort%=\n"
        "s_sleep 4\n"
        "buffer_load_dword v107, off, s[36:39], s81 sc1\n"
        "s_waitcnt vmcnt(0)\n"
        "s_branch L_par_0%=\n"
        "L_par2_0%=:\n"
        "s_bfe_u32 s46, s80, 0x10018\n"
        "s_lshl_b32 s46, s46, 21\n"
        "s_mov_b32 s53, 0x200000\n"
        "s_bfe_u32 s84, s80, 0x80010\n"
        "s_sub_u32 s84, 255, s84\n"
        "s_bfe_u32 s76, s80, 0x80008\n"
        "s_sub_u32 s76, s76, 32\n"
        "s_max_i32 s76, s76, 66\n"
        "s_bfe_u32 s77, s80, 0x80000\n"
        "s_sub_u32 s77, s77, 158\n"
        "s_bitset0_b32 s49, 30\n"
        "s_bitset0_b32 s50, 30\n"
        "s_bitset0_b32 s56, 30\n"
        "s_bitset0_b32 s64, 30\n"
        "v_mov_b32 v140, 0\n"
        "s_mov_b32 s28, 1000\n"
        "s_branch L_xok_0%=\n"
        "L_x2_0%=:\n"
        "s_add_u32 s69, s58, s82\n"
        "s_sub_u32 s72, s58, s82\n"
        "buffer_load_dwordx4 v[64:67], v124, s[36:39], s69 offen offset:0 sc1\n"
        "buffer_load_dwordx4 v[68:71], v124, s[36:39], s69 offen offset:1024 sc1\n"
        "buffer_load_dwordx4 v[72:75], v124, s[36:39], s72 offen offset:2048 sc1\n"
        "buffer_load_dwordx4 v[76:79], v124, s[36:39], s72 offen offset:3072 sc1\n"
        "s_waitcnt vmcnt(0)\n"
        "s_memrealtime s[26:27]\n"
        "s_waitcnt lgkmcnt(0)\n"
        "s_add_u32 s28, s28, s26\n"
        "L_xok_0%=:\n"
        "v_mfma_f32_16x16x32_bf16 v[84:87], v[64:67], a[0:3], 0\n"
        "v_mfma_f32_16x16x32_bf16 v[88:91], v[64:67], a[8:11], 0\n"
        "v_mfma_f32_16x16x32_bf16 v[56:59], a[144:147], v[64:67], 0\n"
        "s_branch L_win_0%=\n"
        "L_retry_0%=:\n"
        "s_add_u32 s63, s63, 1\n"
        "s_cmp_gt_u32 s63, 0x40000\n"
        "s_cbranch_scc1 L_abort%=\n"
        "s_sleep 2\n"
        "s_waitcnt vmcnt(0)\n"
        "s_waitcnt lgkmcnt(0)\n"
        "buffer_load_dwordx4 v[80:83], v138, s[36:39], s50 offen offset:0 sc1\n"
        "buffer_load_dwordx4 v[64:67], v138, s[36:39], s49 offen offset:0 sc1\n"
        "buffer_load_dwordx4 v[68:71], v138, s[36:39], s49 offen offset:256 sc1\n"
        "buffer_load_dwordx4 v[72:75], v138, s[36:39], s65 offen offset:0 sc1\n"
        "buffer_load_dwordx4 v[76:79], v138, s[36:39], s65 offen offset:256 sc1\n"
        "s_waitcnt vmcnt(4)\n"
        "v_bitop3_b32 v123, 32, v80, v82 bitop3:0x80\n"
        "v_add_u32_dpp v80, v81, v80 row_half_mirror row_mask:0xf bank_mask:0xf\n"
        "v_add_u32_dpp v82, v83, v82 row_half_mirror row_mask:0xf bank_mask:0xf\n"
        "v_cvt_f32_i32 v80, v80\n"
        "v_cvt_f32_i32 v82, v82\n"
        "v_cvt_pk_bf16_f32 v52, v80, v82\n"
        "ds_write_b32 v137, v52\n"
        "s_waitcnt vmcnt(3)\n"
        "v_bitop3_b32 v123, v123, v64, v66 bitop3:0x80\n"
        "v_cvt_f32_i32 v64, v64\n"
        "v_cvt_f32_i32 v65, v65\n"
        "v_cvt_f32_i32 v66, v66\n"
        "v_cvt_f32_i32 v67, v67\n"
        "v_cvt_pk_bf16_f32 v52, v64, v65\n"
        "v_cvt_pk_bf16_f32 v53, v66, v67\n"
        "ds_write_b64 v126, v[52:53] offset:0\n"
        "s_waitcnt vmcnt(2)\n"
        "v_bitop3_b32 v123, v123, v68, v70 bitop3:0x80\n"
        "v_cvt_f32_i32 v68, v68\n"
        "v_cvt_f32_i32 v69, v69\n"
        "v_cvt_f32_i32 v70, v70\n"
        "v_cvt_f32_i32 v71, v71\n"
        "v_cvt_pk_bf16_f32 v54, v68, v69\n"
        "v_cvt_pk_bf16_f32 v55, v70, v71\n"
        "ds_write_b64 v126, v[54:55] offset:512\n"
        "s_waitcnt vmcnt(1)\n"
        "v_bitop3_b32 v123, v123, v72, v74 bitop3:0x80\n"
        "v_cvt_f32_i32 v72, v72\n"
        "v_cvt_f32_i32 v73, v73\n"
        "ds_read_b128 v[48:51], v124 offset:0\n"
        "v_cvt_f32_i32 v74, v74\n"
        "v_cvt_f32_i32 v75, v75\n"
        "v_cvt_pk_bf16_f32 v52, v72, v73\n"
        "v_cvt_pk_bf16_f32 v53, v74, v75\n"
        "ds_write_b64 v126, v[52:53] offset:1024\n"
        "s_waitcnt vmcnt(0)\n"
        "v_bitop3_b32 v123, v123, v76, v78 bitop3:0x80\n"
        "v_cmp_eq_u32 vcc, 0, v123\n"
        "v_mov_b64 v[122:123], 0\n"
        "v_cvt_f32_i32 v76, v76\n"
        "v_cvt_f32_i32 v77, v77\n"
        "v_cvt_f32_i32 v78, v78\n"
        "v_cvt_f32_i32 v79, v79\n"
        "v_cvt_pk_bf16_f32 v54, v76, v77\n"
        "v_cvt_pk_bf16_f32 v55, v78, v79\n"
        "ds_write_b64 v126, v[54:55] offset:1536\n"
        "v_mov_b64 v[92:93], 0\n"
        "v_mov_b64 v[94:95], 0\n"
        "v_mov_b64 v[96:97], 0\n"
        "v_mov_b64 v[98:99], 0\n"
        "s_waitcnt lgkmcnt(0)\n"
        "s_branch L_dec_0%=\n"
        "L_sw0_0%=:\n"
        "s_memrealtime s[26:27]\n"
        "s_waitcnt lgkmcnt(0)\n"
        "s_sub_u32 s28, s28, s26\n"
        "L_sw_0%=:\n"
        "s_add_u32 s63, s63, 1\n"
        "s_cmp_gt_u32 s63, 0x40000\n"
        "s_cbranch_scc1 L_abort%=\n"
        "s_sleep 2\n"
        "buffer_load_dword v140, v124, s[36:39], s29 offen sc1\n"
        "s_waitcnt vmcnt(0)\n"
        "v_cmp_eq_u32 vcc, 0, v140\n"
        "s_cbranch_vccnz L_sw_0%=\n"
        "s_memrealtime s[26:27]\n"
        "s_waitcnt lgkmcnt(0)\n"
        "s_add_u32 s28, s28, s26\n"
        "s_branch L_sok_0%=\n"
        ".p2align 6\n"
        "L_rot_0%=:\n"
        "v_mfma_f32_16x16x32_bf16 v[60:63], a[236:239], v[100:103], v[60:63]\n"
        "v_mfma_f32_16x16x32_bf16 v[48:51], v[196:199], v[100:103], v[48:51]\n"
        "v_mfma_f32_16x16x32_bf16 v[52:55], v[204:207], v[100:103], v[52:55]\n"
        "s_waitcnt lgkmcnt(8)\n"
        "v_mfma_f32_16x16x32_bf16 v[84:87], v[92:95], a[96:99], v[84:87]\n"
        "ds_read_b128 v[100:103], v124 offset:15360\n"
        "v_mfma_f32_16x16x32_bf16 v[88:91], v[92:95], a[104:107], v[88:91]\n"
        "v_mfma_f32_16x16x32_bf16 v[56:59], v[0:3], v[92:95], v[56:59]\n"
        "v_mfma_f32_16x16x32_bf16 v[60:63], v[8:11], v[92:95], v[60:63]\n"
        "v_mfma_f32_16x16x32_bf16 v[48:51], v[208:211], v[92:95], v[48:51]\n"
        "s_waitcnt lgkmcnt(0)\n"
        "s_barrier\n"
        "v_mfma_f32_16x16x32_bf16 v[52:55], v[216:219], v[92:95], v[52:55]\n"
        "v_mov_b64 v[116:117], 0\n"
        "v_mfma_f32_16x16x32_bf16 v[84:87], v[104:107], a[100:103], v[84:87]\n"
        "ds_read_b128 v[92:95], v124 offset:16384\n"
        "v_mov_b64 v[118:119], 0\n"
        "v_mfma_f32_16x16x32_bf16 v[88:91], v[104:107], a[108:111], v[88:91]\n"
        "v_mov_b64 v[120:121], 0\n"
        "v_mfma_f32_16x16x32_bf16 v[56:59], v[4:7], v[104:107], v[56:59]\n"
        "v_mfma_f32_16x16x32_bf16 v[60:63], v[12:15], v[104:107], v[60:63]\n"
        "v_mfma_f32_16x16x32_bf16 v[48:51], v[212:215], v[104:107], v[48:51]\n"
        "v_mfma_f32_16x16x32_bf16 v[52:55], v[220:223], v[104:107], v[52:55]\n"
        "v_mfma_f32_16x16x32_bf16 v[48:51], v[224:227], v[96:99], v[48:51]\n"
        "ds_read_b128 v[104:107], v124 offset:17408\n"
        "v_mfma_f32_16x16x32_bf16 v[52:55], v[232:235], v[96:99], v[52:55]\n"
        "v_mfma_f32_16x16x32_bf16 v[48:51], v[228:231], v[100:103], v[48:51]\n"
        "v_mfma_f32_16x16x32_bf16 v[52:55], v[236:239], v[100:103], v[52:55]\n"
        "s_waitcnt lgkmcnt(1)\n"
        "v_mfma_f32_16x16x32_bf16 v[48:51], v[240:243], v[92:95], v[48:51]\n"
        "v_mfma_f32_16x16x32_bf16 v[52:55], v[248:251], v[92:95], v[52:55]\n"
        "s_waitcnt lgkmcnt(0)\n"
        "v_mfma_f32_16x16x32_bf16 v[48:51], v[244:247], v[104:107], v[48:51]\n"
        "s_cmp_lt_i32 s44, 0\n"
        "s_cbranch_scc1 L_light_0%=\n"
        "v_mfma_f32_16x16x32_bf16 v[52:55], v[252:255], v[104:107], v[52:55]\n"
        "s_waitcnt vmcnt(4)\n"
        "v_bitop3_b32 v123, 32, v80, v82 bitop3:0x80\n"
        "s_nop 0\n"
        "v_add_u32_dpp v80, v81, v80 row_half_mirror row_mask:0xf bank_mask:0xf\n"
        "v_mfma_f32_16x16x32_bf16 v[84:87], v[96:99], a[112:115], v[84:87]\n"
        "s_nop 0\n"
        "v_add_u32_dpp v82, v83, v82 row_half_mirror row_mask:0xf bank_mask:0xf\n"
        "v_mfma_f32_16x16x32_bf16 v[88:91], v[96:99], a[120:123], v[88:91]\n"
        "v_cvt_pk_bf16_f32 v48, v48, v52\n"
        "v_cvt_pk_bf16_f32 v49, v49, v53\n"
        "s_nop 0\n"
        "v_mfma_f32_16x16x32_bf16 v[84:87], v[100:103], a[116:119], v[84:87]\n"
        "v_cvt_pk_bf16_f32 v50, v50, v54\n"
        "v_cvt_pk_bf16_f32 v51, v51, v55\n"
        "v_mfma_f32_16x16x32_bf16 v[88:91], v[100:103], a[124:127], v[88:91]\n"
        "buffer_store_dwordx4 v[48:51], v124, s[36:39], s54 offen offset:1024 sc1\n"
        "v_cvt_f32_i32 v80, v80\n"
        "v_mfma_f32_16x16x32_bf16 v[84:87], v[92:95], a[128:131], v[84:87]\n"
        "s_nop 0\n"
        "v_cvt_f32_i32 v82, v82\n"
        "v_mfma_f32_16x16x32_bf16 v[88:91], v[92:95], a[136:139], v[88:91]\n"
        "s_nop 0\n"
        "v_cvt_pk_bf16_f32 v52, v80, v82\n"
        "v_mfma_f32_16x16x32_bf16 v[84:87], v[104:107], a[132:135], v[84:87]\n"
        "s_nop 0\n"
        "v_mfma_f32_16x16x32_bf16 v[88:91], v[104:107], a[140:143], v[88:91]\n"
        "ds_write_b32 v137, v52\n"
        "s_waitcnt vmcnt(4)\n"
        "v_bitop3_b32 v123, v123, v64, v66 bitop3:0x80\n"
        "v_cvt_f32_i32 v64, v64\n"
        "v_mfma_f32_16x16x32_bf16 v[56:59], v[32:35], v[92:95], v[56:59]\n"
        "v_cvt_f32_i32 v65, v65\n"
        "v_cvt_f32_i32 v66, v66\n"
        "v_mfma_f32_16x16x32_bf16 v[60:63], v[40:43], v[92:95], v[60:63]\n"
        "v_cvt_pk_bf16_f32 v84, v84, v85\n"
        "v_cvt_pk_bf16_f32 v85, v86, v87\n"
        "v_mfma_f32_16x16x32_bf16 v[56:59], v[16:19], v[96:99], v[56:59]\n"
        "v_cvt_pk_bf16_f32 v86, v88, v89\n"
        "v_cvt_pk_bf16_f32 v87, v90, v91\n"
        "v_mfma_f32_16x16x32_bf16 v[60:63], v[24:27], v[96:99], v[60:63]\n"
        "buffer_store_dwordx4 v[84:87], v124, s[36:39], s54 offen offset:2048 sc1\n"
        "v_cvt_f32_i32 v67, v67\n"
        "v_cvt_pk_bf16_f32 v52, v64, v65\n"
        "v_mfma_f32_16x16x32_bf16 v[56:59], v[20:23], v[100:103], v[56:59]\n"
        "v_mov_b64 v[92:93], 0\n"
        "v_mov_b64 v[94:95], 0\n"
        "v_mfma_f32_16x16x32_bf16 v[60:63], v[28:31], v[100:103], v[60:63]\n"
        "v_mov_b64 v[96:97], 0\n"
        "v_mov_b64 v[98:99], 0\n"
        "v_mfma_f32_16x16x32_bf16 v[56:59], v[36:39], v[104:107], v[56:59]\n"
        "v_cvt_pk_bf16_f32 v53, v66, v67\n"
        "ds_write_b64 v126, v[52:53] offset:0\n"
        "s_waitcnt vmcnt(4)\n"
        "v_bitop3_b32 v123, v123, v68, v70 bitop3:0x80\n"
        "v_mfma_f32_16x16x32_bf16 v[60:63], v[44:47], v[104:107], v[60:63]\n"
        "v_cvt_f32_i32 v68, v68\n"
        "v_cvt_f32_i32 v69, v69\n"
        "v_cvt_f32_i32 v70, v70\n"
        "v_cvt_f32_i32 v71, v71\n"
        "v_cvt_pk_bf16_f32 v54, v68, v69\n"
        "v_cvt_pk_bf16_f32 v55, v70, v71\n"
        "ds_write_b64 v126, v[54:55] offset:512\n"
        "s_waitcnt vmcnt(3)\n"
        "v_bitop3_b32 v123, v123, v72, v74 bitop3:0x80\n"
        "v_cvt_f32_i32 v72, v72\n"
        "v_cvt_pk_bf16_f32 v100, v56, v60\n"
        "v_cvt_pk_bf16_f32 v101, v57, v61\n"
        "v_cvt_pk_bf16_f32 v102, v58, v62\n"
        "v_cvt_pk_bf16_f32 v103, v59, v63\n"
        "v_cvt_f32_i32 v73, v73\n"
        "ds_read_b128 v[48:51], v124 offset:0\n"
        "v_cvt_f32_i32 v74, v74\n"
        "v_cvt_f32_i32 v75, v75\n"
        "v_cvt_pk_bf16_f32 v52, v72, v73\n"
        "v_cvt_pk_bf16_f32 v53, v74, v75\n"
        "ds_write_b64 v126, v[52:53] offset:1024\n"
        "s_waitcnt vmcnt(2)\n"
        "v_bitop3_b32 v123, v123, v76, v78 bitop3:0x80\n"
        "v_cmp_eq_u32 vcc, 0, v123\n"
        "v_mov_b64 v[122:123], 0\n"
        "v_cvt_f32_i32 v76, v76\n"
        "v_cvt_f32_i32 v77, v77\n"
        "v_cvt_f32_i32 v78, v78\n"
        "v_cvt_f32_i32 v79, v79\n"
        "v_cvt_pk_bf16_f32 v54, v76, v77\n"
        "v_cvt_pk_bf16_f32 v55, v78, v79\n"
        "ds_write_b64 v126, v[54:55] offset:1536\n"
        "L_dec_0%=:\n"
        "s_waitcnt lgkmcnt(2)\n"
        "v_smfmac_f32_16x16x64_bf16 v[92:95], v[48:51], a[144:151], v134\n"
        "ds_read_b128 v[52:55], v124 offset:1024\n"
        "v_max3_f32 v104, |v80|, |v82|, |v64|\n"
        "v_smfmac_f32_16x16x64_bf16 v[96:99], v[48:51], a[152:159], v134\n"
        "s_cbranch_vccnz L_retry_0%=\n"
        "v_max3_f32 v104, |v65|, |v66|, v104\n"
        "v_smfmac_f32_16x16x64_bf16 v[116:119], v[48:51], a[240:247], v134\n"
        "buffer_load_dwordx4 v[88:91], v124, s[36:39], s55 offen offset:3072 sc1\n"
        "s_add_u32 s29, s29, 1024\n"
        "v_max3_f32 v104, |v67|, |v68|, v104\n"
        "v_smfmac_f32_16x16x64_bf16 v[120:123], v[48:51], a[248:255], v134\n"
        "s_waitcnt lgkmcnt(0)\n"
        "v_max3_f32 v104, |v69|, |v70|, v104\n"
        "s_barrier\n"
        "ds_read_b128 v[56:59], v124 offset:6144\n"
        "v_smfmac_f32_16x16x64_bf16 v[92:95], v[52:55], a[160:167], v134\n"
        "ds_read_b128 v[60:63], v124 offset:5120\n"
        "v_max3_f32 v104, |v71|, |v72|, v104\n"
        "v_smfmac_f32_16x16x64_bf16 v[96:99], v[52:55], a[168:175], v134\n"
        "buffer_load_dwordx4 v[84:87], v124, s[36:39], s55 offen offset:0 sc1\n"
        "ds_read_b128 v[80:83], v124 offset:7168\n"
        "v_max3_f32 v104, |v73|, |v74|, v104\n"
        "v_smfmac_f32_16x16x64_bf16 v[116:119], v[52:55], v[176:183], v134\n"
        "ds_read_b128 v[64:67], v124 offset:8192\n"
        "v_max3_f32 v104, |v75|, |v76|, v104\n"
        "v_smfmac_f32_16x16x64_bf16 v[120:123], v[52:55], v[184:191], v134\n"
        "ds_read_b128 v[68:71], v124 offset:2048\n"
        "v_max3_f32 v104, |v77|, |v78|, v104\n"
        "s_waitcnt lgkmcnt(4)\n"
        "v_smfmac_f32_16x16x64_bf16 v[92:95], v[56:59], v[0:7], v134\n"
        "ds_read_b128 v[72:75], v124 offset:3072\n"
        "v_max_f32_e64 v104, |v79|, v104\n"
        "v_smfmac_f32_16x16x64_bf16 v[96:99], v[56:59], v[8:15], v134\n"
        "ds_read_b128 v[0:3], v125 offset:12288\n"
        "v_max_f32_dpp v104, v104, v104 row_shr:1 row_mask:0xf bank_mask:0xf\n"
        "s_waitcnt lgkmcnt(3)\n"
        "v_smfmac_f32_16x16x64_bf16 v[92:95], v[60:63], a[224:231], v134\n"
        "ds_read_b128 v[76:79], v124 offset:4096\n"
        "v_max_f32_dpp v104, v104, v104 row_shr:2 row_mask:0xf bank_mask:0xf\n"
        "v_smfmac_f32_16x16x64_bf16 v[96:99], v[60:63], a[232:239], v134\n"
        "ds_read_b128 v[4:7], v125 offset:13312\n"
        "v_max_f32_dpp v104, v104, v104 row_shr:4 row_mask:0xf bank_mask:0xf\n"
        "v_smfmac_f32_16x16x64_bf16 v[92:95], v[80:83], v[16:23], v134\n"
        "ds_read_b128 v[8:11], v125 offset:14336\n"
        "v_max_f32_dpp v104, v104, v104 row_shr:8 row_mask:0xf bank_mask:0xf\n"
        "v_smfmac_f32_16x16x64_bf16 v[96:99], v[80:83], v[24:31], v134\n"
        "ds_read_b128 v[12:15], v125 offset:15360\n"
        "v_max_f32_dpp v104, v104, v104 row_bcast:15 row_mask:0xa bank_mask:0xf\n"
        "v_smfmac_f32_16x16x64_bf16 v[92:95], v[64:67], v[32:39], v134\n"
        "ds_read_b128 v[16:19], v125 offset:16384\n"
        "v_max_f32_dpp v104, v104, v104 row_bcast:31 row_mask:0xc bank_mask:0xf\n"
        "v_smfmac_f32_16x16x64_bf16 v[96:99], v[64:67], v[40:47], v134\n"
        "ds_read_b128 v[20:23], v125 offset:17408\n"
        "v_readlane_b32 s85, v104, 63\n"
        "s_waitcnt lgkmcnt(5)\n"
        "v_smfmac_f32_16x16x64_bf16 v[92:95], v[68:71], a[176:183], v134\n"
        "ds_read_b128 v[24:27], v125 offset:18432\n"
        "v_smfmac_f32_16x16x64_bf16 v[96:99], v[68:71], a[184:191], v134\n"
        "ds_read_b128 v[28:31], v125 offset:19456\n"
        "v_mov_b32 v104, s85\n"
        "v_smfmac_f32_16x16x64_bf16 v[92:95], v[72:75], a[192:199], v134\n"
        "ds_read_b128 v[32:35], v125 offset:20480\n"
        "v_smfmac_f32_16x16x64_bf16 v[96:99], v[72:75], a[200:207], v134\n"
        "ds_read_b128 v[36:39], v125 offset:21504\n"
        "s_add_u32 s69, s58, s82\n"
        "v_smfmac_f32_16x16x64_bf16 v[92:95], v[76:79], a[208:215], v134\n"
        "ds_read_b128 v[40:43], v125 offset:22528\n"
        "s_sub_u32 s72, s58, s82\n"
        "v_smfmac_f32_16x16x64_bf16 v[96:99], v[76:79], a[216:223], v134\n"
        "ds_read_b128 v[44:47], v125 offset:23552\n"
        "v_smfmac_f32_16x16x64_bf16 v[116:119], v[64:67], v[240:247], v134\n"
        "ds_write_b32 v142, v104\n"
        "v_add_f32 v92, v92, v94\n"
        "v_add_f32 v93, v93, v95\n"
        "v_smfmac_f32_16x16x64_bf16 v[120:123], v[64:67], v[248:255], v134\n"
        "v_add_f32 v96, v96, v98\n"
        "v_add_f32 v97, v97, v99\n"
        "v_cvt_pk_bf16_f32 v92, v92, v96\n"
        "v_cvt_pk_bf16_f32 v93, v93, v97\n"
        "s_waitcnt lgkmcnt(3)\n"
        "v_smfmac_f32_16x16x64_bf16 v[116:119], v[68:71], v[0:7], v134\n"
        "ds_write2_b32 v127, v92, v93 offset1:72\n"
        "v_smfmac_f32_16x16x64_bf16 v[120:123], v[68:71], v[8:15], v134\n"
        "buffer_store_dwordx4 v[100:103], v124, s[36:39], s54 offen offset:0 sc1\n"
        "v_mov_b64 v[98:99], 0\n"
        "v_smfmac_f32_16x16x64_bf16 v[116:119], v[72:75], v[16:23], v134\n"
        "v_mov_b64 v[96:97], 0\n"
        "v_smfmac_f32_16x16x64_bf16 v[120:123], v[72:75], v[24:31], v134\n"
        "s_waitcnt vmcnt(4)\n"
        "s_waitcnt lgkmcnt(0)\n"
        "v_mov_b64 v[106:107], 0\n"
        "s_barrier\n"
        "ds_read_b128 v[0:3], v128 offset:0\n"
        "v_smfmac_f32_16x16x64_bf16 v[116:119], v[76:79], v[32:39], v134\n"
        "ds_read_b128 v[24:27], v124 offset:20480\n"
        "v_mov_b64 v[104:105], 0\n"
        "v_smfmac_f32_16x16x64_bf16 v[120:123], v[76:79], v[40:47], v134\n"
        "ds_read_b128 v[4:7], v128 offset:64\n"
        "v_mov_b64 v[94:95], 0\n"
        "v_smfmac_f32_16x16x64_bf16 v[116:119], v[60:63], v[192:199], v134\n"
        "ds_read_b128 v[8:11], v128 offset:128\n"
        "v_mov_b64 v[92:93], 0\n"
        "v_smfmac_f32_16x16x64_bf16 v[120:123], v[60:63], v[200:207], v134\n"
        "ds_read_b128 v[12:15], v128 offset:192\n"
        "v_mov_b64 v[20:21], 0\n"
        "v_smfmac_f32_16x16x64_bf16 v[116:119], v[56:59], v[208:215], v134\n"
        "s_waitcnt lgkmcnt(3)\n"
        "v_max3_f32 v24, v24, v25, v26\n"
        "s_waitcnt lgkmcnt(2)\n"
        "v_smfmac_f32_16x16x64_bf16 v[104:107], v[108:111], v[0:7], v134\n"
        "v_max_f32 v24, v24, v27\n"
        "v_smfmac_f32_16x16x64_bf16 v[120:123], v[56:59], v[216:223], v134\n"
        "v_readfirstlane_b32 s84, v24\n"
        "s_waitcnt lgkmcnt(0)\n"
        "v_smfmac_f32_16x16x64_bf16 v[104:107], v[112:115], v[8:15], v134\n"
        "v_mov_b64 v[22:23], 0\n"
        "v_smfmac_f32_16x16x64_bf16 v[116:119], v[80:83], v[224:231], v134\n"
        "s_lshr_b32 s84, s84, 23\n"
        "s_add_u32 s84, s84, s101\n"
        "v_smfmac_f32_16x16x64_bf16 v[120:123], v[80:83], v[232:239], v134\n"
        "s_max_i32 s84, s84, s76\n"
        "s_min_i32 s84, s84, 186\n"
        "v_add_f32 v104, v104, v106\n"
        "v_add_f32 v105, v105, v107\n"
        "v_smfmac_f32_16x16x64_bf16 v[92:95], v[64:67], a[128:135], v134\n"
        "ds_write_b64 v131, v[104:105]\n"
        "v_add_f32 v116, v116, v118\n"
        "v_add_f32 v117, v117, v119\n"
        "v_pk_add_f32 v[120:121], v[120:121], v[122:123]\n"
        "v_cvt_pk_bf16_f32 v116, v116, v120\n"
        "v_cvt_pk_bf16_f32 v117, v117, v121\n"
        "ds_write2_b32 v129, v116, v117 offset1:8\n"
        "v_smfmac_f32_16x16x64_bf16 v[96:99], v[64:67], a[136:143], v134\n"
        "s_mul_i32 s88, s84, 0x800080\n"
        "s_mov_b32 s89, s88\n"
        "v_smfmac_f32_16x16x64_bf16 v[92:95], v[68:71], a[32:39], v134\n"
        "buffer_load_dwordx4 v[64:67], v124, s[36:39], s69 offen offset:0 sc1\n"
        "v_mov_b64 v[104:105], s[88:89]\n"
        "v_smfmac_f32_16x16x64_bf16 v[96:99], v[68:71], a[40:47], v134\n"
        "s_add_u32 s90, s88, s35\n"
        "s_mov_b32 s91, s90\n"
        "v_mov_b64 v[106:107], s[90:91]\n"
        "v_smfmac_f32_16x16x64_bf16 v[92:95], v[48:51], a[0:7], v134\n"
        "s_waitcnt lgkmcnt(0)\n"
        "v_mov_b64 v[100:101], s[88:89]\n"
        "s_barrier\n"
        "ds_read_b128 v[24:27], v130\n"
        "v_smfmac_f32_16x16x64_bf16 v[96:99], v[48:51], a[8:15], v134\n"
        "ds_read_b128 v[28:31], v130 offset:1024\n"
        "v_mov_b64 v[102:103], s[90:91]\n"
        "v_smfmac_f32_16x16x64_bf16 v[92:95], v[52:55], a[16:23], v134\n"
        "ds_read_b128 v[116:119], v124 offset:13568\n"
        "v_mov_b64 v[48:49], 0\n"
        "v_smfmac_f32_16x16x64_bf16 v[96:99], v[52:55], a[24:31], v134\n"
        "ds_read_b128 v[32:35], v141 offset:0\n"
        "v_mov_b64 v[50:51], 0\n"
        "v_smfmac_f32_16x16x64_bf16 v[92:95], v[56:59], a[96:103], v134\n"
        "ds_read_b128 v[36:39], v141 offset:1024\n"
        "v_mov_b64 v[52:53], 0\n"
        "v_smfmac_f32_16x16x64_bf16 v[96:99], v[56:59], a[104:111], v134\n"
        "ds_read_b128 v[40:43], v141 offset:2048\n"
        "v_mov_b64 v[54:55], 0\n"
        "s_waitcnt lgkmcnt(5)\n"
        "v_smfmac_f32_16x16x64_bf16 v[20:23], v[24:27], v[0:7], v134\n"
        "ds_read_b128 v[44:47], v141 offset:3072\n"
        "v_mov_b64 v[56:57], 0\n"
        "s_waitcnt lgkmcnt(4)\n"
        "v_smfmac_f32_16x16x64_bf16 v[20:23], v[28:31], v[8:15], v134\n"
        "buffer_load_dwordx4 v[68:71], v124, s[36:39], s69 offen offset:1024 sc1\n"
        "v_max3_f32 v121, v116, v117, v118\n"
        "v_smfmac_f32_16x16x64_bf16 v[92:95], v[72:75], a[48:55], v134\n"
        "ds_read_b128 v[0:3], v125 offset:24576\n"
        "v_mov_b64 v[58:59], 0\n"
        "v_smfmac_f32_16x16x64_bf16 v[96:99], v[72:75], a[56:63], v134\n"
        "buffer_load_dwordx4 v[72:75], v124, s[36:39], s72 offen offset:2048 sc1\n"
        "v_add_f32 v20, v20, v22\n"
        "v_add_f32 v21, v21, v23\n"
        "v_cndmask_b32 v120, v20, v21, s[92:93]\n"
        "v_mul_f32 v120, s74, v120\n"
        "v_max3_f32 v121, v121, v119, v120\n"
        "v_smfmac_f32_16x16x64_bf16 v[92:95], v[76:79], a[64:71], v134\n"
        "ds_read_b128 v[4:7], v125 offset:25600\n"
        "v_max_f32_dpp v121, v121, v121 quad_perm:[2,3,0,1] row_mask:0xf bank_mask:0xf\n"
        "v_mov_b32 v122, v121\n"
        "v_smfmac_f32_16x16x64_bf16 v[96:99], v[76:79], a[72:79], v134\n"
        "buffer_load_dwordx4 v[76:79], v124, s[36:39], s72 offen offset:3072 sc1\n"
        "v_permlane16_swap_b32 v121, v122\n"
        "v_max_f32 v121, v121, v122\n"
        "v_mov_b32 v122, v121\n"
        "v_smfmac_f32_16x16x64_bf16 v[92:95], v[80:83], a[112:119], v134\n"
        "ds_read_b128 v[8:11], v125 offset:26624\n"
        "v_permlane32_swap_b32 v121, v122\n"
        "v_smfmac_f32_16x16x64_bf16 v[96:99], v[80:83], a[120:127], v134\n"
        "v_max_f32 v121, v121, v122\n"
        "v_mul_f32_e64 v123, -s73, v121\n"
        "v_fma_f32 v120, v120, s73, v123\n"
        "v_fma_f32 v116, v116, s73, v123\n"
        "v_smfmac_f32_16x16x64_bf16 v[92:95], v[60:63], a[80:87], v134\n"
        "ds_read_b128 v[12:15], v125 offset:27648\n"
        "v_fma_f32 v117, v117, s73, v123\n"
        "v_fma_f32 v118, v118, s73, v123\n"
        "v_fma_f32 v119, v119, s73, v123\n"
        "v_exp_f32 v120, v120\n"
        "v_smfmac_f32_16x16x64_bf16 v[96:99], v[60:63], a[88:95], v134\n"
        "ds_read_b128 v[16:19], v125 offset:28672\n"
        "v_exp_f32 v116, v116\n"
        "v_mul_f32 v120, s74, v120\n"
        "v_exp_f32 v117, v117\n"
        "v_exp_f32 v118, v118\n"
        "v_exp_f32 v119, v119\n"
        "s_waitcnt vmcnt(5)\n"
        "v_mov_b64 v[108:109], v[86:87]\n"
        "v_mov_b64 v[112:113], v[90:91]\n"
        "v_cvt_pk_bf16_f32 v120, v120, v120\n"
        "v_cvt_pk_bf16_f32 v60, v116, v117\n"
        "v_cvt_pk_bf16_f32 v62, v118, v119\n"
        "v_and_b32 v61, v120, v135\n"
        "v_and_b32 v63, v120, v136\n"
        "v_pk_add_f32 v[92:93], v[92:93], v[94:95]\n"
        "v_pk_add_f32 v[96:97], v[96:97], v[98:99]\n"
        "v_smfmac_f32_16x16x64_bf16 v[56:59], v[60:63], v[100:107], v134\n"
        "ds_read_b128 v[20:23], v125 offset:29696\n"
        "v_cvt_pk_bf16_f32 v86, v92, v93\n"
        "v_cvt_pk_bf16_f32 v87, v92, v93\n"
        "v_mov_b64 v[90:91], v[86:87]\n"
        "v_cvt_pk_bf16_f32 v110, v96, v97\n"
        "v_cvt_pk_bf16_f32 v111, v96, v97\n"
        "v_smfmac_f32_16x16x64_bf16 v[48:51], v[60:63], v[84:91], v134\n"
        "ds_read_b128 v[24:27], v125 offset:30720\n"
        "v_mov_b64 v[114:115], v[110:111]\n"
        "v_add_f32 v56, v56, v58\n"
        "v_add_f32 v57, v57, v59\n"
        "v_smfmac_f32_16x16x64_bf16 v[52:55], v[60:63], v[108:115], v134\n"
        "ds_read_b128 v[28:31], v125 offset:31744\n"
        "s_nop 0\n"
        "v_rcp_f32 v56, v56\n"
        "v_rcp_f32 v57, v57\n"
        "v_pk_add_f32 v[48:49], v[48:49], v[50:51]\n"
        "v_pk_mul_f32 v[48:49], v[48:49], v[56:57]\n"
        "v_mov_b32 v110, v86\n"
        "v_mov_b64 v[92:93], s[70:71]\n"
        "v_pk_add_f32 v[52:53], v[52:53], v[54:55]\n"
        "v_pk_mul_f32 v[52:53], v[52:53], v[56:57]\n"
        "v_cvt_pk_bf16_f32 v48, v48, v52\n"
        "v_cvt_pk_bf16_f32 v49, v49, v53\n"
        "ds_write2_b32 v139, v48, v49 offset1:4\n"
        "v_mov_b64 v[94:95], 0\n"
        "buffer_store_dwordx2 v[110:111], v126, s[36:39], s60 offen sc1\n"
        "v_mov_b64 v[96:97], 0\n"
        "v_mov_b64 v[98:99], 0\n"
        "v_mov_b64 v[100:101], s[70:71]\n"
        "v_mov_b64 v[102:103], 0\n"
        "v_mov_b64 v[104:105], 0\n"
        "s_waitcnt lgkmcnt(0)\n"
        "v_mov_b64 v[106:107], 0\n"
        "s_barrier\n"
        "ds_read_b128 v[108:111], v124 offset:18432\n"
        "v_mov_b64 v[118:119], 0\n"
        "ds_read_b128 v[112:115], v124 offset:19456\n"
        "v_mov_b64 v[118:119], 0\n"
        "s_nop 0\n"
        "v_mov_b64 v[116:117], s[78:79]\n"
        "s_nop 0\n"
        "v_mov_b64 v[118:119], 0\n"
        "s_nop 0\n"
        "v_mov_b64 v[116:117], s[78:79]\n"
        "s_nop 0\n"
        "s_waitcnt lgkmcnt(1)\n"
        "v_smfmac_f32_16x16x64_bf16 v[116:119], v[108:111], v[32:39], v134\n"
        "s_waitcnt lgkmcnt(0)\n"
        "v_smfmac_f32_16x16x64_bf16 v[116:119], v[112:115], v[40:47], v134\n"
        "v_smfmac_f32_16x16x64_bf16 v[92:95], v[108:111], v[144:151], v134\n"
        "v_smfmac_f32_16x16x64_bf16 v[96:99], v[108:111], v[152:159], v134\n"
        "buffer_load_dword v140, v124, s[36:39], s29 offen sc1\n"
        "v_add_f32 v116, v116, v118\n"
        "v_add_f32 v117, v117, v119\n"
        "v_smfmac_f32_16x16x64_bf16 v[92:95], v[112:115], v[160:167], v134\n"
        "v_cvt_rpi_i32_f32 v118, v116\n"
        "v_bfi_b32 v118, 63, 2, v118\n"
        "v_cvt_rpi_i32_f32 v119, v117\n"
        "v_smfmac_f32_16x16x64_bf16 v[96:99], v[112:115], v[168:175], v134\n"
        "buffer_atomic_add_x2 v[118:119], v133, s[36:39], s52 offen\n"
        "v_smfmac_f32_16x16x64_bf16 v[100:103], v[108:111], v[0:7], v134\n"
        "v_add_f32 v92, v92, v94\n"
        "v_add_f32 v93, v93, v95\n"
        "v_cvt_rpi_i32_f32 v94, v92\n"
        "v_smfmac_f32_16x16x64_bf16 v[104:107], v[108:111], v[8:15], v134\n"
        "v_bfi_b32 v94, 63, 1, v94\n"
        "v_add_f32 v96, v96, v98\n"
        "v_smfmac_f32_16x16x64_bf16 v[100:103], v[112:115], v[16:23], v134\n"
        "v_cvt_rpi_i32_f32 v95, v96\n"
        "v_smfmac_f32_16x16x64_bf16 v[104:107], v[112:115], v[24:31], v134\n"
        "buffer_atomic_add_x2 v[94:95], v133, s[36:39], s51 offen\n"
        "v_add_f32 v97, v97, v99\n"
        "v_cvt_rpi_i32_f32 v98, v93\n"
        "v_bfi_b32 v98, 63, 1, v98\n"
        "v_cvt_rpi_i32_f32 v99, v97\n"
        "s_waitcnt vmcnt(7)\n"
        "v_mfma_f32_16x16x32_bf16 v[84:87], v[64:67], a[0:3], 0\n"
        "buffer_atomic_add_x2 v[98:99], v133, s[36:39], s66 offen\n"
        "v_add_f32 v100, v100, v102\n"
        "v_add_f32 v101, v101, v103\n"
        "v_add_f32 v104, v104, v106\n"
        "v_cvt_rpi_i32_f32 v102, v100\n"
        "v_bfi_b32 v102, 63, 1, v102\n"
        "v_cvt_rpi_i32_f32 v103, v104\n"
        "v_mfma_f32_16x16x32_bf16 v[88:91], v[64:67], a[8:11], 0\n"
        "buffer_atomic_add_x2 v[102:103], v133, s[36:39], s67 offen\n"
        "v_add_f32 v105, v105, v107\n"
        "v_cvt_rpi_i32_f32 v106, v101\n"
        "v_bfi_b32 v106, 63, 1, v106\n"
        "v_cvt_rpi_i32_f32 v107, v105\n"
        "v_mfma_f32_16x16x32_bf16 v[56:59], a[144:147], v[64:67], 0\n"
        "buffer_atomic_add_x2 v[106:107], v133, s[36:39], s68 offen\n"
        "L_win_0%=:\n"
        "v_mfma_f32_16x16x32_bf16 v[60:63], a[152:155], v[64:67], 0\n"
        "v_mfma_f32_16x16x32_bf16 v[48:51], a[240:243], v[64:67], 0\n"
        "ds_write_b128 v124, v[64:67] offset:0\n"
        "v_mfma_f32_16x16x32_bf16 v[52:55], a[248:251], v[64:67], 0\n"
        "s_waitcnt vmcnt(9)\n"
        "v_mfma_f32_16x16x32_bf16 v[84:87], v[68:71], a[4:7], v[84:87]\n"
        "ds_write_b128 v124, v[68:71] offset:1024\n"
        "v_mfma_f32_16x16x32_bf16 v[88:91], v[68:71], a[12:15], v[88:91]\n"
        "v_mfma_f32_16x16x32_bf16 v[56:59], a[148:151], v[68:71], v[56:59]\n"
        "s_waitcnt vmcnt(8)\n"
        "ds_write_b128 v124, v[72:75] offset:2048\n"
        "v_mfma_f32_16x16x32_bf16 v[60:63], a[156:159], v[68:71], v[60:63]\n"
        "v_mfma_f32_16x16x32_bf16 v[48:51], a[244:247], v[68:71], v[48:51]\n"
        "s_waitcnt vmcnt(7)\n"
        "ds_write_b128 v124, v[76:79] offset:3072\n"
        "v_mfma_f32_16x16x32_bf16 v[52:55], a[252:255], v[68:71], v[52:55]\n"
        "v_mfma_f32_16x16x32_bf16 v[84:87], v[72:75], a[16:19], v[84:87]\n"
        "v_mfma_f32_16x16x32_bf16 v[88:91], v[72:75], a[24:27], v[88:91]\n"
        "v_mfma_f32_16x16x32_bf16 v[56:59], a[160:163], v[72:75], v[56:59]\n"
        "v_mfma_f32_16x16x32_bf16 v[60:63], a[168:171], v[72:75], v[60:63]\n"
        "v_mfma_f32_16x16x32_bf16 v[48:51], v[176:179], v[72:75], v[48:51]\n"
        "ds_read_b128 v[0:3], v125 offset:12288\n"
        "v_mfma_f32_16x16x32_bf16 v[52:55], v[184:187], v[72:75], v[52:55]\n"
        "ds_read_b128 v[8:11], v125 offset:14336\n"
        "v_mfma_f32_16x16x32_bf16 v[84:87], v[76:79], a[20:23], v[84:87]\n"
        "s_waitcnt lgkmcnt(2)\n"
        "s_barrier\n"
        "v_mfma_f32_16x16x32_bf16 v[88:91], v[76:79], a[28:31], v[88:91]\n"
        "ds_read_b128 v[92:95], v124 offset:4096\n"
        "v_mov_b32 v116, s62\n"
        "v_mfma_f32_16x16x32_bf16 v[56:59], a[164:167], v[76:79], v[56:59]\n"
        "s_add_u32 s49, s49, 163840\n"
        "ds_read_b128 v[104:107], v124 offset:5120\n"
        "v_mfma_f32_16x16x32_bf16 v[60:63], a[172:175], v[76:79], v[60:63]\n"
        "s_add_u32 s50, s50, 163840\n"
        "ds_read_b128 v[4:7], v125 offset:13312\n"
        "v_mfma_f32_16x16x32_bf16 v[48:51], v[180:183], v[76:79], v[48:51]\n"
        "s_xor_b32 s49, s49, s86\n"
        "ds_read_b128 v[12:15], v125 offset:15360\n"
        "v_mfma_f32_16x16x32_bf16 v[52:55], v[188:191], v[76:79], v[52:55]\n"
        "s_xor_b32 s65, s49, 8192\n"
        "s_add_u32 s56, s56, 786432\n"
        "s_waitcnt lgkmcnt(3)\n"
        "v_mfma_f32_16x16x32_bf16 v[84:87], v[92:95], a[32:35], v[84:87]\n"
        "s_add_u32 s64, s64, 786432\n"
        "ds_read_b128 v[96:99], v124 offset:6144\n"
        "v_mfma_f32_16x16x32_bf16 v[88:91], v[92:95], a[40:43], v[88:91]\n"
        "ds_read_b128 v[100:103], v124 offset:7168\n"
        "v_mfma_f32_16x16x32_bf16 v[56:59], a[176:179], v[92:95], v[56:59]\n"
        "ds_read_b128 v[16:19], v125 offset:16384\n"
        "v_mfma_f32_16x16x32_bf16 v[60:63], a[184:187], v[92:95], v[60:63]\n"
        "s_add_u32 s51, s51, 163840\n"
        "ds_read_b128 v[20:23], v125 offset:17408\n"
        "v_mfma_f32_16x16x32_bf16 v[48:51], v[0:3], v[92:95], v[48:51]\n"
        "s_add_u32 s52, s52, 163840\n"
        "ds_read_b128 v[24:27], v125 offset:18432\n"
        "v_mfma_f32_16x16x32_bf16 v[52:55], v[8:11], v[92:95], v[52:55]\n"
        "ds_read_b128 v[28:31], v125 offset:19456\n"
        "s_waitcnt lgkmcnt(8)\n"
        "v_mfma_f32_16x16x32_bf16 v[84:87], v[104:107], a[36:39], v[84:87]\n"
        "s_add_u32 s83, s83, 163840\n"
        "ds_read_b128 v[92:95], v124 offset:8192\n"
        "v_mfma_f32_16x16x32_bf16 v[88:91], v[104:107], a[44:47], v[88:91]\n"
        "s_waitcnt vmcnt(6)\n"
        "s_mov_b64 exec, 1\n"
        "buffer_atomic_umax v116, off, s[36:39], s61\n"
        "s_mov_b64 exec, -1\n"
        "v_mfma_f32_16x16x32_bf16 v[56:59], a[180:183], v[104:107], v[56:59]\n"
        "s_cmp_eq_u32 s44, 63\n"
        "s_cbranch_scc1 L_end%=\n"
        "v_mfma_f32_16x16x32_bf16 v[60:63], a[188:191], v[104:107], v[60:63]\n"
        "s_xor_b32 s86, s83, s46\n"
        "ds_read_b128 v[32:35], v125 offset:20480\n"
        "s_waitcnt lgkmcnt(9)\n"
        "v_mfma_f32_16x16x32_bf16 v[48:51], v[4:7], v[104:107], v[48:51]\n"
        "s_and_b32 s86, s86, s53\n"
        "ds_read_b128 v[36:39], v125 offset:21504\n"
        "s_waitcnt lgkmcnt(9)\n"
        "v_mfma_f32_16x16x32_bf16 v[52:55], v[12:15], v[104:107], v[52:55]\n"
        "ds_read_b128 v[40:43], v125 offset:22528\n"
        "s_waitcnt lgkmcnt(9)\n"
        "v_mfma_f32_16x16x32_bf16 v[84:87], v[96:99], a[48:51], v[84:87]\n"
        "s_lshr_b32 s86, s86, 8\n"
        "ds_read_b128 v[104:107], v124 offset:9216\n"
        "v_mfma_f32_16x16x32_bf16 v[88:91], v[96:99], a[56:59], v[88:91]\n"
        "s_xor_b32 s86, s86, s34\n"
        "ds_read_b128 v[44:47], v125 offset:23552\n"
        "v_mfma_f32_16x16x32_bf16 v[56:59], a[192:195], v[96:99], v[56:59]\n"
        "s_xor_b32 s34, s34, s86\n"
        "ds_read_b128 v[0:3], v125 offset:0\n"
        "v_mfma_f32_16x16x32_bf16 v[60:63], a[200:203], v[96:99], v[60:63]\n"
        "ds_read_b128 v[4:7], v125 offset:1024\n"
        "s_waitcnt vmcnt(6)\n"
        "v_cmp_eq_u32 vcc, 0, v140\n"
        "s_waitcnt lgkmcnt(11)\n"
        "v_mfma_f32_16x16x32_bf16 v[48:51], v[16:19], v[96:99], v[48:51]\n"
        "s_cbranch_vccnz L_sw0_0%=\n"
        "L_sok_0%=:\n"
        "s_waitcnt lgkmcnt(9)\n"
        "v_mfma_f32_16x16x32_bf16 v[52:55], v[24:27], v[96:99], v[52:55]\n"
        "v_readlane_b32 s81, v140, s99\n"
        "v_max_u32_dpp v140, v140, v140 row_shr:1 row_mask:0xf bank_mask:0xf\n"
        "v_mfma_f32_16x16x32_bf16 v[84:87], v[100:103], a[52:55], v[84:87]\n"
        "ds_read_b128 v[96:99], v124 offset:10240\n"
        "v_max_u32_dpp v140, v140, v140 row_shr:2 row_mask:0xf bank_mask:0xf\n"
        "v_mfma_f32_16x16x32_bf16 v[88:91], v[100:103], a[60:63], v[88:91]\n"
        "buffer_load_dwordx4 v[108:111], v132, s[36:39], s56 offen offset:0 sc1\n"
        "v_max_u32_dpp v140, v140, v140 row_shr:4 row_mask:0xf bank_mask:0xf\n"
        "v_mfma_f32_16x16x32_bf16 v[56:59], a[196:199], v[100:103], v[56:59]\n"
        "ds_read_b128 v[8:11], v125 offset:2048\n"
        "v_max_u32_dpp v140, v140, v140 row_shr:8 row_mask:0xf bank_mask:0xf\n"
        "v_mfma_f32_16x16x32_bf16 v[60:63], a[204:207], v[100:103], v[60:63]\n"
        "buffer_load_dwordx4 v[112:115], v132, s[36:39], s64 offen offset:0 sc1\n"
        "v_max_u32_dpp v140, v140, v140 row_bcast:15 row_mask:0xa bank_mask:0xf\n"
        "v_mfma_f32_16x16x32_bf16 v[48:51], v[20:23], v[100:103], v[48:51]\n"
        "ds_read_b128 v[12:15], v125 offset:3072\n"
        "v_max_u32_dpp v140, v140, v140 row_bcast:31 row_mask:0xc bank_mask:0xf\n"
        "s_waitcnt lgkmcnt(11)\n"
        "v_mfma_f32_16x16x32_bf16 v[52:55], v[28:31], v[100:103], v[52:55]\n"
        "v_readlane_b32 s33, v140, 63\n"
        "s_lshl1_add_u32 s28, s28, s81\n"
        "s_waitcnt lgkmcnt(10)\n"
        "v_mfma_f32_16x16x32_bf16 v[84:87], v[92:95], a[64:67], v[84:87]\n"
        "ds_read_b128 v[100:103], v124 offset:11264\n"
        "s_add_u32 s28, s28, 36\n"
        "s_sub_i32 s33, s33, s28\n"
        "v_mfma_f32_16x16x32_bf16 v[88:91], v[92:95], a[72:75], v[88:91]\n"
        "s_min_i32 s33, s33, 500\n"
        "s_mov_b32 s28, 0\n"
        "L_dl_0%=:\n"
        "s_sub_i32 s33, s33, 3\n"
        "s_cmp_gt_i32 s33, 0\n"
        "s_cbranch_scc1 L_dl_0%=\n"
        "v_mfma_f32_16x16x32_bf16 v[56:59], a[208:211], v[92:95], v[56:59]\n"
        "s_xor_b32 s51, s51, s86\n"
        "buffer_load_dwordx4 v[80:83], v138, s[36:39], s50 offen offset:0 sc1\n"
        "v_mfma_f32_16x16x32_bf16 v[60:63], a[216:219], v[92:95], v[60:63]\n"
        "s_add_u32 s55, s55, 786432\n"
        "ds_read_b128 v[16:19], v125 offset:4096\n"
        "s_waitcnt lgkmcnt(11)\n"
        "v_mfma_f32_16x16x32_bf16 v[48:51], v[32:35], v[92:95], v[48:51]\n"
        "s_add_u32 s54, s54, 786432\n"
        "buffer_load_dwordx4 v[64:67], v138, s[36:39], s49 offen offset:0 sc1\n"
        "s_waitcnt lgkmcnt(9)\n"
        "v_mfma_f32_16x16x32_bf16 v[52:55], v[40:43], v[92:95], v[52:55]\n"
        "ds_read_b128 v[20:23], v125 offset:5120\n"
        "s_waitcnt lgkmcnt(9)\n"
        "v_mfma_f32_16x16x32_bf16 v[84:87], v[104:107], a[68:71], v[84:87]\n"
        "s_add_u32 s60, s60, 131072\n"
        "ds_read_b128 v[92:95], v124 offset:12288\n"
        "v_mfma_f32_16x16x32_bf16 v[88:91], v[104:107], a[76:79], v[88:91]\n"
        "s_add_u32 s58, s58, 36864\n"
        "buffer_load_dwordx4 v[68:71], v138, s[36:39], s49 offen offset:256 sc1\n"
        "v_mfma_f32_16x16x32_bf16 v[56:59], a[212:215], v[104:107], v[56:59]\n"
        "s_xor_b32 s66, s51, 1024\n"
        "ds_read_b128 v[24:27], v125 offset:6144\n"
        "v_mfma_f32_16x16x32_bf16 v[60:63], a[220:223], v[104:107], v[60:63]\n"
        "s_xor_b32 s67, s51, 8192\n"
        "buffer_load_dwordx4 v[72:75], v138, s[36:39], s65 offen offset:0 sc1\n"
        "v_mfma_f32_16x16x32_bf16 v[48:51], v[36:39], v[104:107], v[48:51]\n"
        "s_xor_b32 s68, s51, 9216\n"
        "ds_read_b128 v[28:31], v125 offset:7168\n"
        "s_waitcnt lgkmcnt(11)\n"
        "v_mfma_f32_16x16x32_bf16 v[52:55], v[44:47], v[104:107], v[52:55]\n"
        "buffer_load_dwordx4 v[76:79], v138, s[36:39], s65 offen offset:256 sc1\n"
        "s_waitcnt lgkmcnt(8)\n"
        "v_mfma_f32_16x16x32_bf16 v[84:87], v[96:99], a[80:83], v[84:87]\n"
        "s_sub_u32 s75, 127, s84\n"
        "ds_read_b128 v[104:107], v124 offset:13312\n"
        "v_mfma_f32_16x16x32_bf16 v[88:91], v[96:99], a[88:91], v[88:91]\n"
        "s_sub_u32 s101, s77, s75\n"
        "ds_read_b128 v[32:35], v125 offset:8192\n"
        "v_mfma_f32_16x16x32_bf16 v[56:59], a[224:227], v[96:99], v[56:59]\n"
        "s_mul_i32 s35, s75, 0x800080\n"
        "ds_read_b128 v[36:39], v125 offset:9216\n"
        "v_mfma_f32_16x16x32_bf16 v[60:63], a[232:235], v[96:99], v[60:63]\n"
        "s_lshl_b32 s80, s75, 23\n"
        "ds_read_b128 v[40:43], v125 offset:10240\n"
        "v_mfma_f32_16x16x32_bf16 v[48:51], v[192:195], v[96:99], v[48:51]\n"
        "s_sub_u32 s73, 0x3e0293ee, s80\n"
        "ds_read_b128 v[44:47], v125 offset:11264\n"
        "v_mfma_f32_16x16x32_bf16 v[52:55], v[200:203], v[96:99], v[52:55]\n"
        "s_sub_u32 s74, 0x3f800000, s80\n"
        "s_add_u32 s80, s75, 128\n"
        "s_waitcnt lgkmcnt(10)\n"
        "v_mfma_f32_16x16x32_bf16 v[84:87], v[100:103], a[84:87], v[84:87]\n"
        "s_lshl_b32 s80, s80, 8\n"
        "ds_read_b128 v[96:99], v124 offset:14336\n"
        "v_mfma_f32_16x16x32_bf16 v[88:91], v[100:103], a[92:95], v[88:91]\n"
        "s_add_u32 s44, s44, 1\n"
        "s_add_u32 s61, s61, 32\n"
        "s_add_u32 s62, s80, 1\n"
        "v_mfma_f32_16x16x32_bf16 v[56:59], a[228:231], v[100:103], v[56:59]\n"
        "s_branch L_rot_0%=\n"
        "L_light_1%=:\n"
        "v_mfma_f32_16x16x32_bf16 v[52:55], v[252:255], v[104:107], v[52:55]\n"
        "v_mfma_f32_16x16x32_bf16 v[84:87], v[96:99], a[112:115], v[84:87]\n"
        "v_mfma_f32_16x16x32_bf16 v[88:91], v[96:99], a[120:123], v[88:91]\n"
        "v_cvt_pk_bf16_f32 v48, v48, v52\n"
        "v_cvt_pk_bf16_f32 v49, v49, v53\n"
        "v_mfma_f32_16x16x32_bf16 v[84:87], v[100:103], a[116:119], v[84:87]\n"
        "v_cvt_pk_bf16_f32 v50, v50, v54\n"
        "v_cvt_pk_bf16_f32 v51, v51, v55\n"
        "v_mfma_f32_16x16x32_bf16 v[88:91], v[100:103], a[124:127], v[88:91]\n"
        "v_mfma_f32_16x16x32_bf16 v[84:87], v[92:95], a[128:131], v[84:87]\n"
        "buffer_store_dwordx4 v[48:51], v124, s[36:39], s54 offen offset:1024 sc1\n"
        "v_mfma_f32_16x16x32_bf16 v[88:91], v[92:95], a[136:139], v[88:91]\n"
        "v_mfma_f32_16x16x32_bf16 v[84:87], v[104:107], a[132:135], v[84:87]\n"
        "v_mfma_f32_16x16x32_bf16 v[88:91], v[104:107], a[140:143], v[88:91]\n"
        "v_mfma_f32_16x16x32_bf16 v[56:59], v[32:35], v[92:95], v[56:59]\n"
        "v_mfma_f32_16x16x32_bf16 v[60:63], v[40:43], v[92:95], v[60:63]\n"
        "v_cvt_pk_bf16_f32 v84, v84, v85\n"
        "v_cvt_pk_bf16_f32 v85, v86, v87\n"
        "v_mfma_f32_16x16x32_bf16 v[56:59], v[16:19], v[96:99], v[56:59]\n"
        "v_cvt_pk_bf16_f32 v86, v88, v89\n"
        "v_cvt_pk_bf16_f32 v87, v90, v91\n"
        "v_mfma_f32_16x16x32_bf16 v[60:63], v[24:27], v[96:99], v[60:63]\n"
        "v_mfma_f32_16x16x32_bf16 v[56:59], v[20:23], v[100:103], v[56:59]\n"
        "buffer_store_dwordx4 v[84:87], v124, s[36:39], s54 offen offset:2048 sc1\n"
        "v_mov_b64 v[92:93], 0\n"
        "v_mfma_f32_16x16x32_bf16 v[60:63], v[28:31], v[100:103], v[60:63]\n"
        "v_mov_b64 v[94:95], 0\n"
        "v_mov_b64 v[96:97], 0\n"
        "v_mfma_f32_16x16x32_bf16 v[56:59], v[36:39], v[104:107], v[56:59]\n"
        "v_mov_b64 v[98:99], 0\n"
        "v_mfma_f32_16x16x32_bf16 v[60:63], v[44:47], v[104:107], v[60:63]\n"
        "s_nop 7\n"
        "v_cvt_pk_bf16_f32 v100, v56, v60\n"
        "v_cvt_pk_bf16_f32 v101, v57, v61\n"
        "v_cvt_pk_bf16_f32 v102, v58, v62\n"
        "v_cvt_pk_bf16_f32 v103, v59, v63\n"
        "buffer_store_dwordx4 v[100:103], v124, s[36:39], s54 offen offset:0 sc1\n"
        "s_barrier\n"
        "s_add_u32 s69, s58, s82\n"
        "s_sub_u32 s72, s58, s82\n"
        "s_waitcnt vmcnt(6)\n"
        "buffer_load_dwordx4 v[64:67], v124, s[36:39], s69 offen offset:0 sc1\n"
        "s_waitcnt vmcnt(6)\n"
        "buffer_load_dwordx4 v[68:71], v124, s[36:39], s69 offen offset:1024 sc1\n"
        "s_waitcnt vmcnt(6)\n"
        "buffer_load_dwordx4 v[72:75], v124, s[36:39], s72 offen offset:2048 sc1\n"
        "s_waitcnt vmcnt(6)\n"
        "buffer_load_dwordx4 v[76:79], v124, s[36:39], s72 offen offset:3072 sc1\n"
        "s_mov_b32 s81, 64\n"
        "buffer_load_dword v107, off, s[36:39], s81 sc1\n"
        "s_memrealtime s[26:27]\n"
        "s_waitcnt vmcnt(5)\n"
        "s_waitcnt lgkmcnt(0)\n"
        "s_barrier\n"
        "s_add_u32 s29, s29, 1024\n"
        "s_waitcnt vmcnt(0)\n"
        "L_par_1%=:\n"
        "v_readfirstlane_b32 s80, v107\n"
        "s_nop 0\n"
        "s_cmp_lt_i32 s80, 0\n"
        "s_cbranch_scc1 L_par2_1%=\n"
        "s_add_u32 s63, s63, 1\n"
        "s_cmp_gt_u32 s63, 0x40000\n"
        "s_cbranch_scc1 L_abort%=\n"
        "s_sleep 4\n"
        "buffer_load_dword v107, off, s[36:39], s81 sc1\n"
        "s_waitcnt vmcnt(0)\n"
        "s_branch L_par_1%=\n"
        "L_par2_1%=:\n"
        "s_bfe_u32 s46, s80, 0x10018\n"
        "s_lshl_b32 s46, s46, 21\n"
        "s_mov_b32 s53, 0x200000\n"
        "s_bfe_u32 s84, s80, 0x80010\n"
        "s_sub_u32 s84, 255, s84\n"
        "s_bfe_u32 s76, s80, 0x80008\n"
        "s_sub_u32 s76, s76, 32\n"
        "s_max_i32 s76, s76, 66\n"
        "s_bfe_u32 s77, s80, 0x80000\n"
        "s_sub_u32 s77, s77, 158\n"
        "s_bitset0_b32 s49, 30\n"
        "s_bitset0_b32 s50, 30\n"
        "s_bitset0_b32 s56, 30\n"
        "s_bitset0_b32 s64, 30\n"
        "v_mov_b32 v140, 0\n"
        "s_mov_b32 s28, 1000\n"
        "s_branch L_xok_1%=\n"
        "L_x2_1%=:\n"
        "s_add_u32 s69, s58, s82\n"
        "s_sub_u32 s72, s58, s82\n"
        "buffer_load_dwordx4 v[64:67], v124, s[36:39], s69 offen offset:0 sc1\n"
        "buffer_load_dwordx4 v[68:71], v124, s[36:39], s69 offen offset:1024 sc1\n"
        "buffer_load_dwordx4 v[72:75], v124, s[36:39], s72 offen offset:2048 sc1\n"
        "buffer_load_dwordx4 v[76:79], v124, s[36:39], s72 offen offset:3072 sc1\n"
        "s_waitcnt vmcnt(0)\n"
        "s_memrealtime s[26:27]\n"
        "s_waitcnt lgkmcnt(0)\n"
        "s_add_u32 s28, s28, s26\n"
        "L_xok_1%=:\n"
        "v_mfma_f32_16x16x32_bf16 v[84:87], v[64:67], a[0:3], 0\n"
        "v_mfma_f32_16x16x32_bf16 v[88:91], v[64:67], a[8:11], 0\n"
        "v_mfma_f32_16x16x32_bf16 v[56:59], a[144:147], v[64:67], 0\n"
        "s_branch L_win_1%=\n"
        "L_retry_1%=:\n"
        "s_add_u32 s63, s63, 1\n"
        "s_cmp_gt_u32 s63, 0x40000\n"
        "s_cbranch_scc1 L_abort%=\n"
        "s_sleep 2\n"
        "s_waitcnt vmcnt(0)\n"
        "s_waitcnt lgkmcnt(0)\n"
        "buffer_load_dwordx4 v[80:83], v138, s[36:39], s50 offen offset:0 sc1\n"
        "buffer_load_dwordx4 v[64:67], v138, s[36:39], s49 offen offset:0 sc1\n"
        "buffer_load_dwordx4 v[68:71], v138, s[36:39], s49 offen offset:256 sc1\n"
        "buffer_load_dwordx4 v[72:75], v138, s[36:39], s65 offen offset:0 sc1\n"
        "buffer_load_dwordx4 v[76:79], v138, s[36:39], s65 offen offset:256 sc1\n"
        "s_waitcnt vmcnt(4)\n"
        "v_bitop3_b32 v123, 32, v80, v82 bitop3:0x80\n"
        "v_add_u32_dpp v80, v81, v80 row_half_mirror row_mask:0xf bank_mask:0xf\n"
        "v_add_u32_dpp v82, v83, v82 row_half_mirror row_mask:0xf bank_mask:0xf\n"
        "v_cvt_f32_i32 v80, v80\n"
        "v_cvt_f32_i32 v82, v82\n"
        "v_cvt_pk_bf16_f32 v52, v80, v82\n"
        "ds_write_b32 v137, v52\n"
        "s_waitcnt vmcnt(3)\n"
        "v_bitop3_b32 v123, v123, v64, v66 bitop3:0x80\n"
        "v_cvt_f32_i32 v64, v64\n"
        "v_cvt_f32_i32 v65, v65\n"
        "v_cvt_f32_i32 v66, v66\n"
        "v_cvt_f32_i32 v67, v67\n"
        "v_cvt_pk_bf16_f32 v52, v64, v65\n"
        "v_cvt_pk_bf16_f32 v53, v66, v67\n"
        "ds_write_b64 v126, v[52:53] offset:0\n"
        "s_waitcnt vmcnt(2)\n"
        "v_bitop3_b32 v123, v123, v68, v70 bitop3:0x80\n"
        "v_cvt_f32_i32 v68, v68\n"
        "v_cvt_f32_i32 v69, v69\n"
        "v_cvt_f32_i32 v70, v70\n"
        "v_cvt_f32_i32 v71, v71\n"
        "v_cvt_pk_bf16_f32 v54, v68, v69\n"
        "v_cvt_pk_bf16_f32 v55, v70, v71\n"
        "ds_write_b64 v126, v[54:55] offset:512\n"
        "s_waitcnt vmcnt(1)\n"
        "v_bitop3_b32 v123, v123, v72, v74 bitop3:0x80\n"
        "v_cvt_f32_i32 v72, v72\n"
        "v_cvt_f32_i32 v73, v73\n"
        "ds_read_b128 v[48:51], v124 offset:2048\n"
        "v_cvt_f32_i32 v74, v74\n"
        "v_cvt_f32_i32 v75, v75\n"
        "v_cvt_pk_bf16_f32 v52, v72, v73\n"
        "v_cvt_pk_bf16_f32 v53, v74, v75\n"
        "ds_write_b64 v126, v[52:53] offset:1024\n"
        "s_waitcnt vmcnt(0)\n"
        "v_bitop3_b32 v123, v123, v76, v78 bitop3:0x80\n"
        "v_cmp_eq_u32 vcc, 0, v123\n"
        "v_mov_b64 v[122:123], 0\n"
        "v_cvt_f32_i32 v76, v76\n"
        "v_cvt_f32_i32 v77, v77\n"
        "v_cvt_f32_i32 v78, v78\n"
        "v_cvt_f32_i32 v79, v79\n"
        "v_cvt_pk_bf16_f32 v54, v76, v77\n"
        "v_cvt_pk_bf16_f32 v55, v78, v79\n"
        "ds_write_b64 v126, v[54:55] offset:1536\n"
        "v_mov_b64 v[92:93], 0\n"
        "v_mov_b64 v[94:95], 0\n"
        "v_mov_b64 v[96:97], 0\n"
        "v_mov_b64 v[98:99], 0\n"
        "s_waitcnt lgkmcnt(0)\n"
        "s_branch L_dec_1%=\n"
        "L_sw0_1%=:\n"
        "s_memrealtime s[26:27]\n"
        "s_waitcnt lgkmcnt(0)\n"
        "s_sub_u32 s28, s28, s26\n"
        "L_sw_1%=:\n"
        "s_add_u32 s63, s63, 1\n"
        "s_cmp_gt_u32 s63, 0x40000\n"
        "s_cbranch_scc1 L_abort%=\n"
        "s_sleep 2\n"
        "buffer_load_dword v140, v124, s[36:39], s29 offen sc1\n"
        "s_waitcnt vmcnt(0)\n"
        "v_cmp_eq_u32 vcc, 0, v140\n"
        "s_cbranch_vccnz L_sw_1%=\n"
        "s_memrealtime s[26:27]\n"
        "s_waitcnt lgkmcnt(0)\n"
        "s_add_u32 s28, s28, s26\n"
        "s_branch L_sok_1%=\n"
        ".p2align 6\n"
        "L_rot_1%=:\n"
        "v_mfma_f32_16x16x32_bf16 v[60:63], a[236:239], v[100:103], v[60:63]\n"
        "v_mfma_f32_16x16x32_bf16 v[48:51], v[196:199], v[100:103], v[48:51]\n"
        "v_mfma_f32_16x16x32_bf16 v[52:55], v[204:207], v[100:103], v[52:55]\n"
        "s_waitcnt lgkmcnt(9)\n"
        "v_mfma_f32_16x16x32_bf16 v[84:87], v[92:95], a[96:99], v[84:87]\n"
        "ds_read_b128 v[100:103], v124 offset:11264\n"
        "v_mfma_f32_16x16x32_bf16 v[88:91], v[92:95], a[104:107], v[88:91]\n"
        "v_mfma_f32_16x16x32_bf16 v[56:59], v[0:3], v[92:95], v[56:59]\n"
        "v_mfma_f32_16x16x32_bf16 v[60:63], v[8:11], v[92:95], v[60:63]\n"
        "v_mfma_f32_16x16x32_bf16 v[48:51], v[208:211], v[92:95], v[48:51]\n"
        "s_waitcnt lgkmcnt(0)\n"
        "s_barrier\n"
        "v_mfma_f32_16x16x32_bf16 v[52:55], v[216:219], v[92:95], v[52:55]\n"
        "v_mov_b64 v[116:117], 0\n"
        "v_mfma_f32_16x16x32_bf16 v[84:87], v[104:107], a[100:103], v[84:87]\n"
        "ds_read_b128 v[92:95], v124 offset:16384\n"
        "v_mov_b64 v[118:119], 0\n"
        "v_mfma_f32_16x16x32_bf16 v[88:91], v[104:107], a[108:111], v[88:91]\n"
        "v_mov_b64 v[120:121], 0\n"
        "v_mfma_f32_16x16x32_bf16 v[56:59], v[4:7], v[104:107], v[56:59]\n"
        "v_mfma_f32_16x16x32_bf16 v[60:63], v[12:15], v[104:107], v[60:63]\n"
        "v_mfma_f32_16x16x32_bf16 v[48:51], v[212:215], v[104:107], v[48:51]\n"
        "v_mfma_f32_16x16x32_bf16 v[52:55], v[220:223], v[104:107], v[52:55]\n"
        "v_mfma_f32_16x16x32_bf16 v[48:51], v[224:227], v[96:99], v[48:51]\n"
        "ds_read_b128 v[104:107], v124 offset:17408\n"
        "v_mfma_f32_16x16x32_bf16 v[52:55], v[232:235], v[96:99], v[52:55]\n"
        "v_mfma_f32_16x16x32_bf16 v[48:51], v[228:231], v[100:103], v[48:51]\n"
        "v_mfma_f32_16x16x32_bf16 v[52:55], v[236:239], v[100:103], v[52:55]\n"
        "s_waitcnt lgkmcnt(1)\n"
        "v_mfma_f32_16x16x32_bf16 v[48:51], v[240:243], v[92:95], v[48:51]\n"
        "v_mfma_f32_16x16x32_bf16 v[52:55], v[248:251], v[92:95], v[52:55]\n"
        "s_waitcnt lgkmcnt(0)\n"
        "v_mfma_f32_16x16x32_bf16 v[48:51], v[244:247], v[104:107], v[48:51]\n"
        "s_cmp_lt_i32 s44, 0\n"
        "s_cbranch_scc1 L_light_1%=\n"
        "v_mfma_f32_16x16x32_bf16 v[52:55], v[252:255], v[104:107], v[52:55]\n"
        "s_waitcnt vmcnt(4)\n"
        "v_bitop3_b32 v123, 32, v80, v82 bitop3:0x80\n"
        "s_nop 0\n"
        "v_add_u32_dpp v80, v81, v80 row_half_mirror row_mask:0xf bank_mask:0xf\n"
        "v_mfma_f32_16x16x32_bf16 v[84:87], v[96:99], a[112:115], v[84:87]\n"
        "s_nop 0\n"
        "v_add_u32_dpp v82, v83, v82 row_half_mirror row_mask:0xf bank_mask:0xf\n"
        "v_mfma_f32_16x16x32_bf16 v[88:91], v[96:99], a[120:123], v[88:91]\n"
        "v_cvt_pk_bf16_f32 v48, v48, v52\n"
        "v_cvt_pk_bf16_f32 v49, v49, v53\n"
        "s_nop 0\n"
        "v_mfma_f32_16x16x32_bf16 v[84:87], v[100:103], a[116:119], v[84:87]\n"
        "v_cvt_pk_bf16_f32 v50, v50, v54\n"
        "v_cvt_pk_bf16_f32 v51, v51, v55\n"
        "v_mfma_f32_16x16x32_bf16 v[88:91], v[100:103], a[124:127], v[88:91]\n"
        "v_cvt_f32_i32 v80, v80\n"
        "s_nop 0\n"
        "v_cvt_f32_i32 v82, v82\n"
        "v_mfma_f32_16x16x32_bf16 v[84:87], v[92:95], a[128:131], v[84:87]\n"
        "buffer_store_dwordx4 v[48:51], v124, s[36:39], s54 offen offset:1024 sc1\n"
        "v_mfma_f32_16x16x32_bf16 v[88:91], v[92:95], a[136:139], v[88:91]\n"
        "s_nop 0\n"
        "v_cvt_pk_bf16_f32 v52, v80, v82\n"
        "v_mfma_f32_16x16x32_bf16 v[84:87], v[104:107], a[132:135], v[84:87]\n"
        "s_nop 0\n"
        "v_mfma_f32_16x16x32_bf16 v[88:91], v[104:107], a[140:143], v[88:91]\n"
        "ds_write_b32 v137, v52\n"
        "s_waitcnt vmcnt(4)\n"
        "v_bitop3_b32 v123, v123, v64, v66 bitop3:0x80\n"
        "v_cvt_f32_i32 v64, v64\n"
        "v_mfma_f32_16x16x32_bf16 v[56:59], v[32:35], v[92:95], v[56:59]\n"
        "v_cvt_f32_i32 v65, v65\n"
        "v_cvt_f32_i32 v66, v66\n"
        "v_mfma_f32_16x16x32_bf16 v[60:63], v[40:43], v[92:95], v[60:63]\n"
        "v_cvt_pk_bf16_f32 v84, v84, v85\n"
        "v_cvt_pk_bf16_f32 v85, v86, v87\n"
        "v_mfma_f32_16x16x32_bf16 v[56:59], v[16:19], v[96:99], v[56:59]\n"
        "v_cvt_pk_bf16_f32 v86, v88, v89\n"
        "v_cvt_pk_bf16_f32 v87, v90, v91\n"
        "v_mfma_f32_16x16x32_bf16 v[60:63], v[24:27], v[96:99], v[60:63]\n"
        "v_cvt_f32_i32 v67, v67\n"
        "v_cvt_pk_bf16_f32 v52, v64, v65\n"
        "v_mfma_f32_16x16x32_bf16 v[56:59], v[20:23], v[100:103], v[56:59]\n"
        "buffer_store_dwordx4 v[84:87], v124, s[36:39], s54 offen offset:2048 sc1\n"
        "v_mov_b64 v[92:93], 0\n"
        "v_cvt_pk_bf16_f32 v53, v66, v67\n"
        "v_mfma_f32_16x16x32_bf16 v[60:63], v[28:31], v[100:103], v[60:63]\n"
        "v_mov_b64 v[94:95], 0\n"
        "v_mov_b64 v[96:97], 0\n"
        "ds_write_b64 v126, v[52:53] offset:0\n"
        "v_mfma_f32_16x16x32_bf16 v[56:59], v[36:39], v[104:107], v[56:59]\n"
        "v_mov_b64 v[98:99], 0\n"
        "s_waitcnt vmcnt(4)\n"
        "v_bitop3_b32 v123, v123, v68, v70 bitop3:0x80\n"
        "v_mfma_f32_16x16x32_bf16 v[60:63], v[44:47], v[104:107], v[60:63]\n"
        "v_cvt_f32_i32 v68, v68\n"
        "v_cvt_f32_i32 v69, v69\n"
        "v_cvt_f32_i32 v70, v70\n"
        "v_cvt_f32_i32 v71, v71\n"
        "v_cvt_pk_bf16_f32 v54, v68, v69\n"
        "v_cvt_pk_bf16_f32 v55, v70, v71\n"
        "ds_write_b64 v126, v[54:55] offset:512\n"
        "s_waitcnt vmcnt(3)\n"
        "v_bitop3_b32 v123, v123, v72, v74 bitop3:0x80\n"
        "v_cvt_f32_i32 v72, v72\n"
        "v_cvt_pk_bf16_f32 v100, v56, v60\n"
        "v_cvt_pk_bf16_f32 v101, v57, v61\n"
        "v_cvt_pk_bf16_f32 v102, v58, v62\n"
        "v_cvt_pk_bf16_f32 v103, v59, v63\n"
        "v_cvt_f32_i32 v73, v73\n"
        "ds_read_b128 v[48:51], v124 offset:2048\n"
        "v_cvt_f32_i32 v74, v74\n"
        "v_cvt_f32_i32 v75, v75\n"
        "v_cvt_pk_bf16_f32 v52, v72, v73\n"
        "v_cvt_pk_bf16_f32 v53, v74, v75\n"
        "ds_write_b64 v126, v[52:53] offset:1024\n"
        "s_waitcnt vmcnt(2)\n"
        "v_bitop3_b32 v123, v123, v76, v78 bitop3:0x80\n"
        "v_cmp_eq_u32 vcc, 0, v123\n"
        "v_mov_b64 v[122:123], 0\n"
        "v_cvt_f32_i32 v76, v76\n"
        "v_cvt_f32_i32 v77, v77\n"
        "v_cvt_f32_i32 v78, v78\n"
        "v_cvt_f32_i32 v79, v79\n"
        "v_cvt_pk_bf16_f32 v54, v76, v77\n"
        "v_cvt_pk_bf16_f32 v55, v78, v79\n"
        "ds_write_b64 v126, v[54:55] offset:1536\n"
        "L_dec_1%=:\n"
        "s_waitcnt lgkmcnt(2)\n"
        "v_smfmac_f32_16x16x64_bf16 v[92:95], v[48:51], a[144:151], v134\n"
        "ds_read_b128 v[52:55], v124 offset:3072\n"
        "v_max3_f32 v104, |v80|, |v82|, |v64|\n"
        "v_smfmac_f32_16x16x64_bf16 v[96:99], v[48:51], a[152:159], v134\n"
        "s_cbranch_vccnz L_retry_1%=\n"
        "v_max3_f32 v104, |v65|, |v66|, v104\n"
        "v_smfmac_f32_16x16x64_bf16 v[116:119], v[48:51], a[240:247], v134\n"
        "s_add_u32 s29, s29, 1024\n"
        "v_max3_f32 v104, |v67|, |v68|, v104\n"
        "v_smfmac_f32_16x16x64_bf16 v[120:123], v[48:51], a[248:255], v134\n"
        "s_waitcnt lgkmcnt(0)\n"
        "v_max3_f32 v104, |v69|, |v70|, v104\n"
        "s_barrier\n"
        "ds_read_b128 v[56:59], v124 offset:4096\n"
        "v_smfmac_f32_16x16x64_bf16 v[92:95], v[52:55], a[160:167], v134\n"
        "buffer_load_dwordx4 v[88:91], v124, s[36:39], s55 offen offset:3072 sc1\n"
        "ds_read_b128 v[60:63], v124 offset:7168\n"
        "v_max3_f32 v104, |v71|, |v72|, v104\n"
        "v_smfmac_f32_16x16x64_bf16 v[96:99], v[52:55], a[168:175], v134\n"
        "ds_read_b128 v[80:83], v124 offset:5120\n"
        "v_max3_f32 v104, |v73|, |v74|, v104\n"
        "v_smfmac_f32_16x16x64_bf16 v[116:119], v[52:55], v[176:183], v134\n"
        "buffer_load_dwordx4 v[84:87], v124, s[36:39], s55 offen offset:0 sc1\n"
        "ds_read_b128 v[64:67], v124 offset:8192\n"
        "v_max3_f32 v104, |v75|, |v76|, v104\n"
        "v_smfmac_f32_16x16x64_bf16 v[120:123], v[52:55], v[184:191], v134\n"
        "ds_read_b128 v[68:71], v124 offset:0\n"
        "v_max3_f32 v104, |v77|, |v78|, v104\n"
        "s_waitcnt lgkmcnt(4)\n"
        "v_smfmac_f32_16x16x64_bf16 v[92:95], v[56:59], v[0:7], v134\n"
        "ds_read_b128 v[72:75], v124 offset:1024\n"
        "v_max_f32_e64 v104, |v79|, v104\n"
        "v_smfmac_f32_16x16x64_bf16 v[96:99], v[56:59], v[8:15], v134\n"
        "ds_read_b128 v[0:3], v125 offset:12288\n"
        "v_max_f32_dpp v104, v104, v104 row_shr:1 row_mask:0xf bank_mask:0xf\n"
        "s_waitcnt lgkmcnt(3)\n"
        "v_smfmac_f32_16x16x64_bf16 v[92:95], v[60:63], a[224:231], v134\n"
        "ds_read_b128 v[76:79], v124 offset:6144\n"
        "v_max_f32_dpp v104, v104, v104 row_shr:2 row_mask:0xf bank_mask:0xf\n"
        "v_smfmac_f32_16x16x64_bf16 v[96:99], v[60:63], a[232:239], v134\n"
        "ds_read_b128 v[4:7], v125 offset:13312\n"
        "v_max_f32_dpp v104, v104, v104 row_shr:4 row_mask:0xf bank_mask:0xf\n"
        "v_smfmac_f32_16x16x64_bf16 v[92:95], v[80:83], v[16:23], v134\n"
        "ds_read_b128 v[8:11], v125 offset:14336\n"
        "v_max_f32_dpp v104, v104, v104 row_shr:8 row_mask:0xf bank_mask:0xf\n"
        "v_smfmac_f32_16x16x64_bf16 v[96:99], v[80:83], v[24:31], v134\n"
        "ds_read_b128 v[12:15], v125 offset:15360\n"
        "v_max_f32_dpp v104, v104, v104 row_bcast:15 row_mask:0xa bank_mask:0xf\n"
        "v_smfmac_f32_16x16x64_bf16 v[92:95], v[64:67], v[32:39], v134\n"
        "ds_read_b128 v[16:19], v125 offset:16384\n"
        "v_max_f32_dpp v104, v104, v104 row_bcast:31 row_mask:0xc bank_mask:0xf\n"
        "v_smfmac_f32_16x16x64_bf16 v[96:99], v[64:67], v[40:47], v134\n"
        "ds_read_b128 v[20:23], v125 offset:17408\n"
        "v_readlane_b32 s85, v104, 63\n"
        "s_waitcnt lgkmcnt(5)\n"
        "v_smfmac_f32_16x16x64_bf16 v[92:95], v[68:71], a[176:183], v134\n"
        "ds_read_b128 v[24:27], v125 offset:18432\n"
        "v_smfmac_f32_16x16x64_bf16 v[96:99], v[68:71], a[184:191], v134\n"
        "ds_read_b128 v[28:31], v125 offset:19456\n"
        "v_mov_b32 v104, s85\n"
        "v_smfmac_f32_16x16x64_bf16 v[92:95], v[72:75], a[192:199], v134\n"
        "ds_read_b128 v[32:35], v125 offset:20480\n"
        "v_smfmac_f32_16x16x64_bf16 v[96:99], v[72:75], a[200:207], v134\n"
        "ds_read_b128 v[36:39], v125 offset:21504\n"
        "s_add_u32 s69, s58, s82\n"
        "v_smfmac_f32_16x16x64_bf16 v[92:95], v[76:79], a[208:215], v134\n"
        "ds_read_b128 v[40:43], v125 offset:22528\n"
        "s_sub_u32 s72, s58, s82\n"
        "v_smfmac_f32_16x16x64_bf16 v[96:99], v[76:79], a[216:223], v134\n"
        "ds_read_b128 v[44:47], v125 offset:23552\n"
        "v_smfmac_f32_16x16x64_bf16 v[116:119], v[64:67], v[240:247], v134\n"
        "ds_write_b32 v142, v104\n"
        "v_add_f32 v92, v92, v94\n"
        "v_add_f32 v93, v93, v95\n"
        "v_smfmac_f32_16x16x64_bf16 v[120:123], v[64:67], v[248:255], v134\n"
        "v_add_f32 v96, v96, v98\n"
        "v_add_f32 v97, v97, v99\n"
        "v_cvt_pk_bf16_f32 v92, v92, v96\n"
        "v_cvt_pk_bf16_f32 v93, v93, v97\n"
        "s_waitcnt lgkmcnt(3)\n"
        "v_smfmac_f32_16x16x64_bf16 v[116:119], v[68:71], v[0:7], v134\n"
        "ds_write2_b32 v127, v92, v93 offset1:72\n"
        "v_smfmac_f32_16x16x64_bf16 v[120:123], v[68:71], v[8:15], v134\n"
        "v_mov_b64 v[98:99], 0\n"
        "v_smfmac_f32_16x16x64_bf16 v[116:119], v[72:75], v[16:23], v134\n"
        "buffer_store_dwordx4 v[100:103], v124, s[36:39], s54 offen offset:0 sc1\n"
        "v_mov_b64 v[96:97], 0\n"
        "v_smfmac_f32_16x16x64_bf16 v[120:123], v[72:75], v[24:31], v134\n"
        "s_waitcnt vmcnt(4)\n"
        "s_waitcnt lgkmcnt(0)\n"
        "v_mov_b64 v[106:107], 0\n"
        "s_barrier\n"
        "ds_read_b128 v[0:3], v128 offset:0\n"
        "v_smfmac_f32_16x16x64_bf16 v[116:119], v[76:79], v[32:39], v134\n"
        "ds_read_b128 v[24:27], v124 offset:20480\n"
        "v_mov_b64 v[104:105], 0\n"
        "v_smfmac_f32_16x16x64_bf16 v[120:123], v[76:79], v[40:47], v134\n"
        "ds_read_b128 v[4:7], v128 offset:64\n"
        "v_mov_b64 v[94:95], 0\n"
        "v_smfmac_f32_16x16x64_bf16 v[116:119], v[60:63], v[192:199], v134\n"
        "ds_read_b128 v[8:11], v128 offset:128\n"
        "v_mov_b64 v[92:93], 0\n"
        "v_smfmac_f32_16x16x64_bf16 v[120:123], v[60:63], v[200:207], v134\n"
        "ds_read_b128 v[12:15], v128 offset:192\n"
        "v_mov_b64 v[20:21], 0\n"
        "v_smfmac_f32_16x16x64_bf16 v[116:119], v[56:59], v[208:215], v134\n"
        "s_waitcnt lgkmcnt(3)\n"
        "v_max3_f32 v24, v24, v25, v26\n"
        "s_waitcnt lgkmcnt(2)\n"
        "v_smfmac_f32_16x16x64_bf16 v[104:107], v[108:111], v[0:7], v134\n"
        "v_max_f32 v24, v24, v27\n"
        "v_smfmac_f32_16x16x64_bf16 v[120:123], v[56:59], v[216:223], v134\n"
        "v_readfirstlane_b32 s84, v24\n"
        "s_waitcnt lgkmcnt(0)\n"
        "v_smfmac_f32_16x16x64_bf16 v[104:107], v[112:115], v[8:15], v134\n"
        "v_mov_b64 v[22:23], 0\n"
        "v_smfmac_f32_16x16x64_bf16 v[116:119], v[80:83], v[224:231], v134\n"
        "s_lshr_b32 s84, s84, 23\n"
        "s_add_u32 s84, s84, s101\n"
        "v_smfmac_f32_16x16x64_bf16 v[120:123], v[80:83], v[232:239], v134\n"
        "s_max_i32 s84, s84, s76\n"
        "s_min_i32 s84, s84, 186\n"
        "v_add_f32 v104, v104, v106\n"
        "v_add_f32 v105, v105, v107\n"
        "v_smfmac_f32_16x16x64_bf16 v[92:95], v[64:67], a[128:135], v134\n"
        "ds_write_b64 v131, v[104:105]\n"
        "v_add_f32 v116, v116, v118\n"
        "v_add_f32 v117, v117, v119\n"
        "v_pk_add_f32 v[120:121], v[120:121], v[122:123]\n"
        "v_cvt_pk_bf16_f32 v116, v116, v120\n"
        "v_cvt_pk_bf16_f32 v117, v117, v121\n"
        "ds_write2_b32 v129, v116, v117 offset1:8\n"
        "v_smfmac_f32_16x16x64_bf16 v[96:99], v[64:67], a[136:143], v134\n"
        "s_mul_i32 s88, s84, 0x800080\n"
        "s_mov_b32 s89, s88\n"
        "v_smfmac_f32_16x16x64_bf16 v[92:95], v[68:71], a[32:39], v134\n"
        "s_add_u32 s90, s88, s35\n"
        "s_mov_b32 s91, s90\n"
        "v_mov_b64 v[104:105], s[88:89]\n"
        "v_smfmac_f32_16x16x64_bf16 v[96:99], v[68:71], a[40:47], v134\n"
        "buffer_load_dwordx4 v[64:67], v124, s[36:39], s69 offen offset:0 sc1\n"
        "v_mov_b64 v[106:107], s[90:91]\n"
        "v_smfmac_f32_16x16x64_bf16 v[92:95], v[48:51], a[0:7], v134\n"
        "s_waitcnt lgkmcnt(0)\n"
        "v_mov_b64 v[100:101], s[88:89]\n"
        "s_barrier\n"
        "ds_read_b128 v[24:27], v130\n"
        "v_smfmac_f32_16x16x64_bf16 v[96:99], v[48:51], a[8:15], v134\n"
        "ds_read_b128 v[28:31], v130 offset:1024\n"
        "v_mov_b64 v[102:103], s[90:91]\n"
        "v_smfmac_f32_16x16x64_bf16 v[92:95], v[52:55], a[16:23], v134\n"
        "ds_read_b128 v[116:119], v124 offset:13568\n"
        "v_mov_b64 v[48:49], 0\n"
        "v_smfmac_f32_16x16x64_bf16 v[96:99], v[52:55], a[24:31], v134\n"
        "ds_read_b128 v[32:35], v141 offset:0\n"
        "v_mov_b64 v[50:51], 0\n"
        "v_smfmac_f32_16x16x64_bf16 v[92:95], v[56:59], a[96:103], v134\n"
        "ds_read_b128 v[36:39], v141 offset:1024\n"
        "v_mov_b64 v[52:53], 0\n"
        "v_smfmac_f32_16x16x64_bf16 v[96:99], v[56:59], a[104:111], v134\n"
        "ds_read_b128 v[40:43], v141 offset:2048\n"
        "v_mov_b64 v[54:55], 0\n"
        "s_waitcnt lgkmcnt(5)\n"
        "v_smfmac_f32_16x16x64_bf16 v[20:23], v[24:27], v[0:7], v134\n"
        "ds_read_b128 v[44:47], v141 offset:3072\n"
        "v_mov_b64 v[56:57], 0\n"
        "s_waitcnt lgkmcnt(4)\n"
        "v_smfmac_f32_16x16x64_bf16 v[20:23], v[28:31], v[8:15], v134\n"
        "ds_read_b128 v[0:3], v125 offset:24576\n"
        "v_max3_f32 v121, v116, v117, v118\n"
        "v_smfmac_f32_16x16x64_bf16 v[92:95], v[72:75], a[48:55], v134\n"
        "buffer_load_dwordx4 v[68:71], v124, s[36:39], s69 offen offset:1024 sc1\n"
        "v_mov_b64 v[58:59], 0\n"
        "v_smfmac_f32_16x16x64_bf16 v[96:99], v[72:75], a[56:63], v134\n"
        "ds_read_b128 v[4:7], v125 offset:25600\n"
        "v_add_f32 v20, v20, v22\n"
        "v_add_f32 v21, v21, v23\n"
        "v_cndmask_b32 v120, v20, v21, s[92:93]\n"
        "v_mul_f32 v120, s74, v120\n"
        "v_max3_f32 v121, v121, v119, v120\n"
        "v_smfmac_f32_16x16x64_bf16 v[92:95], v[76:79], a[64:71], v134\n"
        "buffer_load_dwordx4 v[72:75], v124, s[36:39], s72 offen offset:2048 sc1\n"
        "v_max_f32_dpp v121, v121, v121 quad_perm:[2,3,0,1] row_mask:0xf bank_mask:0xf\n"
        "v_mov_b32 v122, v121\n"
        "v_smfmac_f32_16x16x64_bf16 v[96:99], v[76:79], a[72:79], v134\n"
        "ds_read_b128 v[8:11], v125 offset:26624\n"
        "v_permlane16_swap_b32 v121, v122\n"
        "v_max_f32 v121, v121, v122\n"
        "v_mov_b32 v122, v121\n"
        "v_smfmac_f32_16x16x64_bf16 v[92:95], v[80:83], a[112:119], v134\n"
        "buffer_load_dwordx4 v[76:79], v124, s[36:39], s72 offen offset:3072 sc1\n"
        "v_permlane32_swap_b32 v121, v122\n"
        "v_smfmac_f32_16x16x64_bf16 v[96:99], v[80:83], a[120:127], v134\n"
        "ds_read_b128 v[12:15], v125 offset:27648\n"
        "v_max_f32 v121, v121, v122\n"
        "v_mul_f32_e64 v123, -s73, v121\n"
        "v_fma_f32 v120, v120, s73, v123\n"
        "v_fma_f32 v116, v116, s73, v123\n"
        "v_smfmac_f32_16x16x64_bf16 v[92:95], v[60:63], a[80:87], v134\n"
        "v_fma_f32 v117, v117, s73, v123\n"
        "v_fma_f32 v118, v118, s73, v123\n"
        "v_fma_f32 v119, v119, s73, v123\n"
        "v_exp_f32 v120, v120\n"
        "v_smfmac_f32_16x16x64_bf16 v[96:99], v[60:63], a[88:95], v134\n"
        "ds_read_b128 v[16:19], v125 offset:28672\n"
        "v_exp_f32 v116, v116\n"
        "v_mul_f32 v120, s74, v120\n"
        "v_exp_f32 v117, v117\n"
        "v_exp_f32 v118, v118\n"
        "v_exp_f32 v119, v119\n"
        "s_waitcnt vmcnt(5)\n"
        "v_mov_b64 v[108:109], v[86:87]\n"
        "v_mov_b64 v[112:113], v[90:91]\n"
        "v_cvt_pk_bf16_f32 v120, v120, v120\n"
        "v_cvt_pk_bf16_f32 v60, v116, v117\n"
        "v_cvt_pk_bf16_f32 v62, v118, v119\n"
        "v_and_b32 v61, v120, v135\n"
        "v_and_b32 v63, v120, v136\n"
        "v_pk_add_f32 v[92:93], v[92:93], v[94:95]\n"
        "v_pk_add_f32 v[96:97], v[96:97], v[98:99]\n"
        "v_smfmac_f32_16x16x64_bf16 v[56:59], v[60:63], v[100:107], v134\n"
        "ds_read_b128 v[20:23], v125 offset:29696\n"
        "v_cvt_pk_bf16_f32 v86, v92, v93\n"
        "v_cvt_pk_bf16_f32 v87, v92, v93\n"
        "v_mov_b64 v[90:91], v[86:87]\n"
        "v_cvt_pk_bf16_f32 v110, v96, v97\n"
        "v_cvt_pk_bf16_f32 v111, v96, v97\n"
        "v_smfmac_f32_16x16x64_bf16 v[48:51], v[60:63], v[84:91], v134\n"
        "ds_read_b128 v[24:27], v125 offset:30720\n"
        "v_mov_b64 v[114:115], v[110:111]\n"
        "v_add_f32 v56, v56, v58\n"
        "v_add_f32 v57, v57, v59\n"
        "v_smfmac_f32_16x16x64_bf16 v[52:55], v[60:63], v[108:115], v134\n"
        "ds_read_b128 v[28:31], v125 offset:31744\n"
        "s_nop 0\n"
        "v_rcp_f32 v56, v56\n"
        "v_rcp_f32 v57, v57\n"
        "v_pk_add_f32 v[48:49], v[48:49], v[50:51]\n"
        "v_pk_mul_f32 v[48:49], v[48:49], v[56:57]\n"
        "v_mov_b32 v110, v86\n"
        "v_mov_b64 v[92:93], s[70:71]\n"
        "v_pk_add_f32 v[52:53], v[52:53], v[54:55]\n"
        "v_pk_mul_f32 v[52:53], v[52:53], v[56:57]\n"
        "v_cvt_pk_bf16_f32 v48, v48, v52\n"
        "v_cvt_pk_bf16_f32 v49, v49, v53\n"
        "ds_write2_b32 v139, v48, v49 offset1:4\n"
        "v_mov_b64 v[94:95], 0\n"
        "v_mov_b64 v[96:97], 0\n"
        "v_mov_b64 v[98:99], 0\n"
        "buffer_store_dwordx2 v[110:111], v126, s[36:39], s60 offen sc1\n"
        "v_mov_b64 v[100:101], s[70:71]\n"
        "v_mov_b64 v[102:103], 0\n"
        "v_mov_b64 v[104:105], 0\n"
        "s_waitcnt lgkmcnt(0)\n"
        "v_mov_b64 v[106:107], 0\n"
        "s_barrier\n"
        "ds_read_b128 v[108:111], v124 offset:18432\n"
        "v_mov_b64 v[118:119], 0\n"
        "ds_read_b128 v[112:115], v124 offset:19456\n"
        "v_mov_b64 v[118:119], 0\n"
        "s_nop 0\n"
        "v_mov_b64 v[116:117], s[78:79]\n"
        "s_nop 0\n"
        "v_mov_b64 v[118:119], 0\n"
        "s_nop 0\n"
        "v_mov_b64 v[116:117], s[78:79]\n"
        "s_nop 0\n"
        "s_waitcnt lgkmcnt(1)\n"
        "v_smfmac_f32_16x16x64_bf16 v[116:119], v[108:111], v[32:39], v134\n"
        "s_waitcnt lgkmcnt(0)\n"
        "v_smfmac_f32_16x16x64_bf16 v[116:119], v[112:115], v[40:47], v134\n"
        "v_smfmac_f32_16x16x64_bf16 v[92:95], v[108:111], v[144:151], v134\n"
        "v_smfmac_f32_16x16x64_bf16 v[96:99], v[108:111], v[152:159], v134\n"
        "v_add_f32 v116, v116, v118\n"
        "v_add_f32 v117, v117, v119\n"
        "v_smfmac_f32_16x16x64_bf16 v[92:95], v[112:115], v[160:167], v134\n"
        "buffer_load_dword v140, v124, s[36:39], s29 offen sc1\n"
        "v_cvt_rpi_i32_f32 v118, v116\n"
        "v_bfi_b32 v118, 63, 2, v118\n"
        "v_cvt_rpi_i32_f32 v119, v117\n"
        "v_smfmac_f32_16x16x64_bf16 v[96:99], v[112:115], v[168:175], v134\n"
        "buffer_atomic_add_x2 v[118:119], v133, s[36:39], s52 offen\n"
        "v_smfmac_f32_16x16x64_bf16 v[100:103], v[108:111], v[0:7], v134\n"
        "v_add_f32 v92, v92, v94\n"
        "v_add_f32 v93, v93, v95\n"
        "v_cvt_rpi_i32_f32 v94, v92\n"
        "v_smfmac_f32_16x16x64_bf16 v[104:107], v[108:111], v[8:15], v134\n"
        "v_bfi_b32 v94, 63, 1, v94\n"
        "v_add_f32 v96, v96, v98\n"
        "v_smfmac_f32_16x16x64_bf16 v[100:103], v[112:115], v[16:23], v134\n"
        "v_cvt_rpi_i32_f32 v95, v96\n"
        "v_smfmac_f32_16x16x64_bf16 v[104:107], v[112:115], v[24:31], v134\n"
        "buffer_atomic_add_x2 v[94:95], v133, s[36:39], s51 offen\n"
        "v_add_f32 v97, v97, v99\n"
        "v_cvt_rpi_i32_f32 v98, v93\n"
        "v_bfi_b32 v98, 63, 1, v98\n"
        "v_cvt_rpi_i32_f32 v99, v97\n"
        "s_waitcnt vmcnt(7)\n"
        "v_mfma_f32_16x16x32_bf16 v[84:87], v[64:67], a[0:3], 0\n"
        "buffer_atomic_add_x2 v[98:99], v133, s[36:39], s66 offen\n"
        "v_add_f32 v100, v100, v102\n"
        "v_add_f32 v101, v101, v103\n"
        "v_add_f32 v104, v104, v106\n"
        "v_cvt_rpi_i32_f32 v102, v100\n"
        "v_bfi_b32 v102, 63, 1, v102\n"
        "v_cvt_rpi_i32_f32 v103, v104\n"
        "v_mfma_f32_16x16x32_bf16 v[88:91], v[64:67], a[8:11], 0\n"
        "buffer_atomic_add_x2 v[102:103], v133, s[36:39], s67 offen\n"
        "v_add_f32 v105, v105, v107\n"
        "v_cvt_rpi_i32_f32 v106, v101\n"
        "v_bfi_b32 v106, 63, 1, v106\n"
        "v_cvt_rpi_i32_f32 v107, v105\n"
        "v_mfma_f32_16x16x32_bf16 v[56:59], a[144:147], v[64:67], 0\n"
        "buffer_atomic_add_x2 v[106:107], v133, s[36:39], s68 offen\n"
        "L_win_1%=:\n"
        "v_mfma_f32_16x16x32_bf16 v[60:63], a[152:155], v[64:67], 0\n"
        "v_mfma_f32_16x16x32_bf16 v[48:51], a[240:243], v[64:67], 0\n"
        "v_mfma_f32_16x16x32_bf16 v[52:55], a[248:251], v[64:67], 0\n"
        "ds_write_b128 v124, v[64:67] offset:4096\n"
        "s_waitcnt vmcnt(9)\n"
        "v_mfma_f32_16x16x32_bf16 v[84:87], v[68:71], a[4:7], v[84:87]\n"
        "v_mfma_f32_16x16x32_bf16 v[88:91], v[68:71], a[12:15], v[88:91]\n"
        "ds_write_b128 v124, v[68:71] offset:5120\n"
        "v_mfma_f32_16x16x32_bf16 v[56:59], a[148:151], v[68:71], v[56:59]\n"
        "v_mfma_f32_16x16x32_bf16 v[60:63], a[156:159], v[68:71], v[60:63]\n"
        "s_waitcnt vmcnt(8)\n"
        "ds_write_b128 v124, v[72:75] offset:6144\n"
        "v_mfma_f32_16x16x32_bf16 v[48:51], a[244:247], v[68:71], v[48:51]\n"
        "v_mfma_f32_16x16x32_bf16 v[52:55], a[252:255], v[68:71], v[52:55]\n"
        "s_waitcnt vmcnt(7)\n"
        "ds_write_b128 v124, v[76:79] offset:7168\n"
        "v_mfma_f32_16x16x32_bf16 v[84:87], v[72:75], a[16:19], v[84:87]\n"
        "v_mfma_f32_16x16x32_bf16 v[88:91], v[72:75], a[24:27], v[88:91]\n"
        "v_mfma_f32_16x16x32_bf16 v[56:59], a[160:163], v[72:75], v[56:59]\n"
        "v_mfma_f32_16x16x32_bf16 v[60:63], a[168:171], v[72:75], v[60:63]\n"
        "v_mfma_f32_16x16x32_bf16 v[48:51], v[176:179], v[72:75], v[48:51]\n"
        "ds_read_b128 v[0:3], v125 offset:12288\n"
        "v_mfma_f32_16x16x32_bf16 v[52:55], v[184:187], v[72:75], v[52:55]\n"
        "ds_read_b128 v[8:11], v125 offset:14336\n"
        "v_mfma_f32_16x16x32_bf16 v[84:87], v[76:79], a[20:23], v[84:87]\n"
        "s_waitcnt lgkmcnt(2)\n"
        "s_barrier\n"
        "v_mfma_f32_16x16x32_bf16 v[88:91], v[76:79], a[28:31], v[88:91]\n"
        "ds_read_b128 v[92:95], v124 offset:0\n"
        "v_mov_b32 v116, s62\n"
        "v_mfma_f32_16x16x32_bf16 v[56:59], a[164:167], v[76:79], v[56:59]\n"
        "s_add_u32 s49, s49, 163840\n"
        "ds_read_b128 v[104:107], v124 offset:1024\n"
        "v_mfma_f32_16x16x32_bf16 v[60:63], a[172:175], v[76:79], v[60:63]\n"
        "s_add_u32 s50, s50, 163840\n"
        "ds_read_b128 v[4:7], v125 offset:13312\n"
        "v_mfma_f32_16x16x32_bf16 v[48:51], v[180:183], v[76:79], v[48:51]\n"
        "s_xor_b32 s49, s49, s86\n"
        "ds_read_b128 v[12:15], v125 offset:15360\n"
        "v_mfma_f32_16x16x32_bf16 v[52:55], v[188:191], v[76:79], v[52:55]\n"
        "s_xor_b32 s65, s49, 8192\n"
        "s_add_u32 s56, s56, 786432\n"
        "s_waitcnt lgkmcnt(3)\n"
        "v_mfma_f32_16x16x32_bf16 v[84:87], v[92:95], a[32:35], v[84:87]\n"
        "s_add_u32 s64, s64, 786432\n"
        "ds_read_b128 v[96:99], v124 offset:2048\n"
        "v_mfma_f32_16x16x32_bf16 v[88:91], v[92:95], a[40:43], v[88:91]\n"
        "ds_read_b128 v[100:103], v124 offset:3072\n"
        "v_mfma_f32_16x16x32_bf16 v[56:59], a[176:179], v[92:95], v[56:59]\n"
        "ds_read_b128 v[16:19], v125 offset:16384\n"
        "v_mfma_f32_16x16x32_bf16 v[60:63], a[184:187], v[92:95], v[60:63]\n"
        "s_add_u32 s51, s51, 163840\n"
        "ds_read_b128 v[20:23], v125 offset:17408\n"
        "v_mfma_f32_16x16x32_bf16 v[48:51], v[0:3], v[92:95], v[48:51]\n"
        "s_add_u32 s52, s52, 163840\n"
        "ds_read_b128 v[24:27], v125 offset:18432\n"
        "v_mfma_f32_16x16x32_bf16 v[52:55], v[8:11], v[92:95], v[52:55]\n"
        "ds_read_b128 v[28:31], v125 offset:19456\n"
        "s_waitcnt lgkmcnt(8)\n"
        "v_mfma_f32_16x16x32_bf16 v[84:87], v[104:107], a[36:39], v[84:87]\n"
        "s_add_u32 s83, s83, 163840\n"
        "ds_read_b128 v[92:95], v124 offset:12288\n"
        "v_mfma_f32_16x16x32_bf16 v[88:91], v[104:107], a[44:47], v[88:91]\n"
        "s_xor_b32 s86, s83, s46\n"
        "s_and_b32 s86, s86, s53\n"
        "s_lshr_b32 s86, s86, 8\n"
        "v_mfma_f32_16x16x32_bf16 v[56:59], a[180:183], v[104:107], v[56:59]\n"
        "s_waitcnt vmcnt(6)\n"
        "s_mov_b64 exec, 1\n"
        "buffer_atomic_umax v116, off, s[36:39], s61\n"
        "s_mov_b64 exec, -1\n"
        "v_mfma_f32_16x16x32_bf16 v[60:63], a[188:191], v[104:107], v[60:63]\n"
        "s_cmp_eq_u32 s44, 63\n"
        "s_cbranch_scc1 L_end%=\n"
        "s_waitcnt lgkmcnt(8)\n"
        "v_mfma_f32_16x16x32_bf16 v[48:51], v[4:7], v[104:107], v[48:51]\n"
        "s_xor_b32 s86, s86, s34\n"
        "ds_read_b128 v[32:35], v125 offset:20480\n"
        "s_waitcnt lgkmcnt(8)\n"
        "v_mfma_f32_16x16x32_bf16 v[52:55], v[12:15], v[104:107], v[52:55]\n"
        "ds_read_b128 v[36:39], v125 offset:21504\n"
        "s_waitcnt lgkmcnt(8)\n"
        "v_mfma_f32_16x16x32_bf16 v[84:87], v[96:99], a[48:51], v[84:87]\n"
        "s_xor_b32 s34, s34, s86\n"
        "ds_read_b128 v[104:107], v124 offset:13312\n"
        "v_mfma_f32_16x16x32_bf16 v[88:91], v[96:99], a[56:59], v[88:91]\n"
        "s_xor_b32 s51, s51, s86\n"
        "ds_read_b128 v[40:43], v125 offset:22528\n"
        "v_mfma_f32_16x16x32_bf16 v[56:59], a[192:195], v[96:99], v[56:59]\n"
        "s_add_u32 s55, s55, 786432\n"
        "ds_read_b128 v[44:47], v125 offset:23552\n"
        "v_mfma_f32_16x16x32_bf16 v[60:63], a[200:203], v[96:99], v[60:63]\n"
        "ds_read_b128 v[0:3], v125 offset:0\n"
        "s_waitcnt vmcnt(6)\n"
        "v_cmp_eq_u32 vcc, 0, v140\n"
        "s_waitcnt lgkmcnt(10)\n"
        "v_mfma_f32_16x16x32_bf16 v[48:51], v[16:19], v[96:99], v[48:51]\n"
        "s_cbranch_vccnz L_sw0_1%=\n"
        "L_sok_1%=:\n"
        "s_waitcnt lgkmcnt(8)\n"
        "v_mfma_f32_16x16x32_bf16 v[52:55], v[24:27], v[96:99], v[52:55]\n"
        "v_readlane_b32 s81, v140, s99\n"
        "v_max_u32_dpp v140, v140, v140 row_shr:1 row_mask:0xf bank_mask:0xf\n"
        "v_mfma_f32_16x16x32_bf16 v[84:87], v[100:103], a[52:55], v[84:87]\n"
        "ds_read_b128 v[96:99], v124 offset:14336\n"
        "v_max_u32_dpp v140, v140, v140 row_shr:2 row_mask:0xf bank_mask:0xf\n"
        "v_mfma_f32_16x16x32_bf16 v[88:91], v[100:103], a[60:63], v[88:91]\n"
        "ds_read_b128 v[4:7], v125 offset:1024\n"
        "v_max_u32_dpp v140, v140, v140 row_shr:4 row_mask:0xf bank_mask:0xf\n"
        "v_mfma_f32_16x16x32_bf16 v[56:59], a[196:199], v[100:103], v[56:59]\n"
        "buffer_load_dwordx4 v[108:111], v132, s[36:39], s56 offen offset:0 sc1\n"
        "v_max_u32_dpp v140, v140, v140 row_shr:8 row_mask:0xf bank_mask:0xf\n"
        "v_mfma_f32_16x16x32_bf16 v[60:63], a[204:207], v[100:103], v[60:63]\n"
        "ds_read_b128 v[8:11], v125 offset:2048\n"
        "v_max_u32_dpp v140, v140, v140 row_bcast:15 row_mask:0xa bank_mask:0xf\n"
        "v_mfma_f32_16x16x32_bf16 v[48:51], v[20:23], v[100:103], v[48:51]\n"
        "buffer_load_dwordx4 v[112:115], v132, s[36:39], s64 offen offset:0 sc1\n"
        "v_max_u32_dpp v140, v140, v140 row_bcast:31 row_mask:0xc bank_mask:0xf\n"
        "s_waitcnt lgkmcnt(10)\n"
        "v_mfma_f32_16x16x32_bf16 v[52:55], v[28:31], v[100:103], v[52:55]\n"
        "v_readlane_b32 s33, v140, 63\n"
        "s_lshl1_add_u32 s28, s28, s81\n"
        "s_waitcnt lgkmcnt(9)\n"
        "v_mfma_f32_16x16x32_bf16 v[84:87], v[92:95], a[64:67], v[84:87]\n"
        "ds_read_b128 v[100:103], v124 offset:15360\n"
        "s_add_u32 s28, s28, 36\n"
        "s_sub_i32 s33, s33, s28\n"
        "v_mfma_f32_16x16x32_bf16 v[88:91], v[92:95], a[72:75], v[88:91]\n"
        "s_min_i32 s33, s33, 500\n"
        "s_mov_b32 s28, 0\n"
        "L_dl_1%=:\n"
        "s_sub_i32 s33, s33, 3\n"
        "s_cmp_gt_i32 s33, 0\n"
        "s_cbranch_scc1 L_dl_1%=\n"
        "v_mfma_f32_16x16x32_bf16 v[56:59], a[208:211], v[92:95], v[56:59]\n"
        "s_add_u32 s54, s54, 786432\n"
        "ds_read_b128 v[12:15], v125 offset:3072\n"
        "v_mfma_f32_16x16x32_bf16 v[60:63], a[216:219], v[92:95], v[60:63]\n"
        "s_add_u32 s60, s60, 131072\n"
        "buffer_load_dwordx4 v[80:83], v138, s[36:39], s50 offen offset:0 sc1\n"
        "s_waitcnt lgkmcnt(10)\n"
        "v_mfma_f32_16x16x32_bf16 v[48:51], v[32:35], v[92:95], v[48:51]\n"
        "s_add_u32 s58, s58, 36864\n"
        "ds_read_b128 v[16:19], v125 offset:4096\n"
        "s_waitcnt lgkmcnt(8)\n"
        "v_mfma_f32_16x16x32_bf16 v[52:55], v[40:43], v[92:95], v[52:55]\n"
        "buffer_load_dwordx4 v[64:67], v138, s[36:39], s49 offen offset:0 sc1\n"
        "v_mfma_f32_16x16x32_bf16 v[84:87], v[104:107], a[68:71], v[84:87]\n"
        "s_xor_b32 s66, s51, 1024\n"
        "ds_read_b128 v[92:95], v124 offset:8192\n"
        "v_mfma_f32_16x16x32_bf16 v[88:91], v[104:107], a[76:79], v[88:91]\n"
        "s_xor_b32 s67, s51, 8192\n"
        "buffer_load_dwordx4 v[68:71], v138, s[36:39], s49 offen offset:256 sc1\n"
        "v_mfma_f32_16x16x32_bf16 v[56:59], a[212:215], v[104:107], v[56:59]\n"
        "s_xor_b32 s68, s51, 9216\n"
        "ds_read_b128 v[20:23], v125 offset:5120\n"
        "v_mfma_f32_16x16x32_bf16 v[60:63], a[220:223], v[104:107], v[60:63]\n"
        "s_sub_u32 s75, 127, s84\n"
        "buffer_load_dwordx4 v[72:75], v138, s[36:39], s65 offen offset:0 sc1\n"
        "v_mfma_f32_16x16x32_bf16 v[48:51], v[36:39], v[104:107], v[48:51]\n"
        "s_sub_u32 s101, s77, s75\n"
        "ds_read_b128 v[24:27], v125 offset:6144\n"
        "s_waitcnt lgkmcnt(10)\n"
        "v_mfma_f32_16x16x32_bf16 v[52:55], v[44:47], v[104:107], v[52:55]\n"
        "buffer_load_dwordx4 v[76:79], v138, s[36:39], s65 offen offset:256 sc1\n"
        "s_waitcnt lgkmcnt(8)\n"
        "v_mfma_f32_16x16x32_bf16 v[84:87], v[96:99], a[80:83], v[84:87]\n"
        "s_mul_i32 s35, s75, 0x800080\n"
        "ds_read_b128 v[104:107], v124 offset:9216\n"
        "v_mfma_f32_16x16x32_bf16 v[88:91], v[96:99], a[88:91], v[88:91]\n"
        "s_lshl_b32 s80, s75, 23\n"
        "ds_read_b128 v[28:31], v125 offset:7168\n"
        "v_mfma_f32_16x16x32_bf16 v[56:59], a[224:227], v[96:99], v[56:59]\n"
        "s_sub_u32 s73, 0x3e0293ee, s80\n"
        "ds_read_b128 v[32:35], v125 offset:8192\n"
        "v_mfma_f32_16x16x32_bf16 v[60:63], a[232:235], v[96:99], v[60:63]\n"
        "s_sub_u32 s74, 0x3f800000, s80\n"
        "ds_read_b128 v[36:39], v125 offset:9216\n"
        "v_mfma_f32_16x16x32_bf16 v[48:51], v[192:195], v[96:99], v[48:51]\n"
        "s_add_u32 s80, s75, 128\n"
        "ds_read_b128 v[40:43], v125 offset:10240\n"
        "v_mfma_f32_16x16x32_bf16 v[52:55], v[200:203], v[96:99], v[52:55]\n"
        "ds_read_b128 v[44:47], v125 offset:11264\n"
        "s_waitcnt lgkmcnt(11)\n"
        "v_mfma_f32_16x16x32_bf16 v[84:87], v[100:103], a[84:87], v[84:87]\n"
        "s_lshl_b32 s80, s80, 8\n"
        "ds_read_b128 v[96:99], v124 offset:10240\n"
        "v_mfma_f32_16x16x32_bf16 v[88:91], v[100:103], a[92:95], v[88:91]\n"
        "s_add_u32 s44, s44, 1\n"
        "s_add_u32 s61, s61, 32\n"
        "s_add_u32 s62, s80, 1\n"
        "v_mfma_f32_16x16x32_bf16 v[56:59], a[228:231], v[100:103], v[56:59]\n"
        "s_branch L_rot_1%=\n"
        "L_light_2%=:\n"
        "v_mfma_f32_16x16x32_bf16 v[52:55], v[252:255], v[104:107], v[52:55]\n"
        "v_mfma_f32_16x16x32_bf16 v[84:87], v[96:99], a[112:115], v[84:87]\n"
        "v_mfma_f32_16x16x32_bf16 v[88:91], v[96:99], a[120:123], v[88:91]\n"
        "v_cvt_pk_bf16_f32 v48, v48, v52\n"
        "v_cvt_pk_bf16_f32 v49, v49, v53\n"
        "v_mfma_f32_16x16x32_bf16 v[84:87], v[100:103], a[116:119], v[84:87]\n"
        "v_cvt_pk_bf16_f32 v50, v50, v54\n"
        "v_cvt_pk_bf16_f32 v51, v51, v55\n"
        "v_mfma_f32_16x16x32_bf16 v[88:91], v[100:103], a[124:127], v[88:91]\n"
        "v_mfma_f32_16x16x32_bf16 v[84:87], v[92:95], a[128:131], v[84:87]\n"
        "v_mfma_f32_16x16x32_bf16 v[88:91], v[92:95], a[136:139], v[88:91]\n"
        "buffer_store_dwordx4 v[48:51], v124, s[36:39], s54 offen offset:1024 sc1\n"
        "v_mfma_f32_16x16x32_bf16 v[84:87], v[104:107], a[132:135], v[84:87]\n"
        "v_mfma_f32_16x16x32_bf16 v[88:91], v[104:107], a[140:143], v[88:91]\n"
        "v_mfma_f32_16x16x32_bf16 v[56:59], v[32:35], v[92:95], v[56:59]\n"
        "v_mfma_f32_16x16x32_bf16 v[60:63], v[40:43], v[92:95], v[60:63]\n"
        "v_cvt_pk_bf16_f32 v84, v84, v85\n"
        "v_cvt_pk_bf16_f32 v85, v86, v87\n"
        "v_mfma_f32_16x16x32_bf16 v[56:59], v[16:19], v[96:99], v[56:59]\n"
        "v_cvt_pk_bf16_f32 v86, v88, v89\n"
        "v_cvt_pk_bf16_f32 v87, v90, v91\n"
        "v_mfma_f32_16x16x32_bf16 v[60:63], v[24:27], v[96:99], v[60:63]\n"
        "v_mfma_f32_16x16x32_bf16 v[56:59], v[20:23], v[100:103], v[56:59]\n"
        "v_mov_b64 v[92:93], 0\n"
        "v_mov_b64 v[94:95], 0\n"
        "v_mfma_f32_16x16x32_bf16 v[60:63], v[28:31], v[100:103], v[60:63]\n"
        "buffer_store_dwordx4 v[84:87], v124, s[36:39], s54 offen offset:2048 sc1\n"
        "v_mov_b64 v[96:97], 0\n"
        "v_mfma_f32_16x16x32_bf16 v[56:59], v[36:39], v[104:107], v[56:59]\n"
        "v_mov_b64 v[98:99], 0\n"
        "v_mfma_f32_16x16x32_bf16 v[60:63], v[44:47], v[104:107], v[60:63]\n"
        "s_nop 7\n"
        "v_cvt_pk_bf16_f32 v100, v56, v60\n"
        "v_cvt_pk_bf16_f32 v101, v57, v61\n"
        "v_cvt_pk_bf16_f32 v102, v58, v62\n"
        "v_cvt_pk_bf16_f32 v103, v59, v63\n"
        "buffer_store_dwordx4 v[100:103], v124, s[36:39], s54 offen offset:0 sc1\n"
        "s_barrier\n"
        "s_add_u32 s69, s58, s82\n"
        "s_sub_u32 s72, s58, s82\n"
        "s_add_u32 s87, s58, 8192\n"
        "s_waitcnt vmcnt(6)\n"
        "buffer_load_dwordx4 v[64:67], v124, s[36:39], s69 offen offset:0 sc1\n"
        "s_waitcnt vmcnt(6)\n"
        "buffer_load_dwordx4 v[68:71], v124, s[36:39], s69 offen offset:1024 sc1\n"
        "s_waitcnt vmcnt(6)\n"
        "buffer_load_dwordx4 v[72:75], v124, s[36:39], s72 offen offset:2048 sc1\n"
        "s_waitcnt vmcnt(6)\n"
        "buffer_load_dwordx4 v[76:79], v124, s[36:39], s72 offen offset:3072 sc1\n"
        "buffer_load_dwordx4 v[80:83], v124, s[36:39], s87 offen offset:0 sc1\n"
        "s_mov_b32 s81, 64\n"
        "buffer_load_dword v107, off, s[36:39], s81 sc1\n"
        "s_memrealtime s[26:27]\n"
        "s_waitcnt vmcnt(6)\n"
        "s_waitcnt lgkmcnt(0)\n"
        "s_barrier\n"
        "s_add_u32 s29, s29, 1024\n"
        "s_waitcnt vmcnt(0)\n"
        "L_par_2%=:\n"
        "v_readfirstlane_b32 s80, v107\n"
        "s_nop 0\n"
        "s_cmp_lt_i32 s80, 0\n"
        "s_cbranch_scc1 L_par2_2%=\n"
        "s_add_u32 s63, s63, 1\n"
        "s_cmp_gt_u32 s63, 0x40000\n"
        "s_cbranch_scc1 L_abort%=\n"
        "s_sleep 4\n"
        "buffer_load_dword v107, off, s[36:39], s81 sc1\n"
        "s_waitcnt vmcnt(0)\n"
        "s_branch L_par_2%=\n"
        "L_par2_2%=:\n"
        "s_bfe_u32 s46, s80, 0x10018\n"
        "s_lshl_b32 s46, s46, 21\n"
        "s_mov_b32 s53, 0x200000\n"
        "s_bfe_u32 s84, s80, 0x80010\n"
        "s_sub_u32 s84, 255, s84\n"
        "s_bfe_u32 s76, s80, 0x80008\n"
        "s_sub_u32 s76, s76, 32\n"
        "s_max_i32 s76, s76, 66\n"
        "s_bfe_u32 s77, s80, 0x80000\n"
        "s_sub_u32 s77, s77, 158\n"
        "s_bitset0_b32 s49, 30\n"
        "s_bitset0_b32 s50, 30\n"
        "s_bitset0_b32 s56, 30\n"
        "s_bitset0_b32 s64, 30\n"
        "v_mov_b32 v140, 0\n"
        "s_mov_b32 s28, 1000\n"
        "s_branch L_xok_2%=\n"
        "L_x2_2%=:\n"
        "s_add_u32 s69, s58, s82\n"
        "s_sub_u32 s72, s58, s82\n"
        "s_add_u32 s87, s58, 8192\n"
        "buffer_load_dwordx4 v[64:67], v124, s[36:39], s69 offen offset:0 sc1\n"
        "buffer_load_dwordx4 v[68:71], v124, s[36:39], s69 offen offset:1024 sc1\n"
        "buffer_load_dwordx4 v[72:75], v124, s[36:39], s72 offen offset:2048 sc1\n"
        "buffer_load_dwordx4 v[76:79], v124, s[36:39], s72 offen offset:3072 sc1\n"
        "buffer_load_dwordx4 v[80:83], v124, s[36:39], s87 offen offset:0 sc1\n"
        "s_waitcnt vmcnt(0)\n"
        "s_memrealtime s[26:27]\n"
        "s_waitcnt lgkmcnt(0)\n"
        "s_add_u32 s28, s28, s26\n"
        "L_xok_2%=:\n"
        "v_mfma_f32_16x16x32_bf16 v[84:87], v[64:67], a[0:3], 0\n"
        "v_mfma_f32_16x16x32_bf16 v[88:91], v[64:67], a[8:11], 0\n"
        "v_mfma_f32_16x16x32_bf16 v[56:59], a[144:147], v[64:67], 0\n"
        "s_branch L_win_2%=\n"
        "L_retry_2%=:\n"
        "s_add_u32 s63, s63, 1\n"
        "s_cmp_gt_u32 s63, 0x40000\n"
        "s_cbranch_scc1 L_abort%=\n"
        "s_sleep 2\n"
        "s_waitcnt vmcnt(0)\n"
        "s_waitcnt lgkmcnt(0)\n"
        "buffer_load_dwordx4 v[80:83], v138, s[36:39], s50 offen offset:0 sc1\n"
        "buffer_load_dwordx4 v[64:67], v138, s[36:39], s49 offen offset:0 sc1\n"
        "buffer_load_dwordx4 v[68:71], v138, s[36:39], s49 offen offset:256 sc1\n"
        "buffer_load_dwordx4 v[72:75], v138, s[36:39], s65 offen offset:0 sc1\n"
        "buffer_load_dwordx4 v[76:79], v138, s[36:39], s65 offen offset:256 sc1\n"
        "s_waitcnt vmcnt(4)\n"
        "v_bitop3_b32 v123, 32, v80, v82 bitop3:0x80\n"
        "v_add_u32_dpp v80, v81, v80 row_half_mirror row_mask:0xf bank_mask:0xf\n"
        "v_add_u32_dpp v82, v83, v82 row_half_mirror row_mask:0xf bank_mask:0xf\n"
        "v_cvt_f32_i32 v80, v80\n"
        "v_cvt_f32_i32 v82, v82\n"
        "v_cvt_pk_bf16_f32 v52, v80, v82\n"
        "ds_write_b32 v137, v52\n"
        "s_waitcnt vmcnt(3)\n"
        "v_bitop3_b32 v123, v123, v64, v66 bitop3:0x80\n"
        "v_cvt_f32_i32 v64, v64\n"
        "v_cvt_f32_i32 v65, v65\n"
        "v_cvt_f32_i32 v66, v66\n"
        "v_cvt_f32_i32 v67, v67\n"
        "v_cvt_pk_bf16_f32 v52, v64, v65\n"
        "v_cvt_pk_bf16_f32 v53, v66, v67\n"
        "ds_write_b64 v126, v[52:53] offset:0\n"
        "s_waitcnt vmcnt(2)\n"
        "v_bitop3_b32 v123, v123, v68, v70 bitop3:0x80\n"
        "v_cvt_f32_i32 v68, v68\n"
        "v_cvt_f32_i32 v69, v69\n"
        "v_cvt_f32_i32 v70, v70\n"
        "v_cvt_f32_i32 v71, v71\n"
        "v_cvt_pk_bf16_f32 v54, v68, v69\n"
        "v_cvt_pk_bf16_f32 v55, v70, v71\n"
        "ds_write_b64 v126, v[54:55] offset:512\n"
        "s_waitcnt vmcnt(1)\n"
        "v_bitop3_b32 v123, v123, v72, v74 bitop3:0x80\n"
        "v_cvt_f32_i32 v72, v72\n"
        "v_cvt_f32_i32 v73, v73\n"
        "ds_read_b128 v[48:51], v124 offset:4096\n"
        "v_cvt_f32_i32 v74, v74\n"
        "v_cvt_f32_i32 v75, v75\n"
        "v_cvt_pk_bf16_f32 v52, v72, v73\n"
        "v_cvt_pk_bf16_f32 v53, v74, v75\n"
        "ds_write_b64 v126, v[52:53] offset:1024\n"
        "s_waitcnt vmcnt(0)\n"
        "v_bitop3_b32 v123, v123, v76, v78 bitop3:0x80\n"
        "v_cmp_eq_u32 vcc, 0, v123\n"
        "v_mov_b64 v[122:123], 0\n"
        "v_cvt_f32_i32 v76, v76\n"
        "v_cvt_f32_i32 v77, v77\n"
        "v_cvt_f32_i32 v78, v78\n"
        "v_cvt_f32_i32 v79, v79\n"
        "v_cvt_pk_bf16_f32 v54, v76, v77\n"
        "v_cvt_pk_bf16_f32 v55, v78, v79\n"
        "ds_write_b64 v126, v[54:55] offset:1536\n"
        "v_mov_b64 v[92:93], 0\n"
        "v_mov_b64 v[94:95], 0\n"
        "v_mov_b64 v[96:97], 0\n"
        "v_mov_b64 v[98:99], 0\n"
        "s_waitcnt lgkmcnt(0)\n"
        "s_branch L_dec_2%=\n"
        "L_sw0_2%=:\n"
        "s_memrealtime s[26:27]\n"
        "s_waitcnt lgkmcnt(0)\n"
        "s_sub_u32 s28, s28, s26\n"
        "L_sw_2%=:\n"
        "s_add_u32 s63, s63, 1\n"
        "s_cmp_gt_u32 s63, 0x40000\n"
        "s_cbranch_scc1 L_abort%=\n"
        "s_sleep 2\n"
        "buffer_load_dword v140, v124, s[36:39], s29 offen sc1\n"
        "s_waitcnt vmcnt(0)\n"
        "v_cmp_eq_u32 vcc, 0, v140\n"
        "s_cbranch_vccnz L_sw_2%=\n"
        "s_memrealtime s[26:27]\n"
        "s_waitcnt lgkmcnt(0)\n"
        "s_add_u32 s28, s28, s26\n"
        "s_branch L_sok_2%=\n"
        ".p2align 6\n"
        "L_rot_2%=:\n"
        "v_mfma_f32_16x16x32_bf16 v[60:63], a[236:239], v[100:103], v[60:63]\n"
        "v_mfma_f32_16x16x32_bf16 v[48:51], v[196:199], v[100:103], v[48:51]\n"
        "v_mfma_f32_16x16x32_bf16 v[52:55], v[204:207], v[100:103], v[52:55]\n"
        "s_waitcnt lgkmcnt(10)\n"
        "v_mfma_f32_16x16x32_bf16 v[84:87], v[92:95], a[96:99], v[84:87]\n"
        "ds_read_b128 v[100:103], v124 offset:7168\n"
        "v_mfma_f32_16x16x32_bf16 v[88:91], v[92:95], a[104:107], v[88:91]\n"
        "v_mfma_f32_16x16x32_bf16 v[56:59], v[0:3], v[92:95], v[56:59]\n"
        "v_mfma_f32_16x16x32_bf16 v[60:63], v[8:11], v[92:95], v[60:63]\n"
        "v_mfma_f32_16x16x32_bf16 v[48:51], v[208:211], v[92:95], v[48:51]\n"
        "s_waitcnt lgkmcnt(0)\n"
        "s_barrier\n"
        "v_mfma_f32_16x16x32_bf16 v[52:55], v[216:219], v[92:95], v[52:55]\n"
        "v_mov_b64 v[116:117], 0\n"
        "v_mfma_f32_16x16x32_bf16 v[84:87], v[104:107], a[100:103], v[84:87]\n"
        "ds_read_b128 v[92:95], v124 offset:16384\n"
        "v_mov_b64 v[118:119], 0\n"
        "v_mfma_f32_16x16x32_bf16 v[88:91], v[104:107], a[108:111], v[88:91]\n"
        "v_mov_b64 v[120:121], 0\n"
        "v_mfma_f32_16x16x32_bf16 v[56:59], v[4:7], v[104:107], v[56:59]\n"
        "v_mfma_f32_16x16x32_bf16 v[60:63], v[12:15], v[104:107], v[60:63]\n"
        "v_mfma_f32_16x16x32_bf16 v[48:51], v[212:215], v[104:107], v[48:51]\n"
        "v_mfma_f32_16x16x32_bf16 v[52:55], v[220:223], v[104:107], v[52:55]\n"
        "v_mfma_f32_16x16x32_bf16 v[48:51], v[224:227], v[96:99], v[48:51]\n"
        "ds_read_b128 v[104:107], v124 offset:17408\n"
        "v_mfma_f32_16x16x32_bf16 v[52:55], v[232:235], v[96:99], v[52:55]\n"
        "v_mfma_f32_16x16x32_bf16 v[48:51], v[228:231], v[100:103], v[48:51]\n"
        "v_mfma_f32_16x16x32_bf16 v[52:55], v[236:239], v[100:103], v[52:55]\n"
        "s_waitcnt lgkmcnt(1)\n"
        "v_mfma_f32_16x16x32_bf16 v[48:51], v[240:243], v[92:95], v[48:51]\n"
        "v_mfma_f32_16x16x32_bf16 v[52:55], v[248:251], v[92:95], v[52:55]\n"
        "s_waitcnt lgkmcnt(0)\n"
        "v_mfma_f32_16x16x32_bf16 v[48:51], v[244:247], v[104:107], v[48:51]\n"
        "s_cmp_lt_i32 s44, 0\n"
        "s_cbranch_scc1 L_light_2%=\n"
        "v_mfma_f32_16x16x32_bf16 v[52:55], v[252:255], v[104:107], v[52:55]\n"
        "s_waitcnt vmcnt(4)\n"
        "v_bitop3_b32 v123, 32, v80, v82 bitop3:0x80\n"
        "s_nop 0\n"
        "v_add_u32_dpp v80, v81, v80 row_half_mirror row_mask:0xf bank_mask:0xf\n"
        "v_mfma_f32_16x16x32_bf16 v[84:87], v[96:99], a[112:115], v[84:87]\n"
        "s_nop 0\n"
        "v_add_u32_dpp v82, v83, v82 row_half_mirror row_mask:0xf bank_mask:0xf\n"
        "v_mfma_f32_16x16x32_bf16 v[88:91], v[96:99], a[120:123], v[88:91]\n"
        "v_cvt_pk_bf16_f32 v48, v48, v52\n"
        "v_cvt_pk_bf16_f32 v49, v49, v53\n"
        "s_nop 0\n"
        "v_mfma_f32_16x16x32_bf16 v[84:87], v[100:103], a[116:119], v[84:87]\n"
        "v_cvt_pk_bf16_f32 v50, v50, v54\n"
        "v_cvt_pk_bf16_f32 v51, v51, v55\n"
        "v_mfma_f32_16x16x32_bf16 v[88:91], v[100:103], a[124:127], v[88:91]\n"
        "v_cvt_f32_i32 v80, v80\n"
        "s_nop 0\n"
        "v_cvt_f32_i32 v82, v82\n"
        "v_mfma_f32_16x16x32_bf16 v[84:87], v[92:95], a[128:131], v[84:87]\n"
        "s_nop 0\n"
        "v_cvt_pk_bf16_f32 v52, v80, v82\n"
        "v_mfma_f32_16x16x32_bf16 v[88:91], v[92:95], a[136:139], v[88:91]\n"
        "buffer_store_dwordx4 v[48:51], v124, s[36:39], s54 offen offset:1024 sc1\n"
        "v_mfma_f32_16x16x32_bf16 v[84:87], v[104:107], a[132:135], v[84:87]\n"
        "s_nop 0\n"
        "v_mfma_f32_16x16x32_bf16 v[88:91], v[104:107], a[140:143], v[88:91]\n"
        "ds_write_b32 v137, v52\n"
        "s_waitcnt vmcnt(4)\n"
        "v_bitop3_b32 v123, v123, v64, v66 bitop3:0x80\n"
        "v_cvt_f32_i32 v64, v64\n"
        "v_mfma_f32_16x16x32_bf16 v[56:59], v[32:35], v[92:95], v[56:59]\n"
        "v_cvt_f32_i32 v65, v65\n"
        "v_cvt_f32_i32 v66, v66\n"
        "v_mfma_f32_16x16x32_bf16 v[60:63], v[40:43], v[92:95], v[60:63]\n"
        "v_cvt_pk_bf16_f32 v84, v84, v85\n"
        "v_cvt_pk_bf16_f32 v85, v86, v87\n"
        "v_mfma_f32_16x16x32_bf16 v[56:59], v[16:19], v[96:99], v[56:59]\n"
        "v_cvt_pk_bf16_f32 v86, v88, v89\n"
        "v_cvt_pk_bf16_f32 v87, v90, v91\n"
        "v_mfma_f32_16x16x32_bf16 v[60:63], v[24:27], v[96:99], v[60:63]\n"
        "v_cvt_f32_i32 v67, v67\n"
        "v_cvt_pk_bf16_f32 v52, v64, v65\n"
        "v_mfma_f32_16x16x32_bf16 v[56:59], v[20:23], v[100:103], v[56:59]\n"
        "v_mov_b64 v[92:93], 0\n"
        "v_mov_b64 v[94:95], 0\n"
        "v_mfma_f32_16x16x32_bf16 v[60:63], v[28:31], v[100:103], v[60:63]\n"
        "buffer_store_dwordx4 v[84:87], v124, s[36:39], s54 offen offset:2048 sc1\n"
        "v_mov_b64 v[96:97], 0\n"
        "v_cvt_pk_bf16_f32 v53, v66, v67\n"
        "v_mfma_f32_16x16x32_bf16 v[56:59], v[36:39], v[104:107], v[56:59]\n"
        "v_mov_b64 v[98:99], 0\n"
        "ds_write_b64 v126, v[52:53] offset:0\n"
        "s_waitcnt vmcnt(4)\n"
        "v_bitop3_b32 v123, v123, v68, v70 bitop3:0x80\n"
        "v_mfma_f32_16x16x32_bf16 v[60:63], v[44:47], v[104:107], v[60:63]\n"
        "v_cvt_f32_i32 v68, v68\n"
        "v_cvt_f32_i32 v69, v69\n"
        "v_cvt_f32_i32 v70, v70\n"
        "v_cvt_f32_i32 v71, v71\n"
        "v_cvt_pk_bf16_f32 v54, v68, v69\n"
        "v_cvt_pk_bf16_f32 v55, v70, v71\n"
        "ds_write_b64 v126, v[54:55] offset:512\n"
        "s_waitcnt vmcnt(3)\n"
        "v_bitop3_b32 v123, v123, v72, v74 bitop3:0x80\n"
        "v_cvt_f32_i32 v72, v72\n"
        "v_cvt_pk_bf16_f32 v100, v56, v60\n"
        "v_cvt_pk_bf16_f32 v101, v57, v61\n"
        "v_cvt_pk_bf16_f32 v102, v58, v62\n"
        "v_cvt_pk_bf16_f32 v103, v59, v63\n"
        "v_cvt_f32_i32 v73, v73\n"
        "ds_read_b128 v[48:51], v124 offset:4096\n"
        "v_cvt_f32_i32 v74, v74\n"
        "v_cvt_f32_i32 v75, v75\n"
        "v_cvt_pk_bf16_f32 v52, v72, v73\n"
        "v_cvt_pk_bf16_f32 v53, v74, v75\n"
        "ds_write_b64 v126, v[52:53] offset:1024\n"
        "s_waitcnt vmcnt(2)\n"
        "v_bitop3_b32 v123, v123, v76, v78 bitop3:0x80\n"
        "v_cmp_eq_u32 vcc, 0, v123\n"
        "v_mov_b64 v[122:123], 0\n"
        "v_cvt_f32_i32 v76, v76\n"
        "v_cvt_f32_i32 v77, v77\n"
        "v_cvt_f32_i32 v78, v78\n"
        "v_cvt_f32_i32 v79, v79\n"
        "v_cvt_pk_bf16_f32 v54, v76, v77\n"
        "v_cvt_pk_bf16_f32 v55, v78, v79\n"
        "ds_write_b64 v126, v[54:55] offset:1536\n"
        "L_dec_2%=:\n"
        "s_waitcnt lgkmcnt(2)\n"
        "v_smfmac_f32_16x16x64_bf16 v[92:95], v[48:51], a[144:151], v134\n"
        "ds_read_b128 v[52:55], v124 offset:5120\n"
        "v_max3_f32 v104, |v80|, |v82|, |v64|\n"
        "v_smfmac_f32_16x16x64_bf16 v[96:99], v[48:51], a[152:159], v134\n"
        "s_cbranch_vccnz L_retry_2%=\n"
        "v_max3_f32 v104, |v65|, |v66|, v104\n"
        "v_smfmac_f32_16x16x64_bf16 v[116:119], v[48:51], a[240:247], v134\n"
        "s_add_u32 s29, s29, 1024\n"
        "v_max3_f32 v104, |v67|, |v68|, v104\n"
        "v_smfmac_f32_16x16x64_bf16 v[120:123], v[48:51], a[248:255], v134\n"
        "s_waitcnt lgkmcnt(0)\n"
        "v_max3_f32 v104, |v69|, |v70|, v104\n"
        "s_barrier\n"
        "ds_read_b128 v[56:59], v124 offset:2048\n"
        "v_smfmac_f32_16x16x64_bf16 v[92:95], v[52:55], a[160:167], v134\n"
        "ds_read_b128 v[60:63], v124 offset:1024\n"
        "v_max3_f32 v104, |v71|, |v72|, v104\n"
        "v_smfmac_f32_16x16x64_bf16 v[96:99], v[52:55], a[168:175], v134\n"
        "buffer_load_dwordx4 v[88:91], v124, s[36:39], s55 offen offset:3072 sc1\n"
        "ds_read_b128 v[80:83], v124 offset:3072\n"
        "v_max3_f32 v104, |v73|, |v74|, v104\n"
        "v_smfmac_f32_16x16x64_bf16 v[116:119], v[52:55], v[176:183], v134\n"
        "ds_read_b128 v[64:67], v124 offset:8192\n"
        "v_max3_f32 v104, |v75|, |v76|, v104\n"
        "v_smfmac_f32_16x16x64_bf16 v[120:123], v[52:55], v[184:191], v134\n"
        "buffer_load_dwordx4 v[84:87], v124, s[36:39], s55 offen offset:0 sc1\n"
        "ds_read_b128 v[68:71], v124 offset:6144\n"
        "v_max3_f32 v104, |v77|, |v78|, v104\n"
        "s_waitcnt lgkmcnt(4)\n"
        "v_smfmac_f32_16x16x64_bf16 v[92:95], v[56:59], v[0:7], v134\n"
        "ds_read_b128 v[72:75], v124 offset:7168\n"
        "v_max_f32_e64 v104, |v79|, v104\n"
        "v_smfmac_f32_16x16x64_bf16 v[96:99], v[56:59], v[8:15], v134\n"
        "ds_read_b128 v[0:3], v125 offset:12288\n"
        "v_max_f32_dpp v104, v104, v104 row_shr:1 row_mask:0xf bank_mask:0xf\n"
        "s_waitcnt lgkmcnt(3)\n"
        "v_smfmac_f32_16x16x64_bf16 v[92:95], v[60:63], a[224:231], v134\n"
        "ds_read_b128 v[76:79], v124 offset:0\n"
        "v_max_f32_dpp v104, v104, v104 row_shr:2 row_mask:0xf bank_mask:0xf\n"
        "v_smfmac_f32_16x16x64_bf16 v[96:99], v[60:63], a[232:239], v134\n"
        "ds_read_b128 v[4:7], v125 offset:13312\n"
        "v_max_f32_dpp v104, v104, v104 row_shr:4 row_mask:0xf bank_mask:0xf\n"
        "v_smfmac_f32_16x16x64_bf16 v[92:95], v[80:83], v[16:23], v134\n"
        "ds_read_b128 v[8:11], v125 offset:14336\n"
        "v_max_f32_dpp v104, v104, v104 row_shr:8 row_mask:0xf bank_mask:0xf\n"
        "v_smfmac_f32_16x16x64_bf16 v[96:99], v[80:83], v[24:31], v134\n"
        "ds_read_b128 v[12:15], v125 offset:15360\n"
        "v_max_f32_dpp v104, v104, v104 row_bcast:15 row_mask:0xa bank_mask:0xf\n"
        "v_smfmac_f32_16x16x64_bf16 v[92:95], v[64:67], v[32:39], v134\n"
        "ds_read_b128 v[16:19], v125 offset:16384\n"
        "v_max_f32_dpp v104, v104, v104 row_bcast:31 row_mask:0xc bank_mask:0xf\n"
        "v_smfmac_f32_16x16x64_bf16 v[96:99], v[64:67], v[40:47], v134\n"
        "ds_read_b128 v[20:23], v125 offset:17408\n"
        "v_readlane_b32 s85, v104, 63\n"
        "s_waitcnt lgkmcnt(5)\n"
        "v_smfmac_f32_16x16x64_bf16 v[92:95], v[68:71], a[176:183], v134\n"
        "ds_read_b128 v[24:27], v125 offset:18432\n"
        "v_smfmac_f32_16x16x64_bf16 v[96:99], v[68:71], a[184:191], v134\n"
        "ds_read_b128 v[28:31], v125 offset:19456\n"
        "v_mov_b32 v104, s85\n"
        "v_smfmac_f32_16x16x64_bf16 v[92:95], v[72:75], a[192:199], v134\n"
        "ds_read_b128 v[32:35], v125 offset:20480\n"
        "v_smfmac_f32_16x16x64_bf16 v[96:99], v[72:75], a[200:207], v134\n"
        "ds_read_b128 v[36:39], v125 offset:21504\n"
        "s_add_u32 s69, s58, s82\n"
        "v_smfmac_f32_16x16x64_bf16 v[92:95], v[76:79], a[208:215], v134\n"
        "ds_read_b128 v[40:43], v125 offset:22528\n"
        "s_sub_u32 s72, s58, s82\n"
        "v_smfmac_f32_16x16x64_bf16 v[96:99], v[76:79], a[216:223], v134\n"
        "ds_read_b128 v[44:47], v125 offset:23552\n"
        "s_add_u32 s87, s58, 8192\n"
        "v_smfmac_f32_16x16x64_bf16 v[116:119], v[64:67], v[240:247], v134\n"
        "ds_write_b32 v142, v104\n"
        "v_add_f32 v92, v92, v94\n"
        "v_add_f32 v93, v93, v95\n"
        "v_smfmac_f32_16x16x64_bf16 v[120:123], v[64:67], v[248:255], v134\n"
        "v_add_f32 v96, v96, v98\n"
        "v_add_f32 v97, v97, v99\n"
        "v_cvt_pk_bf16_f32 v92, v92, v96\n"
        "v_cvt_pk_bf16_f32 v93, v93, v97\n"
        "s_waitcnt lgkmcnt(3)\n"
        "v_smfmac_f32_16x16x64_bf16 v[116:119], v[68:71], v[0:7], v134\n"
        "ds_write2_b32 v127, v92, v93 offset1:72\n"
        "v_smfmac_f32_16x16x64_bf16 v[120:123], v[68:71], v[8:15], v134\n"
        "v_mov_b64 v[98:99], 0\n"
        "v_smfmac_f32_16x16x64_bf16 v[116:119], v[72:75], v[16:23], v134\n"
        "v_mov_b64 v[96:97], 0\n"
        "v_smfmac_f32_16x16x64_bf16 v[120:123], v[72:75], v[24:31], v134\n"
        "s_waitcnt vmcnt(3)\n"
        "s_waitcnt lgkmcnt(0)\n"
        "v_mov_b64 v[106:107], 0\n"
        "s_barrier\n"
        "ds_read_b128 v[0:3], v128 offset:0\n"
        "v_smfmac_f32_16x16x64_bf16 v[116:119], v[76:79], v[32:39], v134\n"
        "ds_read_b128 v[24:27], v124 offset:20480\n"
        "v_mov_b64 v[104:105], 0\n"
        "v_smfmac_f32_16x16x64_bf16 v[120:123], v[76:79], v[40:47], v134\n"
        "ds_read_b128 v[4:7], v128 offset:64\n"
        "v_mov_b64 v[94:95], 0\n"
        "v_smfmac_f32_16x16x64_bf16 v[116:119], v[60:63], v[192:199], v134\n"
        "ds_read_b128 v[8:11], v128 offset:128\n"
        "v_mov_b64 v[92:93], 0\n"
        "v_smfmac_f32_16x16x64_bf16 v[120:123], v[60:63], v[200:207], v134\n"
        "ds_read_b128 v[12:15], v128 offset:192\n"
        "v_mov_b64 v[20:21], 0\n"
        "v_smfmac_f32_16x16x64_bf16 v[116:119], v[56:59], v[208:215], v134\n"
        "s_waitcnt lgkmcnt(3)\n"
        "v_max3_f32 v24, v24, v25, v26\n"
        "s_waitcnt lgkmcnt(2)\n"
        "v_smfmac_f32_16x16x64_bf16 v[104:107], v[108:111], v[0:7], v134\n"
        "v_max_f32 v24, v24, v27\n"
        "v_smfmac_f32_16x16x64_bf16 v[120:123], v[56:59], v[216:223], v134\n"
        "v_readfirstlane_b32 s84, v24\n"
        "s_waitcnt lgkmcnt(0)\n"
        "v_smfmac_f32_16x16x64_bf16 v[104:107], v[112:115], v[8:15], v134\n"
        "v_mov_b64 v[22:23], 0\n"
        "v_smfmac_f32_16x16x64_bf16 v[116:119], v[80:83], v[224:231], v134\n"
        "s_lshr_b32 s84, s84, 23\n"
        "s_add_u32 s84, s84, s101\n"
        "v_smfmac_f32_16x16x64_bf16 v[120:123], v[80:83], v[232:239], v134\n"
        "s_max_i32 s84, s84, s76\n"
        "s_min_i32 s84, s84, 186\n"
        "v_add_f32 v104, v104, v106\n"
        "v_add_f32 v105, v105, v107\n"
        "v_smfmac_f32_16x16x64_bf16 v[92:95], v[64:67], a[128:135], v134\n"
        "ds_write_b64 v131, v[104:105]\n"
        "v_add_f32 v116, v116, v118\n"
        "v_add_f32 v117, v117, v119\n"
        "v_pk_add_f32 v[120:121], v[120:121], v[122:123]\n"
        "v_cvt_pk_bf16_f32 v116, v116, v120\n"
        "v_cvt_pk_bf16_f32 v117, v117, v121\n"
        "ds_write2_b32 v129, v116, v117 offset1:8\n"
        "v_smfmac_f32_16x16x64_bf16 v[96:99], v[64:67], a[136:143], v134\n"
        "s_mul_i32 s88, s84, 0x800080\n"
        "s_mov_b32 s89, s88\n"
        "v_smfmac_f32_16x16x64_bf16 v[92:95], v[68:71], a[32:39], v134\n"
        "buffer_store_dwordx4 v[100:103], v124, s[36:39], s54 offen offset:0 sc1\n"
        "v_mov_b64 v[104:105], s[88:89]\n"
        "v_smfmac_f32_16x16x64_bf16 v[96:99], v[68:71], a[40:47], v134\n"
        "s_add_u32 s90, s88, s35\n"
        "s_mov_b32 s91, s90\n"
        "v_mov_b64 v[106:107], s[90:91]\n"
        "v_smfmac_f32_16x16x64_bf16 v[92:95], v[48:51], a[0:7], v134\n"
        "s_waitcnt lgkmcnt(0)\n"
        "v_mov_b64 v[100:101], s[88:89]\n"
        "s_barrier\n"
        "ds_read_b128 v[24:27], v130\n"
        "v_smfmac_f32_16x16x64_bf16 v[96:99], v[48:51], a[8:15], v134\n"
        "ds_read_b128 v[28:31], v130 offset:1024\n"
        "v_mov_b64 v[102:103], s[90:91]\n"
        "v_smfmac_f32_16x16x64_bf16 v[92:95], v[52:55], a[16:23], v134\n"
        "ds_read_b128 v[116:119], v124 offset:13568\n"
        "v_mov_b64 v[48:49], 0\n"
        "v_smfmac_f32_16x16x64_bf16 v[96:99], v[52:55], a[24:31], v134\n"
        "buffer_load_dwordx4 v[64:67], v124, s[36:39], s69 offen offset:0 sc1\n"
        "v_mov_b64 v[50:51], 0\n"
        "v_smfmac_f32_16x16x64_bf16 v[92:95], v[56:59], a[96:103], v134\n"
        "v_mov_b64 v[52:53], 0\n"
        "v_smfmac_f32_16x16x64_bf16 v[96:99], v[56:59], a[104:111], v134\n"
        "buffer_load_dwordx4 v[68:71], v124, s[36:39], s69 offen offset:1024 sc1\n"
        "v_mov_b64 v[54:55], 0\n"
        "s_waitcnt lgkmcnt(2)\n"
        "v_smfmac_f32_16x16x64_bf16 v[20:23], v[24:27], v[0:7], v134\n"
        "v_mov_b64 v[56:57], 0\n"
        "s_waitcnt lgkmcnt(0)\n"
        "v_smfmac_f32_16x16x64_bf16 v[20:23], v[28:31], v[8:15], v134\n"
        "v_max3_f32 v121, v116, v117, v118\n"
        "v_smfmac_f32_16x16x64_bf16 v[92:95], v[72:75], a[48:55], v134\n"
        "ds_read_b128 v[0:3], v125 offset:24576\n"
        "v_mov_b64 v[58:59], 0\n"
        "v_smfmac_f32_16x16x64_bf16 v[96:99], v[72:75], a[56:63], v134\n"
        "buffer_load_dwordx4 v[72:75], v124, s[36:39], s72 offen offset:2048 sc1\n"
        "v_add_f32 v20, v20, v22\n"
        "v_add_f32 v21, v21, v23\n"
        "v_cndmask_b32 v120, v20, v21, s[92:93]\n"
        "v_mul_f32 v120, s74, v120\n"
        "v_max3_f32 v121, v121, v119, v120\n"
        "v_smfmac_f32_16x16x64_bf16 v[92:95], v[76:79], a[64:71], v134\n"
        "ds_read_b128 v[4:7], v125 offset:25600\n"
        "v_max_f32_dpp v121, v121, v121 quad_perm:[2,3,0,1] row_mask:0xf bank_mask:0xf\n"
        "v_mov_b32 v122, v121\n"
        "v_smfmac_f32_16x16x64_bf16 v[96:99], v[76:79], a[72:79], v134\n"
        "buffer_load_dwordx4 v[76:79], v124, s[36:39], s72 offen offset:3072 sc1\n"
        "v_permlane16_swap_b32 v121, v122\n"
        "v_max_f32 v121, v121, v122\n"
        "v_mov_b32 v122, v121\n"
        "v_smfmac_f32_16x16x64_bf16 v[92:95], v[80:83], a[112:119], v134\n"
        "ds_read_b128 v[8:11], v125 offset:26624\n"
        "v_permlane32_swap_b32 v121, v122\n"
        "v_smfmac_f32_16x16x64_bf16 v[96:99], v[80:83], a[120:127], v134\n"
        "buffer_load_dwordx4 v[80:83], v124, s[36:39], s87 offen offset:0 sc1\n"
        "v_max_f32 v121, v121, v122\n"
        "v_mul_f32_e64 v123, -s73, v121\n"
        "v_fma_f32 v120, v120, s73, v123\n"
        "v_fma_f32 v116, v116, s73, v123\n"
        "v_smfmac_f32_16x16x64_bf16 v[92:95], v[60:63], a[80:87], v134\n"
        "ds_read_b128 v[12:15], v125 offset:27648\n"
        "v_fma_f32 v117, v117, s73, v123\n"
        "v_fma_f32 v118, v118, s73, v123\n"
        "v_fma_f32 v119, v119, s73, v123\n"
        "v_exp_f32 v120, v120\n"
        "v_smfmac_f32_16x16x64_bf16 v[96:99], v[60:63], a[88:95], v134\n"
        "ds_read_b128 v[16:19], v125 offset:28672\n"
        "v_exp_f32 v116, v116\n"
        "v_mul_f32 v120, s74, v120\n"
        "v_exp_f32 v117, v117\n"
        "v_exp_f32 v118, v118\n"
        "v_exp_f32 v119, v119\n"
        "s_waitcnt vmcnt(6)\n"
        "v_mov_b64 v[108:109], v[86:87]\n"
        "v_mov_b64 v[112:113], v[90:91]\n"
        "v_cvt_pk_bf16_f32 v120, v120, v120\n"
        "v_cvt_pk_bf16_f32 v60, v116, v117\n"
        "v_cvt_pk_bf16_f32 v62, v118, v119\n"
        "v_and_b32 v61, v120, v135\n"
        "v_and_b32 v63, v120, v136\n"
        "v_pk_add_f32 v[92:93], v[92:93], v[94:95]\n"
        "v_pk_add_f32 v[96:97], v[96:97], v[98:99]\n"
        "v_smfmac_f32_16x16x64_bf16 v[56:59], v[60:63], v[100:107], v134\n"
        "ds_read_b128 v[20:23], v125 offset:29696\n"
        "v_cvt_pk_bf16_f32 v86, v92, v93\n"
        "v_cvt_pk_bf16_f32 v87, v92, v93\n"
        "v_mov_b64 v[90:91], v[86:87]\n"
        "v_cvt_pk_bf16_f32 v110, v96, v97\n"
        "v_cvt_pk_bf16_f32 v111, v96, v97\n"
        "v_smfmac_f32_16x16x64_bf16 v[48:51], v[60:63], v[84:91], v134\n"
        "ds_read_b128 v[28:31], v125 offset:31744\n"
        "v_mov_b64 v[114:115], v[110:111]\n"
        "v_add_f32 v56, v56, v58\n"
        "v_add_f32 v57, v57, v59\n"
        "v_smfmac_f32_16x16x64_bf16 v[52:55], v[60:63], v[108:115], v134\n"
        "s_nop 1\n"
        "v_rcp_f32 v56, v56\n"
        "v_rcp_f32 v57, v57\n"
        "v_pk_add_f32 v[48:49], v[48:49], v[50:51]\n"
        "v_pk_mul_f32 v[48:49], v[48:49], v[56:57]\n"
        "v_mov_b32 v110, v86\n"
        "v_mov_b64 v[92:93], s[70:71]\n"
        "v_pk_add_f32 v[52:53], v[52:53], v[54:55]\n"
        "v_pk_mul_f32 v[52:53], v[52:53], v[56:57]\n"
        "v_cvt_pk_bf16_f32 v48, v48, v52\n"
        "v_cvt_pk_bf16_f32 v49, v49, v53\n"
        "ds_write2_b32 v139, v48, v49 offset1:4\n"
        "v_mov_b64 v[94:95], 0\n"
        "v_mov_b64 v[96:97], 0\n"
        "v_mov_b64 v[98:99], 0\n"
        "v_mov_b64 v[100:101], s[70:71]\n"
        "v_mov_b64 v[102:103], 0\n"
        "buffer_store_dwordx2 v[110:111], v126, s[36:39], s60 offen sc1\n"
        "v_mov_b64 v[104:105], 0\n"
        "s_waitcnt lgkmcnt(0)\n"
        "v_mov_b64 v[106:107], 0\n"
        "s_barrier\n"
        "ds_read_b128 v[108:111], v124 offset:18432\n"
        "v_mov_b64 v[106:107], 0\n"
        "ds_read_b128 v[112:115], v124 offset:19456\n"
        "v_mov_b64 v[106:107], 0\n"
        "s_bitcmp1_b32 s59, 30\n"
        "s_cbranch_scc1 L_s1_2%=\n"
        "buffer_store_dwordx4 v[24:27], v124, s[36:39], s59 offen offset:0 sc1\n"
        "L_s1_2%=:\n"
        "v_mov_b64 v[106:107], 0\n"
        "v_mov_b64 v[106:107], 0\n"
        "ds_read_b128 v[24:27], v125 offset:30720\n"
        "v_mov_b64 v[106:107], 0\n"
        "s_waitcnt lgkmcnt(2)\n"
        "v_smfmac_f32_16x16x64_bf16 v[92:95], v[108:111], v[144:151], v134\n"
        "v_smfmac_f32_16x16x64_bf16 v[96:99], v[108:111], v[152:159], v134\n"
        "s_waitcnt lgkmcnt(1)\n"
        "v_smfmac_f32_16x16x64_bf16 v[92:95], v[112:115], v[160:167], v134\n"
        "v_smfmac_f32_16x16x64_bf16 v[96:99], v[112:115], v[168:175], v134\n"
        "v_smfmac_f32_16x16x64_bf16 v[100:103], v[108:111], v[0:7], v134\n"
        "v_add_f32 v92, v92, v94\n"
        "v_add_f32 v93, v93, v95\n"
        "v_cvt_rpi_i32_f32 v94, v92\n"
        "v_smfmac_f32_16x16x64_bf16 v[104:107], v[108:111], v[8:15], v134\n"
        "buffer_load_dword v140, v124, s[36:39], s29 offen sc1\n"
        "v_bfi_b32 v94, 63, 1, v94\n"
        "v_add_f32 v96, v96, v98\n"
        "v_smfmac_f32_16x16x64_bf16 v[100:103], v[112:115], v[16:23], v134\n"
        "v_cvt_rpi_i32_f32 v95, v96\n"
        "s_waitcnt lgkmcnt(0)\n"
        "v_smfmac_f32_16x16x64_bf16 v[104:107], v[112:115], v[24:31], v134\n"
        "buffer_atomic_add_x2 v[94:95], v133, s[36:39], s51 offen\n"
        "v_add_f32 v97, v97, v99\n"
        "v_cvt_rpi_i32_f32 v98, v93\n"
        "v_bfi_b32 v98, 63, 1, v98\n"
        "v_cvt_rpi_i32_f32 v99, v97\n"
        "s_waitcnt vmcnt(7)\n"
        "v_mfma_f32_16x16x32_bf16 v[84:87], v[64:67], a[0:3], 0\n"
        "buffer_atomic_add_x2 v[98:99], v133, s[36:39], s66 offen\n"
        "v_add_f32 v100, v100, v102\n"
        "v_add_f32 v101, v101, v103\n"
        "v_add_f32 v104, v104, v106\n"
        "v_cvt_rpi_i32_f32 v102, v100\n"
        "v_bfi_b32 v102, 63, 1, v102\n"
        "v_cvt_rpi_i32_f32 v103, v104\n"
        "v_mfma_f32_16x16x32_bf16 v[88:91], v[64:67], a[8:11], 0\n"
        "buffer_atomic_add_x2 v[102:103], v133, s[36:39], s67 offen\n"
        "v_add_f32 v105, v105, v107\n"
        "v_cvt_rpi_i32_f32 v106, v101\n"
        "v_bfi_b32 v106, 63, 1, v106\n"
        "v_cvt_rpi_i32_f32 v107, v105\n"
        "v_mfma_f32_16x16x32_bf16 v[56:59], a[144:147], v[64:67], 0\n"
        "buffer_atomic_add_x2 v[106:107], v133, s[36:39], s68 offen\n"
        "L_win_2%=:\n"
        "v_mfma_f32_16x16x32_bf16 v[60:63], a[152:155], v[64:67], 0\n"
        "v_mfma_f32_16x16x32_bf16 v[48:51], a[240:243], v[64:67], 0\n"
        "ds_write_b128 v124, v[64:67] offset:8192\n"
        "v_mfma_f32_16x16x32_bf16 v[52:55], a[248:251], v[64:67], 0\n"
        "s_waitcnt vmcnt(9)\n"
        "v_mfma_f32_16x16x32_bf16 v[84:87], v[68:71], a[4:7], v[84:87]\n"
        "ds_write_b128 v124, v[68:71] offset:9216\n"
        "v_mfma_f32_16x16x32_bf16 v[88:91], v[68:71], a[12:15], v[88:91]\n"
        "v_mfma_f32_16x16x32_bf16 v[56:59], a[148:151], v[68:71], v[56:59]\n"
        "s_waitcnt vmcnt(8)\n"
        "ds_write_b128 v124, v[72:75] offset:10240\n"
        "v_mfma_f32_16x16x32_bf16 v[60:63], a[156:159], v[68:71], v[60:63]\n"
        "v_mfma_f32_16x16x32_bf16 v[48:51], a[244:247], v[68:71], v[48:51]\n"
        "s_waitcnt vmcnt(7)\n"
        "ds_write_b128 v124, v[76:79] offset:11264\n"
        "v_mfma_f32_16x16x32_bf16 v[52:55], a[252:255], v[68:71], v[52:55]\n"
        "v_mfma_f32_16x16x32_bf16 v[84:87], v[72:75], a[16:19], v[84:87]\n"
        "s_waitcnt vmcnt(6)\n"
        "ds_write_b128 v124, v[80:83] offset:16384\n"
        "v_mfma_f32_16x16x32_bf16 v[88:91], v[72:75], a[24:27], v[88:91]\n"
        "v_mfma_f32_16x16x32_bf16 v[56:59], a[160:163], v[72:75], v[56:59]\n"
        "v_mfma_f32_16x16x32_bf16 v[60:63], a[168:171], v[72:75], v[60:63]\n"
        "v_mfma_f32_16x16x32_bf16 v[48:51], v[176:179], v[72:75], v[48:51]\n"
        "ds_read_b128 v[0:3], v125 offset:12288\n"
        "v_mfma_f32_16x16x32_bf16 v[52:55], v[184:187], v[72:75], v[52:55]\n"
        "ds_read_b128 v[8:11], v125 offset:14336\n"
        "v_mfma_f32_16x16x32_bf16 v[84:87], v[76:79], a[20:23], v[84:87]\n"
        "s_waitcnt lgkmcnt(2)\n"
        "s_barrier\n"
        "v_mfma_f32_16x16x32_bf16 v[88:91], v[76:79], a[28:31], v[88:91]\n"
        "ds_read_b128 v[92:95], v124 offset:12288\n"
        "v_mov_b32 v116, s62\n"
        "v_mfma_f32_16x16x32_bf16 v[56:59], a[164:167], v[76:79], v[56:59]\n"
        "s_add_u32 s49, s49, 163840\n"
        "ds_read_b128 v[104:107], v124 offset:13312\n"
        "v_mfma_f32_16x16x32_bf16 v[60:63], a[172:175], v[76:79], v[60:63]\n"
        "s_add_u32 s50, s50, 163840\n"
        "ds_read_b128 v[4:7], v125 offset:13312\n"
        "v_mfma_f32_16x16x32_bf16 v[48:51], v[180:183], v[76:79], v[48:51]\n"
        "s_xor_b32 s49, s49, s86\n"
        "ds_read_b128 v[12:15], v125 offset:15360\n"
        "v_mfma_f32_16x16x32_bf16 v[52:55], v[188:191], v[76:79], v[52:55]\n"
        "s_xor_b32 s65, s49, 8192\n"
        "s_add_u32 s56, s56, 786432\n"
        "s_waitcnt lgkmcnt(3)\n"
        "v_mfma_f32_16x16x32_bf16 v[84:87], v[92:95], a[32:35], v[84:87]\n"
        "s_add_u32 s64, s64, 786432\n"
        "ds_read_b128 v[96:99], v124 offset:14336\n"
        "v_mfma_f32_16x16x32_bf16 v[88:91], v[92:95], a[40:43], v[88:91]\n"
        "ds_read_b128 v[100:103], v124 offset:15360\n"
        "v_mfma_f32_16x16x32_bf16 v[56:59], a[176:179], v[92:95], v[56:59]\n"
        "ds_read_b128 v[16:19], v125 offset:16384\n"
        "v_mfma_f32_16x16x32_bf16 v[60:63], a[184:187], v[92:95], v[60:63]\n"
        "s_add_u32 s51, s51, 163840\n"
        "ds_read_b128 v[20:23], v125 offset:17408\n"
        "v_mfma_f32_16x16x32_bf16 v[48:51], v[0:3], v[92:95], v[48:51]\n"
        "s_add_u32 s83, s83, 163840\n"
        "ds_read_b128 v[24:27], v125 offset:18432\n"
        "v_mfma_f32_16x16x32_bf16 v[52:55], v[8:11], v[92:95], v[52:55]\n"
        "ds_read_b128 v[28:31], v125 offset:19456\n"
        "s_waitcnt lgkmcnt(8)\n"
        "v_mfma_f32_16x16x32_bf16 v[84:87], v[104:107], a[36:39], v[84:87]\n"
        "s_xor_b32 s86, s83, s46\n"
        "ds_read_b128 v[92:95], v124 offset:0\n"
        "v_mfma_f32_16x16x32_bf16 v[88:91], v[104:107], a[44:47], v[88:91]\n"
        "s_and_b32 s86, s86, s53\n"
        "s_lshr_b32 s86, s86, 8\n"
        "s_xor_b32 s86, s86, s34\n"
        "v_mfma_f32_16x16x32_bf16 v[56:59], a[180:183], v[104:107], v[56:59]\n"
        "s_xor_b32 s34, s34, s86\n"
        "s_xor_b32 s51, s51, s86\n"
        "s_add_u32 s55, s55, 786432\n"
        "v_mfma_f32_16x16x32_bf16 v[60:63], a[188:191], v[104:107], v[60:63]\n"
        "s_waitcnt vmcnt(5)\n"
        "s_mov_b64 exec, 1\n"
        "buffer_atomic_umax v116, off, s[36:39], s61\n"
        "s_mov_b64 exec, -1\n"
        "s_waitcnt lgkmcnt(8)\n"
        "v_mfma_f32_16x16x32_bf16 v[48:51], v[4:7], v[104:107], v[48:51]\n"
        "s_cmp_eq_u32 s44, 63\n"
        "s_cbranch_scc1 L_end%=\n"
        "s_waitcnt lgkmcnt(7)\n"
        "v_mfma_f32_16x16x32_bf16 v[52:55], v[12:15], v[104:107], v[52:55]\n"
        "ds_read_b128 v[32:35], v125 offset:20480\n"
        "s_waitcnt lgkmcnt(7)\n"
        "v_mfma_f32_16x16x32_bf16 v[84:87], v[96:99], a[48:51], v[84:87]\n"
        "s_add_u32 s54, s54, 786432\n"
        "ds_read_b128 v[104:107], v124 offset:1024\n"
        "v_mfma_f32_16x16x32_bf16 v[88:91], v[96:99], a[56:59], v[88:91]\n"
        "s_add_u32 s59, s59, 131072\n"
        "ds_read_b128 v[36:39], v125 offset:21504\n"
        "v_mfma_f32_16x16x32_bf16 v[56:59], a[192:195], v[96:99], v[56:59]\n"
        "s_add_u32 s60, s60, 131072\n"
        "ds_read_b128 v[40:43], v125 offset:22528\n"
        "v_mfma_f32_16x16x32_bf16 v[60:63], a[200:203], v[96:99], v[60:63]\n"
        "ds_read_b128 v[44:47], v125 offset:23552\n"
        "s_waitcnt vmcnt(5)\n"
        "v_cmp_eq_u32 vcc, 0, v140\n"
        "s_waitcnt lgkmcnt(9)\n"
        "v_mfma_f32_16x16x32_bf16 v[48:51], v[16:19], v[96:99], v[48:51]\n"
        "s_cbranch_vccnz L_sw0_2%=\n"
        "L_sok_2%=:\n"
        "s_waitcnt lgkmcnt(7)\n"
        "v_mfma_f32_16x16x32_bf16 v[52:55], v[24:27], v[96:99], v[52:55]\n"
        "v_readlane_b32 s81, v140, s99\n"
        "v_max_u32_dpp v140, v140, v140 row_shr:1 row_mask:0xf bank_mask:0xf\n"
        "v_mfma_f32_16x16x32_bf16 v[84:87], v[100:103], a[52:55], v[84:87]\n"
        "ds_read_b128 v[96:99], v124 offset:2048\n"
        "v_max_u32_dpp v140, v140, v140 row_shr:2 row_mask:0xf bank_mask:0xf\n"
        "v_mfma_f32_16x16x32_bf16 v[88:91], v[100:103], a[60:63], v[88:91]\n"
        "buffer_load_dwordx4 v[108:111], v132, s[36:39], s56 offen offset:0 sc1\n"
        "v_max_u32_dpp v140, v140, v140 row_shr:4 row_mask:0xf bank_mask:0xf\n"
        "v_mfma_f32_16x16x32_bf16 v[56:59], a[196:199], v[100:103], v[56:59]\n"
        "ds_read_b128 v[0:3], v125 offset:0\n"
        "v_max_u32_dpp v140, v140, v140 row_shr:8 row_mask:0xf bank_mask:0xf\n"
        "v_mfma_f32_16x16x32_bf16 v[60:63], a[204:207], v[100:103], v[60:63]\n"
        "buffer_load_dwordx4 v[112:115], v132, s[36:39], s64 offen offset:0 sc1\n"
        "v_max_u32_dpp v140, v140, v140 row_bcast:15 row_mask:0xa bank_mask:0xf\n"
        "v_mfma_f32_16x16x32_bf16 v[48:51], v[20:23], v[100:103], v[48:51]\n"
        "ds_read_b128 v[4:7], v125 offset:1024\n"
        "v_max_u32_dpp v140, v140, v140 row_bcast:31 row_mask:0xc bank_mask:0xf\n"
        "s_waitcnt lgkmcnt(9)\n"
        "v_mfma_f32_16x16x32_bf16 v[52:55], v[28:31], v[100:103], v[52:55]\n"
        "v_readlane_b32 s33, v140, 63\n"
        "s_lshl1_add_u32 s28, s28, s81\n"
        "s_waitcnt lgkmcnt(8)\n"
        "v_mfma_f32_16x16x32_bf16 v[84:87], v[92:95], a[64:67], v[84:87]\n"
        "ds_read_b128 v[100:103], v124 offset:3072\n"
        "s_add_u32 s28, s28, 36\n"
        "s_sub_i32 s33, s33, s28\n"
        "v_mfma_f32_16x16x32_bf16 v[88:91], v[92:95], a[72:75], v[88:91]\n"
        "s_min_i32 s33, s33, 500\n"
        "s_mov_b32 s28, 0\n"
        "L_dl_2%=:\n"
        "s_sub_i32 s33, s33, 3\n"
        "s_cmp_gt_i32 s33, 0\n"
        "s_cbranch_scc1 L_dl_2%=\n"
        "v_mfma_f32_16x16x32_bf16 v[56:59], a[208:211], v[92:95], v[56:59]\n"
        "s_add_u32 s58, s58, 36864\n"
        "buffer_load_dwordx4 v[80:83], v138, s[36:39], s50 offen offset:0 sc1\n"
        "v_mfma_f32_16x16x32_bf16 v[60:63], a[216:219], v[92:95], v[60:63]\n"
        "s_xor_b32 s66, s51, 1024\n"
        "ds_read_b128 v[8:11], v125 offset:2048\n"
        "s_waitcnt lgkmcnt(9)\n"
        "v_mfma_f32_16x16x32_bf16 v[48:51], v[32:35], v[92:95], v[48:51]\n"
        "s_xor_b32 s67, s51, 8192\n"
        "buffer_load_dwordx4 v[64:67], v138, s[36:39], s49 offen offset:0 sc1\n"
        "s_waitcnt lgkmcnt(6)\n"
        "v_mfma_f32_16x16x32_bf16 v[52:55], v[40:43], v[92:95], v[52:55]\n"
        "ds_read_b128 v[12:15], v125 offset:3072\n"
        "v_mfma_f32_16x16x32_bf16 v[84:87], v[104:107], a[68:71], v[84:87]\n"
        "s_xor_b32 s68, s51, 9216\n"
        "ds_read_b128 v[92:95], v124 offset:4096\n"
        "v_mfma_f32_16x16x32_bf16 v[88:91], v[104:107], a[76:79], v[88:91]\n"
        "s_sub_u32 s75, 127, s84\n"
        "buffer_load_dwordx4 v[68:71], v138, s[36:39], s49 offen offset:256 sc1\n"
        "v_mfma_f32_16x16x32_bf16 v[56:59], a[212:215], v[104:107], v[56:59]\n"
        "s_sub_u32 s101, s77, s75\n"
        "ds_read_b128 v[16:19], v125 offset:4096\n"
        "v_mfma_f32_16x16x32_bf16 v[60:63], a[220:223], v[104:107], v[60:63]\n"
        "s_mul_i32 s35, s75, 0x800080\n"
        "buffer_load_dwordx4 v[72:75], v138, s[36:39], s65 offen offset:0 sc1\n"
        "v_mfma_f32_16x16x32_bf16 v[48:51], v[36:39], v[104:107], v[48:51]\n"
        "s_lshl_b32 s80, s75, 23\n"
        "ds_read_b128 v[20:23], v125 offset:5120\n"
        "s_waitcnt lgkmcnt(9)\n"
        "v_mfma_f32_16x16x32_bf16 v[52:55], v[44:47], v[104:107], v[52:55]\n"
        "buffer_load_dwordx4 v[76:79], v138, s[36:39], s65 offen offset:256 sc1\n"
        "s_waitcnt lgkmcnt(8)\n"
        "v_mfma_f32_16x16x32_bf16 v[84:87], v[96:99], a[80:83], v[84:87]\n"
        "s_sub_u32 s73, 0x3e0293ee, s80\n"
        "ds_read_b128 v[104:107], v124 offset:5120\n"
        "v_mfma_f32_16x16x32_bf16 v[88:91], v[96:99], a[88:91], v[88:91]\n"
        "s_sub_u32 s74, 0x3f800000, s80\n"
        "ds_read_b128 v[24:27], v125 offset:6144\n"
        "v_mfma_f32_16x16x32_bf16 v[56:59], a[224:227], v[96:99], v[56:59]\n"
        "s_add_u32 s80, s75, 128\n"
        "ds_read_b128 v[28:31], v125 offset:7168\n"
        "v_mfma_f32_16x16x32_bf16 v[60:63], a[232:235], v[96:99], v[60:63]\n"
        "s_lshl_b32 s80, s80, 8\n"
        "ds_read_b128 v[32:35], v125 offset:8192\n"
        "v_mfma_f32_16x16x32_bf16 v[48:51], v[192:195], v[96:99], v[48:51]\n"
        "s_add_u32 s62, s80, 1\n"
        "ds_read_b128 v[36:39], v125 offset:9216\n"
        "v_mfma_f32_16x16x32_bf16 v[52:55], v[200:203], v[96:99], v[52:55]\n"
        "ds_read_b128 v[40:43], v125 offset:10240\n"
        "s_waitcnt lgkmcnt(11)\n"
        "v_mfma_f32_16x16x32_bf16 v[84:87], v[100:103], a[84:87], v[84:87]\n"
        "s_add_u32 s44, s44, 1\n"
        "ds_read_b128 v[96:99], v124 offset:6144\n"
        "v_mfma_f32_16x16x32_bf16 v[88:91], v[100:103], a[92:95], v[88:91]\n"
        "s_add_u32 s61, s61, 32\n"
        "ds_read_b128 v[44:47], v125 offset:11264\n"
        "v_mfma_f32_16x16x32_bf16 v[56:59], a[228:231], v[100:103], v[56:59]\n"
        "s_branch L_rot_2%=\n"
        "L_light_3%=:\n"
        "v_mfma_f32_16x16x32_bf16 v[52:55], v[252:255], v[104:107], v[52:55]\n"
        "v_mfma_f32_16x16x32_bf16 v[84:87], v[96:99], a[112:115], v[84:87]\n"
        "v_mfma_f32_16x16x32_bf16 v[88:91], v[96:99], a[120:123], v[88:91]\n"
        "v_cvt_pk_bf16_f32 v48, v48, v52\n"
        "v_cvt_pk_bf16_f32 v49, v49, v53\n"
        "v_mfma_f32_16x16x32_bf16 v[84:87], v[100:103], a[116:119], v[84:87]\n"
        "v_cvt_pk_bf16_f32 v50, v50, v54\n"
        "v_cvt_pk_bf16_f32 v51, v51, v55\n"
        "v_mfma_f32_16x16x32_bf16 v[88:91], v[100:103], a[124:127], v[88:91]\n"
        "v_mfma_f32_16x16x32_bf16 v[84:87], v[92:95], a[128:131], v[84:87]\n"
        "v_mfma_f32_16x16x32_bf16 v[88:91], v[92:95], a[136:139], v[88:91]\n"
        "v_mfma_f32_16x16x32_bf16 v[84:87], v[104:107], a[132:135], v[84:87]\n"
        "buffer_store_dwordx4 v[48:51], v124, s[36:39], s54 offen offset:1024 sc1\n"
        "v_mfma_f32_16x16x32_bf16 v[88:91], v[104:107], a[140:143], v[88:91]\n"
        "v_mfma_f32_16x16x32_bf16 v[56:59], v[32:35], v[92:95], v[56:59]\n"
        "v_mfma_f32_16x16x32_bf16 v[60:63], v[40:43], v[92:95], v[60:63]\n"
        "v_cvt_pk_bf16_f32 v84, v84, v85\n"
        "v_cvt_pk_bf16_f32 v85, v86, v87\n"
        "v_mfma_f32_16x16x32_bf16 v[56:59], v[16:19], v[96:99], v[56:59]\n"
        "v_cvt_pk_bf16_f32 v86, v88, v89\n"
        "v_cvt_pk_bf16_f32 v87, v90, v91\n"
        "v_mfma_f32_16x16x32_bf16 v[60:63], v[24:27], v[96:99], v[60:63]\n"
        "v_mfma_f32_16x16x32_bf16 v[56:59], v[20:23], v[100:103], v[56:59]\n"
        "v_mov_b64 v[92:93], 0\n"
        "v_mov_b64 v[94:95], 0\n"
        "v_mfma_f32_16x16x32_bf16 v[60:63], v[28:31], v[100:103], v[60:63]\n"
        "v_mov_b64 v[96:97], 0\n"
        "v_mov_b64 v[98:99], 0\n"
        "v_mfma_f32_16x16x32_bf16 v[56:59], v[36:39], v[104:107], v[56:59]\n"
        "buffer_store_dwordx4 v[84:87], v124, s[36:39], s54 offen offset:2048 sc1\n"
        "v_mfma_f32_16x16x32_bf16 v[60:63], v[44:47], v[104:107], v[60:63]\n"
        "s_nop 7\n"
        "v_cvt_pk_bf16_f32 v100, v56, v60\n"
        "v_cvt_pk_bf16_f32 v101, v57, v61\n"
        "v_cvt_pk_bf16_f32 v102, v58, v62\n"
        "v_cvt_pk_bf16_f32 v103, v59, v63\n"
        "buffer_store_dwordx4 v[100:103], v124, s[36:39], s54 offen offset:0 sc1\n"
        "s_waitcnt vmcnt(3)\n"
        "v_cmp_ge_u32 vcc, s31, v140\n"
        "s_and_b32 vcc_lo, vcc_lo, 0x1ff\n"
        "s_cbranch_scc0 L_xl_3%=\n"
        "s_branch L_xl2_3%=\n"
        "L_xl1_3%=:\n"
        "s_add_u32 s63, s63, 1\n"
        "s_cmp_gt_u32 s63, 0x40000\n"
        "s_cbranch_scc1 L_abort%=\n"
        "s_sleep 4\n"
        "L_xl2_3%=:\n"
        "v_mov_b32 v140, 0\n"
        "buffer_atomic_add v140, v143, s[36:39], s57 offen sc0\n"
        "s_waitcnt vmcnt(0)\n"
        "v_cmp_ge_u32 vcc, s31, v140\n"
        "s_and_b32 vcc_lo, vcc_lo, 0x1ff\n"
        "s_cbranch_scc1 L_xl1_3%=\n"
        "L_xl_3%=:\n"
        "s_barrier\n"
        "s_add_u32 s69, s58, s82\n"
        "s_sub_u32 s72, s58, s82\n"
        "s_add_u32 s87, s58, 5120\n"
        "buffer_load_dwordx4 v[64:67], v124, s[36:39], s69 offen offset:0 sc1\n"
        "buffer_load_dwordx4 v[68:71], v124, s[36:39], s69 offen offset:1024 sc1\n"
        "buffer_load_dwordx4 v[72:75], v124, s[36:39], s72 offen offset:2048 sc1\n"
        "buffer_load_dwordx4 v[76:79], v124, s[36:39], s72 offen offset:3072 sc1\n"
        "buffer_load_dwordx4 v[80:83], v124, s[36:39], s87 offen offset:0 sc1\n"
        "s_mov_b32 s81, 64\n"
        "buffer_load_dword v107, off, s[36:39], s81 sc1\n"
        "s_memrealtime s[26:27]\n"
        "s_waitcnt vmcnt(6)\n"
        "s_waitcnt lgkmcnt(0)\n"
        "s_barrier\n"
        "v_lshl_or_b32 v20, s26, 1, 1\n"
        "s_add_u32 s29, s29, 1024\n"
        "s_add_u32 s80, s29, s30\n"
        "s_mov_b64 exec, 1\n"
        "buffer_atomic_umax v20, off, s[36:39], s80\n"
        "s_mov_b64 exec, -1\n"
        "s_waitcnt vmcnt(1)\n"
        "L_par_3%=:\n"
        "v_readfirstlane_b32 s80, v107\n"
        "s_nop 0\n"
        "s_cmp_lt_i32 s80, 0\n"
        "s_cbranch_scc1 L_par2_3%=\n"
        "s_add_u32 s63, s63, 1\n"
        "s_cmp_gt_u32 s63, 0x40000\n"
        "s_cbranch_scc1 L_abort%=\n"
        "s_sleep 4\n"
        "buffer_load_dword v107, off, s[36:39], s81 sc1\n"
        "s_waitcnt vmcnt(0)\n"
        "s_branch L_par_3%=\n"
        "L_par2_3%=:\n"
        "s_bfe_u32 s46, s80, 0x10018\n"
        "s_lshl_b32 s46, s46, 21\n"
        "s_mov_b32 s53, 0x200000\n"
        "s_bfe_u32 s84, s80, 0x80010\n"
        "s_sub_u32 s84, 255, s84\n"
        "s_bfe_u32 s76, s80, 0x80008\n"
        "s_sub_u32 s76, s76, 32\n"
        "s_max_i32 s76, s76, 66\n"
        "s_bfe_u32 s77, s80, 0x80000\n"
        "s_sub_u32 s77, s77, 158\n"
        "s_bitset0_b32 s49, 30\n"
        "s_bitset0_b32 s50, 30\n"
        "s_bitset0_b32 s56, 30\n"
        "s_bitset0_b32 s64, 30\n"
        "v_mov_b32 v140, 0\n"
        "s_mov_b32 s28, 1000\n"
        "s_branch L_xok_3%=\n"
        "L_x2_3%=:\n"
        "s_add_u32 s69, s58, s82\n"
        "s_sub_u32 s72, s58, s82\n"
        "s_add_u32 s87, s58, 5120\n"
        "buffer_load_dwordx4 v[64:67], v124, s[36:39], s69 offen offset:0 sc1\n"
        "buffer_load_dwordx4 v[68:71], v124, s[36:39], s69 offen offset:1024 sc1\n"
        "buffer_load_dwordx4 v[72:75], v124, s[36:39], s72 offen offset:2048 sc1\n"
        "buffer_load_dwordx4 v[76:79], v124, s[36:39], s72 offen offset:3072 sc1\n"
        "buffer_load_dwordx4 v[80:83], v124, s[36:39], s87 offen offset:0 sc1\n"
        "s_waitcnt vmcnt(0)\n"
        "s_memrealtime s[26:27]\n"
        "s_waitcnt lgkmcnt(0)\n"
        "s_add_u32 s28, s28, s26\n"
        "L_xok_3%=:\n"
        "v_mfma_f32_16x16x32_bf16 v[84:87], v[64:67], a[0:3], 0\n"
        "v_mfma_f32_16x16x32_bf16 v[88:91], v[64:67], a[8:11], 0\n"
        "v_mfma_f32_16x16x32_bf16 v[56:59], a[144:147], v[64:67], 0\n"
        "s_branch L_win_3%=\n"
        "L_retry_3%=:\n"
        "s_add_u32 s63, s63, 1\n"
        "s_cmp_gt_u32 s63, 0x40000\n"
        "s_cbranch_scc1 L_abort%=\n"
        "s_sleep 2\n"
        "s_waitcnt vmcnt(0)\n"
        "s_waitcnt lgkmcnt(0)\n"
        "buffer_load_dwordx4 v[80:83], v138, s[36:39], s50 offen offset:0 sc1\n"
        "buffer_load_dwordx4 v[64:67], v138, s[36:39], s49 offen offset:0 sc1\n"
        "buffer_load_dwordx4 v[68:71], v138, s[36:39], s49 offen offset:256 sc1\n"
        "buffer_load_dwordx4 v[72:75], v138, s[36:39], s65 offen offset:0 sc1\n"
        "buffer_load_dwordx4 v[76:79], v138, s[36:39], s65 offen offset:256 sc1\n"
        "s_waitcnt vmcnt(4)\n"
        "v_bitop3_b32 v123, 32, v80, v82 bitop3:0x80\n"
        "v_add_u32_dpp v80, v81, v80 row_half_mirror row_mask:0xf bank_mask:0xf\n"
        "v_add_u32_dpp v82, v83, v82 row_half_mirror row_mask:0xf bank_mask:0xf\n"
        "v_cvt_f32_i32 v80, v80\n"
        "v_cvt_f32_i32 v82, v82\n"
        "v_cvt_pk_bf16_f32 v52, v80, v82\n"
        "ds_write_b32 v137, v52\n"
        "s_waitcnt vmcnt(3)\n"
        "v_bitop3_b32 v123, v123, v64, v66 bitop3:0x80\n"
        "v_cvt_f32_i32 v64, v64\n"
        "v_cvt_f32_i32 v65, v65\n"
        "v_cvt_f32_i32 v66, v66\n"
        "v_cvt_f32_i32 v67, v67\n"
        "v_cvt_pk_bf16_f32 v52, v64, v65\n"
        "v_cvt_pk_bf16_f32 v53, v66, v67\n"
        "ds_write_b64 v126, v[52:53] offset:0\n"
        "s_waitcnt vmcnt(2)\n"
        "v_bitop3_b32 v123, v123, v68, v70 bitop3:0x80\n"
        "v_cvt_f32_i32 v68, v68\n"
        "v_cvt_f32_i32 v69, v69\n"
        "v_cvt_f32_i32 v70, v70\n"
        "v_cvt_f32_i32 v71, v71\n"
        "v_cvt_pk_bf16_f32 v54, v68, v69\n"
        "v_cvt_pk_bf16_f32 v55, v70, v71\n"
        "ds_write_b64 v126, v[54:55] offset:512\n"
        "s_waitcnt vmcnt(1)\n"
        "v_bitop3_b32 v123, v123, v72, v74 bitop3:0x80\n"
        "v_cvt_f32_i32 v72, v72\n"
        "v_cvt_f32_i32 v73, v73\n"
        "ds_read_b128 v[48:51], v124 offset:6144\n"
        "v_cvt_f32_i32 v74, v74\n"
        "v_cvt_f32_i32 v75, v75\n"
        "v_cvt_pk_bf16_f32 v52, v72, v73\n"
        "v_cvt_pk_bf16_f32 v53, v74, v75\n"
        "ds_write_b64 v126, v[52:53] offset:1024\n"
        "s_waitcnt vmcnt(0)\n"
        "v_bitop3_b32 v123, v123, v76, v78 bitop3:0x80\n"
        "v_cmp_eq_u32 vcc, 0, v123\n"
        "v_mov_b64 v[122:123], 0\n"
        "v_cvt_f32_i32 v76, v76\n"
        "v_cvt_f32_i32 v77, v77\n"
        "v_cvt_f32_i32 v78, v78\n"
        "v_cvt_f32_i32 v79, v79\n"
        "v_cvt_pk_bf16_f32 v54, v76, v77\n"
        "v_cvt_pk_bf16_f32 v55, v78, v79\n"
        "ds_write_b64 v126, v[54:55] offset:1536\n"
        "v_mov_b64 v[92:93], 0\n"
        "v_mov_b64 v[94:95], 0\n"
        "v_mov_b64 v[96:97], 0\n"
        "v_mov_b64 v[98:99], 0\n"
        "s_waitcnt lgkmcnt(0)\n"
        "s_branch L_dec_3%=\n"
        "L_sw0_3%=:\n"
        "s_memrealtime s[26:27]\n"
        "s_waitcnt lgkmcnt(0)\n"
        "s_sub_u32 s28, s28, s26\n"
        "L_sw_3%=:\n"
        "s_add_u32 s63, s63, 1\n"
        "s_cmp_gt_u32 s63, 0x40000\n"
        "s_cbranch_scc1 L_abort%=\n"
        "s_sleep 2\n"
        "buffer_load_dword v140, v124, s[36:39], s29 offen sc1\n"
        "s_waitcnt vmcnt(0)\n"
        "v_cmp_eq_u32 vcc, 0, v140\n"
        "s_cbranch_vccnz L_sw_3%=\n"
        "s_memrealtime s[26:27]\n"
        "s_waitcnt lgkmcnt(0)\n"
        "s_add_u32 s28, s28, s26\n"
        "s_branch L_sok_3%=\n"
        "L_xs_3%=:\n"
        "s_branch L_xs2_3%=\n"
        "L_xs1_3%=:\n"
        "s_add_u32 s63, s63, 1\n"
        "s_cmp_gt_u32 s63, 0x40000\n"
        "s_cbranch_scc1 L_abort%=\n"
        "s_sleep 4\n"
        "L_xs2_3%=:\n"
        "v_mov_b32 v140, 0\n"
        "buffer_atomic_add v140, v143, s[36:39], s57 offen sc0\n"
        "s_waitcnt vmcnt(0)\n"
        "v_cmp_ge_u32 vcc, s31, v140\n"
        "s_and_b32 vcc_lo, vcc_lo, 0x1ff\n"
        "s_cbranch_scc1 L_xs1_3%=\n"
        "s_branch L_xr_3%=\n"
        ".p2align 6\n"
        "L_rot_3%=:\n"
        "v_mfma_f32_16x16x32_bf16 v[48:51], v[196:199], v[100:103], v[48:51]\n"
        "v_mfma_f32_16x16x32_bf16 v[52:55], v[204:207], v[100:103], v[52:55]\n"
        "s_waitcnt lgkmcnt(10)\n"
        "v_mfma_f32_16x16x32_bf16 v[84:87], v[92:95], a[96:99], v[84:87]\n"
        "ds_read_b128 v[100:103], v124 offset:3072\n"
        "v_mfma_f32_16x16x32_bf16 v[88:91], v[92:95], a[104:107], v[88:91]\n"
        "v_mfma_f32_16x16x32_bf16 v[56:59], v[0:3], v[92:95], v[56:59]\n"
        "v_mfma_f32_16x16x32_bf16 v[60:63], v[8:11], v[92:95], v[60:63]\n"
        "v_mfma_f32_16x16x32_bf16 v[48:51], v[208:211], v[92:95], v[48:51]\n"
        "s_waitcnt lgkmcnt(0)\n"
        "s_barrier\n"
        "v_mfma_f32_16x16x32_bf16 v[52:55], v[216:219], v[92:95], v[52:55]\n"
        "v_mov_b64 v[116:117], 0\n"
        "v_mfma_f32_16x16x32_bf16 v[84:87], v[104:107], a[100:103], v[84:87]\n"
        "ds_read_b128 v[92:95], v124 offset:16384\n"
        "v_mov_b32 v140, 0\n"
        "v_mfma_f32_16x16x32_bf16 v[88:91], v[104:107], a[108:111], v[88:91]\n"
        "buffer_atomic_add v140, v143, s[36:39], s57 offen sc0\n"
        "v_mov_b64 v[118:119], 0\n"
        "v_mfma_f32_16x16x32_bf16 v[56:59], v[4:7], v[104:107], v[56:59]\n"
        "v_mov_b64 v[120:121], 0\n"
        "v_mfma_f32_16x16x32_bf16 v[60:63], v[12:15], v[104:107], v[60:63]\n"
        "v_mfma_f32_16x16x32_bf16 v[48:51], v[212:215], v[104:107], v[48:51]\n"
        "v_mfma_f32_16x16x32_bf16 v[52:55], v[220:223], v[104:107], v[52:55]\n"
        "v_mfma_f32_16x16x32_bf16 v[48:51], v[224:227], v[96:99], v[48:51]\n"
        "ds_read_b128 v[104:107], v124 offset:17408\n"
        "v_mfma_f32_16x16x32_bf16 v[52:55], v[232:235], v[96:99], v[52:55]\n"
        "v_mfma_f32_16x16x32_bf16 v[48:51], v[228:231], v[100:103], v[48:51]\n"
        "v_mfma_f32_16x16x32_bf16 v[52:55], v[236:239], v[100:103], v[52:55]\n"
        "s_waitcnt lgkmcnt(1)\n"
        "v_mfma_f32_16x16x32_bf16 v[48:51], v[240:243], v[92:95], v[48:51]\n"
        "v_mfma_f32_16x16x32_bf16 v[52:55], v[248:251], v[92:95], v[52:55]\n"
        "s_waitcnt lgkmcnt(0)\n"
        "v_mfma_f32_16x16x32_bf16 v[48:51], v[244:247], v[104:107], v[48:51]\n"
        "s_cmp_lt_i32 s44, 0\n"
        "s_cbranch_scc1 L_light_3%=\n"
        "v_mfma_f32_16x16x32_bf16 v[52:55], v[252:255], v[104:107], v[52:55]\n"
        "s_waitcnt vmcnt(5)\n"
        "v_bitop3_b32 v123, 32, v80, v82 bitop3:0x80\n"
        "s_nop 0\n"
        "v_add_u32_dpp v80, v81, v80 row_half_mirror row_mask:0xf bank_mask:0xf\n"
        "v_mfma_f32_16x16x32_bf16 v[84:87], v[96:99], a[112:115], v[84:87]\n"
        "s_nop 0\n"
        "v_add_u32_dpp v82, v83, v82 row_half_mirror row_mask:0xf bank_mask:0xf\n"
        "v_mfma_f32_16x16x32_bf16 v[88:91], v[96:99], a[120:123], v[88:91]\n"
        "v_cvt_pk_bf16_f32 v48, v48, v52\n"
        "v_cvt_pk_bf16_f32 v49, v49, v53\n"
        "s_nop 0\n"
        "v_mfma_f32_16x16x32_bf16 v[84:87], v[100:103], a[116:119], v[84:87]\n"
        "v_cvt_pk_bf16_f32 v50, v50, v54\n"
        "v_cvt_pk_bf16_f32 v51, v51, v55\n"
        "v_mfma_f32_16x16x32_bf16 v[88:91], v[100:103], a[124:127], v[88:91]\n"
        "v_cvt_f32_i32 v80, v80\n"
        "s_nop 0\n"
        "v_cvt_f32_i32 v82, v82\n"
        "v_mfma_f32_16x16x32_bf16 v[84:87], v[92:95], a[128:131], v[84:87]\n"
        "s_nop 0\n"
        "v_cvt_pk_bf16_f32 v52, v80, v82\n"
        "v_mfma_f32_16x16x32_bf16 v[88:91], v[92:95], a[136:139], v[88:91]\n"
        "s_nop 0\n"
        "v_mfma_f32_16x16x32_bf16 v[84:87], v[104:107], a[132:135], v[84:87]\n"
        "buffer_store_dwordx4 v[48:51], v124, s[36:39], s54 offen offset:1024 sc1\n"
        "v_mfma_f32_16x16x32_bf16 v[88:91], v[104:107], a[140:143], v[88:91]\n"
        "ds_write_b32 v137, v52\n"
        "s_waitcnt vmcnt(5)\n"
        "v_bitop3_b32 v123, v123, v64, v66 bitop3:0x80\n"
        "v_cvt_f32_i32 v64, v64\n"
        "v_mfma_f32_16x16x32_bf16 v[56:59], v[32:35], v[92:95], v[56:59]\n"
        "v_cvt_f32_i32 v65, v65\n"
        "v_cvt_f32_i32 v66, v66\n"
        "v_mfma_f32_16x16x32_bf16 v[60:63], v[40:43], v[92:95], v[60:63]\n"
        "v_cvt_pk_bf16_f32 v84, v84, v85\n"
        "v_cvt_pk_bf16_f32 v85, v86, v87\n"
        "v_mfma_f32_16x16x32_bf16 v[56:59], v[16:19], v[96:99], v[56:59]\n"
        "v_cvt_pk_bf16_f32 v86, v88, v89\n"
        "v_cvt_pk_bf16_f32 v87, v90, v91\n"
        "v_mfma_f32_16x16x32_bf16 v[60:63], v[24:27], v[96:99], v[60:63]\n"
        "v_cvt_f32_i32 v67, v67\n"
        "v_cvt_pk_bf16_f32 v52, v64, v65\n"
        "v_mfma_f32_16x16x32_bf16 v[56:59], v[20:23], v[100:103], v[56:59]\n"
        "v_mov_b64 v[92:93], 0\n"
        "v_mov_b64 v[94:95], 0\n"
        "v_mfma_f32_16x16x32_bf16 v[60:63], v[28:31], v[100:103], v[60:63]\n"
        "v_mov_b64 v[96:97], 0\n"
        "v_mov_b64 v[98:99], 0\n"
        "v_mfma_f32_16x16x32_bf16 v[56:59], v[36:39], v[104:107], v[56:59]\n"
        "buffer_store_dwordx4 v[84:87], v124, s[36:39], s54 offen offset:2048 sc1\n"
        "v_cvt_pk_bf16_f32 v53, v66, v67\n"
        "v_mfma_f32_16x16x32_bf16 v[60:63], v[44:47], v[104:107], v[60:63]\n"
        "ds_write_b64 v126, v[52:53] offset:0\n"
        "s_waitcnt vmcnt(5)\n"
        "v_bitop3_b32 v123, v123, v68, v70 bitop3:0x80\n"
        "v_cvt_f32_i32 v68, v68\n"
        "v_cvt_f32_i32 v69, v69\n"
        "v_cvt_f32_i32 v70, v70\n"
        "v_cvt_f32_i32 v71, v71\n"
        "v_cvt_pk_bf16_f32 v54, v68, v69\n"
        "v_cvt_pk_bf16_f32 v55, v70, v71\n"
        "ds_write_b64 v126, v[54:55] offset:512\n"
        "s_waitcnt vmcnt(4)\n"
        "v_bitop3_b32 v123, v123, v72, v74 bitop3:0x80\n"
        "v_cvt_pk_bf16_f32 v100, v56, v60\n"
        "v_cvt_pk_bf16_f32 v101, v57, v61\n"
        "v_cvt_pk_bf16_f32 v102, v58, v62\n"
        "v_cvt_pk_bf16_f32 v103, v59, v63\n"
        "v_cvt_f32_i32 v72, v72\n"
        "v_cvt_f32_i32 v73, v73\n"
        "ds_read_b128 v[48:51], v124 offset:6144\n"
        "v_cvt_f32_i32 v74, v74\n"
        "v_cvt_f32_i32 v75, v75\n"
        "v_cvt_pk_bf16_f32 v52, v72, v73\n"
        "v_cvt_pk_bf16_f32 v53, v74, v75\n"
        "ds_write_b64 v126, v[52:53] offset:1024\n"
        "s_waitcnt vmcnt(3)\n"
        "v_bitop3_b32 v123, v123, v76, v78 bitop3:0x80\n"
        "v_cmp_eq_u32 vcc, 0, v123\n"
        "v_mov_b64 v[122:123], 0\n"
        "v_cvt_f32_i32 v76, v76\n"
        "v_cvt_f32_i32 v77, v77\n"
        "v_cvt_f32_i32 v78, v78\n"
        "v_cvt_f32_i32 v79, v79\n"
        "v_cvt_pk_bf16_f32 v54, v76, v77\n"
        "v_cvt_pk_bf16_f32 v55, v78, v79\n"
        "ds_write_b64 v126, v[54:55] offset:1536\n"
        "L_dec_3%=:\n"
        "s_waitcnt lgkmcnt(2)\n"
        "v_smfmac_f32_16x16x64_bf16 v[92:95], v[48:51], a[144:151], v134\n"
        "ds_read_b128 v[52:55], v124 offset:7168\n"
        "v_max3_f32 v104, |v80|, |v82|, |v64|\n"
        "v_smfmac_f32_16x16x64_bf16 v[96:99], v[48:51], a[152:159], v134\n"
        "s_cbranch_vccnz L_retry_3%=\n"
        "v_max3_f32 v104, |v65|, |v66|, v104\n"
        "v_smfmac_f32_16x16x64_bf16 v[116:119], v[48:51], a[240:247], v134\n"
        "s_add_u32 s29, s29, 1024\n"
        "s_add_u32 s80, s29, s30\n"
        "v_max3_f32 v104, |v67|, |v68|, v104\n"
        "v_smfmac_f32_16x16x64_bf16 v[120:123], v[48:51], a[248:255], v134\n"
        "s_waitcnt lgkmcnt(0)\n"
        "v_max3_f32 v104, |v69|, |v70|, v104\n"
        "s_barrier\n"
        "ds_read_b128 v[56:59], v124 offset:0\n"
        "v_smfmac_f32_16x16x64_bf16 v[92:95], v[52:55], a[160:167], v134\n"
        "ds_read_b128 v[60:63], v124 offset:3072\n"
        "v_max3_f32 v104, |v71|, |v72|, v104\n"
        "v_smfmac_f32_16x16x64_bf16 v[96:99], v[52:55], a[168:175], v134\n"
        "ds_read_b128 v[80:83], v124 offset:1024\n"
        "v_max3_f32 v104, |v73|, |v74|, v104\n"
        "v_smfmac_f32_16x16x64_bf16 v[116:119], v[52:55], v[176:183], v134\n"
        "buffer_load_dwordx4 v[88:91], v124, s[36:39], s55 offen offset:3072 sc1\n"
        "ds_read_b128 v[64:67], v124 offset:8192\n"
        "v_max3_f32 v104, |v75|, |v76|, v104\n"
        "v_smfmac_f32_16x16x64_bf16 v[120:123], v[52:55], v[184:191], v134\n"
        "ds_read_b128 v[68:71], v124 offset:4096\n"
        "v_max3_f32 v104, |v77|, |v78|, v104\n"
        "s_waitcnt lgkmcnt(4)\n"
        "v_smfmac_f32_16x16x64_bf16 v[92:95], v[56:59], v[0:7], v134\n"
        "buffer_load_dwordx4 v[84:87], v124, s[36:39], s55 offen offset:0 sc1\n"
        "ds_read_b128 v[72:75], v124 offset:5120\n"
        "v_max_f32_e64 v104, |v79|, v104\n"
        "v_smfmac_f32_16x16x64_bf16 v[96:99], v[56:59], v[8:15], v134\n"
        "ds_read_b128 v[0:3], v125 offset:12288\n"
        "v_max_f32_dpp v104, v104, v104 row_shr:1 row_mask:0xf bank_mask:0xf\n"
        "s_waitcnt lgkmcnt(3)\n"
        "v_smfmac_f32_16x16x64_bf16 v[92:95], v[60:63], a[224:231], v134\n"
        "ds_read_b128 v[76:79], v124 offset:2048\n"
        "v_max_f32_dpp v104, v104, v104 row_shr:2 row_mask:0xf bank_mask:0xf\n"
        "v_smfmac_f32_16x16x64_bf16 v[96:99], v[60:63], a[232:239], v134\n"
        "ds_read_b128 v[4:7], v125 offset:13312\n"
        "v_max_f32_dpp v104, v104, v104 row_shr:4 row_mask:0xf bank_mask:0xf\n"
        "v_smfmac_f32_16x16x64_bf16 v[92:95], v[80:83], v[16:23], v134\n"
        "ds_read_b128 v[8:11], v125 offset:14336\n"
        "v_max_f32_dpp v104, v104, v104 row_shr:8 row_mask:0xf bank_mask:0xf\n"
        "v_smfmac_f32_16x16x64_bf16 v[96:99], v[80:83], v[24:31], v134\n"
        "ds_read_b128 v[12:15], v125 offset:15360\n"
        "v_max_f32_dpp v104, v104, v104 row_bcast:15 row_mask:0xa bank_mask:0xf\n"
        "v_smfmac_f32_16x16x64_bf16 v[92:95], v[64:67], v[32:39], v134\n"
        "ds_read_b128 v[16:19], v125 offset:16384\n"
        "v_max_f32_dpp v104, v104, v104 row_bcast:31 row_mask:0xc bank_mask:0xf\n"
        "v_smfmac_f32_16x16x64_bf16 v[96:99], v[64:67], v[40:47], v134\n"
        "ds_read_b128 v[20:23], v125 offset:17408\n"
        "v_readlane_b32 s85, v104, 63\n"
        "s_waitcnt lgkmcnt(5)\n"
        "v_smfmac_f32_16x16x64_bf16 v[92:95], v[68:71], a[176:183], v134\n"
        "ds_read_b128 v[24:27], v125 offset:18432\n"
        "v_smfmac_f32_16x16x64_bf16 v[96:99], v[68:71], a[184:191], v134\n"
        "ds_read_b128 v[28:31], v125 offset:19456\n"
        "v_mov_b32 v104, s85\n"
        "v_smfmac_f32_16x16x64_bf16 v[92:95], v[72:75], a[192:199], v134\n"
        "ds_read_b128 v[32:35], v125 offset:20480\n"
        "v_smfmac_f32_16x16x64_bf16 v[96:99], v[72:75], a[200:207], v134\n"
        "ds_read_b128 v[36:39], v125 offset:21504\n"
        "s_add_u32 s69, s58, s82\n"
        "v_smfmac_f32_16x16x64_bf16 v[92:95], v[76:79], a[208:215], v134\n"
        "ds_read_b128 v[40:43], v125 offset:22528\n"
        "s_sub_u32 s72, s58, s82\n"
        "v_smfmac_f32_16x16x64_bf16 v[96:99], v[76:79], a[216:223], v134\n"
        "ds_read_b128 v[44:47], v125 offset:23552\n"
        "s_add_u32 s87, s58, 5120\n"
        "v_smfmac_f32_16x16x64_bf16 v[116:119], v[64:67], v[240:247], v134\n"
        "ds_write_b32 v142, v104\n"
        "v_add_f32 v92, v92, v94\n"
        "v_add_f32 v93, v93, v95\n"
        "v_smfmac_f32_16x16x64_bf16 v[120:123], v[64:67], v[248:255], v134\n"
        "v_add_f32 v96, v96, v98\n"
        "v_add_f32 v97, v97, v99\n"
        "v_cvt_pk_bf16_f32 v92, v92, v96\n"
        "v_cvt_pk_bf16_f32 v93, v93, v97\n"
        "s_waitcnt lgkmcnt(3)\n"
        "v_smfmac_f32_16x16x64_bf16 v[116:119], v[68:71], v[0:7], v134\n"
        "ds_write2_b32 v127, v92, v93 offset1:72\n"
        "s_waitcnt vmcnt(4)\n"
        "v_cmp_ge_u32 vcc, s31, v140\n"
        "v_smfmac_f32_16x16x64_bf16 v[120:123], v[68:71], v[8:15], v134\n"
        "s_and_b32 vcc_lo, vcc_lo, 0x1ff\n"
        "s_cbranch_scc1 L_xs_3%=\n"
        "L_xr_3%=:\n"
        "s_memrealtime s[26:27]\n"
        "v_smfmac_f32_16x16x64_bf16 v[116:119], v[72:75], v[16:23], v134\n"
        "v_mov_b64 v[96:97], 0\n"
        "v_mov_b64 v[106:107], 0\n"
        "v_smfmac_f32_16x16x64_bf16 v[120:123], v[72:75], v[24:31], v134\n"
        "s_waitcnt vmcnt(3)\n"
        "s_waitcnt lgkmcnt(0)\n"
        "v_lshl_or_b32 v20, s26, 1, 1\n"
        "s_barrier\n"
        "ds_read_b128 v[0:3], v128 offset:0\n"
        "v_smfmac_f32_16x16x64_bf16 v[116:119], v[76:79], v[32:39], v134\n"
        "s_mov_b64 exec, 1\n"
        "buffer_atomic_umax v20, off, s[36:39], s80\n"
        "s_mov_b64 exec, -1\n"
        "ds_read_b128 v[24:27], v124 offset:20480\n"
        "v_mov_b64 v[104:105], 0\n"
        "v_smfmac_f32_16x16x64_bf16 v[120:123], v[76:79], v[40:47], v134\n"
        "ds_read_b128 v[4:7], v128 offset:64\n"
        "v_mov_b64 v[94:95], 0\n"
        "v_smfmac_f32_16x16x64_bf16 v[116:119], v[60:63], v[192:199], v134\n"
        "ds_read_b128 v[8:11], v128 offset:128\n"
        "v_mov_b64 v[92:93], 0\n"
        "v_smfmac_f32_16x16x64_bf16 v[120:123], v[60:63], v[200:207], v134\n"
        "ds_read_b128 v[12:15], v128 offset:192\n"
        "v_mov_b64 v[20:21], 0\n"
        "v_smfmac_f32_16x16x64_bf16 v[116:119], v[56:59], v[208:215], v134\n"
        "s_waitcnt lgkmcnt(3)\n"
        "v_max3_f32 v24, v24, v25, v26\n"
        "v_mov_b64 v[98:99], 0\n"
        "s_waitcnt lgkmcnt(2)\n"
        "v_smfmac_f32_16x16x64_bf16 v[104:107], v[108:111], v[0:7], v134\n"
        "v_max_f32 v24, v24, v27\n"
        "v_smfmac_f32_16x16x64_bf16 v[120:123], v[56:59], v[216:223], v134\n"
        "v_readfirstlane_b32 s84, v24\n"
        "s_waitcnt lgkmcnt(0)\n"
        "v_smfmac_f32_16x16x64_bf16 v[104:107], v[112:115], v[8:15], v134\n"
        "v_mov_b64 v[22:23], 0\n"
        "v_smfmac_f32_16x16x64_bf16 v[116:119], v[80:83], v[224:231], v134\n"
        "s_lshr_b32 s84, s84, 23\n"
        "s_add_u32 s84, s84, s101\n"
        "v_smfmac_f32_16x16x64_bf16 v[120:123], v[80:83], v[232:239], v134\n"
        "s_max_i32 s84, s84, s76\n"
        "s_min_i32 s84, s84, 186\n"
        "v_add_f32 v104, v104, v106\n"
        "v_add_f32 v105, v105, v107\n"
        "v_smfmac_f32_16x16x64_bf16 v[92:95], v[64:67], a[128:135], v134\n"
        "ds_write_b64 v131, v[104:105]\n"
        "v_add_f32 v116, v116, v118\n"
        "v_add_f32 v117, v117, v119\n"
        "v_pk_add_f32 v[120:121], v[120:121], v[122:123]\n"
        "v_cvt_pk_bf16_f32 v116, v116, v120\n"
        "v_cvt_pk_bf16_f32 v117, v117, v121\n"
        "ds_write2_b32 v129, v116, v117 offset1:8\n"
        "v_smfmac_f32_16x16x64_bf16 v[96:99], v[64:67], a[136:143], v134\n"
        "s_mul_i32 s88, s84, 0x800080\n"
        "s_mov_b32 s89, s88\n"
        "v_smfmac_f32_16x16x64_bf16 v[92:95], v[68:71], a[32:39], v134\n"
        "s_add_u32 s90, s88, s35\n"
        "s_mov_b32 s91, s90\n"
        "v_mov_b64 v[104:105], s[88:89]\n"
        "v_smfmac_f32_16x16x64_bf16 v[96:99], v[68:71], a[40:47], v134\n"
        "buffer_store_dwordx4 v[100:103], v124, s[36:39], s54 offen offset:0 sc1\n"
        "v_mov_b64 v[106:107], s[90:91]\n"
        "v_smfmac_f32_16x16x64_bf16 v[92:95], v[48:51], a[0:7], v134\n"
        "s_waitcnt lgkmcnt(0)\n"
        "v_mov_b64 v[100:101], s[88:89]\n"
        "s_barrier\n"
        "ds_read_b128 v[24:27], v130\n"
        "v_smfmac_f32_16x16x64_bf16 v[96:99], v[48:51], a[8:15], v134\n"
        "ds_read_b128 v[28:31], v130 offset:1024\n"
        "v_mov_b64 v[102:103], s[90:91]\n"
        "v_smfmac_f32_16x16x64_bf16 v[92:95], v[52:55], a[16:23], v134\n"
        "ds_read_b128 v[116:119], v124 offset:13568\n"
        "v_mov_b64 v[48:49], 0\n"
        "v_smfmac_f32_16x16x64_bf16 v[96:99], v[52:55], a[24:31], v134\n"
        "v_mov_b64 v[50:51], 0\n"
        "v_smfmac_f32_16x16x64_bf16 v[92:95], v[56:59], a[96:103], v134\n"
        "buffer_load_dwordx4 v[64:67], v124, s[36:39], s69 offen offset:0 sc1\n"
        "v_mov_b64 v[52:53], 0\n"
        "v_smfmac_f32_16x16x64_bf16 v[96:99], v[56:59], a[104:111], v134\n"
        "v_mov_b64 v[54:55], 0\n"
        "s_waitcnt lgkmcnt(2)\n"
        "v_smfmac_f32_16x16x64_bf16 v[20:23], v[24:27], v[0:7], v134\n"
        "buffer_load_dwordx4 v[68:71], v124, s[36:39], s69 offen offset:1024 sc1\n"
        "v_mov_b64 v[56:57], 0\n"
        "s_waitcnt lgkmcnt(0)\n"
        "v_smfmac_f32_16x16x64_bf16 v[20:23], v[28:31], v[8:15], v134\n"
        "ds_read_b128 v[0:3], v125 offset:24576\n"
        "v_max3_f32 v121, v116, v117, v118\n"
        "v_smfmac_f32_16x16x64_bf16 v[92:95], v[72:75], a[48:55], v134\n"
        "v_mov_b64 v[58:59], 0\n"
        "v_smfmac_f32_16x16x64_bf16 v[96:99], v[72:75], a[56:63], v134\n"
        "ds_read_b128 v[4:7], v125 offset:25600\n"
        "v_add_f32 v20, v20, v22\n"
        "v_add_f32 v21, v21, v23\n"
        "v_cndmask_b32 v120, v20, v21, s[92:93]\n"
        "v_mul_f32 v120, s74, v120\n"
        "v_max3_f32 v121, v121, v119, v120\n"
        "v_smfmac_f32_16x16x64_bf16 v[92:95], v[76:79], a[64:71], v134\n"
        "buffer_load_dwordx4 v[72:75], v124, s[36:39], s72 offen offset:2048 sc1\n"
        "v_max_f32_dpp v121, v121, v121 quad_perm:[2,3,0,1] row_mask:0xf bank_mask:0xf\n"
        "v_mov_b32 v122, v121\n"
        "v_smfmac_f32_16x16x64_bf16 v[96:99], v[76:79], a[72:79], v134\n"
        "ds_read_b128 v[8:11], v125 offset:26624\n"
        "v_permlane16_swap_b32 v121, v122\n"
        "v_max_f32 v121, v121, v122\n"
        "v_mov_b32 v122, v121\n"
        "v_smfmac_f32_16x16x64_bf16 v[92:95], v[80:83], a[112:119], v134\n"
        "buffer_load_dwordx4 v[76:79], v124, s[36:39], s72 offen offset:3072 sc1\n"
        "v_permlane32_swap_b32 v121, v122\n"
        "v_smfmac_f32_16x16x64_bf16 v[96:99], v[80:83], a[120:127], v134\n"
        "ds_read_b128 v[12:15], v125 offset:27648\n"
        "v_max_f32 v121, v121, v122\n"
        "v_mul_f32_e64 v123, -s73, v121\n"
        "v_fma_f32 v120, v120, s73, v123\n"
        "v_fma_f32 v116, v116, s73, v123\n"
        "v_smfmac_f32_16x16x64_bf16 v[92:95], v[60:63], a[80:87], v134\n"
        "buffer_load_dwordx4 v[80:83], v124, s[36:39], s87 offen offset:0 sc1\n"
        "v_fma_f32 v117, v117, s73, v123\n"
        "v_fma_f32 v118, v118, s73, v123\n"
        "v_fma_f32 v119, v119, s73, v123\n"
        "v_exp_f32 v120, v120\n"
        "v_smfmac_f32_16x16x64_bf16 v[96:99], v[60:63], a[88:95], v134\n"
        "ds_read_b128 v[16:19], v125 offset:28672\n"
        "v_exp_f32 v116, v116\n"
        "v_mul_f32 v120, s74, v120\n"
        "v_exp_f32 v117, v117\n"
        "v_exp_f32 v118, v118\n"
        "v_exp_f32 v119, v119\n"
        "s_waitcnt vmcnt(7)\n"
        "v_mov_b64 v[108:109], v[86:87]\n"
        "v_mov_b64 v[112:113], v[90:91]\n"
        "v_cvt_pk_bf16_f32 v120, v120, v120\n"
        "v_cvt_pk_bf16_f32 v60, v116, v117\n"
        "v_cvt_pk_bf16_f32 v62, v118, v119\n"
        "v_and_b32 v61, v120, v135\n"
        "v_and_b32 v63, v120, v136\n"
        "v_pk_add_f32 v[92:93], v[92:93], v[94:95]\n"
        "v_pk_add_f32 v[96:97], v[96:97], v[98:99]\n"
        "v_smfmac_f32_16x16x64_bf16 v[56:59], v[60:63], v[100:107], v134\n"
        "ds_read_b128 v[20:23], v125 offset:29696\n"
        "v_cvt_pk_bf16_f32 v86, v92, v93\n"
        "v_cvt_pk_bf16_f32 v87, v92, v93\n"
        "v_mov_b64 v[90:91], v[86:87]\n"
        "v_cvt_pk_bf16_f32 v110, v96, v97\n"
        "v_cvt_pk_bf16_f32 v111, v96, v97\n"
        "v_smfmac_f32_16x16x64_bf16 v[48:51], v[60:63], v[84:91], v134\n"
        "ds_read_b128 v[24:27], v125 offset:30720\n"
        "v_mov_b64 v[114:115], v[110:111]\n"
        "v_add_f32 v56, v56, v58\n"
        "v_add_f32 v57, v57, v59\n"
        "v_smfmac_f32_16x16x64_bf16 v[52:55], v[60:63], v[108:115], v134\n"
        "s_nop 1\n"
        "v_rcp_f32 v56, v56\n"
        "v_rcp_f32 v57, v57\n"
        "v_pk_add_f32 v[48:49], v[48:49], v[50:51]\n"
        "v_pk_mul_f32 v[48:49], v[48:49], v[56:57]\n"
        "v_mov_b32 v110, v86\n"
        "v_mov_b64 v[92:93], s[70:71]\n"
        "v_pk_add_f32 v[52:53], v[52:53], v[54:55]\n"
        "v_pk_mul_f32 v[52:53], v[52:53], v[56:57]\n"
        "v_cvt_pk_bf16_f32 v48, v48, v52\n"
        "v_cvt_pk_bf16_f32 v49, v49, v53\n"
        "ds_write2_b32 v139, v48, v49 offset1:4\n"
        "v_mov_b64 v[94:95], 0\n"
        "v_mov_b64 v[96:97], 0\n"
        "v_mov_b64 v[98:99], 0\n"
        "v_mov_b64 v[100:101], s[70:71]\n"
        "v_mov_b64 v[102:103], 0\n"
        "v_mov_b64 v[104:105], 0\n"
        "buffer_store_dwordx2 v[110:111], v126, s[36:39], s60 offen sc1\n"
        "s_waitcnt lgkmcnt(0)\n"
        "v_mov_b64 v[106:107], 0\n"
        "s_barrier\n"
        "ds_read_b128 v[108:111], v124 offset:18432\n"
        "v_mov_b64 v[106:107], 0\n"
        "ds_read_b128 v[112:115], v124 offset:19456\n"
        "v_mov_b64 v[106:107], 0\n"
        "s_bitcmp1_b32 s59, 30\n"
        "s_cbranch_scc1 L_s1_3%=\n"
        "buffer_store_dwordx4 v[28:31], v124, s[36:39], s59 offen offset:1024 sc1\n"
        "L_s1_3%=:\n"
        "v_mov_b64 v[106:107], 0\n"
        "v_mov_b64 v[106:107], 0\n"
        "ds_read_b128 v[28:31], v125 offset:31744\n"
        "v_mov_b64 v[106:107], 0\n"
        "s_waitcnt lgkmcnt(2)\n"
        "v_smfmac_f32_16x16x64_bf16 v[92:95], v[108:111], v[144:151], v134\n"
        "v_smfmac_f32_16x16x64_bf16 v[96:99], v[108:111], v[152:159], v134\n"
        "s_waitcnt lgkmcnt(1)\n"
        "v_smfmac_f32_16x16x64_bf16 v[92:95], v[112:115], v[160:167], v134\n"
        "v_smfmac_f32_16x16x64_bf16 v[96:99], v[112:115], v[168:175], v134\n"
        "v_smfmac_f32_16x16x64_bf16 v[100:103], v[108:111], v[0:7], v134\n"
        "v_add_f32 v92, v92, v94\n"
        "v_add_f32 v93, v93, v95\n"
        "v_cvt_rpi_i32_f32 v94, v92\n"
        "v_smfmac_f32_16x16x64_bf16 v[104:107], v[108:111], v[8:15], v134\n"
        "v_bfi_b32 v94, 63, 1, v94\n"
        "v_add_f32 v96, v96, v98\n"
        "v_smfmac_f32_16x16x64_bf16 v[100:103], v[112:115], v[16:23], v134\n"
        "buffer_load_dword v140, v124, s[36:39], s29 offen sc1\n"
        "v_cvt_rpi_i32_f32 v95, v96\n"
        "s_waitcnt lgkmcnt(0)\n"
        "v_smfmac_f32_16x16x64_bf16 v[104:107], v[112:115], v[24:31], v134\n"
        "buffer_atomic_add_x2 v[94:95], v133, s[36:39], s51 offen\n"
        "v_add_f32 v97, v97, v99\n"
        "v_cvt_rpi_i32_f32 v98, v93\n"
        "v_bfi_b32 v98, 63, 1, v98\n"
        "v_cvt_rpi_i32_f32 v99, v97\n"
        "s_waitcnt vmcnt(7)\n"
        "v_mfma_f32_16x16x32_bf16 v[84:87], v[64:67], a[0:3], 0\n"
        "buffer_atomic_add_x2 v[98:99], v133, s[36:39], s66 offen\n"
        "v_add_f32 v100, v100, v102\n"
        "v_add_f32 v101, v101, v103\n"
        "v_add_f32 v104, v104, v106\n"
        "v_cvt_rpi_i32_f32 v102, v100\n"
        "v_bfi_b32 v102, 63, 1, v102\n"
        "v_cvt_rpi_i32_f32 v103, v104\n"
        "v_mfma_f32_16x16x32_bf16 v[88:91], v[64:67], a[8:11], 0\n"
        "buffer_atomic_add_x2 v[102:103], v133, s[36:39], s67 offen\n"
        "v_add_f32 v105, v105, v107\n"
        "v_cvt_rpi_i32_f32 v106, v101\n"
        "v_bfi_b32 v106, 63, 1, v106\n"
        "v_cvt_rpi_i32_f32 v107, v105\n"
        "v_mfma_f32_16x16x32_bf16 v[56:59], a[144:147], v[64:67], 0\n"
        "buffer_atomic_add_x2 v[106:107], v133, s[36:39], s68 offen\n"
        "L_win_3%=:\n"
        "v_mfma_f32_16x16x32_bf16 v[60:63], a[152:155], v[64:67], 0\n"
        "v_mfma_f32_16x16x32_bf16 v[48:51], a[240:243], v[64:67], 0\n"
        "v_mfma_f32_16x16x32_bf16 v[52:55], a[248:251], v[64:67], 0\n"
        "ds_write_b128 v124, v[64:67] offset:12288\n"
        "s_waitcnt vmcnt(9)\n"
        "v_mfma_f32_16x16x32_bf16 v[84:87], v[68:71], a[4:7], v[84:87]\n"
        "v_mfma_f32_16x16x32_bf16 v[88:91], v[68:71], a[12:15], v[88:91]\n"
        "ds_write_b128 v124, v[68:71] offset:13312\n"
        "v_mfma_f32_16x16x32_bf16 v[56:59], a[148:151], v[68:71], v[56:59]\n"
        "v_mfma_f32_16x16x32_bf16 v[60:63], a[156:159], v[68:71], v[60:63]\n"
        "s_waitcnt vmcnt(8)\n"
        "ds_write_b128 v124, v[72:75] offset:14336\n"
        "v_mfma_f32_16x16x32_bf16 v[48:51], a[244:247], v[68:71], v[48:51]\n"
        "v_mfma_f32_16x16x32_bf16 v[52:55], a[252:255], v[68:71], v[52:55]\n"
        "s_waitcnt vmcnt(7)\n"
        "ds_write_b128 v124, v[76:79] offset:15360\n"
        "v_mfma_f32_16x16x32_bf16 v[84:87], v[72:75], a[16:19], v[84:87]\n"
        "v_mfma_f32_16x16x32_bf16 v[88:91], v[72:75], a[24:27], v[88:91]\n"
        "s_waitcnt vmcnt(6)\n"
        "ds_write_b128 v124, v[80:83] offset:17408\n"
        "v_mfma_f32_16x16x32_bf16 v[56:59], a[160:163], v[72:75], v[56:59]\n"
        "v_mfma_f32_16x16x32_bf16 v[60:63], a[168:171], v[72:75], v[60:63]\n"
        "v_mfma_f32_16x16x32_bf16 v[48:51], v[176:179], v[72:75], v[48:51]\n"
        "ds_read_b128 v[0:3], v125 offset:12288\n"
        "v_mfma_f32_16x16x32_bf16 v[52:55], v[184:187], v[72:75], v[52:55]\n"
        "ds_read_b128 v[8:11], v125 offset:14336\n"
        "v_mfma_f32_16x16x32_bf16 v[84:87], v[76:79], a[20:23], v[84:87]\n"
        "s_waitcnt lgkmcnt(2)\n"
        "s_barrier\n"
        "v_mfma_f32_16x16x32_bf16 v[88:91], v[76:79], a[28:31], v[88:91]\n"
        "ds_read_b128 v[92:95], v124 offset:8192\n"
        "v_mov_b32 v116, s62\n"
        "v_mfma_f32_16x16x32_bf16 v[56:59], a[164:167], v[76:79], v[56:59]\n"
        "s_add_u32 s49, s49, 163840\n"
        "ds_read_b128 v[104:107], v124 offset:9216\n"
        "v_mfma_f32_16x16x32_bf16 v[60:63], a[172:175], v[76:79], v[60:63]\n"
        "s_add_u32 s50, s50, 163840\n"
        "ds_read_b128 v[4:7], v125 offset:13312\n"
        "v_mfma_f32_16x16x32_bf16 v[48:51], v[180:183], v[76:79], v[48:51]\n"
        "s_xor_b32 s49, s49, s86\n"
        "ds_read_b128 v[12:15], v125 offset:15360\n"
        "v_mfma_f32_16x16x32_bf16 v[52:55], v[188:191], v[76:79], v[52:55]\n"
        "s_xor_b32 s65, s49, 8192\n"
        "s_add_u32 s56, s56, 786432\n"
        "s_waitcnt lgkmcnt(3)\n"
        "v_mfma_f32_16x16x32_bf16 v[84:87], v[92:95], a[32:35], v[84:87]\n"
        "s_add_u32 s64, s64, 786432\n"
        "ds_read_b128 v[96:99], v124 offset:10240\n"
        "v_mfma_f32_16x16x32_bf16 v[88:91], v[92:95], a[40:43], v[88:91]\n"
        "ds_read_b128 v[100:103], v124 offset:11264\n"
        "v_mfma_f32_16x16x32_bf16 v[56:59], a[176:179], v[92:95], v[56:59]\n"
        "ds_read_b128 v[16:19], v125 offset:16384\n"
        "v_mfma_f32_16x16x32_bf16 v[60:63], a[184:187], v[92:95], v[60:63]\n"
        "s_add_u32 s51, s51, 163840\n"
        "ds_read_b128 v[20:23], v125 offset:17408\n"
        "v_mfma_f32_16x16x32_bf16 v[48:51], v[0:3], v[92:95], v[48:51]\n"
        "s_add_u32 s83, s83, 163840\n"
        "ds_read_b128 v[24:27], v125 offset:18432\n"
        "v_mfma_f32_16x16x32_bf16 v[52:55], v[8:11], v[92:95], v[52:55]\n"
        "ds_read_b128 v[28:31], v125 offset:19456\n"
        "s_waitcnt lgkmcnt(8)\n"
        "v_mfma_f32_16x16x32_bf16 v[84:87], v[104:107], a[36:39], v[84:87]\n"
        "s_xor_b32 s86, s83, s46\n"
        "ds_read_b128 v[92:95], v124 offset:4096\n"
        "v_mfma_f32_16x16x32_bf16 v[88:91], v[104:107], a[44:47], v[88:91]\n"
        "s_and_b32 s86, s86, s53\n"
        "s_lshr_b32 s86, s86, 8\n"
        "s_xor_b32 s86, s86, s34\n"
        "v_mfma_f32_16x16x32_bf16 v[56:59], a[180:183], v[104:107], v[56:59]\n"
        "s_xor_b32 s34, s34, s86\n"
        "s_xor_b32 s51, s51, s86\n"
        "s_add_u32 s55, s55, 786432\n"
        "v_mfma_f32_16x16x32_bf16 v[60:63], a[188:191], v[104:107], v[60:63]\n"
        "s_add_u32 s54, s54, 786432\n"
        "ds_read_b128 v[32:35], v125 offset:20480\n"
        "s_waitcnt lgkmcnt(9)\n"
        "v_mfma_f32_16x16x32_bf16 v[48:51], v[4:7], v[104:107], v[48:51]\n"
        "s_waitcnt vmcnt(5)\n"
        "s_mov_b64 exec, 1\n"
        "buffer_atomic_umax v116, off, s[36:39], s61\n"
        "s_mov_b64 exec, -1\n"
        "s_waitcnt lgkmcnt(8)\n"
        "v_mfma_f32_16x16x32_bf16 v[52:55], v[12:15], v[104:107], v[52:55]\n"
        "ds_read_b128 v[36:39], v125 offset:21504\n"
        "s_waitcnt lgkmcnt(8)\n"
        "v_mfma_f32_16x16x32_bf16 v[84:87], v[96:99], a[48:51], v[84:87]\n"
        "s_add_u32 s59, s59, 131072\n"
        "ds_read_b128 v[104:107], v124 offset:5120\n"
        "v_mfma_f32_16x16x32_bf16 v[88:91], v[96:99], a[56:59], v[88:91]\n"
        "s_cmp_eq_u32 s44, 63\n"
        "s_cbranch_scc1 L_end%=\n"
        "v_mfma_f32_16x16x32_bf16 v[56:59], a[192:195], v[96:99], v[56:59]\n"
        "s_add_u32 s60, s60, 131072\n"
        "ds_read_b128 v[40:43], v125 offset:22528\n"
        "v_mfma_f32_16x16x32_bf16 v[60:63], a[200:203], v[96:99], v[60:63]\n"
        "ds_read_b128 v[44:47], v125 offset:23552\n"
        "s_waitcnt vmcnt(5)\n"
        "v_cmp_eq_u32 vcc, 0, v140\n"
        "s_waitcnt lgkmcnt(9)\n"
        "v_mfma_f32_16x16x32_bf16 v[48:51], v[16:19], v[96:99], v[48:51]\n"
        "s_cbranch_vccnz L_sw0_3%=\n"
        "L_sok_3%=:\n"
        "s_waitcnt lgkmcnt(7)\n"
        "v_mfma_f32_16x16x32_bf16 v[52:55], v[24:27], v[96:99], v[52:55]\n"
        "v_readlane_b32 s81, v140, s99\n"
        "v_max_u32_dpp v140, v140, v140 row_shr:1 row_mask:0xf bank_mask:0xf\n"
        "v_mfma_f32_16x16x32_bf16 v[84:87], v[100:103], a[52:55], v[84:87]\n"
        "ds_read_b128 v[96:99], v124 offset:6144\n"
        "v_max_u32_dpp v140, v140, v140 row_shr:2 row_mask:0xf bank_mask:0xf\n"
        "v_mfma_f32_16x16x32_bf16 v[88:91], v[100:103], a[60:63], v[88:91]\n"
        "ds_read_b128 v[0:3], v125 offset:0\n"
        "v_max_u32_dpp v140, v140, v140 row_shr:4 row_mask:0xf bank_mask:0xf\n"
        "v_mfma_f32_16x16x32_bf16 v[56:59], a[196:199], v[100:103], v[56:59]\n"
        "buffer_load_dwordx4 v[108:111], v132, s[36:39], s56 offen offset:0 sc1\n"
        "v_max_u32_dpp v140, v140, v140 row_shr:8 row_mask:0xf bank_mask:0xf\n"
        "v_mfma_f32_16x16x32_bf16 v[60:63], a[204:207], v[100:103], v[60:63]\n"
        "ds_read_b128 v[4:7], v125 offset:1024\n"
        "v_max_u32_dpp v140, v140, v140 row_bcast:15 row_mask:0xa bank_mask:0xf\n"
        "v_mfma_f32_16x16x32_bf16 v[48:51], v[20:23], v[100:103], v[48:51]\n"
        "buffer_load_dwordx4 v[112:115], v132, s[36:39], s64 offen offset:0 sc1\n"
        "v_max_u32_dpp v140, v140, v140 row_bcast:31 row_mask:0xc bank_mask:0xf\n"
        "s_waitcnt lgkmcnt(9)\n"
        "v_mfma_f32_16x16x32_bf16 v[52:55], v[28:31], v[100:103], v[52:55]\n"
        "v_readlane_b32 s33, v140, 63\n"
        "s_lshl1_add_u32 s28, s28, s81\n"
        "s_waitcnt lgkmcnt(8)\n"
        "v_mfma_f32_16x16x32_bf16 v[84:87], v[92:95], a[64:67], v[84:87]\n"
        "ds_read_b128 v[100:103], v124 offset:7168\n"
        "s_add_u32 s28, s28, 36\n"
        "s_sub_i32 s33, s33, s28\n"
        "v_mfma_f32_16x16x32_bf16 v[88:91], v[92:95], a[72:75], v[88:91]\n"
        "s_min_i32 s33, s33, 500\n"
        "s_mov_b32 s28, 0\n"
        "L_dl_3%=:\n"
        "s_sub_i32 s33, s33, 3\n"
        "s_cmp_gt_i32 s33, 0\n"
        "s_cbranch_scc1 L_dl_3%=\n"
        "v_mfma_f32_16x16x32_bf16 v[56:59], a[208:211], v[92:95], v[56:59]\n"
        "s_add_u32 s58, s58, 36864\n"
        "ds_read_b128 v[8:11], v125 offset:2048\n"
        "v_mfma_f32_16x16x32_bf16 v[60:63], a[216:219], v[92:95], v[60:63]\n"
        "s_xor_b32 s66, s51, 1024\n"
        "buffer_load_dwordx4 v[80:83], v138, s[36:39], s50 offen offset:0 sc1\n"
        "s_waitcnt lgkmcnt(9)\n"
        "v_mfma_f32_16x16x32_bf16 v[48:51], v[32:35], v[92:95], v[48:51]\n"
        "s_xor_b32 s67, s51, 8192\n"
        "ds_read_b128 v[12:15], v125 offset:3072\n"
        "s_waitcnt lgkmcnt(7)\n"
        "v_mfma_f32_16x16x32_bf16 v[52:55], v[40:43], v[92:95], v[52:55]\n"
        "buffer_load_dwordx4 v[64:67], v138, s[36:39], s49 offen offset:0 sc1\n"
        "v_mfma_f32_16x16x32_bf16 v[84:87], v[104:107], a[68:71], v[84:87]\n"
        "s_xor_b32 s68, s51, 9216\n"
        "ds_read_b128 v[92:95], v124 offset:0\n"
        "v_mfma_f32_16x16x32_bf16 v[88:91], v[104:107], a[76:79], v[88:91]\n"
        "s_sub_u32 s75, 127, s84\n"
        "buffer_load_dwordx4 v[68:71], v138, s[36:39], s49 offen offset:256 sc1\n"
        "v_mfma_f32_16x16x32_bf16 v[56:59], a[212:215], v[104:107], v[56:59]\n"
        "s_sub_u32 s101, s77, s75\n"
        "ds_read_b128 v[16:19], v125 offset:4096\n"
        "v_mfma_f32_16x16x32_bf16 v[60:63], a[220:223], v[104:107], v[60:63]\n"
        "s_mul_i32 s35, s75, 0x800080\n"
        "buffer_load_dwordx4 v[72:75], v138, s[36:39], s65 offen offset:0 sc1\n"
        "v_mfma_f32_16x16x32_bf16 v[48:51], v[36:39], v[104:107], v[48:51]\n"
        "s_lshl_b32 s80, s75, 23\n"
        "ds_read_b128 v[20:23], v125 offset:5120\n"
        "s_waitcnt lgkmcnt(9)\n"
        "v_mfma_f32_16x16x32_bf16 v[52:55], v[44:47], v[104:107], v[52:55]\n"
        "buffer_load_dwordx4 v[76:79], v138, s[36:39], s65 offen offset:256 sc1\n"
        "s_waitcnt lgkmcnt(8)\n"
        "v_mfma_f32_16x16x32_bf16 v[84:87], v[96:99], a[80:83], v[84:87]\n"
        "s_sub_u32 s73, 0x3e0293ee, s80\n"
        "ds_read_b128 v[104:107], v124 offset:1024\n"
        "v_mfma_f32_16x16x32_bf16 v[88:91], v[96:99], a[88:91], v[88:91]\n"
        "s_sub_u32 s74, 0x3f800000, s80\n"
        "ds_read_b128 v[24:27], v125 offset:6144\n"
        "v_mfma_f32_16x16x32_bf16 v[56:59], a[224:227], v[96:99], v[56:59]\n"
        "s_add_u32 s80, s75, 128\n"
        "ds_read_b128 v[28:31], v125 offset:7168\n"
        "v_mfma_f32_16x16x32_bf16 v[60:63], a[232:235], v[96:99], v[60:63]\n"
        "s_lshl_b32 s80, s80, 8\n"
        "ds_read_b128 v[32:35], v125 offset:8192\n"
        "v_mfma_f32_16x16x32_bf16 v[48:51], v[192:195], v[96:99], v[48:51]\n"
        "s_add_u32 s62, s80, 1\n"
        "ds_read_b128 v[36:39], v125 offset:9216\n"
        "v_mfma_f32_16x16x32_bf16 v[52:55], v[200:203], v[96:99], v[52:55]\n"
        "ds_read_b128 v[40:43], v125 offset:10240\n"
        "s_waitcnt lgkmcnt(11)\n"
        "v_mfma_f32_16x16x32_bf16 v[84:87], v[100:103], a[84:87], v[84:87]\n"
        "s_add_u32 s44, s44, 1\n"
        "ds_read_b128 v[96:99], v124 offset:2048\n"
        "v_mfma_f32_16x16x32_bf16 v[88:91], v[100:103], a[92:95], v[88:91]\n"
        "s_add_u32 s61, s61, 32\n"
        "ds_read_b128 v[44:47], v125 offset:11264\n"
        "v_mfma_f32_16x16x32_bf16 v[56:59], a[228:231], v[100:103], v[56:59]\n"
        "s_xor_b32 s57, s57, 4096\n"
        "s_add_u32 s31, s31, 1\n"
        "s_and_b32 s31, s31, 63\n"
        "v_mfma_f32_16x16x32_bf16 v[60:63], a[236:239], v[100:103], v[60:63]\n"
        "s_branch L_rot_3%=\n"
        "L_abort%=:\n"
        "v_mov_b32 v0, 1\n"
        "s_mov_b32 s80, 0\n"
        "buffer_store_dword v0, off, s[36:39], s80 sc1\n"
        "L_end%=:\n"
        "s_waitcnt vmcnt(0)\n"
        "s_waitcnt lgkmcnt(0)\n"
        :: "s"(ws), "s"(Wq), "s"(Wk), "s"(Wv), "s"(Wo), "s"(id), "s"(ack)
        : "v0", "v1", "v2", "v3", "v4", "v5", "v6", "v7", "v8", "v9", "v10", "v11", "v12", "v13", "v14", "v15", "v16", "v17", "v18", "v19", "v20", "v21", "v22", "v23",
          "v24", "v25", "v26", "v27", "v28", "v29", "v30", "v31", "v32", "v33", "v34", "v35", "v36", "v37", "v38", "v39", "v40", "v41", "v42", "v43", "v44", "v45", "v46", "v47",
          "v48", "v49", "v50", "v51", "v52", "v53", "v54", "v55", "v56", "v57", "v58", "v59", "v60", "v61", "v62", "v63", "v64", "v65", "v66", "v67", "v68", "v69", "v70", "v71",
          "v72", "v73", "v74", "v75", "v76", "v77", "v78", "v79", "v80", "v81", "v82", "v83", "v84", "v85", "v86", "v87", "v88", "v89", "v90", "v91", "v92", "v93", "v94", "v95",
          "v96", "v97", "v98", "v99", "v100", "v101", "v102", "v103", "v104", "v105", "v106", "v107", "v108", "v109", "v110", "v111", "v112", "v113", "v114", "v115", "v116", "v117", "v118", "v119",
          "v120", "v121", "v122", "v123", "v124", "v125", "v126", "v127", "v128", "v129", "v130", "v131", "v132", "v133", "v134", "v135", "v136", "v137", "v138", "v139", "v140", "v141", "v142", "v143",
          "v144", "v145", "v146", "v147", "v148", "v149", "v150", "v151", "v152", "v153", "v154", "v155", "v156", "v157", "v158", "v159", "v160", "v161", "v162", "v163", "v164", "v165", "v166", "v167",
          "v168", "v169", "v170", "v171", "v172", "v173", "v174", "v175", "v176", "v177", "v178", "v179", "v180", "v181", "v182", "v183", "v184", "v185", "v186", "v187", "v188", "v189", "v190", "v191",
          "v192", "v193", "v194", "v195", "v196", "v197", "v198", "v199", "v200", "v201", "v202", "v203", "v204", "v205", "v206", "v207", "v208", "v209", "v210", "v211", "v212", "v213", "v214", "v215",
          "v216", "v217", "v218", "v219", "v220", "v221", "v222", "v223", "v224", "v225", "v226", "v227", "v228", "v229", "v230", "v231", "v232", "v233", "v234", "v235", "v236", "v237", "v238", "v239",
          "v240", "v241", "v242", "v243", "v244", "v245", "v246", "v247", "v248", "v249", "v250", "v251", "v252", "v253", "v254", "v255", "a0", "a1", "a2", "a3", "a4", "a5", "a6", "a7",
          "a8", "a9", "a10", "a11", "a12", "a13", "a14", "a15", "a16", "a17", "a18", "a19", "a20", "a21", "a22", "a23", "a24", "a25", "a26", "a27", "a28", "a29", "a30", "a31",
          "a32", "a33", "a34", "a35", "a36", "a37", "a38", "a39", "a40", "a41", "a42", "a43", "a44", "a45", "a46", "a47", "a48", "a49", "a50", "a51", "a52", "a53", "a54", "a55",
          "a56", "a57", "a58", "a59", "a60", "a61", "a62", "a63", "a64", "a65", "a66", "a67", "a68", "a69", "a70", "a71", "a72", "a73", "a74", "a75", "a76", "a77", "a78", "a79",
          "a80", "a81", "a82", "a83", "a84", "a85", "a86", "a87", "a88", "a89", "a90", "a91", "a92", "a93", "a94", "a95", "a96", "a97", "a98", "a99", "a100", "a101", "a102", "a103",
          "a104", "a105", "a106", "a107", "a108", "a109", "a110", "a111", "a112", "a113", "a114", "a115", "a116", "a117", "a118", "a119", "a120", "a121", "a122", "a123", "a124", "a125", "a126", "a127",
          "a128", "a129", "a130", "a131", "a132", "a133", "a134", "a135", "a136", "a137", "a138", "a139", "a140", "a141", "a142", "a143", "a144", "a145", "a146", "a147", "a148", "a149", "a150", "a151",
          "a152", "a153", "a154", "a155", "a156", "a157", "a158", "a159", "a160", "a161", "a162", "a163", "a164", "a165", "a166", "a167", "a168", "a169", "a170", "a171", "a172", "a173", "a174", "a175",
          "a176", "a177", "a178", "a179", "a180", "a181", "a182", "a183", "a184", "a185", "a186", "a187", "a188", "a189", "a190", "a191", "a192", "a193", "a194", "a195", "a196", "a197", "a198", "a199",
          "a200", "a201", "a202", "a203", "a204", "a205", "a206", "a207", "a208", "a209", "a210", "a211", "a212", "a213", "a214", "a215", "a216", "a217", "a218", "a219", "a220", "a221", "a222", "a223",
          "a224", "a225", "a226", "a227", "a228", "a229", "a230", "a231", "a232", "a233", "a234", "a235", "a236", "a237", "a238", "a239", "a240", "a241", "a242", "a243", "a244", "a245", "a246", "a247",
          "a248", "a249", "a250", "a251", "a252", "a253", "a254", "a255", "s20", "s21", "s22", "s23", "s24", "s25", "s26", "s27", "s28", "s29", "s30", "s31", "s32", "s33", "s34", "s35",
          "s36", "s37", "s38", "s39", "s40", "s41", "s42", "s43", "s44", "s45", "s46", "s47", "s48", "s49", "s50", "s51", "s52", "s53", "s54", "s55", "s56", "s57", "s58", "s59",
          "s60", "s61", "s62", "s63", "s64", "s65", "s66", "s67", "s68", "s69", "s70", "s71", "s72", "s73", "s74", "s75", "s76", "s77", "s78", "s79", "s80", "s81", "s82", "s83",
          "s84", "s85", "s86", "s87", "s88", "s89", "s90", "s91", "s92", "s93", "s94", "s95", "s96", "s97", "s98", "s99", "s100", "s101", "vcc", "scc", "m0", "memory");
}
constexpr u32 OOB = 0x40000000u;
#define BID (blockIdx.x ^ 248u)

constexpr u32 XNB_W = OFF_XNB, FX_W = 0;
__device__ __forceinline__ float bf2f(u16 v) { return __uint_as_float(((u32)v) << 16); }
__device__ __forceinline__ float bflo(u32 v) { return __uint_as_float(v << 16); }
__device__ __forceinline__ float bfhi(u32 v) { return __uint_as_float(v & 0xffff0000u); }
__device__ __forceinline__ u32 pack2(float a, float b) { bfv2 v = {(__bf16)a, (__bf16)b}; return __builtin_bit_cast(u32, v); }
__device__ __forceinline__ float rbf(float f) { return (float)(__bf16)f; }
__device__ __forceinline__ v4u ld16(rsrc_t r, u32 off) { return __builtin_amdgcn_raw_buffer_load_b128(r, off, 0, 16); }
__device__ __forceinline__ v2u ld8(rsrc_t r, u32 off) { return __builtin_amdgcn_raw_buffer_load_b64(r, off, 0, 16); }
__device__ __forceinline__ void st16(rsrc_t r, u32 off, v4u v) { __builtin_amdgcn_raw_buffer_store_b128(v, r, off, 0, 16); }
__device__ __forceinline__ void st8(rsrc_t r, u32 off, v2u v) { __builtin_amdgcn_raw_buffer_store_b64(v, r, off, 0, 16); }
__device__ __forceinline__ void st4(rsrc_t r, u32 off, u32 v) { __builtin_amdgcn_raw_buffer_store_b32(v, r, off, 0, 16); }
#define DRAIN() asm volatile("s_waitcnt vmcnt(0)" ::: "memory")
#define CBAR() asm volatile("" ::: "memory")
#define SCHED() __builtin_amdgcn_sched_barrier(0)
#define VMW(n) asm volatile("s_waitcnt vmcnt(%0)" :: "n"(n) : "memory")
#define LBAR() asm volatile("s_waitcnt lgkmcnt(0)\n\ts_barrier" ::: "memory")
__device__ __forceinline__ u32 ldw(char* ws, u32 off) { return __hip_atomic_load((gu32*)(ws + off), RLX_AGENT); }
constexpr u32 SPIN_CAP = 1u << 20;
#define SLP_A 2
#define SLP_S 6
__device__ __forceinline__ bool spin_fail(char* ws, u32& spins) {
    ++spins;
    if ((spins & 63u) == 0u) {
        if (ldw(ws, OFF_ABORT)) return true;
        if (spins > SPIN_CAP) { __hip_atomic_store((gu32*)(ws + OFF_ABORT), 1u, RLX_AGENT); return true; }
    }
    return false;
}
#define NAP_A 2
#define NAP_W 2
#define NAP_C 1
__device__ __forceinline__ void nap(int n) { for (int i = 0; i < n; ++i) __builtin_amdgcn_s_sleep(127); }
__device__ __forceinline__ f32x4 mfma32(bf16x8 a, bf16x8 b, f32x4 c) { return __builtin_amdgcn_mfma_f32_16x16x32_bf16(a, b, c, 0, 0, 0); }
__device__ __forceinline__ f32x4 mfma16(bf16x4 a, bf16x4 b, f32x4 c) { return __builtin_amdgcn_mfma_f32_16x16x16bf16_1k(a, b, c, 0, 0, 0); }
template <bool AG, bool SW, bool FIRST>
__device__ __forceinline__ void mmaw(f32x4& acc, const bf16x8& w, const bf16x8& x) {
    if constexpr (FIRST) {
        if constexpr (AG && !SW) asm volatile("v_mfma_f32_16x16x32_bf16 %0, %1, %2, 0" : "=&v"(acc) : "a"(w), "v"(x));
        else if constexpr (AG && SW) asm volatile("v_mfma_f32_16x16x32_bf16 %0, %2, %1, 0" : "=&v"(acc) : "a"(w), "v"(x));
        else if constexpr (!SW) asm volatile("v_mfma_f32_16x16x32_bf16 %0, %1, %2, 0" : "=&v"(acc) : "v"(w), "v"(x));
        else asm volatile("v_mfma_f32_16x16x32_bf16 %0, %2, %1, 0" : "=&v"(acc) : "v"(w), "v"(x));
    } else {
        if constexpr (AG && !SW) asm volatile("v_mfma_f32_16x16x32_bf16 %0, %1, %2, %0" : "+v"(acc) : "a"(w), "v"(x));
        else if constexpr (AG && SW) asm volatile("v_mfma_f32_16x16x32_bf16 %0, %2, %1, %0" : "+v"(acc) : "a"(w), "v"(x));
        else if constexpr (!SW) asm volatile("v_mfma_f32_16x16x32_bf16 %0, %1, %2, %0" : "+v"(acc) : "v"(w), "v"(x));
        else asm volatile("v_mfma_f32_16x16x32_bf16 %0, %2, %1, %0" : "+v"(acc) : "v"(w), "v"(x));
    }
}
#define NOP2() asm volatile("s_nop 1")
#define NOP8() asm volatile("s_nop 7")
#define PIN(x) asm volatile("" : "+v"(x))
#define PINA(x) asm volatile("" : "+a"(x))
template <bool PERM = false>
__device__ __forceinline__ void load_pair(const u16* W, int ld, int krow, int ncol, bf16x8& f0, bf16x8& f1) {
    u32 d[8];
#pragma unroll
    for (int j = 0; j < 8; ++j) d[j] = *(const u32*)(W + (size_t)(PERM ? krow + 16 * (j & 1) + (j >> 1) : krow + j) * ld + ncol);
    v4u a, b;
#pragma unroll
    for (int q = 0; q < 4; ++q) {
        a[q] = __builtin_amdgcn_perm(d[2 * q + 1], d[2 * q], 0x05040100u);
        b[q] = __builtin_amdgcn_perm(d[2 * q + 1], d[2 * q], 0x07060302u);
    }
    f0 = __builtin_bit_cast(bf16x8, a);
    f1 = __builtin_bit_cast(bf16x8, b);
}
template <bool PERM = false>
__device__ __forceinline__ void load_quad(const u16* W, int ld, int krow, int ncol, bf16x8& f0, bf16x8& f1, bf16x8& f2, bf16x8& f3) {
    v2u d[8];
#pragma unroll
    for (int j = 0; j < 8; ++j) d[j] = *(const v2u*)(W + (size_t)(PERM ? krow + 16 * (j & 1) + (j >> 1) : krow + j) * ld + ncol);
    v4u a, b, c, e;
#pragma unroll
    for (int q = 0; q < 4; ++q) {
        a[q] = __builtin_amdgcn_perm(d[2 * q + 1][0], d[2 * q][0], 0x05040100u);
        b[q] = __builtin_amdgcn_perm(d[2 * q + 1][0], d[2 * q][0], 0x07060302u);
        c[q] = __builtin_amdgcn_perm(d[2 * q + 1][1], d[2 * q][1], 0x05040100u);
        e[q] = __builtin_amdgcn_perm(d[2 * q + 1][1], d[2 * q][1], 0x07060302u);
    }
    f0 = __builtin_bit_cast(bf16x8, a); f1 = __builtin_bit_cast(bf16x8, b);
    f2 = __builtin_bit_cast(bf16x8, c); f3 = __builtin_bit_cast(bf16x8, e);
}
__device__ __forceinline__ v4u pack_quad(f32x4 t0, f32x4 t1, f32x4 t2, f32x4 t3, bool hf) {
    return v4u{pack2(hf ? t0[2] : t0[0], hf ? t1[2] : t1[0]), pack2(hf ? t2[2] : t2[0], hf ? t3[2] : t3[0]),
               pack2(hf ? t0[3] : t0[1], hf ? t1[3] : t1[1]), pack2(hf ? t2[3] : t2[1], hf ? t3[3] : t3[1])};
}
__device__ __forceinline__ v4u pack_pair(f32x4 t0, f32x4 t1) {
    v4u o;
#pragma unroll
    for (int r = 0; r < 4; ++r) o[r] = pack2(t0[r], t1[r]);
    return o;
}
__device__ __forceinline__ float vmax(float a, float b) { float r; asm("v_max_f32 %0, %1, %2" : "=v"(r) : "v"(a), "v"(b)); return r; }
__device__ __forceinline__ float xmax(float v) {
    auto a = __builtin_amdgcn_permlane16_swap(__float_as_uint(v), __float_as_uint(v), false, false);
    v = vmax(__uint_as_float(a[0]), __uint_as_float(a[1]));
    auto b = __builtin_amdgcn_permlane32_swap(__float_as_uint(v), __float_as_uint(v), false, false);
    return vmax(__uint_as_float(b[0]), __uint_as_float(b[1]));
}
__device__ __forceinline__ float xsum(float v) {
    auto a = __builtin_amdgcn_permlane16_swap(__float_as_uint(v), __float_as_uint(v), false, false);
    v = __uint_as_float(a[0]) + __uint_as_float(a[1]);
    auto b = __builtin_amdgcn_permlane32_swap(__float_as_uint(v), __float_as_uint(v), false, false);
    return __uint_as_float(b[0]) + __uint_as_float(b[1]);
}
template <int CTRL> __device__ __forceinline__ float dpp(float v) {
    return __uint_as_float(__builtin_amdgcn_update_dpp(0u, __float_as_uint(v), CTRL, 0xf, 0xf, false));
}
__device__ __forceinline__ float wave_sum(float v) {
    v += dpp<0xB1>(v);
    v += dpp<0x4E>(v);
    v += dpp<0x141>(v);
    v += dpp<0x140>(v);
    return xsum(v);
}
__device__ __forceinline__ f32x2 un2(u32 w) { return f32x2{bflo(w), bfhi(w)}; }
__device__ __forceinline__ u32 pack2(f32x2 v) { return pack2(v[0], v[1]); }
__device__ __forceinline__ void ln_par(float s, float q, f32x2& a, f32x2& b) {
    const float mean = s * (1.f / D_), inv = __builtin_amdgcn_rcpf(rbf(__builtin_amdgcn_sqrtf(rbf(rbf(fmaxf(q * (1.f / D_) - mean * mean, 0.f)) + 1e-5f))));
    a = f32x2{inv, inv};
    b = -rbf(mean) * a;
}
__device__ __forceinline__ void ln_row(const f32x2 (&v)[6], v2u (&y)[3]) {
    f32x2 s = v[0], q = v[0] * v[0], a, b;
#pragma unroll
    for (int i = 1; i < 6; ++i) { s += v[i]; q += v[i] * v[i]; }
    ln_par(wave_sum(s[0] + s[1]), wave_sum(q[0] + q[1]), a, b);
#pragma unroll
    for (int i = 0; i < 6; ++i) y[i >> 1][i & 1] = pack2(v[i] * a + b);
}

__device__ __forceinline__ u32 prog(u32 k) { return OFF_PROG + (k & 7) * 4096 + (k >> 3) * 256; }
#define CR 2
#define CA 1
#define CP 1
#define CB 1
constexpr u32 FOA = 8 * CX, FR = FOA + 8 * CA, FPB = FR + 2 * CR, FOB = FPB + 16 * CP, NPROG = FOB + 8 * CB;
static_assert(NPROG <= 128);
__device__ __forceinline__ u32 rdw(rsrc_t rs, u32 off) { return (u32)__builtin_amdgcn_raw_ptr_buffer_atomic_add_i32(0, rs, off, 0, 0); }
__device__ int buf_umax(int, rsrc_t, int, int, int) __asm("llvm.amdgcn.raw.ptr.buffer.atomic.umax.i32");
__device__ int buf_swap(int, rsrc_t, int, int, int) __asm("llvm.amdgcn.raw.ptr.buffer.atomic.swap.i32");
__device__ __forceinline__ void put(rsrc_t rs, u32 off, u32 v) { buf_swap(v, rs, off, 0, 0); }
__device__ __forceinline__ void tell(rsrc_t rs, u32 mine, u32 done) { buf_umax(done, rs, mine, 0, 0); }
template <int N> __device__ __forceinline__ u32 words(u32 off) { const u32 lane = threadIdx.x & 63; return lane < N ? off + lane * 4 : OOB; }
template <int N> __device__ __forceinline__ bool reached(u32 f, u32 need) { return ((u32)__ballot(f < need) & (u32)((1ull << N) - 1)) == 0u; }
template <int N> __device__ __forceinline__ u32 wait_prog(char* ws, rsrc_t rs, u32 off, u32 need) {
    u32 spins = 0, f;
    while (!reached<N>(f = rdw(rs, off), need)) {
        if (spin_fail(ws, spins)) break;
        __builtin_amdgcn_s_sleep(SLP_A);
    }
    CBAR();
    return __builtin_amdgcn_readfirstlane(f);
}
#define PS 8
#define AS 0
#define BS 2
template <int N, bool ANN, int P> __device__ __forceinline__ u32 watch(char* ws, rsrc_t rs, u32 mine, u32 done, u32 off, u32 need) {
    u32 f = rdw(rs, off), spins = 0;
    if (ANN) { VMW(1); tell(rs, mine, done); PIN(f); }
    if (!reached<N>(f, need))
        do {
            if (P) __builtin_amdgcn_s_sleep(P);
            f = rdw(rs, off);
        } while (!reached<N>(f, need) && !spin_fail(ws, spins));
    CBAR();
    return __builtin_amdgcn_readfirstlane(f);
}
template <int N> __device__ __forceinline__ void meet(char* ws, rsrc_t rs, bool me, u32 mine, u32 done, u32 off, u32 need) {
    if (me) watch<N, true, BS>(ws, rs, mine, done, off, need);
    else { DRAIN(); tell(rs, mine, done); }
    asm volatile("s_barrier" ::: "memory");
}
__device__ __forceinline__ bool told(const char* p) { return __builtin_amdgcn_readfirstlane(*(const u32*)p); }
__device__ __forceinline__ u32 my_word() { return words<1>(OFF_ACK + (BID * 4 + (threadIdx.x >> 6)) * 4); }
__device__ __forceinline__ u32 ask(rsrc_t rs) { DRAIN(); return rdw(rs, my_word()); }
__device__ __forceinline__ void go(rsrc_t rs, u32 a) {
    for (u32 spins = 0; !reached<1>(a, 1u) && spins < SPIN_CAP; ++spins) { __builtin_amdgcn_s_sleep(SLP_A); a = rdw(rs, my_word()); }
    CBAR();
    put(rs, my_word(), 2u);
}
#define CLR 16
template <int E = 1> __device__ __forceinline__ void clear(rsrc_t rs, u32 off, int n) {
#pragma unroll 5
    for (int i = 0; i < n; ++i) __builtin_amdgcn_raw_buffer_store_b128(v4u{0u, 0u, 0u, 0u}, rs, off + (i * 32 + (threadIdx.x >> 3)) * (128 * E) + (threadIdx.x & 7) * 16, 0, CLR);
}
__device__ __forceinline__ void role_lead(char* ws, rsrc_t rs) {
    const u32 tid = threadIdx.x;
    clear(rs, 0, 3);
#pragma unroll
    for (u32 i = FX_W ? CX / 4 : 0; i < (NPROG + 31) / 32; ++i) st16(rs, prog(i * 32 + (tid >> 3)) + (tid & 7) * 16, v4u{0u, 0u, 0u, 0u});
    DRAIN();
    __syncthreads();
#pragma unroll
    for (int k = 0; k < 4; ++k) put(rs, OFF_ACK + (k * 256 + tid) * 4, 1u);
    DRAIN();
    u32 pend = tid < 248 || tid >= 252 ? 15u : 7u, spins = 0;
    bool z = tid < 128;
    while (__any(pend)) {
        u32 old[4];
#pragma unroll
        for (int k = 0; k < 4; ++k) old[k] = __hip_atomic_fetch_max((gu32*)(ws + OFF_ACK) + k * 256 + tid, 1u, RLX_AGENT);
#pragma unroll
        for (int k = 0; k < 4; ++k) if (old[k] == 2u || (old[k] == 0u && !(k == 2 && tid < 128))) pend &= ~(1u << k);
        if (z && !__any(pend & 4u)) { tell(rs, words<1>(OFF_Z + (tid >> 6) * 4), 1u); z = false; }
        if (spin_fail(ws, spins)) break;
        __builtin_amdgcn_s_sleep(SLP_A);
    }
}
typedef short s16x4 __attribute__((ext_vector_type(4)));
__device__ __forceinline__ v2u tr_read(const char* p) {
    return __builtin_bit_cast(v2u, __builtin_amdgcn_ds_read_tr16_b64_v4i16((__attribute__((address_space(3))) s16x4*)p));
}
__device__ __forceinline__ bf16x8 asf(v4u v) { return __builtin_bit_cast(bf16x8, v); }

constexpr u32 TILE = 18432;
__device__ __forceinline__ u32 quad(int lane) { return ((lane >> 2) & 3) * 4608 + (lane >> 4) * 64 + (lane & 3) * 16; }
__device__ __forceinline__ v4u ldt(rsrc_t r, u32 vo, u32 so) { return __builtin_amdgcn_raw_buffer_load_b128(r, vo, so, 16); }
__device__ __forceinline__ void stt(rsrc_t r, u32 vo, u32 so, v4u v) {
    __builtin_amdgcn_raw_buffer_store_b128(v, r, vo, so, 16);
    asm volatile("s_nop 1" :: "v"(v) : "memory");
}

__device__ __forceinline__ void ld_tile(int tile, const u16* x0, v4u (&t)[18]) {
    const int lane = threadIdx.x & 63;
#pragma unroll
    for (int ks = 0; ks < 18; ++ks) t[ks] = *(const v4u*)(x0 + (size_t)(tile * 16 + (lane & 15)) * D_ + ks * 32 + (lane >> 4) * 8);
}
__device__ __forceinline__ void ln_tile(v4u (&t)[18]) {
    f32x2 s = {0.f, 0.f}, q = {0.f, 0.f}, a, b;
#pragma unroll
    for (int i = 0; i < 72; ++i) { const f32x2 v = un2(t[i >> 2][i & 3]); s += v; q += v * v; }
    ln_par(xsum(s[0] + s[1]), xsum(q[0] + q[1]), a, b);
#pragma unroll
    for (int i = 0; i < 72; ++i) t[i >> 2][i & 3] = pack2(un2(t[i >> 2][i & 3]) * a + b);
}
__device__ __forceinline__ void role_t0(int tile, const u16* x0, char* ws, rsrc_t rs) {
    v4u t[18];
    ld_tile(tile, x0, t);
    const u32 a = ask(rs);
    ln_tile(t);
    go(rs, a);
#pragma unroll
    for (int ks = 0; ks < 18; ++ks) stt(rs, quad(threadIdx.x & 63) + (ks & 15) * 256, OFF_XNA + tile * TILE + (ks >> 4) * 4096, t[ks]);
    DRAIN();
    tell(rs, words<16>(OFF_CXNA + tile * 64), NSEG);
}

constexpr int SL = 56;
template <int NR, bool FIN, int S0 = 0, int S1 = NSEG>
__device__ __forceinline__ void role_r(int idx, const u16* x0, u16* out, char* ws, rsrc_t rs, char* smem) {
    const int tid = threadIdx.x, lane = tid & 63, wave = __builtin_amdgcn_readfirstlane(tid >> 6);
    const bool act = lane < 48;
    const int lc = act ? lane : 0;
    const int rl = (idx * 4 + wave) * NR;
    const u32 po = act ? lane * 16 : OOB, xo = act ? (lane >> 1) * 64 + (lane & 1) * 8 : OOB;
    const u32 fp = words<24>(prog(FPB + idx % CP * 16 + (FIN ? 4 : 0) + (rl >> 3) + (lane < 12 ? 0 : 8)) + 16), fo = lane < CR ? prog(FR + lane * 2 + (rl >> 4)) + (rl & 15) * 4 : OOB;
    if (!FIN) {
        v2u xr[2][3];
#pragma unroll
        for (int i = 0; i < 6; ++i) xr[i / 3][i % 3] = *(const v2u*)(x0 + ((i / 3) * SEG + rl) * D_ + ((i % 3) * 48 + lc) * 4);
        const u32 a = ask(rs);
#pragma unroll
        for (int u = 0; u < 2; ++u) {
            f32x2 v[6];
#pragma unroll
            for (int i = 0; i < 6; ++i) v[i] = un2(act ? xr[u][i >> 1][i & 1] : 0u);
            v2u y[3];
            ln_row(v, y);
#pragma unroll
            for (int j = 0; j < 3; ++j) if (act) *(v2u*)(smem + 9216 + u * 4608 + (j * 24 + (lc >> 1)) * 64 + wave * 16 + (lc & 1) * 8) = y[j];
        }
        LBAR();
#pragma unroll
        for (int i = 0; i < 3; ++i) {
            const int c = tid + 256 * i;
            if (c < 576) st16(rs, OFF_XNA + (idx >> 2) * TILE + (idx & 3) * 4608 + (c < 288 ? c * 16 : 2 * TILE - 4608 + c * 16), *(const v4u*)(smem + 9216 + c * 16));
        }
        DRAIN();
        go(rs, a);
        tell(rs, lane < 2 ? OFF_CXNA + lane * 128 + rl * 4 : OOB, NSEG);
    }
    bool owe = false;
    const u32 f1 = wave ? OOB : fp;
    meet<24>(ws, rs, !wave, OOB, 0, fp, S0 + 1);
    const rsrc_t ro = __builtin_amdgcn_make_buffer_rsrc(out, 0, XB, 0x00020000);
    v2u xn[NR][3];
    auto ldx = [&](int s) {
#pragma unroll
        for (int i = 0; i < 3 * NR; ++i)
            xn[i / 3][i % 3] = __builtin_amdgcn_raw_buffer_load_b64(rs, xo + (i % 3) * 1536, OFF_X + (FIN ? 2 : 0) * XB + (s * 2 + (rl >> 4)) * TILE + ((rl >> 2) & 3) * 4608 + ((rl + i / 3) & 3) * 16, 16);
    };
    ldx(S0);
#pragma unroll 1
    for (int s = S0; s < S1; ++s) {
        v2u xr[NR][3];
        v4u pr[NR][4][3];
#pragma unroll
        for (int u = 0; u < NR; ++u) {
#pragma unroll
            for (int p = 0; p < 4; ++p)
#pragma unroll
                for (int j = 0; j < 3; ++j) pr[u][p][j] = ldt(rs, po + j * 768, OFF_PB + (((FIN ? 64 + s : s) * 4 + p) * SEG + rl + u) * D_ * 4);
        }
        SCHED();
#pragma unroll
        for (int i = 0; i < 3 * NR; ++i) xr[i / 3][i % 3] = xn[i / 3][i % 3];
        ldx(min(s + 1, NSEG - 1));
        SCHED();
        if (!FIN && owe) { VMW(15 * NR); tell(rs, fo, s); }
        u32 fn;
        if (FIN) { CBAR(); fn = rdw(rs, f1); }
        char* sm = smem + (s & 1) * 9216;
#pragma unroll
        for (int u = 0; u < NR; ++u) {
            f32x2 v[6];
#pragma unroll
            for (int i = 0; i < 6; ++i) {
                auto P = [&](int p) { return f32x2{__uint_as_float(pr[u][p][i >> 1][2 * (i & 1)]), __uint_as_float(pr[u][p][i >> 1][2 * (i & 1) + 1])}; };
                v[i] = ((P(0) + P(1)) + (P(2) + P(3))) + un2(xr[u][i >> 1][i & 1]);
            }
            v2u y[3];
            if (!FIN) { PIN(v[5]); CBAR(); fn = rdw(rs, f1); }
            if (!FIN) ln_row(v, y);
            if (FIN) {
#pragma unroll
                for (int j = 0; j < 3; ++j) __builtin_amdgcn_raw_buffer_store_b64(v2u{pack2(v[2 * j]), pack2(v[2 * j + 1])}, ro, (po >> 1) + j * 384, (s * SEG + rl + u) * (D_ * 2), 0);
            } else if (act) {
#pragma unroll
                for (int j = 0; j < 3; ++j) {
                    const v2u xw = {pack2(v[2 * j]), pack2(v[2 * j + 1])};
                    {
                        *(v2u*)(sm + (j * 24 + (lc >> 1)) * 64 + wave * 16 + (lc & 1) * 8) = y[j];
                        *(v2u*)(sm + 4608 + (j * 24 + (lc >> 1)) * 64 + wave * 16 + (lc & 1) * 8) = xw;
                    }
                }
            }
        }
        SCHED();
        PIN(fn);
        if (!wave) *(u32*)(smem + 32768 + (s & 1) * 4) = reached<24>(fn, s + 2);
        LBAR();
        owe = told(smem + 32768 + (s & 1) * 4);
        if (!FIN) {
#pragma unroll
            for (int i = 0; i < 3; ++i) {
                const int c = tid + 256 * i, c2 = c < 288 ? c : c - 288;
                if (c < 576) st16(rs, (c < 288 ? OFF_XNA : OFF_X) + XB + (s * 2 + (idx >> 2)) * TILE + (idx & 3) * 4608 + c2 * 16, *(const v4u*)(sm + c * 16));
            }
        }
        CBAR();
        if (!owe) {
            if (!FIN) meet<24>(ws, rs, !wave, fo, s + 1, fp, min(s + 2, NSEG));
            else if (s + 1 < S1) { if (!wave) watch<24, false, BS>(ws, rs, 0, 0, fp, s + 2); asm volatile("s_barrier" ::: "memory"); }
        }
    }
}

__device__ __forceinline__ void role_a1(int layer, int hp, const u16* wqkv, char* ws, rsrc_t rs, char* smem) {
    const int tid = threadIdx.x, lane = tid & 63, l15 = lane & 15, g = lane >> 4;
    const int wave = __builtin_amdgcn_readfirstlane(tid >> 6), hd = wave >> 1, m = wave & 1;
    bf16x8 w[18][4];
    v4u x[18];
    u32 a0;
    const u32 fx = wave < 3 ? OOB : words<32>(layer ? prog(FR + hp % CR * 2 + (g & 1)) - (g & 1) * 64 : OFF_CXNA), fs = layer ? 0 : 128, xb = OFF_XNA + layer * XB + m * TILE;
    const u32 fo = lane < CA ? prog(FOA + lane * 8 + (layer * 2 + m) * 2) + hp * 4 : OOB, ql = quad(lane);
    {
        const rsrc_t wr = __builtin_amdgcn_make_buffer_rsrc((void*)wqkv, 0, D_ * NQA * 2, 0x00020000);
        const int c = l15 ^ (2 * (g | hd << 2));
        const u32 vo = c < 15 ? (4 * wave + g) * (NQA * 2) + (c / 5) * (D_ * 2) + ((hp * 72) & ~15) + (c % 5) * 16 : OOB;
#pragma unroll
        for (int i = 0; i < 36; ++i) __builtin_amdgcn_raw_ptr_buffer_load_lds(wr, (__attribute__((address_space(3))) void*)(smem + (4 * i + wave) * 1024), 16, vo, i * 16 * NQA * 2, 0, 0);
        const char* rd[4];
#pragma unroll
        for (int t = 0; t < 4; ++t) {
            const int by = (hp & 1) * 8 + (t == 2 ? 32 + 80 * min(l15 & 3, 2) : hd * 40 + (l15 & 3) * 8 + 80 * min(t, 2));
            rd[t] = smem + (8 * g + (l15 >> 2)) * 256 + (by ^ (32 * ((l15 >> 2) | (g & 1) << 2)));
        }
        a0 = ask(rs);
        LBAR();
#pragma unroll
        for (int ks = 0; ks < 18; ++ks)
#pragma unroll
            for (int t = 0; t < 4; ++t) {
                const v2u a = tr_read(rd[t] + ks * 8192), b = tr_read(rd[t] + ks * 8192 + 1024);
                w[ks][t] = asf(v4u{a[0], a[1], b[0], b[1]});
            }
#pragma unroll
        for (int i = 0; i < 72; ++i) if (i < 60) PINA(w[i >> 2][i & 3]); else PIN(w[i >> 2][i & 3]);
        LBAR();
    }
    char* sK = smem + hd * 8192;
    char* sV = smem + 16384 + hd * 8192;
#pragma unroll
    for (int i = 0; i < 8; ++i) *(v4u*)(smem + (i * 256 + tid) * 16) = v4u{0u, 0u, 0u, 0u};
    LBAR();
    const bool odd = l15 & 1;
    const u32 pm = odd ? 0xffff0000u : 0xffffu;
    bool ok[4];
#pragma unroll
    for (int i = 0; i < 4; ++i) { const int d = 16 + l15 - (16 * (i >> 1) + 4 * g + 2 * (i & 1) + (l15 & 1)); ok[i] = d >= 0 && d <= 14; }
    const u32 vb = (4 * g + (l15 >> 2)) * 48 + (l15 & 3) * 8, vb2 = (4 * g + (l15 >> 2)) * 48 + ((l15 & 3) == 0 ? 32 : 40);
    char* sO = smem + 32768 + m * 1024;
    bool owe = false;
    auto body = [&](int s, auto own) {
        auto ldx = [&](int ks) { if constexpr (!own) x[ks] = ldt(rs, ql + (ks & 15) * 256, xb + s * 2 * TILE + (ks >> 4) * 4096); SCHED(); };
#pragma unroll
        for (int ks = 0; ks < 12; ++ks) ldx(ks);
        if (owe) { VMW(12); tell(rs, fo + (~s & 1) * 4096, s); }
        f32x4 acc[4];
#pragma unroll
        for (int ks = 0; ks < 18; ++ks) {
#pragma unroll
            for (int t = 0; t < 4; ++t) {
                if (ks == 0) mmaw<true, false, true>(acc[t], w[ks][t], asf(x[ks]));
                else if (ks < 15) mmaw<true, false, false>(acc[t], w[ks][t], asf(x[ks]));
                else mmaw<false, false, false>(acc[t], w[ks][t], asf(x[ks]));
            }
            SCHED();
            if (ks < 6) ldx(ks + 12);
        }
        CBAR();
        NOP8();
#pragma unroll
        for (int t = 0; t < 4; ++t) PIN(acc[t]);
        const int T = (2 * s + m) & 7, Tp = (T + 7) & 7;
        const u32 e01 = pack2(hd ? acc[2][2] : acc[2][0], hd ? acc[2][3] : acc[2][1]);
        const v4u qf = {pack2(acc[0][0], acc[0][1]), pack2(acc[0][2], acc[0][3]), g ? 0u : e01, 0u};
        const v4u kf = {pack2(acc[1][0], acc[1][1]), pack2(acc[1][2], acc[1][3]), __builtin_amdgcn_permlane16_swap(e01, e01, false, false)[1], 0u};
        *(v4u*)(sK + T * 1024 + lane * 16) = kf;
        *(v2u*)(sV + T * 1024 + l15 * 48 + g * 8) = v2u{pack2(acc[3][0], acc[3][1]), pack2(acc[3][2], acc[3][3])};
        if (g == 2) *(u32*)(sV + T * 1024 + l15 * 48 + 32) = e01;
        const f32x4 s1 = mfma32(asf(kf), asf(qf), f32x4{0.f, 0.f, 0.f, 0.f});
        LBAR();
        const u32 fn = rdw(rs, fx + (s + 1 < NSEG ? s + 1 : s) * fs);
        CBAR();
        const v4u kp = *(const v4u*)(sK + Tp * 1024 + lane * 16);
        const v2u va0 = tr_read(sV + Tp * 1024 + vb), va1 = tr_read(sV + T * 1024 + vb), vc0 = tr_read(sV + Tp * 1024 + vb2), vc1 = tr_read(sV + T * 1024 + vb2);
        const f32x4 s0 = mfma32(asf(kp), asf(qf), f32x4{0.f, 0.f, 0.f, 0.f});
        float l[4] = {odd ? s0[1] : s0[0], odd ? s0[3] : s0[2], odd ? s1[1] : s1[0], odd ? s1[3] : s1[2]};
        constexpr float SC = 0.23570226039551584f * 1.4426950408889634f;
#pragma unroll
        for (int i = 0; i < 4; ++i) l[i] = ok[i] && (i > 1 || s + m) ? l[i] : -3.0e38f;
        const float mx = -SC * xmax(fmaxf(fmaxf(l[0], l[1]), fmaxf(l[2], l[3])));
        v4u p;
#pragma unroll
        for (int i = 0; i < 4; ++i) { l[i] = __builtin_amdgcn_exp2f(l[i] * SC + mx); p[i] = pack2(l[i], l[i]) & pm; }
        const f32x4 oa = mfma32(asf(v4u{va0[0], va0[1], va1[0], va1[1]}), asf(p), f32x4{0.f, 0.f, 0.f, 0.f});
        const f32x4 ob = mfma32(asf(v4u{vc0[0], vc0[1], vc1[0], vc1[1]}), asf(p), f32x4{0.f, 0.f, 0.f, 0.f});
        const float inv = __builtin_amdgcn_rcpf(xsum((l[0] + l[1]) + (l[2] + l[3])));
        const v2u o = {pack2(oa[0] * inv, oa[1] * inv), pack2(oa[2] * inv, oa[3] * inv)};
        const u32 oe = pack2(ob[0] * inv, ob[1] * inv);
        if (hd) { *(v2u*)(sO + lane * 8) = o; if (g == 0) *(u32*)(sO + 512 + lane * 4) = oe; }
        LBAR();
        if (!hd) {
            const v2u o2 = *(const v2u*)(sO + lane * 8);
            const u32 oe2 = *(const u32*)(sO + 512 + l15 * 4);
            const u32 dst = OFF_OA + layer * XB + (s * 2 + m) * TILE;
            stt(rs, lane * 16, dst + hp * 1024, v4u{o[0], o[1], o2[0], o2[1]});
            if (g == 0) st8(rs, dst + 16384 + hp * 128 + lane * 8, v2u{oe, oe2});
        }
        CBAR();
        if (wave == 3) *(u32*)(smem + 36864) = reached<32>(fn, s + 2);
        LBAR();
        const bool have = told(smem + 36864);
        owe = have && !hd;
        if (!have) meet<32>(ws, rs, wave == 3, hd ? OOB : fo + (s & 1) * 4096, s + 1, fx + (s + 1 < NSEG ? s + 1 : s) * fs, min(s + 2, NSEG));
    };
    go(rs, a0);
    meet<32>(ws, rs, wave == 3, OOB, 0, fx, 1);
    for (int s = 0; s < NSEG; ++s) body(s, std::false_type());
}

__device__ __forceinline__ void role_a2(int layer, int nb, const u16* x0, const u16* woA, char* ws, rsrc_t rs, char* smem) {
    const int tid = threadIdx.x, lane = tid & 63, l15 = lane & 15, g = lane >> 4;
    const int wave = __builtin_amdgcn_readfirstlane(tid >> 6), m = wave & 1;
    bf16x8 w[18][4];
    {
        v4u t[18];
#pragma unroll
        for (int i = 0; i < 18; ++i) t[i] = *(const v4u*)(woA + (size_t)(i * 32 + (tid >> 3)) * D_ + nb * 64 + (tid & 7) * 8);
#pragma unroll
        for (int i = 0; i < 18; ++i) *(v4u*)(smem + (i * 32 + (tid >> 3)) * 144 + (tid & 7) * 16) = t[i];
        __syncthreads();
        const int q = l15 >> 2;
        const char* ra = smem + (4 * g + q) * 144 + (l15 & 3) * 16;
        const char* rb = smem + (36 * g + 16 + q) * 144 + (l15 & 3) * 16;
#pragma unroll
        for (int ks = 0; ks < 18; ++ks)
#pragma unroll
            for (int T = 0; T < 4; ++T) {
                const int co = (T >> 1) * 64 + (T & 1) * 8;
                v2u a, b;
                if (ks < 16) { a = tr_read(ra + ks * 36 * 144 + co); b = tr_read(ra + (ks * 36 + 20) * 144 + co); }
                else { a = tr_read(rb + (ks - 16) * 288 * 144 + co); b = tr_read(rb + ((ks - 16) * 288 + 144) * 144 + co); }
                w[ks][T] = asf(v4u{a[0], a[1], b[0], b[1]});
            }
#pragma unroll
        for (int i = 0; i < 72; ++i) if (i < 60) PINA(w[i >> 2][i & 3]); else PIN(w[i >> 2][i & 3]);
    }
    go(rs, ask(rs));
    wait_prog<2>(ws, rs, words<2>(OFF_Z), 1);
    const u32 fi = words<16>(prog(FOA + nb % CA * 8 + (layer * 2 + m) * 2 + (wave >> 1))), fo = lane < CX ? FX_W + prog(lane * 8 + (layer * 2 + m) * 2 + (wave >> 1)) + nb * 4 : OOB;
    v4u xn[2];
    auto ldr = [&](int s) {
#pragma unroll
        for (int P = 0; P < 2; ++P) xn[P] = *(const v4u*)(x0 + (size_t)(s * SEG + m * 16 + l15) * D_ + nb * 64 + P * 32 + g * 8);
    };
    if (layer == 0) ldr(wave >> 1);
    watch<16, false, PS>(ws, rs, 0, 0, fi, (wave >> 1) + 1);
    for (int s = wave >> 1; s < NSEG; s += 2) {
        const u32 task = (layer * 64 + s) * 2 + m;
        const u32 ro = (s * 2 + m) * TILE + nb * 2048, rq = (s * 2 + m) * TILE + nb * 512;
        v4u x[18], xr[2];
#pragma unroll
        for (int ks = 0; ks < 16; ++ks) x[ks] = ldt(rs, lane * 16 + (ks & 3) * 1024, OFF_OA + layer * XB + (s * 2 + m) * TILE + (ks >> 2) * 4096);
#pragma unroll
        for (int e = 0; e < 2; ++e) {
            const v2u a = __builtin_amdgcn_raw_buffer_load_b64(rs, lane * 8 + e * 1024, OFF_OA + layer * XB + (s * 2 + m) * TILE + 16384, 16);
            const v2u b = __builtin_amdgcn_raw_buffer_load_b64(rs, lane * 8 + e * 1024 + 512, OFF_OA + layer * XB + (s * 2 + m) * TILE + 16384, 16);
            x[16 + e] = v4u{a[0], a[1], b[0], b[1]};
        }
        if (layer != 0) {
#pragma unroll
            for (int P = 0; P < 2; ++P) xr[P] = ldt(rs, quad(lane) + P * 256, OFF_X + XB + rq);
        } else {
            xr[0] = xn[0]; xr[1] = xn[1];
            ldr(min(s + 2, NSEG - 1));
        }
        f32x4 acc[4];
        f32x2 xf[8];
#pragma unroll
        for (int ks = 0; ks < 18; ++ks)
#pragma unroll
            for (int t = 0; t < 4; ++t) {
                if (ks == 0) mmaw<true, false, true>(acc[t], w[ks][t], asf(x[ks]));
                else if (ks < 15) mmaw<true, false, false>(acc[t], w[ks][t], asf(x[ks]));
                else mmaw<false, false, false>(acc[t], w[ks][t], asf(x[ks]));
                if (ks >= 16) { u32 xw = xr[ks & 1][t]; PIN(xw); xf[(ks & 1) * 4 + t] = un2(xw); PIN(xf[(ks & 1) * 4 + t]); }
            }
        NOP8();
        f32x2 v[8];
#pragma unroll
        for (int i = 0; i < 8; ++i) { PIN(acc[i >> 1]); v[i] = f32x2{acc[i >> 1][2 * (i & 1)], acc[i >> 1][2 * (i & 1) + 1]} + xf[i]; }
        f32x2 su = v[0], sq = v[0] * v[0], a, b;
#pragma unroll
        for (int i = 1; i < 8; ++i) { su += v[i]; sq += v[i] * v[i]; }
        const u32 so = OFF_STAT + task * 2304 + l15 * 8;
        asm volatile("buffer_atomic_or_x2 %0, %1, %2, 0 offen" :: "v"(v2u{__float_as_uint(xsum(su[0] + su[1])), __float_as_uint(xsum(sq[0] + sq[1])) | 1u}),
                     "v"(g ? OOB : so + nb * 256), "s"(rs) : "memory");
        u64 st[3] = {0, 0, g ? 1ull << 32 : 0};
        u32 spins = 0;
        __builtin_amdgcn_s_sleep(SLP_S);
        for (;;) {
            CBAR();
            u64 z = 0;
            PIN(z);
#pragma unroll
            for (int i = 0; i < 3; ++i) if ((st[i] >> 32) == 0) st[i] = __hip_atomic_fetch_add((GAS u64*)(ws + (so + (g + 4 * i) * 256)), z, RLX_AGENT);
            if (__all((st[0] >> 32) != 0 && (st[1] >> 32) != 0 && (st[2] >> 32) != 0)) break;
            if (spin_fail(ws, spins)) break;
        }
        ln_par(xsum(__uint_as_float((u32)st[0]) + __uint_as_float((u32)st[1]) + __uint_as_float((u32)st[2])),
               xsum(__uint_as_float(st[0] >> 32) + __uint_as_float(st[1] >> 32) + __uint_as_float(st[2] >> 32)), a, b);
#pragma unroll
        for (int P = 0; P < 2; ++P) stt(rs, lane * 16 + P * 1024, XNB_W + layer * XB + ro, v4u{pack2(v[4 * P] * a + b), pack2(v[4 * P + 1] * a + b), pack2(v[4 * P + 2] * a + b), pack2(v[4 * P + 3] * a + b)});
#pragma unroll
        for (int P = 0; P < 2; ++P) stt(rs, quad(lane) + P * 256, OFF_X + 2 * layer * XB + rq, v4u{pack2(v[4 * P]), pack2(v[4 * P + 1]), pack2(v[4 * P + 2]), pack2(v[4 * P + 3])});
        if ((s & ~1) == 8) wait_prog<1>(ws, rs, words<1>(OFF_Z2), 112);
        watch<16, true, PS>(ws, rs, fo, s + 1, fi, s + 2 < NSEG ? s + 3 : s + 1);
    }
}

__device__ __forceinline__ void role_w(int layer, int kb, int nb, const u16* Wo, char* ws, rsrc_t rs, char* smem) {
    const int tid = threadIdx.x, lane = tid & 63, l15 = lane & 15, g = lane >> 4;
    const int wave = __builtin_amdgcn_readfirstlane(tid >> 6);
    float* sR = (float*)smem;
    bf16x8 w[3][8][2];
#pragma unroll
    for (int i = 0; i < 3; ++i)
#pragma unroll
        for (int ks = 0; ks < 8; ++ks) load_pair<true>(Wo, D_, kb * 1024 + wave * 256 + ks * 32 + 4 * g, nb * 96 + i * 32 + 2 * l15, w[i][ks][0], w[i][ks][1]);
#pragma unroll
    for (int i = 0; i < 48; ++i) PINA(w[i >> 4][(i >> 1) & 7][i & 1]);
    const u32 fo = wave ? OOB : words<8>(prog(FOB + nb % CB * 8 + layer * 4 + kb)), ob = OFF_OB + (layer * 2048 + kb * 8 + wave * 2) * 8192;
    const u32 fp = lane < CP ? prog(FPB + lane * 16 + layer * 4 + wave + (kb >> 1) * 8) + 16 + (kb * 6 + nb) * 4 : OOB;
    bool owe = false;
    go(rs, ask(rs));
    meet<8>(ws, rs, !wave, OOB, 0, fo, 1);
    for (int s = 0; s < NSEG; ++s) {
        v4u tb[16];
        auto ldb = [&](int i) { tb[i] = ldt(rs, lane * 16 + (i & 3) * 1024, ob + s * 262144 + (i >> 2) * 4096); SCHED(); };
#pragma unroll
        for (int i = 0; i < 10; ++i) ldb(i);
        if (owe) { VMW(10); tell(rs, fp, s); }
        f32x4 acc[3][2][2];
        u32 fn;
#pragma unroll
        for (int ks = 0; ks < 8; ++ks) {
#pragma unroll
            for (int m = 0; m < 2; ++m) {
#pragma unroll
                for (int i = 0; i < 3; ++i)
#pragma unroll
                    for (int t = 0; t < 2; ++t) {
                        if (ks == 0) mmaw<true, false, true>(acc[i][t][m], w[i][ks][t], asf(tb[2 * ks + m]));
                        else mmaw<true, false, false>(acc[i][t][m], w[i][ks][t], asf(tb[2 * ks + m]));
                    }
                SCHED();
                if (ks < 3) ldb(2 * ks + m + 10);
            }
        }
        NOP8();
        float* sB = sR + (s & 1) * 12800;
#pragma unroll
        for (int m = 0; m < 2; ++m)
#pragma unroll
            for (int i = 0; i < 3; ++i) {
                if (m && i == 0) { CBAR(); fn = rdw(rs, fo); CBAR(); }
                PIN(acc[i][0][m]); PIN(acc[i][1][m]);
                const f32x4 a0 = acc[i][0][m], a1 = acc[i][1][m];
                float* dst = sB + (wave * 32 + m * 16 + l15) * 100 + i * 32 + 8 * g;
                *(f32x4*)dst = f32x4{a0[0], a1[0], a0[1], a1[1]};
                *(f32x4*)(dst + 4) = f32x4{a0[2], a1[2], a0[3], a1[3]};
            }
        LBAR();
#pragma unroll
        for (int i = 0; i < 3; ++i) {
            const int c = wave * 192 + lane + 64 * i, tok = c / 24, c4 = (c % 24) * 4;
            f32x4 a = *(const f32x4*)(sB + tok * 100 + c4);
#pragma unroll
            for (int wv = 1; wv < 4; ++wv) a += *(const f32x4*)(sB + (wv * 32 + tok) * 100 + c4);
            st16(rs, OFF_PB + ((((layer * 64 + s) * 4 + kb) * SEG + tok) * D_ + nb * 96 + c4) * 4, __builtin_bit_cast(v4u, a));
        }
        CBAR();
        if (!wave) *(u32*)(smem + 102400) = reached<8>(fn, s + 2);
        LBAR();
        owe = told(smem + 102400);
        if (!owe) meet<8>(ws, rs, !wave, fp, s + 1, fo, min(s + 2, NSEG));
    }
}

__device__ __forceinline__ int exp_above(float x) { return (int)((__float_as_uint(x) >> 23) & 255u) - 126; }
__device__ __forceinline__ float sq2(u32 w, float c) { return __builtin_amdgcn_fdot2_f32_bf16(__builtin_bit_cast(bfv2, w), __builtin_bit_cast(bfv2, w), c, false); }
__device__ __forceinline__ float wave_max(float v) {
    v = vmax(v, dpp<0xB1>(v)); v = vmax(v, dpp<0x4E>(v)); v = vmax(v, dpp<0x141>(v)); v = vmax(v, dpp<0x140>(v));
    return xmax(v);
}
__device__ __forceinline__ void par_done(char* ws, rsrc_t rs) {
    const int tid = threadIdx.x;
    DRAIN();
    __syncthreads();
    if (tid < 64) {
        u32 n = 0;
        if (tid == 0) n = __hip_atomic_fetch_add((gu32*)(ws + OFF_CC), 1u, RLX_AGENT);
        if (__builtin_amdgcn_readfirstlane(n) == 63) {
            const v2u fm = ld8(rs, OFF_CH + (tid & 31) * 8);
            const u32 b0 = ldw(ws, OFF_CH + 256);
            u32 ph, spins = 0;
            while ((ph = ldw(ws, OFF_CH + 384)) == 0u && !spin_fail(ws, spins)) __builtin_amdgcn_s_sleep(2);
            const float c = sqrtf(__uint_as_float(fm[0])) * sqrtf(__uint_as_float(fm[1]));
            const float cs = wave_sum(c) * 0.5f;
            const int e35 = min(max(exp_above(43.75f * cs), -100), 100), ek = min(max(exp_above(30.f * cs), -100), 100);
            if (tid == 0) st4(rs, OFF_PAR, 0x80000000u | (ph & 1u) << 24 | b0 << 16 | (u32)(e35 + 128) << 8 | (u32)(ek + 128));
        }
    }
}
#define PAGE_STEP 0
#define CODE_BYTES 83968
__device__ __forceinline__ void touch(int j, int xc, const u16* wqkv, const u16* Wq, const u16* Wk, const u16* Wv, const u16* Wo, rsrc_t rs) {
    const u32 L = j * 256 + threadIdx.x, o = (L >> 3) * NB + xc * 512 + (L & 7) * 64;
    auto get = [](const u16* p) { asm volatile("" :: "v"(*(const u32*)p) : "memory"); };

    get(wqkv + min(L >> 1, 1727u) * D_ + xc * 72 + (L & 1) * 70);
    get(Wv + o);
    if ((j & ~3) == 8) go(rs, ask(rs));
    get(Wo + (xc * 4608 + L) * 64);
    get(Wq + o);
    get(Wk + o);
}
__device__ __forceinline__ void role_par(int h, int p, const u16* mem0, const u16* Wv, const u16* Wo, char* ws, rsrc_t rs, char* smem) {
    const int tid = threadIdx.x;
    float* sR = (float*)smem;
    float* sW = sR + 1024;
    const u32 a0 = ask(rs);
    if (p == 0) {
        v4u w[36];
#pragma unroll
        for (int i = 0; i < 36; ++i) w[i] = *(const v4u*)(Wv + (size_t)(i * 16 + (tid >> 4)) * NB + h * 128 + (tid & 15) * 8);
        float f[4] = {0.f, 0.f, 0.f, 0.f};
#pragma unroll
        for (int i = 0; i < 144; ++i) f[i & 3] = sq2(w[i >> 2][i & 3], f[i & 3]);
        const float t = wave_sum((f[0] + f[1]) + (f[2] + f[3]));
        if ((tid & 63) == 0) sW[tid >> 6] = t;
        __syncthreads();
        go(rs, a0);
        if (tid == 0) st4(rs, OFF_CH + h * 8, __float_as_uint((sW[0] + sW[1]) + (sW[2] + sW[3])));
    } else {
        u32 me[9];
        v4u w[43];
        const int q = h * 72 + min(tid, 71);
        int i0, i1;
        if (q < 2048) { i0 = ((q >> 3) & 7) * D_ + (q >> 8) * 64 + ((q >> 6) & 3) * 16 + (q & 3) * 4 + ((q >> 2) & 1) * 2; i1 = i0 + 1; }
        else { i0 = ((q >> 4) & 3) * 2 * D_ + 512 + ((q >> 6) & 3) * 16 + (q & 15); i1 = i0 + D_; }
        const u16 m0 = mem0[i0], m1 = mem0[i1];
#pragma unroll
        for (int i = 0; i < 9; ++i) me[i] = ((const u32*)mem0)[i * 256 + tid];
#pragma unroll
        for (int i = 0; i < 43; ++i) w[i] = *(const v4u*)(Wo + (size_t)(h * 128 + min(3 * i + tid / 72, 127)) * D_ + (tid % 72) * 8);
        float mm = 0.f;
#pragma unroll
        for (int i = 0; i < 9; ++i) mm = fmaxf(mm, fmaxf(fabsf(bflo(me[i])), fabsf(bfhi(me[i]))));
        mm = wave_max(mm);
        if ((tid & 63) == 0) sW[tid >> 6] = mm;
        LBAR();
        const int b0 = 28 - min(max(exp_above(fmaxf(fmaxf(sW[0], sW[1]), fmaxf(sW[2], sW[3]))), -30), 90);
        f32x4 c = {0.f, 0.f, 0.f, 0.f};
#pragma unroll
        for (int i = 0; i < 172; ++i) c[i & 3] = sq2(w[i >> 2][i & 3], c[i & 3]);
        *(f32x4*)(sR + tid * 4) = c;
        if (tid < 72) {
            const v2u z = {(u32)(int)rintf(ldexpf(bf2f(m0), b0 - 3)) << 6 | 32u, (u32)(int)rintf(ldexpf(bf2f(m1), b0 + 3))};
            const u32 a = (q < 2048 ? (q >> 9) * 16384 + ((q >> 8) & 1) * 8192 + ((q >> 3) & 7) * 1024 + ((q >> 6) & 1) * 512 + ((q >> 7) & 1) * 256
                                    : 65536 + ((q >> 3) & 1) * 512 + ((q >> 4) & 3) * 2048 + ((q >> 6) & 1) * 256 + ((q >> 7) & 1) * 8192) + (q & 7) * 8;
            for (int i = 0; i < 4; ++i) {
                st8(rs, OFF_ACC + (2 + (i >> 1)) * (ACC_SLOT / 2) + (i & 1) * 64 + a, z);
                st8(rs, q < 2048 ? OOB : OFF_ACC + (2 + (i >> 1)) * (ACC_SLOT / 2) + 1024 + (i & 1) * 64 + a, v2u{32u, 0u});
            }
        }
        LBAR();
        go(rs, a0);
        if (tid < 64) {
            float m = 0.f;
#pragma unroll
            for (int i = 0; i < 5; ++i) { const int d = min(tid + 64 * i, 287); m = fmaxf(m, sR[d] + sR[288 + d] + sR[576 + d]); }
            m = wave_max(m);
            if (tid < 2) st4(rs, tid ? OFF_CH + 256 : OFF_CH + h * 8 + 4, tid ? (u32)(b0 + 3 + 128) : __float_as_uint(m));
        }
    }
    par_done(ws, rs);
}
__device__ __forceinline__ void role_side(char* ws, rsrc_t rs, char* smem) {
    const int lane = threadIdx.x & 63, wave = threadIdx.x >> 6;
    go(rs, ask(rs));
    const u32 base = ((OFF_JUNK + 16383u) & ~16383u) + wave * 32768;
    st16(rs, lane < 32 ? base + (lane >> 3) * 8192 + (lane & 7) * 16 : OOB, v4u{0u, 0u, 0u, 0u});
    int vote = 0;
    DRAIN();
#pragma unroll 1
    for (int r = 0; r < 2; ++r) {
        u32 t[2];
        __builtin_amdgcn_raw_ptr_buffer_atomic_add_i32(1, rs, lane < 2 ? base + r * 16384 + lane * 8192 : OOB, 0, 0);
        DRAIN();
#pragma unroll
        for (int e = 0; e < 2; ++e) {
            const u32 a = base + r * 16384 + e * 8192;
            v4u q;
            u64 t0, t1;
            asm volatile("s_memtime %0\n\ts_waitcnt lgkmcnt(0)\n\tbuffer_load_dwordx4 %2, %3, %4, 0 offen sc1\n\ts_waitcnt vmcnt(0)\n\ts_memtime %1\n\ts_waitcnt lgkmcnt(0)"
                         : "=&s"(t0), "=&s"(t1), "=&v"(q) : "v"(lane < 4 ? a + lane * 16 : OOB), "s"(rs) : "memory");
            t[e] = (u32)(t1 - t0);
        }
        const u32 near = t[1] < t[0], b21 = ((u32)(size_t)ws + base + r * 16384) >> 21 & 1;
        vote += (near ^ (BID >> 2 & 1) ^ b21) ? 1 : -1;
    }
    if (lane == 0) *(int*)(smem + wave * 4) = vote;
    __syncthreads();
    if (threadIdx.x == 0) { const int* v = (const int*)smem; st4(rs, OFF_CH + 384, v[0] + v[1] + v[2] + v[3] > 0 ? 3u : 2u); }
}
__device__ __forceinline__ void clear_late(int i, rsrc_t rs) {
    nap(NAP_A);
    clear<2>(rs, OFF_ACC + (10 + 2 * i) * ACC_SLOT, 40);
    go(rs, ask(rs));
    __builtin_amdgcn_raw_ptr_buffer_atomic_add_i32(1, rs, words<1>(OFF_Z2), 0, 0);
}

using v8u = __attribute__((ext_vector_type(8))) unsigned;
template <bool FIRST> __device__ __forceinline__ void mm16(f32x4& acc, v2u a, v2u b) {
    if constexpr (FIRST) asm volatile("v_mfma_f32_16x16x16_bf16 %0, %1, %2, 0" : "=&v"(acc) : "v"(a), "v"(b));
    else asm volatile("v_mfma_f32_16x16x16_bf16 %0, %1, %2, %0" : "+v"(acc) : "v"(a), "v"(b));
}
__device__ __forceinline__ v4u half(const v8u& v, int i) { return i ? v4u{v[4], v[5], v[6], v[7]} : v4u{v[0], v[1], v[2], v[3]}; }
__device__ __forceinline__ void role_att(int layer, int h, char* ws, rsrc_t rs) {
    const int lane = threadIdx.x & 63, l15 = lane & 15, g = lane >> 4;
    const u32 fl = words<6>(OFF_FKVM + ((layer * 32 + h) * 68 + 2) * 32);
    const u32 kvo = ((g >> 1) * 16 + ((l15 >> 3) | ((l15 >> 1) & 2)) * 4 + (l15 & 1) + (g & 1) * 2) * 16, vmo = (lane & 57) * 8 + (lane & 2) * 16 + (lane & 4) * 4;
    constexpr float SC = 0.08838834764831845f * 1.4426950408889634f;
    const u32 fo = lane < CB ? prog(FOB + lane * 8 + layer * 4 + (h >> 3)) + (h & 7) * 4 : OOB;
    v4u k[2][4], v[2][4];
    auto issue = [&](int s, v8u (&q)[2][2]) {
        const u32 blk = OFF_QKV + ((layer * 66 + s) * 32 + h) * 24576;
        v4u t[2][4];
#pragma unroll
        for (int w = 0; w < 4; ++w)
#pragma unroll
            for (int p = 0; p < 2; ++p) { t[p][w] = ldt(rs, lane * 16, blk + w * 6144 + p * 3072); k[p][w] = ldt(rs, lane * 16 + 1024, blk + w * 6144 + p * 3072); }
#pragma unroll
        for (int p = 0; p < 2; ++p)
#pragma unroll
            for (int i = 0; i < 2; ++i) q[p][i] = __builtin_shufflevector(t[p][2 * i], t[p][2 * i + 1], 0, 1, 2, 3, 4, 5, 6, 7);
    };
    auto issuev = [&](int s) {
        const u32 blk = OFF_QKV + ((layer * 66 + s) * 32 + h) * 24576;
#pragma unroll
        for (int p = 0; p < 2; ++p)
#pragma unroll
            for (int w = 0; w < 4; ++w) v[p][w] = ldt(rs, lane * 16 + 2048, blk + w * 6144 + p * 3072);
    };
    auto body = [&](int s, v8u (&q)[2][2], v8u (&qn)[2][2]) {
        const u32 mb = OFF_KVM + ((layer * 64 + s) * 32 + h) * 4096;
        f32x4 sc[3];
#pragma unroll
        for (int w = 0; w < 4; ++w)
#pragma unroll
            for (int u = 0; u < 3; ++u) {
                if (w == 0) mmaw<false, false, true>(sc[u], asf(k[u >> 1][w]), asf(half(q[u > 0][w >> 1], w & 1)));
                else mmaw<false, false, false>(sc[u], asf(k[u >> 1][w]), asf(half(q[u > 0][w >> 1], w & 1)));
            }
        NOP8();
        float mx[2] = {-3.0e38f, -3.0e38f}, sum[2] = {0.f, 0.f};
#pragma unroll
        for (int u = 0; u < 3; ++u) {
            PIN(sc[u]);
#pragma unroll
            for (int r = 0; r < 4; ++r) {
                if (u != 1) sc[u][r] = 4 * g + r <= l15 ? sc[u][r] : -3.0e38f;
                mx[u > 0] = fmaxf(mx[u > 0], sc[u][r]);
            }
        }
        SCHED();
        u32 f = rdw(rs, fl + s * 32);
        SCHED();
        v2u pt[3];
#pragma unroll
        for (int m = 0; m < 2; ++m) mx[m] = xmax(mx[m]) * SC;
#pragma unroll
        for (int u = 0; u < 3; ++u) {
#pragma unroll
            for (int r = 0; r < 4; ++r) { sc[u][r] = __builtin_amdgcn_exp2f(sc[u][r] * SC - mx[u > 0]); sum[u > 0] += sc[u][r]; }
            pt[u] = v2u{pack2(sc[u][0], sc[u][1]), pack2(sc[u][2], sc[u][3])};
            PIN(pt[u]);
        }
        VMW(9);
        tell(rs, fo, s);
        NOP2();
        f32x4 o[2][4][2], sm[2];
#pragma unroll
        for (int u = 0; u < 3; ++u)
#pragma unroll
            for (int w = 0; w < 4; ++w)
#pragma unroll
                for (int t = 0; t < 2; ++t) {
                    const v2u a = {v[u >> 1][w][2 * t], v[u >> 1][w][2 * t + 1]};
                    if (u < 2) mm16<true>(o[u][w][t], a, pt[u]); else mm16<false>(o[1][w][t], a, pt[u]);
                }
#pragma unroll
        for (int m = 0; m < 2; ++m) { sum[m] = xsum(sum[m]); PIN(sum[m]); PIN(mx[m]); }
        NOP8();
        SCHED();
        PIN(f);
        f = reached<6>(f, 1u) ? __builtin_amdgcn_readfirstlane(f) : watch<6, false, AS>(ws, rs, 0, 0, fl + s * 32, 1u);
        CBAR();
        v4u km[4];
        v2u vm[4];
#pragma unroll
        for (int w = 0; w < 4; ++w) km[w] = ldt(rs, kvo + w * 512, mb);
#pragma unroll
        for (int w = 0; w < 4; ++w) vm[w] = __builtin_amdgcn_raw_buffer_load_b64(rs, vmo, mb + 2048 + w * 512, 16);
        SCHED();
        issue(s + 1 < NSEG ? s + 1 : s, qn);
        SCHED();
        const float si = __uint_as_float((255u - ((f >> 8) & 255u)) << 23);
#pragma unroll
        for (int w = 0; w < 4; ++w)
#pragma unroll
            for (int m = 0; m < 2; ++m) {
                if (w == 0) mmaw<false, false, true>(sm[m], asf(km[w]), asf(half(q[m][w >> 1], w & 1)));
                else mmaw<false, false, false>(sm[m], asf(km[w]), asf(half(q[m][w >> 1], w & 1)));
            }
        NOP8();
        v2u pm[2][2];
        float a[2];
#pragma unroll
        for (int m = 0; m < 2; ++m) {
            PIN(sm[m]);
            float m0 = sm[m][0] * (si * SC), m1 = sm[m][1] * (si * SC);
            const float mm = fmaxf(xmax(fmaxf(m0, m1)), mx[m]);
            m0 = __builtin_amdgcn_exp2f(m0 - mm); m1 = __builtin_amdgcn_exp2f(m1 - mm);
            a[m] = __builtin_amdgcn_exp2f(mx[m] - mm);
            const float inv = __builtin_amdgcn_rcpf(xsum(m0 + m1) + a[m] * sum[m]);
            a[m] *= inv;
            const u32 pp = pack2(m0 * inv * si, m1 * inv * si);
            pm[m][0] = v2u{pp, 0u}; pm[m][1] = v2u{0u, pp};
            PIN(pm[m][0]); PIN(pm[m][1]);
        }
        auto scale = [&](int w) {
#pragma unroll
            for (int m = 0; m < 2; ++m)
#pragma unroll
                for (int t = 0; t < 2; ++t) { o[m][w][t] *= a[m]; PIN(o[m][w][t]); }
        };
        auto put = [&](int w) {
#pragma unroll
            for (int m = 0; m < 2; ++m) { PIN(o[m][w][0]); PIN(o[m][w][1]); stt(rs, lane * 16 + m * 1024, OFF_OB + (((layer * 64 + s) * 32 + h) * 4 + w) * 2048, pack_pair(o[m][w][0], o[m][w][1])); }
        };
        scale(0);
#pragma unroll
        for (int w = 0; w < 4; ++w) {
            if (w < 3) scale(w + 1); else NOP2();
#pragma unroll
            for (int m = 0; m < 2; ++m)
#pragma unroll
                for (int t = 0; t < 2; ++t) mm16<false>(o[m][w][t], vm[w], pm[m][t]);
            if (w) put(w - 1);
        }
        NOP8();
        put(3);
        issuev(s + 1 < NSEG ? s + 1 : s);
    };
    v8u A[2][2], B[2][2];
    go(rs, ask(rs));
    wait_prog<2>(ws, rs, words<2>(OFF_Z), 1);
    watch<6, false, PS>(ws, rs, 0, 0, fl - 32, 1u);
    issue(0, A);
    issuev(0);
    for (int s = 0; s < NSEG; s += 2) { body(s, A, B); body(s + 1, B, A); }
    VMW(8);
    tell(rs, fo, NSEG);
}

__global__ __launch_bounds__(256, 1) void mega_kernel(const u16* __restrict__ x0, const u16* __restrict__ wqkv, const u16* __restrict__ woA,
                                                     const u16* __restrict__ mem0, const u16* __restrict__ Wq, const u16* __restrict__ Wk,
                                                     const u16* __restrict__ Wv, const u16* __restrict__ Wo, u16* out, char* ws) {
    __shared__ __attribute__((aligned(16))) char smem[163840];
    const char* code;
    asm volatile("s_getpc_b64 %0" : "=s"(code));
    u32 ins = 0;
    {
        const u32 i = BID >> 3, L = ((i & 1) | (i >> 2) << 1) * 256 + threadIdx.x;
        if ((i & 2) && L * 128 < CODE_BYTES) ins = *(const GAS u32*)(code + L * 128);
    }
    const rsrc_t rs = __builtin_amdgcn_make_buffer_rsrc(ws, 0, WS_BYTES, 0x00020000);
    const int b = BID;
    const int xc = b & 7, ix = b >> 3;
    const int wave = __builtin_amdgcn_readfirstlane(threadIdx.x >> 6);
    put(rs, my_word(), 0u);
    if ((ix & ~3) == 16) {
#pragma unroll
        for (int i = 0; i < 5; ++i) {
            const int u = (b - 128) * 5 + i;
            if (u < 68) clear(rs, OFF_FKVM + u * 4096, 1); else clear<2>(rs, b < 156 ? OFF_STAT + (u - 68) * 8192 : OOB, 1);
        }
        clear<2>(rs, OFF_ACC + 2 * ACC_SLOT + (b - 128) * 40960, 5);
    }
    const u32 late = 0x4b3fccccu;
    if (late >> ix & 1) { touch(__popc(late << (31 - ix)) - 1, xc, wqkv, Wq, Wk, Wv, Wo, rs); asm volatile("" :: "v"(ins)); }
    if (ix == 24 || ix == 25 || ix == 30 || (b >= 250 && b < 254)) clear_late(ix < 26 ? b - 192 : ix == 30 ? b - 224 : b - 226, rs);
#define ROLE(m)
    if (ix < 16) {
        if (ix & 2) { role_par(4 * xc + (ix >> 2), ix & 1, mem0, Wv, Wo, ws, rs, smem); nap(NAP_C); }
        role_chain(ws, Wq, Wk, Wv, Wo, (u32)(((ix >> 1) & 1) << 8 | (4 * xc + (ix >> 2)) << 3 | (ix & 1) << 2 | wave), OFF_ACK + (b * 4 + wave) * 4);
    }
    else if (ix < 22) {
        if (xc & 1) {
            nap(NAP_W);
            ROLE(1) if (ix >= 20) for (int i = 0; i < 2; ++i) role_t0(64 + ((ix - 20) * 8 + xc - i) * 4 + wave, x0, ws, rs);
        }
        ROLE(4) role_w(xc & 1, xc >> 1, ix - 16, Wo, ws, rs, smem);
    }
    else if (ix < 26) { ROLE(ix < 24 ? 1 : 16) role_a1((ix - 22) >> 1, 2 * xc + (ix & 1), wqkv, ws, rs, smem);
        ROLE(8) if (ix == 22) { DRAIN(); role_r<1, true, SL, NSEG>(xc, x0, out, ws, rs, smem); }
    }
    else if (ix < 28) {
        if (ix == 27) nap(NAP_A);
        ROLE(1) if (b != 208) role_t0(((ix - 26) * 8 + xc) * 4 + wave, x0, ws, rs);
        ROLE(2) role_att(ix - 26, 4 * xc + wave, ws, rs);
    }
    else if (ix == 28) { ROLE(8) role_r<1, false>(xc, x0, out, ws, rs, smem); }
    else if (b < 250) { ROLE(b & 8 && b != 249 ? 64 : 32) role_a2(ix < 31 ? ix - 29 : xc, ix < 31 ? xc : 8, x0, woA, ws, rs, smem); }
    else if (b < 254) { ROLE(8) role_r<2, true, 0, SL>(xc - 2, x0, out, ws, rs, smem); }
    else if (b == 254) role_lead(ws, rs);
    else role_side(ws, rs, smem);
}

extern "C" void kernel_launch(void* const* d_in, const int* in_sizes, int n_in, void* d_out, int out_size, void* d_ws,
                              size_t ws_size, hipStream_t stream) {
    mega_kernel<<<256, 256, 0, stream>>>((const u16*)d_in[0], (const u16*)d_in[1], (const u16*)d_in[2], (const u16*)d_in[4],
                                         (const u16*)d_in[5], (const u16*)d_in[6], (const u16*)d_in[7], (const u16*)d_in[8],
                                         (u16*)d_out, (char*)d_ws);
}
```

```cpp
#include <hip/hip_runtime.h>
#include <hip/hip_bf16.h>
#include <stdint.h>
#include <type_traits>

typedef unsigned short u16;
typedef unsigned u32;
typedef unsigned long long u64;
using bf16x8 = __attribute__((ext_vector_type(8))) short;
using bf16x4 = __attribute__((ext_vector_type(4))) short;
using f32x4 = __attribute__((ext_vector_type(4))) float;
using f32x2 = __attribute__((ext_vector_type(2))) float;
using v4u = __attribute__((ext_vector_type(4))) unsigned;
using v2u = __attribute__((ext_vector_type(2))) unsigned;
typedef __bf16 bfv2 __attribute__((ext_vector_type(2)));
#define GAS __attribute__((address_space(1)))
typedef GAS unsigned gu32;
#define RLX_AGENT __ATOMIC_RELAXED, __HIP_MEMORY_SCOPE_AGENT
typedef __amdgpu_buffer_rsrc_t rsrc_t;

constexpr int S_ = 2048, D_ = 576, NSEG = 64, SEG = 32, NB = 4096, NQA = 1728;

constexpr u32 XB = S_ * D_ * 2;
constexpr u32 OFF_ABORT = 0u;
constexpr u32 OFF_PAR = 64u;
constexpr u32 OFF_CC = 128u;
constexpr u32 OFF_Z = 256u;
constexpr u32 OFF_Z2 = 384u;
constexpr u32 OFF_CH = 512u;
constexpr u32 OFF_CXNA = 1024u;
constexpr u32 OFF_FKVM = 12288u;
constexpr u32 OFF_SCT = 151552u;
constexpr u32 OFF_STAT = 290816u;
constexpr u32 SYNC_BYTES = 880640u;
constexpr u32 CX = 4u;
constexpr u32 OFF_PROG = 884736u;
constexpr u32 OFF_ACK = 1015808u;
constexpr u32 OFF_ACC = 1048576u;
constexpr u32 OFF_XNA = 12189696u;
constexpr u32 OFF_XNB = 16908288u;
constexpr u32 OFF_X = 21626880u;
constexpr u32 OFF_OA = 28704768u;
constexpr u32 OFF_PB = 33423360u;
constexpr u32 OFF_OB = 71172096u;
constexpr u32 OFF_QKV = 104726528u;
constexpr u32 OFF_KVM = 208535552u;
constexpr u32 OFF_DBG = 225312768u;
constexpr u32 OFF_JUNK = 225902592u;
constexpr u32 WS_BYTES = 230653952u;
constexpr u32 ACC_SLOT = 163840;
__device__ __forceinline__ void role_chain(char* ws, const u16* Wq, const u16* Wk, const u16* Wv, const u16* Wo, u32 id, u32 ack) {
    asm volatile(
        "s_mov_b64 s[36:37], %0\n"
        "s_mov_b32 s38, 0xdbf8000\n"
        "s_mov_b32 s39, 0x00020000\n"
        "s_mov_b32 s42, 0x480000\n"
        "s_mov_b32 s43, 0x00020000\n"
        "s_and_b32 s45, %5, 3\n"
        "s_bfe_u32 s46, %5, 0x10002\n"
        "s_bfe_u32 s47, %5, 0x50003\n"
        "s_bfe_u32 s48, %5, 0x10008\n"
        "s_mov_b32 s63, 0\n"
        "v_mbcnt_lo_u32_b32 v128, -1, 0\n"
        "v_mbcnt_hi_u32_b32 v128, -1, v128\n"
        "v_and_b32 v129, 15, v128\n"
        "v_lshrrev_b32 v130, 4, v128\n"
        "v_lshlrev_b32 v131, 1, v130\n"
        "v_xor_b32 v131, v131, v129\n"
        "v_lshlrev_b32 v131, 4, v131\n"
        "v_lshl_add_u32 v134, v130, 13, v131\n"
        "v_xor_b32 v135, 0x80, v134\n"
        "v_and_b32 v131, 0xc0, v131\n"
        "v_lshl_or_b32 v140, v131, 14, v134\n"
        "v_xor_b32 v141, 0x200080, v140\n"
        "v_lshrrev_b32 v131, 2, v129\n"
        "v_lshl_add_u32 v132, v130, 3, v131\n"
        "v_lshlrev_b32 v136, 8, v132\n"
        "v_and_b32 v132, 1, v130\n"
        "v_lshl_or_b32 v131, v132, 2, v131\n"
        "v_lshlrev_b32 v131, 1, v131\n"
        "v_bfe_u32 v132, v129, 1, 1\n"
        "s_lshl_b32 s80, s45, 2\n"
        "v_or_b32 v132, s80, v132\n"
        "v_xor_b32 v131, v131, v132\n"
        "v_lshl_add_u32 v136, v131, 4, v136\n"
        "v_and_b32 v132, 1, v129\n"
        "v_lshl_add_u32 v136, v132, 3, v136\n"
        "v_xor_b32 v137, 32, v136\n"
        "s_lshl_b32 s49, s45, 17\n"
        "s_lshl_b32 s80, s47, 8\n"
        "s_add_u32 s49, s49, s80\n"
        "s_lshl_b32 s50, s45, 12\n"
        "s_bfe_u32 s80, s47, 0x10004\n"
        "s_xor_b32 s83, s80, 1\n"
        "s_lshl_b32 s67, s83, 19\n"
        "s_lshl_b32 s68, s80, 19\n"
        "s_add_u32 s67, s67, s49\n"
        "s_add_u32 s68, s68, s49\n"
        "s_mov_b64 s[40:41], %3\n"
        "v_xor_b32 v142, 0x0, v140\n"
        "v_xor_b32 v143, 0x0, v141\n"
        "s_add_u32 m0, s50, 0\n"
        "s_add_u32 s80, s67, 0\n"
        "buffer_load_dwordx4 v142, s[40:43], s80 offen lds\n"
        "s_add_u32 m0, s50, 1024\n"
        "s_add_u32 s80, s67, 32768\n"
        "buffer_load_dwordx4 v142, s[40:43], s80 offen lds\n"
        "s_add_u32 m0, s50, 2048\n"
        "s_add_u32 s80, s67, 65536\n"
        "buffer_load_dwordx4 v143, s[40:43], s80 offen lds\n"
        "s_add_u32 m0, s50, 3072\n"
        "s_add_u32 s80, s67, 98304\n"
        "buffer_load_dwordx4 v143, s[40:43], s80 offen lds\n"
        "v_xor_b32 v142, 0x0, v140\n"
        "v_xor_b32 v143, 0x0, v141\n"
        "s_add_u32 m0, s50, 16384\n"
        "s_add_u32 s80, s68, 0\n"
        "buffer_load_dwordx4 v142, s[40:43], s80 offen lds\n"
        "s_add_u32 m0, s50, 17408\n"
        "s_add_u32 s80, s68, 32768\n"
        "buffer_load_dwordx4 v142, s[40:43], s80 offen lds\n"
        "s_add_u32 m0, s50, 18432\n"
        "s_add_u32 s80, s68, 65536\n"
        "buffer_load_dwordx4 v143, s[40:43], s80 offen lds\n"
        "s_add_u32 m0, s50, 19456\n"
        "s_add_u32 s80, s68, 98304\n"
        "buffer_load_dwordx4 v143, s[40:43], s80 offen lds\n"
        "v_xor_b32 v142, 0x100000, v140\n"
        "v_xor_b32 v143, 0x100000, v141\n"
        "s_add_u32 m0, s50, 32768\n"
        "s_add_u32 s80, s67, 0\n"
        "buffer_load_dwordx4 v142, s[40:43], s80 offen lds\n"
        "s_add_u32 m0, s50, 33792\n"
        "s_add_u32 s80, s67, 32768\n"
        "buffer_load_dwordx4 v142, s[40:43], s80 offen lds\n"
        "s_add_u32 m0, s50, 34816\n"
        "s_add_u32 s80, s67, 65536\n"
        "buffer_load_dwordx4 v143, s[40:43], s80 offen lds\n"
        "s_add_u32 m0, s50, 35840\n"
        "s_add_u32 s80, s67, 98304\n"
        "buffer_load_dwordx4 v143, s[40:43], s80 offen lds\n"
        "s_mov_b64 s[40:41], %3\n"
        "v_xor_b32 v142, 0x100000, v140\n"
        "v_xor_b32 v143, 0x100000, v141\n"
        "s_add_u32 m0, s50, 49152\n"
        "s_add_u32 s80, s68, 0\n"
        "buffer_load_dwordx4 v142, s[40:43], s80 offen lds\n"
        "s_add_u32 m0, s50, 50176\n"
        "s_add_u32 s80, s68, 32768\n"
        "buffer_load_dwordx4 v142, s[40:43], s80 offen lds\n"
        "s_add_u32 m0, s50, 51200\n"
        "s_add_u32 s80, s68, 65536\n"
        "buffer_load_dwordx4 v143, s[40:43], s80 offen lds\n"
        "s_add_u32 m0, s50, 52224\n"
        "s_add_u32 s80, s68, 98304\n"
        "buffer_load_dwordx4 v143, s[40:43], s80 offen lds\n"
        "v_xor_b32 v142, 0x200000, v140\n"
        "v_xor_b32 v143, 0x200000, v141\n"
        "s_add_u32 m0, s50, 65536\n"
        "s_add_u32 s80, s67, 0\n"
        "buffer_load_dwordx4 v142, s[40:43], s80 offen lds\n"
        "s_add_u32 m0, s50, 66560\n"
        "s_add_u32 s80, s67, 32768\n"
        "buffer_load_dwordx4 v142, s[40:43], s80 offen lds\n"
        "s_add_u32 m0, s50, 67584\n"
        "s_add_u32 s80, s67, 65536\n"
        "buffer_load_dwordx4 v143, s[40:43], s80 offen lds\n"
        "s_add_u32 m0, s50, 68608\n"
        "s_add_u32 s80, s67, 98304\n"
        "buffer_load_dwordx4 v143, s[40:43], s80 offen lds\n"
        "v_xor_b32 v142, 0x200000, v140\n"
        "v_xor_b32 v143, 0x200000, v141\n"
        "s_add_u32 m0, s50, 81920\n"
        "s_add_u32 s80, s68, 0\n"
        "buffer_load_dwordx4 v142, s[40:43], s80 offen lds\n"
        "s_add_u32 m0, s50, 82944\n"
        "s_add_u32 s80, s68, 32768\n"
        "buffer_load_dwordx4 v142, s[40:43], s80 offen lds\n"
        "s_add_u32 m0, s50, 83968\n"
        "s_add_u32 s80, s68, 65536\n"
        "buffer_load_dwordx4 v143, s[40:43], s80 offen lds\n"
        "s_add_u32 m0, s50, 84992\n"
        "s_add_u32 s80, s68, 98304\n"
        "buffer_load_dwordx4 v143, s[40:43], s80 offen lds\n"
        "s_mov_b64 s[40:41], %3\n"
        "v_xor_b32 v142, 0x300000, v140\n"
        "v_xor_b32 v143, 0x300000, v141\n"
        "s_add_u32 m0, s50, 98304\n"
        "s_add_u32 s80, s67, 0\n"
        "buffer_load_dwordx4 v142, s[40:43], s80 offen lds\n"
        "s_add_u32 m0, s50, 99328\n"
        "s_add_u32 s80, s67, 32768\n"
        "buffer_load_dwordx4 v142, s[40:43], s80 offen lds\n"
        "s_add_u32 m0, s50, 100352\n"
        "s_add_u32 s80, s67, 65536\n"
        "buffer_load_dwordx4 v143, s[40:43], s80 offen lds\n"
        "s_add_u32 m0, s50, 101376\n"
        "s_add_u32 s80, s67, 98304\n"
        "buffer_load_dwordx4 v143, s[40:43], s80 offen lds\n"
        "v_xor_b32 v142, 0x300000, v140\n"
        "v_xor_b32 v143, 0x300000, v141\n"
        "s_add_u32 m0, s50, 114688\n"
        "s_add_u32 s80, s68, 0\n"
        "buffer_load_dwordx4 v142, s[40:43], s80 offen lds\n"
        "s_add_u32 m0, s50, 115712\n"
        "s_add_u32 s80, s68, 32768\n"
        "buffer_load_dwordx4 v142, s[40:43], s80 offen lds\n"
        "s_add_u32 m0, s50, 116736\n"
        "s_add_u32 s80, s68, 65536\n"
        "buffer_load_dwordx4 v143, s[40:43], s80 offen lds\n"
        "s_add_u32 m0, s50, 117760\n"
        "s_add_u32 s80, s68, 98304\n"
        "buffer_load_dwordx4 v143, s[40:43], s80 offen lds\n"
        "s_add_u32 m0, s50, 131072\n"
        "s_add_u32 s80, s49, 4194304\n"
        "buffer_load_dwordx4 v134, s[40:43], s80 offen lds\n"
        "s_add_u32 m0, s50, 132096\n"
        "s_add_u32 s80, s49, 4227072\n"
        "buffer_load_dwordx4 v134, s[40:43], s80 offen lds\n"
        "s_add_u32 m0, s50, 133120\n"
        "s_add_u32 s80, s49, 4259840\n"
        "buffer_load_dwordx4 v135, s[40:43], s80 offen lds\n"
        "s_add_u32 m0, s50, 134144\n"
        "s_add_u32 s80, s49, 4292608\n"
        "buffer_load_dwordx4 v135, s[40:43], s80 offen lds\n"
        "v_lshrrev_b32 v131, 3, v129\n"
        "v_lshlrev_b32 v131, 5, v131\n"
        "v_and_b32 v132, 3, v129\n"
        "v_lshl_add_u32 v131, v132, 3, v131\n"
        "v_and_b32 v132, 4, v129\n"
        "v_add_u32 v131, v131, v132\n"
        "s_movk_i32 s80, 4608\n"
        "v_mad_u32_u24 v138, v130, s80, v131\n"
        "s_and_b32 s81, s47, 1\n"
        "s_mul_i32 s81, s81, 6\n"
        "v_xor_b32 v131, s81, v129\n"
        "v_and_b32 v131, 14, v131\n"
        "v_lshlrev_b32 v131, 1, v131\n"
        "v_mad_u32_u24 v127, v130, s80, v131\n"
        "s_mul_i32 s51, s47, 147456\n"
        "s_lshl2_add_u32 s80, s46, s45\n"
        "s_lshl_b32 s80, s80, 7\n"
        "s_add_u32 s52, s51, s80\n"
        "s_and_b32 s80, s45, 1\n"
        "s_lshl_b32 s81, s80, 6\n"
        "s_sub_u32 s52, s52, s81\n"
        "s_lshl1_add_u32 s80, s46, s80\n"
        "s_lshl_b32 s80, s80, 5\n"
        "s_add_u32 s51, s51, s80\n"
        "s_add_u32 s51, s51, 1024\n"
        "s_and_b32 s80, s47, 2\n"
        "s_lshl_b32 s80, s80, 6\n"
        "s_add_u32 s52, s52, s80\n"
        "s_xor_b32 s66, s52, 128\n"
        "s_mov_b32 s53, 0x05040100\n"
        "s_mov_b32 s54, 0x07060302\n"
        "v_and_b32 v131, 1, v129\n"
        "s_mov_b32 s80, 0x02020202\n"
        "v_mul_lo_u32 v131, v131, s80\n"
        "v_add_u32 v139, s53, v131\n"
        "v_lshlrev_b32 v131, 4, v128\n"
        "s_and_b32 s80, s45, 1\n"
        "s_lshl_b32 s80, s80, 12\n"
        "s_add_u32 s84, s80, 152576\n"
        "s_mov_b64 s[40:41], %4\n"
        "s_add_u32 s80, s51, 0\n"
        "buffer_load_dword v64, v127, s[40:43], s80 offen offset:0\n"
        "buffer_load_dword v66, v127, s[40:43], s80 offen offset:1152\n"
        "buffer_load_dword v68, v127, s[40:43], s80 offen offset:2304\n"
        "buffer_load_dword v70, v127, s[40:43], s80 offen offset:3456\n"
        "s_add_u32 s80, s51, 18432\n"
        "buffer_load_dword v65, v127, s[40:43], s80 offen offset:0\n"
        "buffer_load_dword v67, v127, s[40:43], s80 offen offset:1152\n"
        "buffer_load_dword v69, v127, s[40:43], s80 offen offset:2304\n"
        "buffer_load_dword v71, v127, s[40:43], s80 offen offset:3456\n"
        "s_add_u32 s80, s51, 36864\n"
        "buffer_load_dword v72, v127, s[40:43], s80 offen offset:0\n"
        "buffer_load_dword v74, v127, s[40:43], s80 offen offset:1152\n"
        "buffer_load_dword v76, v127, s[40:43], s80 offen offset:2304\n"
        "buffer_load_dword v78, v127, s[40:43], s80 offen offset:3456\n"
        "s_add_u32 s80, s51, 55296\n"
        "buffer_load_dword v73, v127, s[40:43], s80 offen offset:0\n"
        "buffer_load_dword v75, v127, s[40:43], s80 offen offset:1152\n"
        "buffer_load_dword v77, v127, s[40:43], s80 offen offset:2304\n"
        "buffer_load_dword v79, v127, s[40:43], s80 offen offset:3456\n"
        "s_add_u32 s80, s51, 73728\n"
        "buffer_load_dword v80, v127, s[40:43], s80 offen offset:0\n"
        "buffer_load_dword v82, v127, s[40:43], s80 offen offset:1152\n"
        "buffer_load_dword v84, v127, s[40:43], s80 offen offset:2304\n"
        "buffer_load_dword v86, v127, s[40:43], s80 offen offset:3456\n"
        "s_add_u32 s80, s51, 92160\n"
        "buffer_load_dword v81, v127, s[40:43], s80 offen offset:0\n"
        "buffer_load_dword v83, v127, s[40:43], s80 offen offset:1152\n"
        "buffer_load_dword v85, v127, s[40:43], s80 offen offset:2304\n"
        "buffer_load_dword v87, v127, s[40:43], s80 offen offset:3456\n"
        "s_add_u32 s80, s51, 110592\n"
        "buffer_load_dword v88, v127, s[40:43], s80 offen offset:0\n"
        "buffer_load_dword v90, v127, s[40:43], s80 offen offset:1152\n"
        "buffer_load_dword v92, v127, s[40:43], s80 offen offset:2304\n"
        "buffer_load_dword v94, v127, s[40:43], s80 offen offset:3456\n"
        "s_add_u32 s80, s51, 129024\n"
        "buffer_load_dword v89, v127, s[40:43], s80 offen offset:0\n"
        "buffer_load_dword v91, v127, s[40:43], s80 offen offset:1152\n"
        "buffer_load_dword v93, v127, s[40:43], s80 offen offset:2304\n"
        "buffer_load_dword v95, v127, s[40:43], s80 offen offset:3456\n"
        "v_add_u32 v132, 0, v136\n"
        "v_add_u32 v133, 0, v137\n"
        "s_waitcnt vmcnt(56)\n"
        "s_barrier\n"
        "ds_read_b64_tr_b16 a[0:1], v132 offset:0\n"
        "ds_read_b64_tr_b16 a[2:3], v132 offset:1024\n"
        "ds_read_b64_tr_b16 a[4:5], v132 offset:8192\n"
        "ds_read_b64_tr_b16 a[6:7], v132 offset:9216\n"
        "ds_read_b64_tr_b16 a[8:9], v133 offset:0\n"
        "ds_read_b64_tr_b16 a[10:11], v133 offset:1024\n"
        "ds_read_b64_tr_b16 a[12:13], v133 offset:8192\n"
        "ds_read_b64_tr_b16 a[14:15], v133 offset:9216\n"
        "ds_read_b64_tr_b16 a[16:17], v132 offset:16384\n"
        "ds_read_b64_tr_b16 a[18:19], v132 offset:17408\n"
        "ds_read_b64_tr_b16 a[20:21], v132 offset:24576\n"
        "ds_read_b64_tr_b16 a[22:23], v132 offset:25600\n"
        "ds_read_b64_tr_b16 a[24:25], v133 offset:16384\n"
        "ds_read_b64_tr_b16 a[26:27], v133 offset:17408\n"
        "ds_read_b64_tr_b16 a[28:29], v133 offset:24576\n"
        "ds_read_b64_tr_b16 a[30:31], v133 offset:25600\n"
        "ds_read_b64_tr_b16 a[32:33], v132 offset:32768\n"
        "ds_read_b64_tr_b16 a[34:35], v132 offset:33792\n"
        "ds_read_b64_tr_b16 a[36:37], v132 offset:40960\n"
        "ds_read_b64_tr_b16 a[38:39], v132 offset:41984\n"
        "ds_read_b64_tr_b16 a[40:41], v133 offset:32768\n"
        "ds_read_b64_tr_b16 a[42:43], v133 offset:33792\n"
        "ds_read_b64_tr_b16 a[44:45], v133 offset:40960\n"
        "ds_read_b64_tr_b16 a[46:47], v133 offset:41984\n"
        "s_mov_b64 s[40:41], %4\n"
        "s_add_u32 s80, s52, 0\n"
        "buffer_load_dword v0, v138, s[40:43], s80 offen offset:0\n"
        "buffer_load_dword v2, v138, s[40:43], s80 offen offset:1152\n"
        "buffer_load_dword v4, v138, s[40:43], s80 offen offset:2304\n"
        "buffer_load_dword v6, v138, s[40:43], s80 offen offset:3456\n"
        "s_add_u32 s80, s52, 18432\n"
        "buffer_load_dword v1, v138, s[40:43], s80 offen offset:0\n"
        "buffer_load_dword v3, v138, s[40:43], s80 offen offset:1152\n"
        "buffer_load_dword v5, v138, s[40:43], s80 offen offset:2304\n"
        "buffer_load_dword v7, v138, s[40:43], s80 offen offset:3456\n"
        "s_add_u32 s80, s52, 36864\n"
        "buffer_load_dword v8, v138, s[40:43], s80 offen offset:0\n"
        "buffer_load_dword v10, v138, s[40:43], s80 offen offset:1152\n"
        "buffer_load_dword v12, v138, s[40:43], s80 offen offset:2304\n"
        "buffer_load_dword v14, v138, s[40:43], s80 offen offset:3456\n"
        "s_add_u32 s80, s52, 55296\n"
        "buffer_load_dword v9, v138, s[40:43], s80 offen offset:0\n"
        "buffer_load_dword v11, v138, s[40:43], s80 offen offset:1152\n"
        "buffer_load_dword v13, v138, s[40:43], s80 offen offset:2304\n"
        "buffer_load_dword v15, v138, s[40:43], s80 offen offset:3456\n"
        "s_add_u32 s80, s52, 73728\n"
        "buffer_load_dword v16, v138, s[40:43], s80 offen offset:0\n"
        "buffer_load_dword v18, v138, s[40:43], s80 offen offset:1152\n"
        "buffer_load_dword v20, v138, s[40:43], s80 offen offset:2304\n"
        "buffer_load_dword v22, v138, s[40:43], s80 offen offset:3456\n"
        "s_add_u32 s80, s52, 92160\n"
        "buffer_load_dword v17, v138, s[40:43], s80 offen offset:0\n"
        "buffer_load_dword v19, v138, s[40:43], s80 offen offset:1152\n"
        "buffer_load_dword v21, v138, s[40:43], s80 offen offset:2304\n"
        "buffer_load_dword v23, v138, s[40:43], s80 offen offset:3456\n"
        "s_add_u32 s80, s52, 110592\n"
        "buffer_load_dword v24, v138, s[40:43], s80 offen offset:0\n"
        "buffer_load_dword v26, v138, s[40:43], s80 offen offset:1152\n"
        "buffer_load_dword v28, v138, s[40:43], s80 offen offset:2304\n"
        "buffer_load_dword v30, v138, s[40:43], s80 offen offset:3456\n"
        "s_add_u32 s80, s52, 129024\n"
        "buffer_load_dword v25, v138, s[40:43], s80 offen offset:0\n"
        "buffer_load_dword v27, v138, s[40:43], s80 offen offset:1152\n"
        "buffer_load_dword v29, v138, s[40:43], s80 offen offset:2304\n"
        "buffer_load_dword v31, v138, s[40:43], s80 offen offset:3456\n"
        "v_add_u32 v132, 49152, v136\n"
        "v_add_u32 v133, 49152, v137\n"
        "s_waitcnt vmcnt(60)\n"
        "s_barrier\n"
        "ds_read_b64_tr_b16 a[48:49], v132 offset:0\n"
        "ds_read_b64_tr_b16 a[50:51], v132 offset:1024\n"
        "ds_read_b64_tr_b16 a[52:53], v132 offset:8192\n"
        "ds_read_b64_tr_b16 a[54:55], v132 offset:9216\n"
        "ds_read_b64_tr_b16 a[56:57], v133 offset:0\n"
        "ds_read_b64_tr_b16 a[58:59], v133 offset:1024\n"
        "ds_read_b64_tr_b16 a[60:61], v133 offset:8192\n"
        "ds_read_b64_tr_b16 a[62:63], v133 offset:9216\n"
        "ds_read_b64_tr_b16 a[64:65], v132 offset:16384\n"
        "ds_read_b64_tr_b16 a[66:67], v132 offset:17408\n"
        "ds_read_b64_tr_b16 a[68:69], v132 offset:24576\n"
        "ds_read_b64_tr_b16 a[70:71], v132 offset:25600\n"
        "ds_read_b64_tr_b16 a[72:73], v133 offset:16384\n"
        "ds_read_b64_tr_b16 a[74:75], v133 offset:17408\n"
        "ds_read_b64_tr_b16 a[76:77], v133 offset:24576\n"
        "ds_read_b64_tr_b16 a[78:79], v133 offset:25600\n"
        "ds_read_b64_tr_b16 a[80:81], v132 offset:32768\n"
        "ds_read_b64_tr_b16 a[82:83], v132 offset:33792\n"
        "ds_read_b64_tr_b16 a[84:85], v132 offset:40960\n"
        "ds_read_b64_tr_b16 a[86:87], v132 offset:41984\n"
        "ds_read_b64_tr_b16 a[88:89], v133 offset:32768\n"
        "ds_read_b64_tr_b16 a[90:91], v133 offset:33792\n"
        "ds_read_b64_tr_b16 a[92:93], v133 offset:40960\n"
        "ds_read_b64_tr_b16 a[94:95], v133 offset:41984\n"
        "s_mov_b64 s[40:41], %4\n"
        "s_add_u32 s80, s66, 0\n"
        "buffer_load_dword v32, v138, s[40:43], s80 offen offset:0\n"
        "buffer_load_dword v34, v138, s[40:43], s80 offen offset:1152\n"
        "buffer_load_dword v36, v138, s[40:43], s80 offen offset:2304\n"
        "buffer_load_dword v38, v138, s[40:43], s80 offen offset:3456\n"
        "s_add_u32 s80, s66, 18432\n"
        "buffer_load_dword v33, v138, s[40:43], s80 offen offset:0\n"
        "buffer_load_dword v35, v138, s[40:43], s80 offen offset:1152\n"
        "buffer_load_dword v37, v138, s[40:43], s80 offen offset:2304\n"
        "buffer_load_dword v39, v138, s[40:43], s80 offen offset:3456\n"
        "s_add_u32 s80, s66, 36864\n"
        "buffer_load_dword v40, v138, s[40:43], s80 offen offset:0\n"
        "buffer_load_dword v42, v138, s[40:43], s80 offen offset:1152\n"
        "buffer_load_dword v44, v138, s[40:43], s80 offen offset:2304\n"
        "buffer_load_dword v46, v138, s[40:43], s80 offen offset:3456\n"
        "s_add_u32 s80, s66, 55296\n"
        "buffer_load_dword v41, v138, s[40:43], s80 offen offset:0\n"
        "buffer_load_dword v43, v138, s[40:43], s80 offen offset:1152\n"
        "buffer_load_dword v45, v138, s[40:43], s80 offen offset:2304\n"
        "buffer_load_dword v47, v138, s[40:43], s80 offen offset:3456\n"
        "s_add_u32 s80, s66, 73728\n"
        "buffer_load_dword v48, v138, s[40:43], s80 offen offset:0\n"
        "buffer_load_dword v50, v138, s[40:43], s80 offen offset:1152\n"
        "buffer_load_dword v52, v138, s[40:43], s80 offen offset:2304\n"
        "buffer_load_dword v54, v138, s[40:43], s80 offen offset:3456\n"
        "s_add_u32 s80, s66, 92160\n"
        "buffer_load_dword v49, v138, s[40:43], s80 offen offset:0\n"
        "buffer_load_dword v51, v138, s[40:43], s80 offen offset:1152\n"
        "buffer_load_dword v53, v138, s[40:43], s80 offen offset:2304\n"
        "buffer_load_dword v55, v138, s[40:43], s80 offen offset:3456\n"
        "s_add_u32 s80, s66, 110592\n"
        "buffer_load_dword v56, v138, s[40:43], s80 offen offset:0\n"
        "buffer_load_dword v58, v138, s[40:43], s80 offen offset:1152\n"
        "buffer_load_dword v60, v138, s[40:43], s80 offen offset:2304\n"
        "buffer_load_dword v62, v138, s[40:43], s80 offen offset:3456\n"
        "s_add_u32 s80, s66, 129024\n"
        "buffer_load_dword v57, v138, s[40:43], s80 offen offset:0\n"
        "buffer_load_dword v59, v138, s[40:43], s80 offen offset:1152\n"
        "buffer_load_dword v61, v138, s[40:43], s80 offen offset:2304\n"
        "buffer_load_dword v63, v138, s[40:43], s80 offen offset:3456\n"
        "v_add_u32 v132, 98304, v136\n"
        "v_add_u32 v133, 98304, v137\n"
        "s_waitcnt vmcnt(60)\n"
        "s_barrier\n"
        "ds_read_b64_tr_b16 a[96:97], v132 offset:0\n"
        "ds_read_b64_tr_b16 a[98:99], v132 offset:1024\n"
        "ds_read_b64_tr_b16 a[100:101], v132 offset:8192\n"
        "ds_read_b64_tr_b16 a[102:103], v132 offset:9216\n"
        "ds_read_b64_tr_b16 a[104:105], v133 offset:0\n"
        "ds_read_b64_tr_b16 a[106:107], v133 offset:1024\n"
        "ds_read_b64_tr_b16 a[108:109], v133 offset:8192\n"
        "ds_read_b64_tr_b16 a[110:111], v133 offset:9216\n"
        "ds_read_b64_tr_b16 a[112:113], v132 offset:16384\n"
        "ds_read_b64_tr_b16 a[114:115], v132 offset:17408\n"
        "ds_read_b64_tr_b16 a[116:117], v132 offset:24576\n"
        "ds_read_b64_tr_b16 a[118:119], v132 offset:25600\n"
        "ds_read_b64_tr_b16 a[120:121], v133 offset:16384\n"
        "ds_read_b64_tr_b16 a[122:123], v133 offset:17408\n"
        "ds_read_b64_tr_b16 a[124:125], v133 offset:24576\n"
        "ds_read_b64_tr_b16 a[126:127], v133 offset:25600\n"
        "ds_read_b64_tr_b16 a[128:129], v132 offset:32768\n"
        "ds_read_b64_tr_b16 a[130:131], v132 offset:33792\n"
        "ds_read_b64_tr_b16 a[132:133], v132 offset:40960\n"
        "ds_read_b64_tr_b16 a[134:135], v132 offset:41984\n"
        "ds_read_b64_tr_b16 a[136:137], v133 offset:32768\n"
        "ds_read_b64_tr_b16 a[138:139], v133 offset:33792\n"
        "ds_read_b64_tr_b16 a[140:141], v133 offset:40960\n"
        "ds_read_b64_tr_b16 a[142:143], v133 offset:41984\n"
        "s_waitcnt lgkmcnt(0)\n"
        "s_barrier\n"
        "s_mov_b64 s[40:41], %1\n"
        "v_xor_b32 v142, 0x0, v140\n"
        "v_xor_b32 v143, 0x0, v141\n"
        "s_add_u32 m0, s50, 0\n"
        "s_add_u32 s80, s67, 0\n"
        "buffer_load_dwordx4 v142, s[40:43], s80 offen lds\n"
        "s_add_u32 m0, s50, 1024\n"
        "s_add_u32 s80, s67, 32768\n"
        "buffer_load_dwordx4 v142, s[40:43], s80 offen lds\n"
        "s_add_u32 m0, s50, 2048\n"
        "s_add_u32 s80, s67, 65536\n"
        "buffer_load_dwordx4 v143, s[40:43], s80 offen lds\n"
        "s_add_u32 m0, s50, 3072\n"
        "s_add_u32 s80, s67, 98304\n"
        "buffer_load_dwordx4 v143, s[40:43], s80 offen lds\n"
        "v_xor_b32 v142, 0x0, v140\n"
        "v_xor_b32 v143, 0x0, v141\n"
        "s_add_u32 m0, s50, 16384\n"
        "s_add_u32 s80, s68, 0\n"
        "buffer_load_dwordx4 v142, s[40:43], s80 offen lds\n"
        "s_add_u32 m0, s50, 17408\n"
        "s_add_u32 s80, s68, 32768\n"
        "buffer_load_dwordx4 v142, s[40:43], s80 offen lds\n"
        "s_add_u32 m0, s50, 18432\n"
        "s_add_u32 s80, s68, 65536\n"
        "buffer_load_dwordx4 v143, s[40:43], s80 offen lds\n"
        "s_add_u32 m0, s50, 19456\n"
        "s_add_u32 s80, s68, 98304\n"
        "buffer_load_dwordx4 v143, s[40:43], s80 offen lds\n"
        "v_xor_b32 v142, 0x100000, v140\n"
        "v_xor_b32 v143, 0x100000, v141\n"
        "s_add_u32 m0, s50, 32768\n"
        "s_add_u32 s80, s67, 0\n"
        "buffer_load_dwordx4 v142, s[40:43], s80 offen lds\n"
        "s_add_u32 m0, s50, 33792\n"
        "s_add_u32 s80, s67, 32768\n"
        "buffer_load_dwordx4 v142, s[40:43], s80 offen lds\n"
        "s_add_u32 m0, s50, 34816\n"
        "s_add_u32 s80, s67, 65536\n"
        "buffer_load_dwordx4 v143, s[40:43], s80 offen lds\n"
        "s_add_u32 m0, s50, 35840\n"
        "s_add_u32 s80, s67, 98304\n"
        "buffer_load_dwordx4 v143, s[40:43], s80 offen lds\n"
        "s_mov_b64 s[40:41], %1\n"
        "v_xor_b32 v142, 0x100000, v140\n"
        "v_xor_b32 v143, 0x100000, v141\n"
        "s_add_u32 m0, s50, 49152\n"
        "s_add_u32 s80, s68, 0\n"
        "buffer_load_dwordx4 v142, s[40:43], s80 offen lds\n"
        "s_add_u32 m0, s50, 50176\n"
        "s_add_u32 s80, s68, 32768\n"
        "buffer_load_dwordx4 v142, s[40:43], s80 offen lds\n"
        "s_add_u32 m0, s50, 51200\n"
        "s_add_u32 s80, s68, 65536\n"
        "buffer_load_dwordx4 v143, s[40:43], s80 offen lds\n"
        "s_add_u32 m0, s50, 52224\n"
        "s_add_u32 s80, s68, 98304\n"
        "buffer_load_dwordx4 v143, s[40:43], s80 offen lds\n"
        "v_xor_b32 v142, 0x200000, v140\n"
        "v_xor_b32 v143, 0x200000, v141\n"
        "s_add_u32 m0, s50, 65536\n"
        "s_add_u32 s80, s67, 0\n"
        "buffer_load_dwordx4 v142, s[40:43], s80 offen lds\n"
        "s_add_u32 m0, s50, 66560\n"
        "s_add_u32 s80, s67, 32768\n"
        "buffer_load_dwordx4 v142, s[40:43], s80 offen lds\n"
        "s_add_u32 m0, s50, 67584\n"
        "s_add_u32 s80, s67, 65536\n"
        "buffer_load_dwordx4 v143, s[40:43], s80 offen lds\n"
        "s_add_u32 m0, s50, 68608\n"
        "s_add_u32 s80, s67, 98304\n"
        "buffer_load_dwordx4 v143, s[40:43], s80 offen lds\n"
        "v_xor_b32 v142, 0x200000, v140\n"
        "v_xor_b32 v143, 0x200000, v141\n"
        "s_add_u32 m0, s50, 81920\n"
        "s_add_u32 s80, s68, 0\n"
        "buffer_load_dwordx4 v142, s[40:43], s80 offen lds\n"
        "s_add_u32 m0, s50, 82944\n"
        "s_add_u32 s80, s68, 32768\n"
        "buffer_load_dwordx4 v142, s[40:43], s80 offen lds\n"
        "s_add_u32 m0, s50, 83968\n"
        "s_add_u32 s80, s68, 65536\n"
        "buffer_load_dwordx4 v143, s[40:43], s80 offen lds\n"
        "s_add_u32 m0, s50, 84992\n"
        "s_add_u32 s80, s68, 98304\n"
        "buffer_load_dwordx4 v143, s[40:43], s80 offen lds\n"
        "s_mov_b64 s[40:41], %2\n"
        "v_xor_b32 v142, 0x0, v140\n"
        "v_xor_b32 v143, 0x0, v141\n"
        "s_add_u32 m0, s50, 98304\n"
        "s_add_u32 s80, s67, 0\n"
        "buffer_load_dwordx4 v142, s[40:43], s80 offen lds\n"
        "s_add_u32 m0, s50, 99328\n"
        "s_add_u32 s80, s67, 32768\n"
        "buffer_load_dwordx4 v142, s[40:43], s80 offen lds\n"
        "s_add_u32 m0, s50, 100352\n"
        "s_add_u32 s80, s67, 65536\n"
        "buffer_load_dwordx4 v143, s[40:43], s80 offen lds\n"
        "s_add_u32 m0, s50, 101376\n"
        "s_add_u32 s80, s67, 98304\n"
        "buffer_load_dwordx4 v143, s[40:43], s80 offen lds\n"
        "v_xor_b32 v142, 0x0, v140\n"
        "v_xor_b32 v143, 0x0, v141\n"
        "s_add_u32 m0, s50, 114688\n"
        "s_add_u32 s80, s68, 0\n"
        "buffer_load_dwordx4 v142, s[40:43], s80 offen lds\n"
        "s_add_u32 m0, s50, 115712\n"
        "s_add_u32 s80, s68, 32768\n"
        "buffer_load_dwordx4 v142, s[40:43], s80 offen lds\n"
        "s_add_u32 m0, s50, 116736\n"
        "s_add_u32 s80, s68, 65536\n"
        "buffer_load_dwordx4 v143, s[40:43], s80 offen lds\n"
        "s_add_u32 m0, s50, 117760\n"
        "s_add_u32 s80, s68, 98304\n"
        "buffer_load_dwordx4 v143, s[40:43], s80 offen lds\n"
        "v_xor_b32 v142, 0x200000, v140\n"
        "v_xor_b32 v143, 0x200000, v141\n"
        "s_add_u32 m0, s50, 131072\n"
        "s_add_u32 s80, s68, 0\n"
        "buffer_load_dwordx4 v142, s[40:43], s80 offen lds\n"
        "s_add_u32 m0, s50, 132096\n"
        "s_add_u32 s80, s68, 32768\n"
        "buffer_load_dwordx4 v142, s[40:43], s80 offen lds\n"
        "s_add_u32 m0, s50, 133120\n"
        "s_add_u32 s80, s68, 65536\n"
        "buffer_load_dwordx4 v143, s[40:43], s80 offen lds\n"
        "s_add_u32 m0, s50, 134144\n"
        "s_add_u32 s80, s68, 98304\n"
        "buffer_load_dwordx4 v143, s[40:43], s80 offen lds\n"
        "s_waitcnt vmcnt(63)\n"
        "v_perm_b32 v96, v65, v64, v139\n"
        "s_waitcnt vmcnt(63)\n"
        "v_perm_b32 v97, v67, v66, v139\n"
        "s_waitcnt vmcnt(63)\n"
        "v_perm_b32 v98, v69, v68, v139\n"
        "s_waitcnt vmcnt(63)\n"
        "v_perm_b32 v99, v71, v70, v139\n"
        "s_waitcnt vmcnt(63)\n"
        "v_perm_b32 v100, v73, v72, v139\n"
        "s_waitcnt vmcnt(63)\n"
        "v_perm_b32 v101, v75, v74, v139\n"
        "s_waitcnt vmcnt(63)\n"
        "v_perm_b32 v102, v77, v76, v139\n"
        "s_waitcnt vmcnt(63)\n"
        "v_perm_b32 v103, v79, v78, v139\n"
        "s_waitcnt vmcnt(63)\n"
        "v_perm_b32 v104, v81, v80, v139\n"
        "s_waitcnt vmcnt(63)\n"
        "v_perm_b32 v105, v83, v82, v139\n"
        "s_waitcnt vmcnt(63)\n"
        "v_perm_b32 v106, v85, v84, v139\n"
        "s_waitcnt vmcnt(63)\n"
        "v_perm_b32 v107, v87, v86, v139\n"
        "s_waitcnt vmcnt(63)\n"
        "v_perm_b32 v108, v89, v88, v139\n"
        "s_waitcnt vmcnt(63)\n"
        "v_perm_b32 v109, v91, v90, v139\n"
        "s_waitcnt vmcnt(63)\n"
        "v_perm_b32 v110, v93, v92, v139\n"
        "s_waitcnt vmcnt(63)\n"
        "v_perm_b32 v111, v95, v94, v139\n"
        "v_add_u32 v127, s84, v131\n"
        "ds_write_b128 v127, v[96:99] offset:0\n"
        "ds_write_b128 v127, v[100:103] offset:1024\n"
        "ds_write_b128 v127, v[104:107] offset:2048\n"
        "ds_write_b128 v127, v[108:111] offset:3072\n"
        "s_waitcnt vmcnt(63)\n"
        "v_perm_b32 v144, v1, v0, s53\n"
        "v_perm_b32 v152, v1, v0, s54\n"
        "s_waitcnt vmcnt(63)\n"
        "v_perm_b32 v145, v3, v2, s53\n"
        "v_perm_b32 v153, v3, v2, s54\n"
        "s_waitcnt vmcnt(63)\n"
        "v_perm_b32 v146, v5, v4, s53\n"
        "v_perm_b32 v154, v5, v4, s54\n"
        "s_waitcnt vmcnt(63)\n"
        "v_perm_b32 v147, v7, v6, s53\n"
        "v_perm_b32 v155, v7, v6, s54\n"
        "s_waitcnt vmcnt(63)\n"
        "v_perm_b32 v148, v9, v8, s53\n"
        "v_perm_b32 v156, v9, v8, s54\n"
        "s_waitcnt vmcnt(63)\n"
        "v_perm_b32 v149, v11, v10, s53\n"
        "v_perm_b32 v157, v11, v10, s54\n"
        "s_waitcnt vmcnt(63)\n"
        "v_perm_b32 v150, v13, v12, s53\n"
        "v_perm_b32 v158, v13, v12, s54\n"
        "s_waitcnt vmcnt(63)\n"
        "v_perm_b32 v151, v15, v14, s53\n"
        "v_perm_b32 v159, v15, v14, s54\n"
        "s_waitcnt vmcnt(63)\n"
        "v_perm_b32 v160, v17, v16, s53\n"
        "v_perm_b32 v168, v17, v16, s54\n"
        "s_waitcnt vmcnt(63)\n"
        "v_perm_b32 v161, v19, v18, s53\n"
        "v_perm_b32 v169, v19, v18, s54\n"
        "s_waitcnt vmcnt(63)\n"
        "v_perm_b32 v162, v21, v20, s53\n"
        "v_perm_b32 v170, v21, v20, s54\n"
        "s_waitcnt vmcnt(63)\n"
        "v_perm_b32 v163, v23, v22, s53\n"
        "v_perm_b32 v171, v23, v22, s54\n"
        "s_waitcnt vmcnt(63)\n"
        "v_perm_b32 v164, v25, v24, s53\n"
        "v_perm_b32 v172, v25, v24, s54\n"
        "s_waitcnt vmcnt(63)\n"
        "v_perm_b32 v165, v27, v26, s53\n"
        "v_perm_b32 v173, v27, v26, s54\n"
        "s_waitcnt vmcnt(63)\n"
        "v_perm_b32 v166, v29, v28, s53\n"
        "v_perm_b32 v174, v29, v28, s54\n"
        "s_waitcnt vmcnt(63)\n"
        "v_perm_b32 v167, v31, v30, s53\n"
        "v_perm_b32 v175, v31, v30, s54\n"
        "s_waitcnt lgkmcnt(0)\n"
        "s_waitcnt vmcnt(63)\n"
        "v_perm_b32 v96, v33, v32, s53\n"
        "v_perm_b32 v104, v33, v32, s54\n"
        "s_waitcnt vmcnt(62)\n"
        "v_perm_b32 v97, v35, v34, s53\n"
        "v_perm_b32 v105, v35, v34, s54\n"
        "s_waitcnt vmcnt(61)\n"
        "v_perm_b32 v98, v37, v36, s53\n"
        "v_perm_b32 v106, v37, v36, s54\n"
        "s_waitcnt vmcnt(60)\n"
        "v_perm_b32 v99, v39, v38, s53\n"
        "v_perm_b32 v107, v39, v38, s54\n"
        "s_waitcnt vmcnt(55)\n"
        "v_perm_b32 v100, v41, v40, s53\n"
        "v_perm_b32 v108, v41, v40, s54\n"
        "s_waitcnt vmcnt(54)\n"
        "v_perm_b32 v101, v43, v42, s53\n"
        "v_perm_b32 v109, v43, v42, s54\n"
        "s_waitcnt vmcnt(53)\n"
        "v_perm_b32 v102, v45, v44, s53\n"
        "v_perm_b32 v110, v45, v44, s54\n"
        "s_waitcnt vmcnt(52)\n"
        "v_perm_b32 v103, v47, v46, s53\n"
        "v_perm_b32 v111, v47, v46, s54\n"
        "s_waitcnt vmcnt(47)\n"
        "v_perm_b32 v112, v49, v48, s53\n"
        "v_perm_b32 v120, v49, v48, s54\n"
        "s_waitcnt vmcnt(46)\n"
        "v_perm_b32 v113, v51, v50, s53\n"
        "v_perm_b32 v121, v51, v50, s54\n"
        "s_waitcnt vmcnt(45)\n"
        "v_perm_b32 v114, v53, v52, s53\n"
        "v_perm_b32 v122, v53, v52, s54\n"
        "s_waitcnt vmcnt(44)\n"
        "v_perm_b32 v115, v55, v54, s53\n"
        "v_perm_b32 v123, v55, v54, s54\n"
        "s_waitcnt vmcnt(39)\n"
        "v_perm_b32 v116, v57, v56, s53\n"
        "v_perm_b32 v124, v57, v56, s54\n"
        "s_waitcnt vmcnt(38)\n"
        "v_perm_b32 v117, v59, v58, s53\n"
        "v_perm_b32 v125, v59, v58, s54\n"
        "s_waitcnt vmcnt(37)\n"
        "v_perm_b32 v118, v61, v60, s53\n"
        "v_perm_b32 v126, v61, v60, s54\n"
        "s_waitcnt vmcnt(36)\n"
        "v_perm_b32 v119, v63, v62, s53\n"
        "v_perm_b32 v127, v63, v62, s54\n"
        "v_add_u32 v132, 0, v136\n"
        "v_add_u32 v133, 0, v137\n"
        "s_waitcnt vmcnt(24)\n"
        "s_barrier\n"
        "ds_read_b64_tr_b16 a[144:145], v132 offset:0\n"
        "ds_read_b64_tr_b16 a[146:147], v132 offset:1024\n"
        "ds_read_b64_tr_b16 a[148:149], v132 offset:8192\n"
        "ds_read_b64_tr_b16 a[150:151], v132 offset:9216\n"
        "ds_read_b64_tr_b16 a[152:153], v133 offset:0\n"
        "ds_read_b64_tr_b16 a[154:155], v133 offset:1024\n"
        "ds_read_b64_tr_b16 a[156:157], v133 offset:8192\n"
        "ds_read_b64_tr_b16 a[158:159], v133 offset:9216\n"
        "ds_read_b64_tr_b16 a[160:161], v132 offset:16384\n"
        "ds_read_b64_tr_b16 a[162:163], v132 offset:17408\n"
        "ds_read_b64_tr_b16 a[164:165], v132 offset:24576\n"
        "ds_read_b64_tr_b16 a[166:167], v132 offset:25600\n"
        "ds_read_b64_tr_b16 a[168:169], v133 offset:16384\n"
        "ds_read_b64_tr_b16 a[170:171], v133 offset:17408\n"
        "ds_read_b64_tr_b16 a[172:173], v133 offset:24576\n"
        "ds_read_b64_tr_b16 a[174:175], v133 offset:25600\n"
        "ds_read_b64_tr_b16 a[176:177], v132 offset:32768\n"
        "ds_read_b64_tr_b16 a[178:179], v132 offset:33792\n"
        "ds_read_b64_tr_b16 a[180:181], v132 offset:40960\n"
        "ds_read_b64_tr_b16 a[182:183], v132 offset:41984\n"
        "ds_read_b64_tr_b16 a[184:185], v133 offset:32768\n"
        "ds_read_b64_tr_b16 a[186:187], v133 offset:33792\n"
        "ds_read_b64_tr_b16 a[188:189], v133 offset:40960\n"
        "ds_read_b64_tr_b16 a[190:191], v133 offset:41984\n"
        "s_waitcnt lgkmcnt(0)\n"
        "s_barrier\n"
        "s_mov_b64 s[40:41], %2\n"
        "v_xor_b32 v142, 0x300000, v140\n"
        "v_xor_b32 v143, 0x300000, v141\n"
        "s_add_u32 m0, s50, 0\n"
        "s_add_u32 s80, s67, 0\n"
        "buffer_load_dwordx4 v142, s[40:43], s80 offen lds\n"
        "s_add_u32 m0, s50, 1024\n"
        "s_add_u32 s80, s67, 32768\n"
        "buffer_load_dwordx4 v142, s[40:43], s80 offen lds\n"
        "s_add_u32 m0, s50, 2048\n"
        "s_add_u32 s80, s67, 65536\n"
        "buffer_load_dwordx4 v143, s[40:43], s80 offen lds\n"
        "s_add_u32 m0, s50, 3072\n"
        "s_add_u32 s80, s67, 98304\n"
        "buffer_load_dwordx4 v143, s[40:43], s80 offen lds\n"
        "v_xor_b32 v142, 0x300000, v140\n"
        "v_xor_b32 v143, 0x300000, v141\n"
        "s_add_u32 m0, s50, 16384\n"
        "s_add_u32 s80, s68, 0\n"
        "buffer_load_dwordx4 v142, s[40:43], s80 offen lds\n"
        "s_add_u32 m0, s50, 17408\n"
        "s_add_u32 s80, s68, 32768\n"
        "buffer_load_dwordx4 v142, s[40:43], s80 offen lds\n"
        "s_add_u32 m0, s50, 18432\n"
        "s_add_u32 s80, s68, 65536\n"
        "buffer_load_dwordx4 v143, s[40:43], s80 offen lds\n"
        "s_add_u32 m0, s50, 19456\n"
        "s_add_u32 s80, s68, 98304\n"
        "buffer_load_dwordx4 v143, s[40:43], s80 offen lds\n"
        "s_add_u32 m0, s50, 32768\n"
        "s_add_u32 s80, s49, 4194304\n"
        "buffer_load_dwordx4 v134, s[40:43], s80 offen lds\n"
        "s_add_u32 m0, s50, 33792\n"
        "s_add_u32 s80, s49, 4227072\n"
        "buffer_load_dwordx4 v134, s[40:43], s80 offen lds\n"
        "s_add_u32 m0, s50, 34816\n"
        "s_add_u32 s80, s49, 4259840\n"
        "buffer_load_dwordx4 v135, s[40:43], s80 offen lds\n"
        "s_add_u32 m0, s50, 35840\n"
        "s_add_u32 s80, s49, 4292608\n"
        "buffer_load_dwordx4 v135, s[40:43], s80 offen lds\n"
        "v_add_u32 v132, 49152, v136\n"
        "v_add_u32 v133, 49152, v137\n"
        "s_waitcnt vmcnt(24)\n"
        "s_barrier\n"
        "ds_read_b64_tr_b16 a[192:193], v132 offset:0\n"
        "ds_read_b64_tr_b16 a[194:195], v132 offset:1024\n"
        "ds_read_b64_tr_b16 a[196:197], v132 offset:8192\n"
        "ds_read_b64_tr_b16 a[198:199], v132 offset:9216\n"
        "ds_read_b64_tr_b16 a[200:201], v133 offset:0\n"
        "ds_read_b64_tr_b16 a[202:203], v133 offset:1024\n"
        "ds_read_b64_tr_b16 a[204:205], v133 offset:8192\n"
        "ds_read_b64_tr_b16 a[206:207], v133 offset:9216\n"
        "ds_read_b64_tr_b16 a[208:209], v132 offset:16384\n"
        "ds_read_b64_tr_b16 a[210:211], v132 offset:17408\n"
        "ds_read_b64_tr_b16 a[212:213], v132 offset:24576\n"
        "ds_read_b64_tr_b16 a[214:215], v132 offset:25600\n"
        "ds_read_b64_tr_b16 a[216:217], v133 offset:16384\n"
        "ds_read_b64_tr_b16 a[218:219], v133 offset:17408\n"
        "ds_read_b64_tr_b16 a[220:221], v133 offset:24576\n"
        "ds_read_b64_tr_b16 a[222:223], v133 offset:25600\n"
        "ds_read_b64_tr_b16 a[224:225], v132 offset:32768\n"
        "ds_read_b64_tr_b16 a[226:227], v132 offset:33792\n"
        "ds_read_b64_tr_b16 a[228:229], v132 offset:40960\n"
        "ds_read_b64_tr_b16 a[230:231], v132 offset:41984\n"
        "ds_read_b64_tr_b16 a[232:233], v133 offset:32768\n"
        "ds_read_b64_tr_b16 a[234:235], v133 offset:33792\n"
        "ds_read_b64_tr_b16 a[236:237], v133 offset:40960\n"
        "ds_read_b64_tr_b16 a[238:239], v133 offset:41984\n"
        "s_waitcnt lgkmcnt(0)\n"
        "s_barrier\n"
        "s_mov_b64 s[40:41], %1\n"
        "v_xor_b32 v142, 0x300000, v140\n"
        "v_xor_b32 v143, 0x300000, v141\n"
        "s_add_u32 m0, s50, 49152\n"
        "s_add_u32 s80, s67, 0\n"
        "buffer_load_dwordx4 v142, s[40:43], s80 offen lds\n"
        "s_add_u32 m0, s50, 50176\n"
        "s_add_u32 s80, s67, 32768\n"
        "buffer_load_dwordx4 v142, s[40:43], s80 offen lds\n"
        "s_add_u32 m0, s50, 51200\n"
        "s_add_u32 s80, s67, 65536\n"
        "buffer_load_dwordx4 v143, s[40:43], s80 offen lds\n"
        "s_add_u32 m0, s50, 52224\n"
        "s_add_u32 s80, s67, 98304\n"
        "buffer_load_dwordx4 v143, s[40:43], s80 offen lds\n"
        "v_xor_b32 v142, 0x300000, v140\n"
        "v_xor_b32 v143, 0x300000, v141\n"
        "s_add_u32 m0, s50, 65536\n"
        "s_add_u32 s80, s68, 0\n"
        "buffer_load_dwordx4 v142, s[40:43], s80 offen lds\n"
        "s_add_u32 m0, s50, 66560\n"
        "s_add_u32 s80, s68, 32768\n"
        "buffer_load_dwordx4 v142, s[40:43], s80 offen lds\n"
        "s_add_u32 m0, s50, 67584\n"
        "s_add_u32 s80, s68, 65536\n"
        "buffer_load_dwordx4 v143, s[40:43], s80 offen lds\n"
        "s_add_u32 m0, s50, 68608\n"
        "s_add_u32 s80, s68, 98304\n"
        "buffer_load_dwordx4 v143, s[40:43], s80 offen lds\n"
        "s_add_u32 m0, s50, 81920\n"
        "s_add_u32 s80, s49, 4194304\n"
        "buffer_load_dwordx4 v134, s[40:43], s80 offen lds\n"
        "s_add_u32 m0, s50, 82944\n"
        "s_add_u32 s80, s49, 4227072\n"
        "buffer_load_dwordx4 v134, s[40:43], s80 offen lds\n"
        "s_add_u32 m0, s50, 83968\n"
        "s_add_u32 s80, s49, 4259840\n"
        "buffer_load_dwordx4 v135, s[40:43], s80 offen lds\n"
        "s_add_u32 m0, s50, 84992\n"
        "s_add_u32 s80, s49, 4292608\n"
        "buffer_load_dwordx4 v135, s[40:43], s80 offen lds\n"
        "v_add_u32 v132, 98304, v136\n"
        "v_add_u32 v133, 98304, v137\n"
        "s_waitcnt vmcnt(24)\n"
        "s_barrier\n"
        "ds_read_b64_tr_b16 a[240:241], v132 offset:0\n"
        "ds_read_b64_tr_b16 a[242:243], v132 offset:1024\n"
        "ds_read_b64_tr_b16 a[244:245], v132 offset:8192\n"
        "ds_read_b64_tr_b16 a[246:247], v132 offset:9216\n"
        "ds_read_b64_tr_b16 a[248:249], v133 offset:0\n"
        "ds_read_b64_tr_b16 a[250:251], v133 offset:1024\n"
        "ds_read_b64_tr_b16 a[252:253], v133 offset:8192\n"
        "ds_read_b64_tr_b16 a[254:255], v133 offset:9216\n"
        "ds_read_b64_tr_b16 v[176:177], v132 offset:16384\n"
        "ds_read_b64_tr_b16 v[178:179], v132 offset:17408\n"
        "ds_read_b64_tr_b16 v[180:181], v132 offset:24576\n"
        "ds_read_b64_tr_b16 v[182:183], v132 offset:25600\n"
        "ds_read_b64_tr_b16 v[184:185], v133 offset:16384\n"
        "ds_read_b64_tr_b16 v[186:187], v133 offset:17408\n"
        "ds_read_b64_tr_b16 v[188:189], v133 offset:24576\n"
        "ds_read_b64_tr_b16 v[190:191], v133 offset:25600\n"
        "ds_read_b64_tr_b16 v[192:193], v132 offset:32768\n"
        "ds_read_b64_tr_b16 v[194:195], v132 offset:33792\n"
        "ds_read_b64_tr_b16 v[196:197], v132 offset:40960\n"
        "ds_read_b64_tr_b16 v[198:199], v132 offset:41984\n"
        "ds_read_b64_tr_b16 v[200:201], v133 offset:32768\n"
        "ds_read_b64_tr_b16 v[202:203], v133 offset:33792\n"
        "ds_read_b64_tr_b16 v[204:205], v133 offset:40960\n"
        "ds_read_b64_tr_b16 v[206:207], v133 offset:41984\n"
        "s_waitcnt lgkmcnt(0)\n"
        "s_barrier\n"
        "s_mov_b64 s[40:41], %2\n"
        "v_xor_b32 v142, 0x100000, v140\n"
        "v_xor_b32 v143, 0x100000, v141\n"
        "s_add_u32 m0, s50, 98304\n"
        "s_add_u32 s80, s67, 0\n"
        "buffer_load_dwordx4 v142, s[40:43], s80 offen lds\n"
        "s_add_u32 m0, s50, 99328\n"
        "s_add_u32 s80, s67, 32768\n"
        "buffer_load_dwordx4 v142, s[40:43], s80 offen lds\n"
        "s_add_u32 m0, s50, 100352\n"
        "s_add_u32 s80, s67, 65536\n"
        "buffer_load_dwordx4 v143, s[40:43], s80 offen lds\n"
        "s_add_u32 m0, s50, 101376\n"
        "s_add_u32 s80, s67, 98304\n"
        "buffer_load_dwordx4 v143, s[40:43], s80 offen lds\n"
        "v_xor_b32 v142, 0x100000, v140\n"
        "v_xor_b32 v143, 0x100000, v141\n"
        "s_add_u32 m0, s50, 114688\n"
        "s_add_u32 s80, s68, 0\n"
        "buffer_load_dwordx4 v142, s[40:43], s80 offen lds\n"
        "s_add_u32 m0, s50, 115712\n"
        "s_add_u32 s80, s68, 32768\n"
        "buffer_load_dwordx4 v142, s[40:43], s80 offen lds\n"
        "s_add_u32 m0, s50, 116736\n"
        "s_add_u32 s80, s68, 65536\n"
        "buffer_load_dwordx4 v143, s[40:43], s80 offen lds\n"
        "s_add_u32 m0, s50, 117760\n"
        "s_add_u32 s80, s68, 98304\n"
        "buffer_load_dwordx4 v143, s[40:43], s80 offen lds\n"
        "v_xor_b32 v142, 0x200000, v140\n"
        "v_xor_b32 v143, 0x200000, v141\n"
        "s_add_u32 m0, s50, 131072\n"
        "s_add_u32 s80, s67, 0\n"
        "buffer_load_dwordx4 v142, s[40:43], s80 offen lds\n"
        "s_add_u32 m0, s50, 132096\n"
        "s_add_u32 s80, s67, 32768\n"
        "buffer_load_dwordx4 v142, s[40:43], s80 offen lds\n"
        "s_add_u32 m0, s50, 133120\n"
        "s_add_u32 s80, s67, 65536\n"
        "buffer_load_dwordx4 v143, s[40:43], s80 offen lds\n"
        "s_add_u32 m0, s50, 134144\n"
        "s_add_u32 s80, s67, 98304\n"
        "buffer_load_dwordx4 v143, s[40:43], s80 offen lds\n"
        "v_add_u32 v132, 0, v136\n"
        "v_add_u32 v133, 0, v137\n"
        "s_waitcnt vmcnt(24)\n"
        "s_barrier\n"
        "ds_read_b64_tr_b16 v[208:209], v132 offset:0\n"
        "ds_read_b64_tr_b16 v[210:211], v132 offset:1024\n"
        "ds_read_b64_tr_b16 v[212:213], v132 offset:8192\n"
        "ds_read_b64_tr_b16 v[214:215], v132 offset:9216\n"
        "ds_read_b64_tr_b16 v[216:217], v133 offset:0\n"
        "ds_read_b64_tr_b16 v[218:219], v133 offset:1024\n"
        "ds_read_b64_tr_b16 v[220:221], v133 offset:8192\n"
        "ds_read_b64_tr_b16 v[222:223], v133 offset:9216\n"
        "ds_read_b64_tr_b16 v[224:225], v132 offset:16384\n"
        "ds_read_b64_tr_b16 v[226:227], v132 offset:17408\n"
        "ds_read_b64_tr_b16 v[228:229], v132 offset:24576\n"
        "ds_read_b64_tr_b16 v[230:231], v132 offset:25600\n"
        "ds_read_b64_tr_b16 v[232:233], v133 offset:16384\n"
        "ds_read_b64_tr_b16 v[234:235], v133 offset:17408\n"
        "ds_read_b64_tr_b16 v[236:237], v133 offset:24576\n"
        "ds_read_b64_tr_b16 v[238:239], v133 offset:25600\n"
        "ds_read_b64_tr_b16 v[240:241], v132 offset:32768\n"
        "ds_read_b64_tr_b16 v[242:243], v132 offset:33792\n"
        "ds_read_b64_tr_b16 v[244:245], v132 offset:40960\n"
        "ds_read_b64_tr_b16 v[246:247], v132 offset:41984\n"
        "ds_read_b64_tr_b16 v[248:249], v133 offset:32768\n"
        "ds_read_b64_tr_b16 v[250:251], v133 offset:33792\n"
        "ds_read_b64_tr_b16 v[252:253], v133 offset:40960\n"
        "ds_read_b64_tr_b16 v[254:255], v133 offset:41984\n"
        "v_add_u32 v132, 49152, v136\n"
        "v_add_u32 v133, 49152, v137\n"
        "s_waitcnt vmcnt(12)\n"
        "s_barrier\n"
        "ds_read_b64_tr_b16 v[0:1], v132 offset:0\n"
        "ds_read_b64_tr_b16 v[2:3], v132 offset:1024\n"
        "ds_read_b64_tr_b16 v[4:5], v132 offset:8192\n"
        "ds_read_b64_tr_b16 v[6:7], v132 offset:9216\n"
        "ds_read_b64_tr_b16 v[8:9], v133 offset:0\n"
        "ds_read_b64_tr_b16 v[10:11], v133 offset:1024\n"
        "ds_read_b64_tr_b16 v[12:13], v133 offset:8192\n"
        "ds_read_b64_tr_b16 v[14:15], v133 offset:9216\n"
        "ds_read_b64_tr_b16 v[16:17], v132 offset:16384\n"
        "ds_read_b64_tr_b16 v[18:19], v132 offset:17408\n"
        "ds_read_b64_tr_b16 v[20:21], v132 offset:24576\n"
        "ds_read_b64_tr_b16 v[22:23], v132 offset:25600\n"
        "ds_read_b64_tr_b16 v[24:25], v133 offset:16384\n"
        "ds_read_b64_tr_b16 v[26:27], v133 offset:17408\n"
        "ds_read_b64_tr_b16 v[28:29], v133 offset:24576\n"
        "ds_read_b64_tr_b16 v[30:31], v133 offset:25600\n"
        "ds_read_b64_tr_b16 v[32:33], v132 offset:32768\n"
        "ds_read_b64_tr_b16 v[34:35], v132 offset:33792\n"
        "ds_read_b64_tr_b16 v[36:37], v132 offset:40960\n"
        "ds_read_b64_tr_b16 v[38:39], v132 offset:41984\n"
        "ds_read_b64_tr_b16 v[40:41], v133 offset:32768\n"
        "ds_read_b64_tr_b16 v[42:43], v133 offset:33792\n"
        "ds_read_b64_tr_b16 v[44:45], v133 offset:40960\n"
        "ds_read_b64_tr_b16 v[46:47], v133 offset:41984\n"
        "v_add_u32 v132, 98304, v136\n"
        "v_add_u32 v133, 98304, v137\n"
        "s_waitcnt vmcnt(0)\n"
        "s_barrier\n"
        "ds_read_b64_tr_b16 v[48:49], v132 offset:0\n"
        "ds_read_b64_tr_b16 v[50:51], v132 offset:1024\n"
        "ds_read_b64_tr_b16 v[52:53], v132 offset:8192\n"
        "ds_read_b64_tr_b16 v[54:55], v132 offset:9216\n"
        "ds_read_b64_tr_b16 v[56:57], v133 offset:0\n"
        "ds_read_b64_tr_b16 v[58:59], v133 offset:1024\n"
        "ds_read_b64_tr_b16 v[60:61], v133 offset:8192\n"
        "ds_read_b64_tr_b16 v[62:63], v133 offset:9216\n"
        "ds_read_b64_tr_b16 v[64:65], v132 offset:16384\n"
        "ds_read_b64_tr_b16 v[66:67], v132 offset:17408\n"
        "ds_read_b64_tr_b16 v[68:69], v132 offset:24576\n"
        "ds_read_b64_tr_b16 v[70:71], v132 offset:25600\n"
        "ds_read_b64_tr_b16 v[72:73], v133 offset:16384\n"
        "ds_read_b64_tr_b16 v[74:75], v133 offset:17408\n"
        "ds_read_b64_tr_b16 v[76:77], v133 offset:24576\n"
        "ds_read_b64_tr_b16 v[78:79], v133 offset:25600\n"
        "ds_read_b64_tr_b16 v[80:81], v132 offset:32768\n"
        "ds_read_b64_tr_b16 v[82:83], v132 offset:33792\n"
        "ds_read_b64_tr_b16 v[84:85], v132 offset:40960\n"
        "ds_read_b64_tr_b16 v[86:87], v132 offset:41984\n"
        "ds_read_b64_tr_b16 v[88:89], v133 offset:32768\n"
        "ds_read_b64_tr_b16 v[90:91], v133 offset:33792\n"
        "ds_read_b64_tr_b16 v[92:93], v133 offset:40960\n"
        "ds_read_b64_tr_b16 v[94:95], v133 offset:41984\n"
        "s_waitcnt lgkmcnt(0)\n"
        "s_barrier\n"
        "s_lshl_b32 s80, s45, 15\n"
        "s_add_u32 s80, s80, 21504\n"
        "v_add_u32 v132, s80, v131\n"
        "v_mov_b32 v134, 0\n"
        "s_mov_b64 exec, 1\n"
        "buffer_atomic_add v134, off, s[36:39], %6 sc0\n"
        "s_mov_b64 exec, -1\n"
        "ds_write_b128 v132, v[0:3] offset:0\n"
        "ds_write_b128 v132, v[4:7] offset:1024\n"
        "ds_write_b128 v132, v[8:11] offset:2048\n"
        "ds_write_b128 v132, v[12:15] offset:3072\n"
        "ds_write_b128 v132, v[16:19] offset:4096\n"
        "ds_write_b128 v132, v[20:23] offset:5120\n"
        "ds_write_b128 v132, v[24:27] offset:6144\n"
        "ds_write_b128 v132, v[28:31] offset:7168\n"
        "ds_write_b128 v132, v[32:35] offset:8192\n"
        "ds_write_b128 v132, v[36:39] offset:9216\n"
        "ds_write_b128 v132, v[40:43] offset:10240\n"
        "ds_write_b128 v132, v[44:47] offset:11264\n"
        "ds_write_b128 v132, v[48:51] offset:12288\n"
        "ds_write_b128 v132, v[52:55] offset:13312\n"
        "ds_write_b128 v132, v[56:59] offset:14336\n"
        "ds_write_b128 v132, v[60:63] offset:15360\n"
        "ds_write_b128 v132, v[64:67] offset:16384\n"
        "ds_write_b128 v132, v[68:71] offset:17408\n"
        "ds_write_b128 v132, v[72:75] offset:18432\n"
        "ds_write_b128 v132, v[76:79] offset:19456\n"
        "ds_write_b128 v132, v[80:83] offset:20480\n"
        "ds_write_b128 v132, v[84:87] offset:21504\n"
        "ds_write_b128 v132, v[88:91] offset:22528\n"
        "ds_write_b128 v132, v[92:95] offset:23552\n"
        "ds_write_b128 v132, v[96:99] offset:24576\n"
        "ds_write_b128 v132, v[100:103] offset:25600\n"
        "ds_write_b128 v132, v[104:107] offset:26624\n"
        "ds_write_b128 v132, v[108:111] offset:27648\n"
        "ds_write_b128 v132, v[112:115] offset:28672\n"
        "ds_write_b128 v132, v[116:119] offset:29696\n"
        "ds_write_b128 v132, v[120:123] offset:30720\n"
        "ds_write_b128 v132, v[124:127] offset:31744\n"
        "s_waitcnt lgkmcnt(0)\n"
        "s_waitcnt lgkmcnt(0)\n"
        "L_hs%=:\n"
        "s_waitcnt vmcnt(0)\n"
        "v_readfirstlane_b32 s81, v134\n"
        "s_nop 0\n"
        "s_cmp_lg_u32 s81, 0\n"
        "s_cbranch_scc1 L_hs2%=\n"
        "s_add_u32 s63, s63, 1\n"
        "s_cmp_gt_u32 s63, 0x40000\n"
        "s_cbranch_scc1 L_abort%=\n"
        "s_sleep 4\n"
        "v_mov_b32 v134, 0\n"
        "s_mov_b64 exec, 1\n"
        "buffer_atomic_add v134, off, s[36:39], %6 sc0\n"
        "s_mov_b64 exec, -1\n"
        "s_branch L_hs%=\n"
        "L_hs2%=:\n"
        "v_mov_b32 v134, 2\n"
        "s_mov_b64 exec, 1\n"
        "buffer_atomic_swap v134, off, s[36:39], %6\n"
        "s_mov_b64 exec, -1\n"
        "v_mbcnt_lo_u32_b32 v0, -1, 0\n"
        "v_mbcnt_hi_u32_b32 v0, -1, v0\n"
        "v_and_b32 v1, 15, v0\n"
        "v_lshrrev_b32 v2, 4, v0\n"
        "v_lshlrev_b32 v124, 4, v0\n"
        "v_add_u32 v125, s80, v124\n"
        "s_mul_i32 s80, s45, 0\n"
        "s_add_u32 s80, s80, 0\n"
        "v_mov_b32 v138, s80\n"
        "v_bfe_u32 v3, v0, 0, 2\n"
        "s_mov_b32 s80, 16\n"
        "v_mad_u32_u24 v138, v3, s80, v138\n"
        "v_bfe_u32 v3, v0, 2, 3\n"
        "s_mov_b32 s80, 1024\n"
        "v_mad_u32_u24 v138, v3, s80, v138\n"
        "v_bfe_u32 v3, v0, 5, 1\n"
        "s_mov_b32 s80, 512\n"
        "v_mad_u32_u24 v138, v3, s80, v138\n"
        "s_mul_i32 s80, s45, 64\n"
        "s_add_u32 s80, s80, 9216\n"
        "v_mov_b32 v127, s80\n"
        "v_bfe_u32 v3, v0, 0, 2\n"
        "s_mov_b32 s80, 8\n"
        "v_mad_u32_u24 v127, v3, s80, v127\n"
        "v_bfe_u32 v3, v0, 2, 1\n"
        "s_mov_b32 s80, 4\n"
        "v_mad_u32_u24 v127, v3, s80, v127\n"
        "v_bfe_u32 v3, v0, 3, 1\n"
        "s_mov_b32 s80, 32\n"
        "v_mad_u32_u24 v127, v3, s80, v127\n"
        "v_bfe_u32 v3, v0, 4, 2\n"
        "s_mov_b32 s80, 576\n"
        "v_mad_u32_u24 v127, v3, s80, v127\n"
        "s_mul_i32 s80, s45, 2048\n"
        "s_add_u32 s80, s80, 0\n"
        "v_mov_b32 v126, s80\n"
        "v_bfe_u32 v3, v0, 0, 1\n"
        "s_mov_b32 s80, 8\n"
        "v_mad_u32_u24 v126, v3, s80, v126\n"
        "v_bfe_u32 v3, v0, 1, 1\n"
        "s_mov_b32 s80, 32\n"
        "v_mad_u32_u24 v126, v3, s80, v126\n"
        "v_bfe_u32 v3, v0, 2, 1\n"
        "s_mov_b32 s80, 16\n"
        "v_mad_u32_u24 v126, v3, s80, v126\n"
        "v_bfe_u32 v3, v0, 3, 3\n"
        "s_mov_b32 s80, 64\n"
        "v_mad_u32_u24 v126, v3, s80, v126\n"
        "s_mul_i32 s80, s45, 0\n"
        "s_add_u32 s80, s80, 9216\n"
        "v_mov_b32 v128, s80\n"
        "v_bfe_u32 v3, v0, 0, 1\n"
        "s_mov_b32 s80, 288\n"
        "v_mad_u32_u24 v128, v3, s80, v128\n"
        "v_bfe_u32 v3, v0, 2, 2\n"
        "s_mov_b32 s80, 576\n"
        "v_mad_u32_u24 v128, v3, s80, v128\n"
        "v_bfe_u32 v3, v0, 4, 2\n"
        "s_mov_b32 s80, 16\n"
        "v_mad_u32_u24 v128, v3, s80, v128\n"
        "s_mul_i32 s80, s45, 512\n"
        "s_add_u32 s80, s80, 11520\n"
        "v_mov_b32 v129, s80\n"
        "v_bfe_u32 v3, v0, 0, 2\n"
        "s_mov_b32 s80, 4\n"
        "v_mad_u32_u24 v129, v3, s80, v129\n"
        "v_bfe_u32 v3, v0, 2, 1\n"
        "s_mov_b32 s80, 16\n"
        "v_mad_u32_u24 v129, v3, s80, v129\n"
        "v_bfe_u32 v3, v0, 3, 1\n"
        "s_mov_b32 s80, 256\n"
        "v_mad_u32_u24 v129, v3, s80, v129\n"
        "v_bfe_u32 v3, v0, 4, 2\n"
        "s_mov_b32 s80, 64\n"
        "v_mad_u32_u24 v129, v3, s80, v129\n"
        "s_mul_i32 s80, s45, 512\n"
        "s_add_u32 s80, s80, 18432\n"
        "v_mov_b32 v139, s80\n"
        "v_bfe_u32 v3, v0, 0, 1\n"
        "s_mov_b32 s80, 32\n"
        "v_mad_u32_u24 v139, v3, s80, v139\n"
        "v_bfe_u32 v3, v0, 1, 2\n"
        "s_mov_b32 s80, 4\n"
        "v_mad_u32_u24 v139, v3, s80, v139\n"
        "v_bfe_u32 v3, v0, 3, 1\n"
        "s_mov_b32 s80, 256\n"
        "v_mad_u32_u24 v139, v3, s80, v139\n"
        "v_bfe_u32 v3, v0, 4, 2\n"
        "s_mov_b32 s80, 64\n"
        "v_mad_u32_u24 v139, v3, s80, v139\n"
        "s_mul_i32 s80, s45, 0\n"
        "s_add_u32 s80, s80, 11520\n"
        "v_mov_b32 v130, s80\n"
        "v_bfe_u32 v3, v0, 0, 1\n"
        "s_mov_b32 s80, 32\n"
        "v_mad_u32_u24 v130, v3, s80, v130\n"
        "v_bfe_u32 v3, v0, 1, 1\n"
        "s_mov_b32 s80, 16\n"
        "v_mad_u32_u24 v130, v3, s80, v130\n"
        "v_bfe_u32 v3, v0, 2, 1\n"
        "s_mov_b32 s80, 128\n"
        "v_mad_u32_u24 v130, v3, s80, v130\n"
        "v_bfe_u32 v3, v0, 3, 1\n"
        "s_mov_b32 s80, 64\n"
        "v_mad_u32_u24 v130, v3, s80, v130\n"
        "v_bfe_u32 v3, v0, 4, 2\n"
        "s_mov_b32 s80, 256\n"
        "v_mad_u32_u24 v130, v3, s80, v130\n"
        "s_mul_i32 s80, s45, 256\n"
        "s_add_u32 s80, s80, 13568\n"
        "v_mov_b32 v131, s80\n"
        "v_bfe_u32 v3, v0, 0, 1\n"
        "s_mov_b32 s80, 16\n"
        "v_mad_u32_u24 v131, v3, s80, v131\n"
        "v_bfe_u32 v3, v0, 2, 2\n"
        "s_mov_b32 s80, 64\n"
        "v_mad_u32_u24 v131, v3, s80, v131\n"
        "v_bfe_u32 v3, v0, 4, 1\n"
        "s_mov_b32 s80, 32\n"
        "v_mad_u32_u24 v131, v3, s80, v131\n"
        "v_bfe_u32 v3, v0, 5, 1\n"
        "s_mov_b32 s80, 8\n"
        "v_mad_u32_u24 v131, v3, s80, v131\n"
        "s_mul_i32 s80, s45, 0\n"
        "s_add_u32 s80, s80, 0\n"
        "v_mov_b32 v132, s80\n"
        "v_bfe_u32 v3, v0, 0, 1\n"
        "s_mov_b32 s80, 16\n"
        "v_mad_u32_u24 v132, v3, s80, v132\n"
        "v_bfe_u32 v3, v0, 1, 1\n"
        "s_mov_b32 s80, 256\n"
        "v_mad_u32_u24 v132, v3, s80, v132\n"
        "v_bfe_u32 v3, v0, 2, 2\n"
        "s_mov_b32 s80, 32\n"
        "v_mad_u32_u24 v132, v3, s80, v132\n"
        "v_bfe_u32 v3, v0, 4, 1\n"
        "s_mov_b32 s80, 512\n"
        "v_mad_u32_u24 v132, v3, s80, v132\n"
        "v_bfe_u32 v3, v0, 5, 1\n"
        "s_mov_b32 s80, 6144\n"
        "v_mad_u32_u24 v132, v3, s80, v132\n"
        "s_mul_i32 s80, s45, 0\n"
        "s_add_u32 s80, s80, 0\n"
        "v_mov_b32 v133, s80\n"
        "v_bfe_u32 v3, v0, 0, 3\n"
        "s_mov_b32 s80, 8\n"
        "v_mad_u32_u24 v133, v3, s80, v133\n"
        "v_bfe_u32 v3, v0, 3, 1\n"
        "s_mov_b32 s80, 512\n"
        "v_mad_u32_u24 v133, v3, s80, v133\n"
        "v_bfe_u32 v3, v0, 4, 2\n"
        "s_mov_b32 s80, 2048\n"
        "v_mad_u32_u24 v133, v3, s80, v133\n"
        "s_mul_i32 s80, s45, 256\n"
        "s_add_u32 s80, s80, 8192\n"
        "v_mov_b32 v137, s80\n"
        "v_bfe_u32 v3, v0, 0, 1\n"
        "s_mov_b32 s80, 32\n"
        "v_mad_u32_u24 v137, v3, s80, v137\n"
        "v_bfe_u32 v3, v0, 1, 1\n"
        "s_mov_b32 s80, 4\n"
        "v_mad_u32_u24 v137, v3, s80, v137\n"
        "v_bfe_u32 v3, v0, 2, 1\n"
        "s_mov_b32 s80, 16\n"
        "v_mad_u32_u24 v137, v3, s80, v137\n"
        "v_bfe_u32 v3, v0, 3, 2\n"
        "s_mov_b32 s80, 64\n"
        "v_mad_u32_u24 v137, v3, s80, v137\n"
        "v_bfe_u32 v3, v0, 5, 1\n"
        "s_mov_b32 s80, 8\n"
        "v_mad_u32_u24 v137, v3, s80, v137\n"
        "s_and_b32 s80, s47, 1\n"
        "v_bfe_u32 v3, v0, 2, 1\n"
        "v_xor_b32 v3, s80, v3\n"
        "v_mul_u32_u24 v3, 36, v3\n"
        "v_xor_b32 v137, v3, v137\n"
        "s_lshl_b32 s66, s80, 10\n"
        "v_xor_b32 v138, s66, v138\n"
        "s_mul_i32 s80, s45, 4\n"
        "s_add_u32 s80, s80, 20480\n"
        "v_mov_b32 v142, s80\n"
        "s_mul_i32 s80, s45, 0\n"
        "s_add_u32 s80, s80, 0\n"
        "v_mov_b32 v143, s80\n"
        "v_bfe_u32 v3, v0, 0, 6\n"
        "s_mov_b32 s80, 4\n"
        "v_mad_u32_u24 v143, v3, s80, v143\n"
        "v_cmp_gt_u32 vcc, 36, v143\n"
        "s_nop 1\n"
        "v_cndmask_b32 v143, 2.0, v143, vcc\n"
        "v_add_u32 v141, s84, v124\n"
        "s_mov_b32 s92, 0xcccccccc\n"
        "s_mov_b32 s93, 0xcccccccc\n"
        "s_lshl_b32 s82, s45, 12\n"
        "s_and_b32 s80, s45, 1\n"
        "s_mul_i32 s80, s80, -1408\n"
        "s_mul_i32 s100, s45, -4608\n"
        "s_add_u32 s100, s100, s80\n"
        "s_add_u32 s100, s100, 1024\n"
        "v_mov_b32 v3, 0x4444\n"
        "v_mov_b32 v4, 0xeeee\n"
        "v_cndmask_b32 v134, v3, v4, s[92:93]\n"
        "v_mov_b32 v3, 0xffff\n"
        "v_mov_b32 v4, 0xffff0000\n"
        "v_cndmask_b32 v3, v3, v4, s[92:93]\n"
        "v_cmp_gt_u32 vcc, 32, v0\n"
        "s_nop 1\n"
        "v_cndmask_b32 v135, 0, v3, vcc\n"
        "v_xor_b32 v136, v135, v3\n"
        "s_mul_i32 s80, s48, 81920\n"
        "s_add_u32 s80, s80, 884736\n"
        "s_lshl_b32 s81, s45, 14\n"
        "s_add_u32 s49, s80, s81\n"
        "s_lshl_b32 s81, s83, 13\n"
        "s_add_u32 s49, s49, s81\n"
        "s_and_b32 s81, s45, 1\n"
        "s_lshl_b32 s81, s81, 8\n"
        "s_add_u32 s50, s80, s81\n"
        "s_lshr_b32 s81, s45, 1\n"
        "s_lshl_b32 s81, s81, 13\n"
        "s_add_u32 s50, s50, s81\n"
        "s_add_u32 s50, s50, 65536\n"
        "s_add_u32 s80, s80, 163840\n"
        "s_lshl_b32 s69, s83, 11\n"
        "s_add_u32 s83, s80, s36\n"
        "s_mov_b32 s86, 0\n"
        "s_mov_b32 s34, 0\n"
        "s_mov_b32 s53, 0\n"
        "s_add_u32 s80, s80, s66\n"
        "s_lshr_b32 s81, s45, 1\n"
        "s_lshl1_add_u32 s81, s46, s81\n"
        "s_lshl_b32 s81, s81, 14\n"
        "s_add_u32 s51, s80, s81\n"
        "s_and_b32 s81, s45, 1\n"
        "s_lshl_b32 s81, s81, 8\n"
        "s_add_u32 s51, s51, s81\n"
        "s_and_b32 s81, s47, 2\n"
        "s_lshl_b32 s81, s81, 12\n"
        "s_add_u32 s51, s51, s81\n"
        "s_lshl_b32 s81, s46, 5\n"
        "s_add_u32 s81, s81, s45\n"
        "s_lshl_b32 s81, s81, 8\n"
        "s_add_u32 s81, s81, s80\n"
        "s_add_u32 s81, s81, 65536\n"
        "s_cmp_lt_u32 s45, 2\n"
        "s_cselect_b32 s52, s81, 0x40000000\n"
        "s_lshl1_add_u32 s81, s47, s46\n"
        "s_mov_b32 s99, s81\n"
        "s_mov_b32 s28, 0\n"
        "s_lshl_b32 s30, s81, 4\n"
        "s_mul_i32 s29, s48, 69632\n"
        "s_add_u32 s29, s29, 150528\n"
        "s_mul_i32 s80, s48, 2112\n"
        "s_add_u32 s80, s80, s47\n"
        "s_mul_i32 s81, s80, 24576\n"
        "s_mul_i32 s85, s45, 6144\n"
        "s_add_u32 s81, s81, s85\n"
        "s_add_u32 s81, s81, 103153664\n"
        "s_add_u32 s55, s81, 2048\n"
        "s_add_u32 s56, s81, s100\n"
        "s_add_u32 s64, s56, 12288\n"
        "s_mul_i32 s85, s46, 3072\n"
        "s_add_u32 s81, s81, s85\n"
        "s_add_u32 s54, s81, 786432\n"
        "s_lshl_b32 s80, s48, 11\n"
        "s_add_u32 s80, s80, s47\n"
        "s_lshl_b32 s80, s80, 12\n"
        "s_add_u32 s80, s80, 208273408\n"
        "s_bfe_u32 s81, %5, 0x20001\n"
        "s_cmp_eq_u32 s81, 1\n"
        "s_cselect_b32 s59, s80, 0x40000000\n"
        "s_add_u32 s80, s80, 2048\n"
        "s_mul_i32 s85, s45, 1536\n"
        "s_sub_u32 s80, s80, s85\n"
        "s_cmp_eq_u32 s46, 1\n"
        "s_cselect_b32 s60, s80, 0x40000000\n"
        "s_lshl_b32 s80, s48, 5\n"
        "s_add_u32 s80, s80, s47\n"
        "s_mul_i32 s80, s80, 544\n"
        "s_xor_b32 s81, s46, 1\n"
        "s_lshl1_add_u32 s80, s81, s80\n"
        "s_add_u32 s80, s80, s45\n"
        "s_lshl_b32 s80, s80, 2\n"
        "s_add_u32 s80, s80, 12288\n"
        "s_and_b32 s81, s59, s60\n"
        "s_bitcmp1_b32 s81, 30\n"
        "s_cselect_b32 s61, 0x40000000, s80\n"
        "s_lshl1_add_u32 s80, s48, s46\n"
        "s_lshl_b32 s57, s80, 13\n"
        "s_and_b32 s80, s47, 3\n"
        "s_lshl_b32 s80, s80, 8\n"
        "s_add_u32 s57, s57, s80\n"
        "s_add_u32 s57, s57, 884736\n"
        "s_mov_b32 s31, 0\n"
        "s_mul_i32 s80, s48, 2359296\n"
        "s_mul_i32 s81, s46, 18432\n"
        "s_add_u32 s81, s81, s82\n"
        "s_add_u32 s80, s80, s81\n"
        "s_add_u32 s58, s80, 16908288\n"
        "s_mov_b32 s82, s69\n"
        "s_mov_b32 s70, 0x41f40000\n"
        "s_mov_b32 s71, 0x41f40000\n"
        "s_mov_b32 s78, 0x41f40000\n"
        "s_mov_b32 s79, 0\n"
        "s_mul_i32 s80, s48, 81920\n"
        "s_mul_i32 s81, s99, 1280\n"
        "s_add_u32 s80, s80, s81\n"
        "s_add_u32 s47, s80, 1376256\n"
        "s_mov_b32 s44, -2\n"
        "s_waitcnt lgkmcnt(0)\n"
        "s_cmp_lg_u32 s45, 0\n"
        "s_cbranch_scc1 L_x1%=\n"
        "s_branch L_xf2%=\n"
        "L_xf1%=:\n"
        "s_add_u32 s63, s63, 1\n"
        "s_cmp_gt_u32 s63, 0x40000\n"
        "s_cbranch_scc1 L_abort%=\n"
        "s_sleep 2\n"
        "L_xf2%=:\n"
        "v_mov_b32 v140, 0\n"
        "buffer_atomic_add v140, v143, s[36:39], s57 offen sc0\n"
        "s_waitcnt vmcnt(0)\n"
        "v_cmp_ge_u32 vcc, s31, v140\n"
        "s_and_b32 vcc_lo, vcc_lo, 0x1ff\n"
        "s_cbranch_scc1 L_xf1%=\n"
        "L_x1%=:\n"
        "s_bitset1_b32 s49, 30\n"
        "s_bitset1_b32 s50, 30\n"
        "s_bitset1_b32 s56, 30\n"
        "s_bitset1_b32 s64, 30\n"
        "s_add_u32 s29, s29, 1024\n"
        "s_add_u32 s80, s29, s30\n"
        "v_mov_b32 v140, 1\n"
        "s_barrier\n"
        "s_memrealtime s[26:27]\n"
        "s_waitcnt lgkmcnt(0)\n"
        "s_sub_u32 s28, s28, s26\n"
        "s_cmp_eq_u32 s45, 0\n"
        "s_cbranch_scc1 L_x2_0%=\n"
        "s_cmp_eq_u32 s45, 1\n"
        "s_cbranch_scc1 L_x2_1%=\n"
        "s_cmp_eq_u32 s45, 2\n"
        "s_cbranch_scc1 L_x2_2%=\n"
        "s_cmp_eq_u32 s45, 3\n"
        "s_cbranch_scc1 L_x2_3%=\n"
        "L_light_0%=:\n"
        "v_mfma_f32_16x16x32_bf16 v[52:55], v[252:255], v[104:107], v[52:55]\n"
        "v_mfma_f32_16x16x32_bf16 v[84:87], v[96:99], a[112:115], v[84:87]\n"
        "v_mfma_f32_16x16x32_bf16 v[88:91], v[96:99], a[120:123], v[88:91]\n"
        "v_cvt_pk_bf16_f32 v48, v48, v52\n"
        "v_cvt_pk_bf16_f32 v49, v49, v53\n"
        "v_mfma_f32_16x16x32_bf16 v[84:87], v[100:103], a[116:119], v[84:87]\n"
        "v_cvt_pk_bf16_f32 v50, v50, v54\n"
        "v_cvt_pk_bf16_f32 v51, v51, v55\n"
        "v_mfma_f32_16x16x32_bf16 v[88:91], v[100:103], a[124:127], v[88:91]\n"
        "buffer_store_dwordx4 v[48:51], v124, s[36:39], s54 offen offset:1024 sc1\n"
        "v_mfma_f32_16x16x32_bf16 v[84:87], v[92:95], a[128:131], v[84:87]\n"
        "v_mfma_f32_16x16x32_bf16 v[88:91], v[92:95], a[136:139], v[88:91]\n"
        "v_mfma_f32_16x16x32_bf16 v[84:87], v[104:107], a[132:135], v[84:87]\n"
        "v_mfma_f32_16x16x32_bf16 v[88:91], v[104:107], a[140:143], v[88:91]\n"
        "v_mfma_f32_16x16x32_bf16 v[56:59], v[32:35], v[92:95], v[56:59]\n"
        "v_mfma_f32_16x16x32_bf16 v[60:63], v[40:43], v[92:95], v[60:63]\n"
        "v_cvt_pk_bf16_f32 v84, v84, v85\n"
        "v_cvt_pk_bf16_f32 v85, v86, v87\n"
        "v_mfma_f32_16x16x32_bf16 v[56:59], v[16:19], v[96:99], v[56:59]\n"
        "v_cvt_pk_bf16_f32 v86, v88, v89\n"
        "v_cvt_pk_bf16_f32 v87, v90, v91\n"
        "v_mfma_f32_16x16x32_bf16 v[60:63], v[24:27], v[96:99], v[60:63]\n"
        "buffer_store_dwordx4 v[84:87], v124, s[36:39], s54 offen offset:2048 sc1\n"
        "v_mfma_f32_16x16x32_bf16 v[56:59], v[20:23], v[100:103], v[56:59]\n"
        "v_mov_b64 v[92:93], 0\n"
        "v_mov_b64 v[94:95], 0\n"
        "v_mfma_f32_16x16x32_bf16 v[60:63], v[28:31], v[100:103], v[60:63]\n"
        "v_mov_b64 v[96:97], 0\n"
        "v_mov_b64 v[98:99], 0\n"
        "v_mfma_f32_16x16x32_bf16 v[56:59], v[36:39], v[104:107], v[56:59]\n"
        "v_mfma_f32_16x16x32_bf16 v[60:63], v[44:47], v[104:107], v[60:63]\n"
        "s_nop 7\n"
        "v_cvt_pk_bf16_f32 v100, v56, v60\n"
        "v_cvt_pk_bf16_f32 v101, v57, v61\n"
        "v_cvt_pk_bf16_f32 v102, v58, v62\n"
        "v_cvt_pk_bf16_f32 v103, v59, v63\n"
        "buffer_store_dwordx4 v[100:103], v124, s[36:39], s54 offen offset:0 sc1\n"
        "s_barrier\n"
        "s_add_u32 s69, s58, s82\n"
        "s_sub_u32 s72, s58, s82\n"
        "s_waitcnt vmcnt(6)\n"
        "buffer_load_dwordx4 v[64:67], v124, s[36:39], s69 offen offset:0 sc1\n"
        "s_waitcnt vmcnt(6)\n"
        "buffer_load_dwordx4 v[68:71], v124, s[36:39], s69 offen offset:1024 sc1\n"
        "s_waitcnt vmcnt(6)\n"
        "buffer_load_dwordx4 v[72:75], v124, s[36:39], s72 offen offset:2048 sc1\n"
        "s_waitcnt vmcnt(6)\n"
        "buffer_load_dwordx4 v[76:79], v124, s[36:39], s72 offen offset:3072 sc1\n"
        "s_mov_b32 s81, 64\n"
        "buffer_load_dword v107, off, s[36:39], s81 sc1\n"
        "s_memrealtime s[26:27]\n"
        "s_waitcnt vmcnt(5)\n"
        "s_waitcnt lgkmcnt(0)\n"
        "s_barrier\n"
        "s_add_u32 s29, s29, 1024\n"
        "s_waitcnt vmcnt(0)\n"
        "L_par_0%=:\n"
        "v_readfirstlane_b32 s80, v107\n"
        "s_nop 0\n"
        "s_cmp_lt_i32 s80, 0\n"
        "s_cbranch_scc1 L_par2_0%=\n"
        "s_add_u32 s63, s63, 1\n"
        "s_cmp_gt_u32 s63, 0x40000\n"
        "s_cbranch_scc1 L_abort%=\n"
        "s_sleep 4\n"
        "buffer_load_dword v107, off, s[36:39], s81 sc1\n"
        "s_waitcnt vmcnt(0)\n"
        "s_branch L_par_0%=\n"
        "L_par2_0%=:\n"
        "s_bfe_u32 s46, s80, 0x10018\n"
        "s_lshl_b32 s46, s46, 21\n"
        "s_mov_b32 s53, 0x200000\n"
        "s_bfe_u32 s84, s80, 0x80010\n"
        "s_sub_u32 s84, 255, s84\n"
        "s_bfe_u32 s76, s80, 0x80008\n"
        "s_sub_u32 s76, s76, 32\n"
        "s_max_i32 s76, s76, 66\n"
        "s_bfe_u32 s77, s80, 0x80000\n"
        "s_sub_u32 s77, s77, 158\n"
        "s_bitset0_b32 s49, 30\n"
        "s_bitset0_b32 s50, 30\n"
        "s_bitset0_b32 s56, 30\n"
        "s_bitset0_b32 s64, 30\n"
        "v_mov_b32 v140, 0\n"
        "s_mov_b32 s28, 1000\n"
        "s_branch L_xok_0%=\n"
        "L_x2_0%=:\n"
        "s_add_u32 s69, s58, s82\n"
        "s_sub_u32 s72, s58, s82\n"
        "buffer_load_dwordx4 v[64:67], v124, s[36:39], s69 offen offset:0 sc1\n"
        "buffer_load_dwordx4 v[68:71], v124, s[36:39], s69 offen offset:1024 sc1\n"
        "buffer_load_dwordx4 v[72:75], v124, s[36:39], s72 offen offset:2048 sc1\n"
        "buffer_load_dwordx4 v[76:79], v124, s[36:39], s72 offen offset:3072 sc1\n"
        "s_waitcnt vmcnt(0)\n"
        "s_memrealtime s[26:27]\n"
        "s_waitcnt lgkmcnt(0)\n"
        "s_add_u32 s28, s28, s26\n"
        "L_xok_0%=:\n"
        "v_mfma_f32_16x16x32_bf16 v[84:87], v[64:67], a[0:3], 0\n"
        "v_mfma_f32_16x16x32_bf16 v[88:91], v[64:67], a[8:11], 0\n"
        "v_mfma_f32_16x16x32_bf16 v[56:59], a[144:147], v[64:67], 0\n"
        "s_branch L_win_0%=\n"
        "L_retry_0%=:\n"
        "s_add_u32 s63, s63, 1\n"
        "s_cmp_gt_u32 s63, 0x40000\n"
        "s_cbranch_scc1 L_abort%=\n"
        "s_sleep 2\n"
        "s_waitcnt vmcnt(0)\n"
        "s_waitcnt lgkmcnt(0)\n"
        "buffer_load_dwordx4 v[80:83], v138, s[36:39], s50 offen offset:0 sc1\n"
        "buffer_load_dwordx4 v[64:67], v138, s[36:39], s49 offen offset:0 sc1\n"
        "buffer_load_dwordx4 v[68:71], v138, s[36:39], s49 offen offset:256 sc1\n"
        "buffer_load_dwordx4 v[72:75], v138, s[36:39], s65 offen offset:0 sc1\n"
        "buffer_load_dwordx4 v[76:79], v138, s[36:39], s65 offen offset:256 sc1\n"
        "s_waitcnt vmcnt(4)\n"
        "v_bitop3_b32 v123, 32, v80, v82 bitop3:0x80\n"
        "v_add_u32_dpp v80, v81, v80 row_half_mirror row_mask:0xf bank_mask:0xf\n"
        "v_add_u32_dpp v82, v83, v82 row_half_mirror row_mask:0xf bank_mask:0xf\n"
        "v_cvt_f32_i32 v80, v80\n"
        "v_cvt_f32_i32 v82, v82\n"
        "v_cvt_pk_bf16_f32 v52, v80, v82\n"
        "ds_write_b32 v137, v52\n"
        "s_waitcnt vmcnt(3)\n"
        "v_bitop3_b32 v123, v123, v64, v66 bitop3:0x80\n"
        "v_cvt_f32_i32 v64, v64\n"
        "v_cvt_f32_i32 v65, v65\n"
        "v_cvt_f32_i32 v66, v66\n"
        "v_cvt_f32_i32 v67, v67\n"
        "v_cvt_pk_bf16_f32 v52, v64, v65\n"
        "v_cvt_pk_bf16_f32 v53, v66, v67\n"
        "ds_write_b64 v126, v[52:53] offset:0\n"
        "s_waitcnt vmcnt(2)\n"
        "v_bitop3_b32 v123, v123, v68, v70 bitop3:0x80\n"
        "v_cvt_f32_i32 v68, v68\n"
        "v_cvt_f32_i32 v69, v69\n"
        "v_cvt_f32_i32 v70, v70\n"
        "v_cvt_f32_i32 v71, v71\n"
        "v_cvt_pk_bf16_f32 v54, v68, v69\n"
        "v_cvt_pk_bf16_f32 v55, v70, v71\n"
        "ds_write_b64 v126, v[54:55] offset:512\n"
        "s_waitcnt vmcnt(1)\n"
        "v_bitop3_b32 v123, v123, v72, v74 bitop3:0x80\n"
        "v_cvt_f32_i32 v72, v72\n"
        "v_cvt_f32_i32 v73, v73\n"
        "ds_read_b128 v[48:51], v124 offset:0\n"
        "v_cvt_f32_i32 v74, v74\n"
        "v_cvt_f32_i32 v75, v75\n"
        "v_cvt_pk_bf16_f32 v52, v72, v73\n"
        "v_cvt_pk_bf16_f32 v53, v74, v75\n"
        "ds_write_b64 v126, v[52:53] offset:1024\n"
        "s_waitcnt vmcnt(0)\n"
        "v_bitop3_b32 v123, v123, v76, v78 bitop3:0x80\n"
        "v_cmp_eq_u32 vcc, 0, v123\n"
        "v_mov_b64 v[122:123], 0\n"
        "v_cvt_f32_i32 v76, v76\n"
        "v_cvt_f32_i32 v77, v77\n"
        "v_cvt_f32_i32 v78, v78\n"
        "v_cvt_f32_i32 v79, v79\n"
        "v_cvt_pk_bf16_f32 v54, v76, v77\n"
        "v_cvt_pk_bf16_f32 v55, v78, v79\n"
        "ds_write_b64 v126, v[54:55] offset:1536\n"
        "v_mov_b64 v[92:93], 0\n"
        "v_mov_b64 v[94:95], 0\n"
        "v_mov_b64 v[96:97], 0\n"
        "v_mov_b64 v[98:99], 0\n"
        "s_waitcnt lgkmcnt(0)\n"
        "s_branch L_dec_0%=\n"
        "L_sw0_0%=:\n"
        "s_memrealtime s[26:27]\n"
        "s_waitcnt lgkmcnt(0)\n"
        "s_sub_u32 s28, s28, s26\n"
        "L_sw_0%=:\n"
        "s_add_u32 s63, s63, 1\n"
        "s_cmp_gt_u32 s63, 0x40000\n"
        "s_cbranch_scc1 L_abort%=\n"
        "s_sleep 2\n"
        "buffer_load_dword v140, v124, s[36:39], s29 offen sc1\n"
        "s_waitcnt vmcnt(0)\n"
        "v_cmp_eq_u32 vcc, 0, v140\n"
        "s_cbranch_vccnz L_sw_0%=\n"
        "s_memrealtime s[26:27]\n"
        "s_waitcnt lgkmcnt(0)\n"
        "s_add_u32 s28, s28, s26\n"
        "s_branch L_sok_0%=\n"
        ".p2align 6\n"
        "L_rot_0%=:\n"
        "v_mfma_f32_16x16x32_bf16 v[60:63], a[236:239], v[100:103], v[60:63]\n"
        "v_mfma_f32_16x16x32_bf16 v[48:51], v[196:199], v[100:103], v[48:51]\n"
        "v_mfma_f32_16x16x32_bf16 v[52:55], v[204:207], v[100:103], v[52:55]\n"
        "s_waitcnt lgkmcnt(8)\n"
        "v_mfma_f32_16x16x32_bf16 v[84:87], v[92:95], a[96:99], v[84:87]\n"
        "ds_read_b128 v[100:103], v124 offset:15360\n"
        "v_mfma_f32_16x16x32_bf16 v[88:91], v[92:95], a[104:107], v[88:91]\n"
        "v_mfma_f32_16x16x32_bf16 v[56:59], v[0:3], v[92:95], v[56:59]\n"
        "v_mfma_f32_16x16x32_bf16 v[60:63], v[8:11], v[92:95], v[60:63]\n"
        "v_mfma_f32_16x16x32_bf16 v[48:51], v[208:211], v[92:95], v[48:51]\n"
        "s_waitcnt lgkmcnt(0)\n"
        "s_barrier\n"
        "v_mfma_f32_16x16x32_bf16 v[52:55], v[216:219], v[92:95], v[52:55]\n"
        "v_mov_b64 v[116:117], 0\n"
        "v_mfma_f32_16x16x32_bf16 v[84:87], v[104:107], a[100:103], v[84:87]\n"
        "ds_read_b128 v[92:95], v124 offset:16384\n"
        "v_mov_b64 v[118:119], 0\n"
        "v_mfma_f32_16x16x32_bf16 v[88:91], v[104:107], a[108:111], v[88:91]\n"
        "v_mov_b64 v[120:121], 0\n"
        "v_mfma_f32_16x16x32_bf16 v[56:59], v[4:7], v[104:107], v[56:59]\n"
        "v_mfma_f32_16x16x32_bf16 v[60:63], v[12:15], v[104:107], v[60:63]\n"
        "v_mfma_f32_16x16x32_bf16 v[48:51], v[212:215], v[104:107], v[48:51]\n"
        "v_mfma_f32_16x16x32_bf16 v[52:55], v[220:223], v[104:107], v[52:55]\n"
        "v_mfma_f32_16x16x32_bf16 v[48:51], v[224:227], v[96:99], v[48:51]\n"
        "ds_read_b128 v[104:107], v124 offset:17408\n"
        "v_mfma_f32_16x16x32_bf16 v[52:55], v[232:235], v[96:99], v[52:55]\n"
        "v_mfma_f32_16x16x32_bf16 v[48:51], v[228:231], v[100:103], v[48:51]\n"
        "v_mfma_f32_16x16x32_bf16 v[52:55], v[236:239], v[100:103], v[52:55]\n"
        "s_waitcnt lgkmcnt(1)\n"
        "v_mfma_f32_16x16x32_bf16 v[48:51], v[240:243], v[92:95], v[48:51]\n"
        "v_mfma_f32_16x16x32_bf16 v[52:55], v[248:251], v[92:95], v[52:55]\n"
        "s_waitcnt lgkmcnt(0)\n"
        "v_mfma_f32_16x16x32_bf16 v[48:51], v[244:247], v[104:107], v[48:51]\n"
        "s_cmp_lt_i32 s44, 0\n"
        "s_cbranch_scc1 L_light_0%=\n"
        "v_mfma_f32_16x16x32_bf16 v[52:55], v[252:255], v[104:107], v[52:55]\n"
        "s_waitcnt vmcnt(4)\n"
        "v_bitop3_b32 v123, 32, v80, v82 bitop3:0x80\n"
        "s_nop 0\n"
        "v_add_u32_dpp v80, v81, v80 row_half_mirror row_mask:0xf bank_mask:0xf\n"
        "v_mfma_f32_16x16x32_bf16 v[84:87], v[96:99], a[112:115], v[84:87]\n"
        "s_nop 0\n"
        "v_add_u32_dpp v82, v83, v82 row_half_mirror row_mask:0xf bank_mask:0xf\n"
        "v_mfma_f32_16x16x32_bf16 v[88:91], v[96:99], a[120:123], v[88:91]\n"
        "v_cvt_pk_bf16_f32 v48, v48, v52\n"
        "v_cvt_pk_bf16_f32 v49, v49, v53\n"
        "s_nop 0\n"
        "v_mfma_f32_16x16x32_bf16 v[84:87], v[100:103], a[116:119], v[84:87]\n"
        "v_cvt_pk_bf16_f32 v50, v50, v54\n"
        "v_cvt_pk_bf16_f32 v51, v51, v55\n"
        "v_mfma_f32_16x16x32_bf16 v[88:91], v[100:103], a[124:127], v[88:91]\n"
        "buffer_store_dwordx4 v[48:51], v124, s[36:39], s54 offen offset:1024 sc1\n"
        "v_cvt_f32_i32 v80, v80\n"
        "v_mfma_f32_16x16x32_bf16 v[84:87], v[92:95], a[128:131], v[84:87]\n"
        "s_nop 0\n"
        "v_cvt_f32_i32 v82, v82\n"
        "v_mfma_f32_16x16x32_bf16 v[88:91], v[92:95], a[136:139], v[88:91]\n"
        "s_nop 0\n"
        "v_cvt_pk_bf16_f32 v52, v80, v82\n"
        "v_mfma_f32_16x16x32_bf16 v[84:87], v[104:107], a[132:135], v[84:87]\n"
        "s_nop 0\n"
        "v_mfma_f32_16x16x32_bf16 v[88:91], v[104:107], a[140:143], v[88:91]\n"
        "ds_write_b32 v137, v52\n"
        "s_waitcnt vmcnt(4)\n"
        "v_bitop3_b32 v123, v123, v64, v66 bitop3:0x80\n"
        "v_cvt_f32_i32 v64, v64\n"
        "v_mfma_f32_16x16x32_bf16 v[56:59], v[32:35], v[92:95], v[56:59]\n"
        "v_cvt_f32_i32 v65, v65\n"
        "v_cvt_f32_i32 v66, v66\n"
        "v_mfma_f32_16x16x32_bf16 v[60:63], v[40:43], v[92:95], v[60:63]\n"
        "v_cvt_pk_bf16_f32 v84, v84, v85\n"
        "v_cvt_pk_bf16_f32 v85, v86, v87\n"
        "v_mfma_f32_16x16x32_bf16 v[56:59], v[16:19], v[96:99], v[56:59]\n"
        "v_cvt_pk_bf16_f32 v86, v88, v89\n"
        "v_cvt_pk_bf16_f32 v87, v90, v91\n"
        "v_mfma_f32_16x16x32_bf16 v[60:63], v[24:27], v[96:99], v[60:63]\n"
        "buffer_store_dwordx4 v[84:87], v124, s[36:39], s54 offen offset:2048 sc1\n"
        "v_cvt_f32_i32 v67, v67\n"
        "v_cvt_pk_bf16_f32 v52, v64, v65\n"
        "v_mfma_f32_16x16x32_bf16 v[56:59], v[20:23], v[100:103], v[56:59]\n"
        "v_mov_b64 v[92:93], 0\n"
        "v_mov_b64 v[94:95], 0\n"
        "v_mfma_f32_16x16x32_bf16 v[60:63], v[28:31], v[100:103], v[60:63]\n"
        "v_mov_b64 v[96:97], 0\n"
        "v_mov_b64 v[98:99], 0\n"
        "v_mfma_f32_16x16x32_bf16 v[56:59], v[36:39], v[104:107], v[56:59]\n"
        "v_cvt_pk_bf16_f32 v53, v66, v67\n"
        "ds_write_b64 v126, v[52:53] offset:0\n"
        "s_waitcnt vmcnt(4)\n"
        "v_bitop3_b32 v123, v123, v68, v70 bitop3:0x80\n"
        "v_mfma_f32_16x16x32_bf16 v[60:63], v[44:47], v[104:107], v[60:63]\n"
        "v_cvt_f32_i32 v68, v68\n"
        "v_cvt_f32_i32 v69, v69\n"
        "v_cvt_f32_i32 v70, v70\n"
        "v_cvt_f32_i32 v71, v71\n"
        "v_cvt_pk_bf16_f32 v54, v68, v69\n"
        "v_cvt_pk_bf16_f32 v55, v70, v71\n"
        "ds_write_b64 v126, v[54:55] offset:512\n"
        "s_waitcnt vmcnt(3)\n"
        "v_bitop3_b32 v123, v123, v72, v74 bitop3:0x80\n"
        "v_cvt_f32_i32 v72, v72\n"
        "v_cvt_pk_bf16_f32 v100, v56, v60\n"
        "v_cvt_pk_bf16_f32 v101, v57, v61\n"
        "v_cvt_pk_bf16_f32 v102, v58, v62\n"
        "v_cvt_pk_bf16_f32 v103, v59, v63\n"
        "v_cvt_f32_i32 v73, v73\n"
        "ds_read_b128 v[48:51], v124 offset:0\n"
        "v_cvt_f32_i32 v74, v74\n"
        "v_cvt_f32_i32 v75, v75\n"
        "v_cvt_pk_bf16_f32 v52, v72, v73\n"
        "v_cvt_pk_bf16_f32 v53, v74, v75\n"
        "ds_write_b64 v126, v[52:53] offset:1024\n"
        "s_waitcnt vmcnt(2)\n"
        "v_bitop3_b32 v123, v123, v76, v78 bitop3:0x80\n"
        "v_cmp_eq_u32 vcc, 0, v123\n"
        "v_mov_b64 v[122:123], 0\n"
        "v_cvt_f32_i32 v76, v76\n"
        "v_cvt_f32_i32 v77, v77\n"
        "v_cvt_f32_i32 v78, v78\n"
        "v_cvt_f32_i32 v79, v79\n"
        "v_cvt_pk_bf16_f32 v54, v76, v77\n"
        "v_cvt_pk_bf16_f32 v55, v78, v79\n"
        "ds_write_b64 v126, v[54:55] offset:1536\n"
        "L_dec_0%=:\n"
        "s_waitcnt lgkmcnt(2)\n"
        "v_smfmac_f32_16x16x64_bf16 v[92:95], v[48:51], a[144:151], v134\n"
        "ds_read_b128 v[52:55], v124 offset:1024\n"
        "v_max3_f32 v104, |v80|, |v82|, |v64|\n"
        "v_smfmac_f32_16x16x64_bf16 v[96:99], v[48:51], a[152:159], v134\n"
        "s_cbranch_vccnz L_retry_0%=\n"
        "v_max3_f32 v104, |v65|, |v66|, v104\n"
        "v_smfmac_f32_16x16x64_bf16 v[116:119], v[48:51], a[240:247], v134\n"
        "buffer_load_dwordx4 v[88:91], v124, s[36:39], s55 offen offset:3072 sc1\n"
        "s_add_u32 s29, s29, 1024\n"
        "v_max3_f32 v104, |v67|, |v68|, v104\n"
        "v_smfmac_f32_16x16x64_bf16 v[120:123], v[48:51], a[248:255], v134\n"
        "s_waitcnt lgkmcnt(0)\n"
        "v_max3_f32 v104, |v69|, |v70|, v104\n"
        "s_barrier\n"
        "ds_read_b128 v[56:59], v124 offset:6144\n"
        "v_smfmac_f32_16x16x64_bf16 v[92:95], v[52:55], a[160:167], v134\n"
        "ds_read_b128 v[60:63], v124 offset:5120\n"
        "v_max3_f32 v104, |v71|, |v72|, v104\n"
        "v_smfmac_f32_16x16x64_bf16 v[96:99], v[52:55], a[168:175], v134\n"
        "buffer_load_dwordx4 v[84:87], v124, s[36:39], s55 offen offset:0 sc1\n"
        "ds_read_b128 v[80:83], v124 offset:7168\n"
        "v_max3_f32 v104, |v73|, |v74|, v104\n"
        "v_smfmac_f32_16x16x64_bf16 v[116:119], v[52:55], v[176:183], v134\n"
        "ds_read_b128 v[64:67], v124 offset:8192\n"
        "v_max3_f32 v104, |v75|, |v76|, v104\n"
        "v_smfmac_f32_16x16x64_bf16 v[120:123], v[52:55], v[184:191], v134\n"
        "ds_read_b128 v[68:71], v124 offset:2048\n"
        "v_max3_f32 v104, |v77|, |v78|, v104\n"
        "s_waitcnt lgkmcnt(4)\n"
        "v_smfmac_f32_16x16x64_bf16 v[92:95], v[56:59], v[0:7], v134\n"
        "ds_read_b128 v[72:75], v124 offset:3072\n"
        "v_max_f32_e64 v104, |v79|, v104\n"
        "v_smfmac_f32_16x16x64_bf16 v[96:99], v[56:59], v[8:15], v134\n"
        "ds_read_b128 v[0:3], v125 offset:12288\n"
        "v_max_f32_dpp v104, v104, v104 row_shr:1 row_mask:0xf bank_mask:0xf\n"
        "s_waitcnt lgkmcnt(3)\n"
        "v_smfmac_f32_16x16x64_bf16 v[92:95], v[60:63], a[224:231], v134\n"
        "ds_read_b128 v[76:79], v124 offset:4096\n"
        "v_max_f32_dpp v104, v104, v104 row_shr:2 row_mask:0xf bank_mask:0xf\n"
        "v_smfmac_f32_16x16x64_bf16 v[96:99], v[60:63], a[232:239], v134\n"
        "ds_read_b128 v[4:7], v125 offset:13312\n"
        "v_max_f32_dpp v104, v104, v104 row_shr:4 row_mask:0xf bank_mask:0xf\n"
        "v_smfmac_f32_16x16x64_bf16 v[92:95], v[80:83], v[16:23], v134\n"
        "ds_read_b128 v[8:11], v125 offset:14336\n"
        "v_max_f32_dpp v104, v104, v104 row_shr:8 row_mask:0xf bank_mask:0xf\n"
        "v_smfmac_f32_16x16x64_bf16 v[96:99], v[80:83], v[24:31], v134\n"
        "ds_read_b128 v[12:15], v125 offset:15360\n"
        "v_max_f32_dpp v104, v104, v104 row_bcast:15 row_mask:0xa bank_mask:0xf\n"
        "v_smfmac_f32_16x16x64_bf16 v[92:95], v[64:67], v[32:39], v134\n"
        "ds_read_b128 v[16:19], v125 offset:16384\n"
        "v_max_f32_dpp v104, v104, v104 row_bcast:31 row_mask:0xc bank_mask:0xf\n"
        "v_smfmac_f32_16x16x64_bf16 v[96:99], v[64:67], v[40:47], v134\n"
        "ds_read_b128 v[20:23], v125 offset:17408\n"
        "v_readlane_b32 s85, v104, 63\n"
        "s_waitcnt lgkmcnt(5)\n"
        "v_smfmac_f32_16x16x64_bf16 v[92:95], v[68:71], a[176:183], v134\n"
        "ds_read_b128 v[24:27], v125 offset:18432\n"
        "v_smfmac_f32_16x16x64_bf16 v[96:99], v[68:71], a[184:191], v134\n"
        "ds_read_b128 v[28:31], v125 offset:19456\n"
        "v_mov_b32 v104, s85\n"
        "v_smfmac_f32_16x16x64_bf16 v[92:95], v[72:75], a[192:199], v134\n"
        "ds_read_b128 v[32:35], v125 offset:20480\n"
        "v_smfmac_f32_16x16x64_bf16 v[96:99], v[72:75], a[200:207], v134\n"
        "ds_read_b128 v[36:39], v125 offset:21504\n"
        "s_add_u32 s69, s58, s82\n"
        "v_smfmac_f32_16x16x64_bf16 v[92:95], v[76:79], a[208:215], v134\n"
        "ds_read_b128 v[40:43], v125 offset:22528\n"
        "s_sub_u32 s72, s58, s82\n"
        "v_smfmac_f32_16x16x64_bf16 v[96:99], v[76:79], a[216:223], v134\n"
        "ds_read_b128 v[44:47], v125 offset:23552\n"
        "v_smfmac_f32_16x16x64_bf16 v[116:119], v[64:67], v[240:247], v134\n"
        "ds_write_b32 v142, v104\n"
        "v_add_f32 v92, v92, v94\n"
        "v_add_f32 v93, v93, v95\n"
        "v_smfmac_f32_16x16x64_bf16 v[120:123], v[64:67], v[248:255], v134\n"
        "v_add_f32 v96, v96, v98\n"
        "v_add_f32 v97, v97, v99\n"
        "v_cvt_pk_bf16_f32 v92, v92, v96\n"
        "v_cvt_pk_bf16_f32 v93, v93, v97\n"
        "s_waitcnt lgkmcnt(3)\n"
        "v_smfmac_f32_16x16x64_bf16 v[116:119], v[68:71], v[0:7], v134\n"
        "ds_write2_b32 v127, v92, v93 offset1:72\n"
        "v_smfmac_f32_16x16x64_bf16 v[120:123], v[68:71], v[8:15], v134\n"
        "buffer_store_dwordx4 v[100:103], v124, s[36:39], s54 offen offset:0 sc1\n"
        "v_mov_b64 v[98:99], 0\n"
        "v_smfmac_f32_16x16x64_bf16 v[116:119], v[72:75], v[16:23], v134\n"
        "v_mov_b64 v[96:97], 0\n"
        "v_smfmac_f32_16x16x64_bf16 v[120:123], v[72:75], v[24:31], v134\n"
        "s_waitcnt vmcnt(4)\n"
        "s_waitcnt lgkmcnt(0)\n"
        "v_mov_b64 v[106:107], 0\n"
        "s_barrier\n"
        "ds_read_b128 v[0:3], v128 offset:0\n"
        "v_smfmac_f32_16x16x64_bf16 v[116:119], v[76:79], v[32:39], v134\n"
        "ds_read_b128 v[24:27], v124 offset:20480\n"
        "v_mov_b64 v[104:105], 0\n"
        "v_smfmac_f32_16x16x64_bf16 v[120:123], v[76:79], v[40:47], v134\n"
        "ds_read_b128 v[4:7], v128 offset:64\n"
        "v_mov_b64 v[94:95], 0\n"
        "v_smfmac_f32_16x16x64_bf16 v[116:119], v[60:63], v[192:199], v134\n"
        "ds_read_b128 v[8:11], v128 offset:128\n"
        "v_mov_b64 v[92:93], 0\n"
        "v_smfmac_f32_16x16x64_bf16 v[120:123], v[60:63], v[200:207], v134\n"
        "ds_read_b128 v[12:15], v128 offset:192\n"
        "v_mov_b64 v[20:21], 0\n"
        "v_smfmac_f32_16x16x64_bf16 v[116:119], v[56:59], v[208:215], v134\n"
        "s_waitcnt lgkmcnt(3)\n"
        "v_max3_f32 v24, v24, v25, v26\n"
        "s_waitcnt lgkmcnt(2)\n"
        "v_smfmac_f32_16x16x64_bf16 v[104:107], v[108:111], v[0:7], v134\n"
        "v_max_f32 v24, v24, v27\n"
        "v_smfmac_f32_16x16x64_bf16 v[120:123], v[56:59], v[216:223], v134\n"
        "v_readfirstlane_b32 s84, v24\n"
        "s_waitcnt lgkmcnt(0)\n"
        "v_smfmac_f32_16x16x64_bf16 v[104:107], v[112:115], v[8:15], v134\n"
        "v_mov_b64 v[22:23], 0\n"
        "v_smfmac_f32_16x16x64_bf16 v[116:119], v[80:83], v[224:231], v134\n"
        "s_lshr_b32 s84, s84, 23\n"
        "s_add_u32 s84, s84, s101\n"
        "v_smfmac_f32_16x16x64_bf16 v[120:123], v[80:83], v[232:239], v134\n"
        "s_max_i32 s84, s84, s76\n"
        "s_min_i32 s84, s84, 186\n"
        "v_add_f32 v104, v104, v106\n"
        "v_add_f32 v105, v105, v107\n"
        "v_smfmac_f32_16x16x64_bf16 v[92:95], v[64:67], a[128:135], v134\n"
        "ds_write_b64 v131, v[104:105]\n"
        "v_add_f32 v116, v116, v118\n"
        "v_add_f32 v117, v117, v119\n"
        "v_pk_add_f32 v[120:121], v[120:121], v[122:123]\n"
        "v_cvt_pk_bf16_f32 v116, v116, v120\n"
        "v_cvt_pk_bf16_f32 v117, v117, v121\n"
        "ds_write2_b32 v129, v116, v117 offset1:8\n"
        "v_smfmac_f32_16x16x64_bf16 v[96:99], v[64:67], a[136:143], v134\n"
        "s_mul_i32 s88, s84, 0x800080\n"
        "s_mov_b32 s89, s88\n"
        "v_smfmac_f32_16x16x64_bf16 v[92:95], v[68:71], a[32:39], v134\n"
        "buffer_load_dwordx4 v[64:67], v124, s[36:39], s69 offen offset:0 sc1\n"
        "v_mov_b64 v[104:105], s[88:89]\n"
        "v_smfmac_f32_16x16x64_bf16 v[96:99], v[68:71], a[40:47], v134\n"
        "s_add_u32 s90, s88, s35\n"
        "s_mov_b32 s91, s90\n"
        "v_mov_b64 v[106:107], s[90:91]\n"
        "v_smfmac_f32_16x16x64_bf16 v[92:95], v[48:51], a[0:7], v134\n"
        "s_waitcnt lgkmcnt(0)\n"
        "v_mov_b64 v[100:101], s[88:89]\n"
        "s_barrier\n"
        "ds_read_b128 v[24:27], v130\n"
        "v_smfmac_f32_16x16x64_bf16 v[96:99], v[48:51], a[8:15], v134\n"
        "ds_read_b128 v[28:31], v130 offset:1024\n"
        "v_mov_b64 v[102:103], s[90:91]\n"
        "v_smfmac_f32_16x16x64_bf16 v[92:95], v[52:55], a[16:23], v134\n"
        "ds_read_b128 v[116:119], v124 offset:13568\n"
        "v_mov_b64 v[48:49], 0\n"
        "v_smfmac_f32_16x16x64_bf16 v[96:99], v[52:55], a[24:31], v134\n"
        "ds_read_b128 v[32:35], v141 offset:0\n"
        "v_mov_b64 v[50:51], 0\n"
        "v_smfmac_f32_16x16x64_bf16 v[92:95], v[56:59], a[96:103], v134\n"
        "ds_read_b128 v[36:39], v141 offset:1024\n"
        "v_mov_b64 v[52:53], 0\n"
        "v_smfmac_f32_16x16x64_bf16 v[96:99], v[56:59], a[104:111], v134\n"
        "ds_read_b128 v[40:43], v141 offset:2048\n"
        "v_mov_b64 v[54:55], 0\n"
        "s_waitcnt lgkmcnt(5)\n"
        "v_smfmac_f32_16x16x64_bf16 v[20:23], v[24:27], v[0:7], v134\n"
        "ds_read_b128 v[44:47], v141 offset:3072\n"
        "v_mov_b64 v[56:57], 0\n"
        "s_waitcnt lgkmcnt(4)\n"
        "v_smfmac_f32_16x16x64_bf16 v[20:23], v[28:31], v[8:15], v134\n"
        "buffer_load_dwordx4 v[68:71], v124, s[36:39], s69 offen offset:1024 sc1\n"
        "v_max3_f32 v121, v116, v117, v118\n"
        "v_smfmac_f32_16x16x64_bf16 v[92:95], v[72:75], a[48:55], v134\n"
        "ds_read_b128 v[0:3], v125 offset:24576\n"
        "v_mov_b64 v[58:59], 0\n"
        "v_smfmac_f32_16x16x64_bf16 v[96:99], v[72:75], a[56:63], v134\n"
        "buffer_load_dwordx4 v[72:75], v124, s[36:39], s72 offen offset:2048 sc1\n"
        "v_add_f32 v20, v20, v22\n"
        "v_add_f32 v21, v21, v23\n"
        "v_cndmask_b32 v120, v20, v21, s[92:93]\n"
        "v_mul_f32 v120, s74, v120\n"
        "v_max3_f32 v121, v121, v119, v120\n"
        "v_smfmac_f32_16x16x64_bf16 v[92:95], v[76:79], a[64:71], v134\n"
        "ds_read_b128 v[4:7], v125 offset:25600\n"
        "v_max_f32_dpp v121, v121, v121 quad_perm:[2,3,0,1] row_mask:0xf bank_mask:0xf\n"
        "v_mov_b32 v122, v121\n"
        "v_smfmac_f32_16x16x64_bf16 v[96:99], v[76:79], a[72:79], v134\n"
        "buffer_load_dwordx4 v[76:79], v124, s[36:39], s72 offen offset:3072 sc1\n"
        "v_permlane16_swap_b32 v121, v122\n"
        "v_max_f32 v121, v121, v122\n"
        "v_mov_b32 v122, v121\n"
        "v_smfmac_f32_16x16x64_bf16 v[92:95], v[80:83], a[112:119], v134\n"
        "ds_read_b128 v[8:11], v125 offset:26624\n"
        "v_permlane32_swap_b32 v121, v122\n"
        "v_smfmac_f32_16x16x64_bf16 v[96:99], v[80:83], a[120:127], v134\n"
        "v_max_f32 v121, v121, v122\n"
        "v_mul_f32_e64 v123, -s73, v121\n"
        "v_fma_f32 v120, v120, s73, v123\n"
        "v_fma_f32 v116, v116, s73, v123\n"
        "v_smfmac_f32_16x16x64_bf16 v[92:95], v[60:63], a[80:87], v134\n"
        "ds_read_b128 v[12:15], v125 offset:27648\n"
        "v_fma_f32 v117, v117, s73, v123\n"
        "v_fma_f32 v118, v118, s73, v123\n"
        "v_fma_f32 v119, v119, s73, v123\n"
        "v_exp_f32 v120, v120\n"
        "v_smfmac_f32_16x16x64_bf16 v[96:99], v[60:63], a[88:95], v134\n"
        "ds_read_b128 v[16:19], v125 offset:28672\n"
        "v_exp_f32 v116, v116\n"
        "v_mul_f32 v120, s74, v120\n"
        "v_exp_f32 v117, v117\n"
        "v_exp_f32 v118, v118\n"
        "v_exp_f32 v119, v119\n"
        "s_waitcnt vmcnt(5)\n"
        "v_mov_b64 v[108:109], v[86:87]\n"
        "v_mov_b64 v[112:113], v[90:91]\n"
        "v_cvt_pk_bf16_f32 v120, v120, v120\n"
        "v_cvt_pk_bf16_f32 v60, v116, v117\n"
        "v_cvt_pk_bf16_f32 v62, v118, v119\n"
        "v_and_b32 v61, v120, v135\n"
        "v_and_b32 v63, v120, v136\n"
        "v_pk_add_f32 v[92:93], v[92:93], v[94:95]\n"
        "v_pk_add_f32 v[96:97], v[96:97], v[98:99]\n"
        "v_smfmac_f32_16x16x64_bf16 v[56:59], v[60:63], v[100:107], v134\n"
        "ds_read_b128 v[20:23], v125 offset:29696\n"
        "v_cvt_pk_bf16_f32 v86, v92, v93\n"
        "v_cvt_pk_bf16_f32 v87, v92, v93\n"
        "v_mov_b64 v[90:91], v[86:87]\n"
        "v_cvt_pk_bf16_f32 v110, v96, v97\n"
        "v_cvt_pk_bf16_f32 v111, v96, v97\n"
        "v_smfmac_f32_16x16x64_bf16 v[48:51], v[60:63], v[84:91], v134\n"
        "ds_read_b128 v[24:27], v125 offset:30720\n"
        "v_mov_b64 v[114:115], v[110:111]\n"
        "v_add_f32 v56, v56, v58\n"
        "v_add_f32 v57, v57, v59\n"
        "v_smfmac_f32_16x16x64_bf16 v[52:55], v[60:63], v[108:115], v134\n"
        "ds_read_b128 v[28:31], v125 offset:31744\n"
        "s_nop 0\n"
        "v_rcp_f32 v56, v56\n"
        "v_rcp_f32 v57, v57\n"
        "v_pk_add_f32 v[48:49], v[48:49], v[50:51]\n"
        "v_pk_mul_f32 v[48:49], v[48:49], v[56:57]\n"
        "v_mov_b32 v110, v86\n"
        "v_mov_b64 v[92:93], s[70:71]\n"
        "v_pk_add_f32 v[52:53], v[52:53], v[54:55]\n"
        "v_pk_mul_f32 v[52:53], v[52:53], v[56:57]\n"
        "v_cvt_pk_bf16_f32 v48, v48, v52\n"
        "v_cvt_pk_bf16_f32 v49, v49, v53\n"
        "ds_write2_b32 v139, v48, v49 offset1:4\n"
        "v_mov_b64 v[94:95], 0\n"
        "buffer_store_dwordx2 v[110:111], v126, s[36:39], s60 offen sc1\n"
        "v_mov_b64 v[96:97], 0\n"
        "v_mov_b64 v[98:99], 0\n"
        "v_mov_b64 v[100:101], s[70:71]\n"
        "v_mov_b64 v[102:103], 0\n"
        "v_mov_b64 v[104:105], 0\n"
        "s_waitcnt lgkmcnt(0)\n"
        "v_mov_b64 v[106:107], 0\n"
        "s_barrier\n"
        "ds_read_b128 v[108:111], v124 offset:18432\n"
        "v_mov_b64 v[118:119], 0\n"
        "ds_read_b128 v[112:115], v124 offset:19456\n"
        "v_mov_b64 v[118:119], 0\n"
        "s_nop 0\n"
        "v_mov_b64 v[116:117], s[78:79]\n"
        "s_nop 0\n"
        "v_mov_b64 v[118:119], 0\n"
        "s_nop 0\n"
        "v_mov_b64 v[116:117], s[78:79]\n"
        "s_nop 0\n"
        "s_waitcnt lgkmcnt(1)\n"
        "v_smfmac_f32_16x16x64_bf16 v[116:119], v[108:111], v[32:39], v134\n"
        "s_waitcnt lgkmcnt(0)\n"
        "v_smfmac_f32_16x16x64_bf16 v[116:119], v[112:115], v[40:47], v134\n"
        "v_smfmac_f32_16x16x64_bf16 v[92:95], v[108:111], v[144:151], v134\n"
        "v_smfmac_f32_16x16x64_bf16 v[96:99], v[108:111], v[152:159], v134\n"
        "buffer_load_dword v140, v124, s[36:39], s29 offen sc1\n"
        "v_add_f32 v116, v116, v118\n"
        "v_add_f32 v117, v117, v119\n"
        "v_smfmac_f32_16x16x64_bf16 v[92:95], v[112:115], v[160:167], v134\n"
        "v_cvt_rpi_i32_f32 v118, v116\n"
        "v_bfi_b32 v118, 63, 2, v118\n"
        "v_cvt_rpi_i32_f32 v119, v117\n"
        "v_smfmac_f32_16x16x64_bf16 v[96:99], v[112:115], v[168:175], v134\n"
        "buffer_atomic_add_x2 v[118:119], v133, s[36:39], s52 offen\n"
        "v_smfmac_f32_16x16x64_bf16 v[100:103], v[108:111], v[0:7], v134\n"
        "v_add_f32 v92, v92, v94\n"
        "v_add_f32 v93, v93, v95\n"
        "v_cvt_rpi_i32_f32 v94, v92\n"
        "v_smfmac_f32_16x16x64_bf16 v[104:107], v[108:111], v[8:15], v134\n"
        "v_bfi_b32 v94, 63, 1, v94\n"
        "v_add_f32 v96, v96, v98\n"
        "v_smfmac_f32_16x16x64_bf16 v[100:103], v[112:115], v[16:23], v134\n"
        "v_cvt_rpi_i32_f32 v95, v96\n"
        "v_smfmac_f32_16x16x64_bf16 v[104:107], v[112:115], v[24:31], v134\n"
        "buffer_atomic_add_x2 v[94:95], v133, s[36:39], s51 offen\n"
        "v_add_f32 v97, v97, v99\n"
        "v_cvt_rpi_i32_f32 v98, v93\n"
        "v_bfi_b32 v98, 63, 1, v98\n"
        "v_cvt_rpi_i32_f32 v99, v97\n"
        "s_waitcnt vmcnt(7)\n"
        "v_mfma_f32_16x16x32_bf16 v[84:87], v[64:67], a[0:3], 0\n"
        "buffer_atomic_add_x2 v[98:99], v133, s[36:39], s66 offen\n"
        "v_add_f32 v100, v100, v102\n"
        "v_add_f32 v101, v101, v103\n"
        "v_add_f32 v104, v104, v106\n"
        "v_cvt_rpi_i32_f32 v102, v100\n"
        "v_bfi_b32 v102, 63, 1, v102\n"
        "v_cvt_rpi_i32_f32 v103, v104\n"
        "v_mfma_f32_16x16x32_bf16 v[88:91], v[64:67], a[8:11], 0\n"
        "buffer_atomic_add_x2 v[102:103], v133, s[36:39], s67 offen\n"
        "v_add_f32 v105, v105, v107\n"
        "v_cvt_rpi_i32_f32 v106, v101\n"
        "v_bfi_b32 v106, 63, 1, v106\n"
        "v_cvt_rpi_i32_f32 v107, v105\n"
        "v_mfma_f32_16x16x32_bf16 v[56:59], a[144:147], v[64:67], 0\n"
        "buffer_atomic_add_x2 v[106:107], v133, s[36:39], s68 offen\n"
        "L_win_0%=:\n"
        "v_mfma_f32_16x16x32_bf16 v[60:63], a[152:155], v[64:67], 0\n"
        "v_mfma_f32_16x16x32_bf16 v[48:51], a[240:243], v[64:67], 0\n"
        "ds_write_b128 v124, v[64:67] offset:0\n"
        "v_mfma_f32_16x16x32_bf16 v[52:55], a[248:251], v[64:67], 0\n"
        "s_waitcnt vmcnt(9)\n"
        "v_mfma_f32_16x16x32_bf16 v[84:87], v[68:71], a[4:7], v[84:87]\n"
        "ds_write_b128 v124, v[68:71] offset:1024\n"
        "v_mfma_f32_16x16x32_bf16 v[88:91], v[68:71], a[12:15], v[88:91]\n"
        "v_mfma_f32_16x16x32_bf16 v[56:59], a[148:151], v[68:71], v[56:59]\n"
        "s_waitcnt vmcnt(8)\n"
        "ds_write_b128 v124, v[72:75] offset:2048\n"
        "v_mfma_f32_16x16x32_bf16 v[60:63], a[156:159], v[68:71], v[60:63]\n"
        "v_mfma_f32_16x16x32_bf16 v[48:51], a[244:247], v[68:71], v[48:51]\n"
        "s_waitcnt vmcnt(7)\n"
        "ds_write_b128 v124, v[76:79] offset:3072\n"
        "v_mfma_f32_16x16x32_bf16 v[52:55], a[252:255], v[68:71], v[52:55]\n"
        "v_mfma_f32_16x16x32_bf16 v[84:87], v[72:75], a[16:19], v[84:87]\n"
        "v_mfma_f32_16x16x32_bf16 v[88:91], v[72:75], a[24:27], v[88:91]\n"
        "v_mfma_f32_16x16x32_bf16 v[56:59], a[160:163], v[72:75], v[56:59]\n"
        "v_mfma_f32_16x16x32_bf16 v[60:63], a[168:171], v[72:75], v[60:63]\n"
        "v_mfma_f32_16x16x32_bf16 v[48:51], v[176:179], v[72:75], v[48:51]\n"
        "ds_read_b128 v[0:3], v125 offset:12288\n"
        "v_mfma_f32_16x16x32_bf16 v[52:55], v[184:187], v[72:75], v[52:55]\n"
        "ds_read_b128 v[8:11], v125 offset:14336\n"
        "v_mfma_f32_16x16x32_bf16 v[84:87], v[76:79], a[20:23], v[84:87]\n"
        "s_waitcnt lgkmcnt(2)\n"
        "s_barrier\n"
        "v_mfma_f32_16x16x32_bf16 v[88:91], v[76:79], a[28:31], v[88:91]\n"
        "ds_read_b128 v[92:95], v124 offset:4096\n"
        "v_mov_b32 v116, s62\n"
        "v_mfma_f32_16x16x32_bf16 v[56:59], a[164:167], v[76:79], v[56:59]\n"
        "s_add_u32 s49, s49, 163840\n"
        "ds_read_b128 v[104:107], v124 offset:5120\n"
        "v_mfma_f32_16x16x32_bf16 v[60:63], a[172:175], v[76:79], v[60:63]\n"
        "s_add_u32 s50, s50, 163840\n"
        "ds_read_b128 v[4:7], v125 offset:13312\n"
        "v_mfma_f32_16x16x32_bf16 v[48:51], v[180:183], v[76:79], v[48:51]\n"
        "s_xor_b32 s49, s49, s86\n"
        "ds_read_b128 v[12:15], v125 offset:15360\n"
        "v_mfma_f32_16x16x32_bf16 v[52:55], v[188:191], v[76:79], v[52:55]\n"
        "s_xor_b32 s65, s49, 8192\n"
        "s_add_u32 s56, s56, 786432\n"
        "s_waitcnt lgkmcnt(3)\n"
        "v_mfma_f32_16x16x32_bf16 v[84:87], v[92:95], a[32:35], v[84:87]\n"
        "s_add_u32 s64, s64, 786432\n"
        "ds_read_b128 v[96:99], v124 offset:6144\n"
        "v_mfma_f32_16x16x32_bf16 v[88:91], v[92:95], a[40:43], v[88:91]\n"
        "ds_read_b128 v[100:103], v124 offset:7168\n"
        "v_mfma_f32_16x16x32_bf16 v[56:59], a[176:179], v[92:95], v[56:59]\n"
        "ds_read_b128 v[16:19], v125 offset:16384\n"
        "v_mfma_f32_16x16x32_bf16 v[60:63], a[184:187], v[92:95], v[60:63]\n"
        "s_add_u32 s51, s51, 163840\n"
        "ds_read_b128 v[20:23], v125 offset:17408\n"
        "v_mfma_f32_16x16x32_bf16 v[48:51], v[0:3], v[92:95], v[48:51]\n"
        "s_add_u32 s52, s52, 163840\n"
        "ds_read_b128 v[24:27], v125 offset:18432\n"
        "v_mfma_f32_16x16x32_bf16 v[52:55], v[8:11], v[92:95], v[52:55]\n"
        "ds_read_b128 v[28:31], v125 offset:19456\n"
        "s_waitcnt lgkmcnt(8)\n"
        "v_mfma_f32_16x16x32_bf16 v[84:87], v[104:107], a[36:39], v[84:87]\n"
        "s_add_u32 s83, s83, 163840\n"
        "ds_read_b128 v[92:95], v124 offset:8192\n"
        "v_mfma_f32_16x16x32_bf16 v[88:91], v[104:107], a[44:47], v[88:91]\n"
        "s_waitcnt vmcnt(6)\n"
        "s_mov_b64 exec, 1\n"
        "buffer_atomic_umax v116, off, s[36:39], s61\n"
        "s_mov_b64 exec, -1\n"
        "v_mfma_f32_16x16x32_bf16 v[56:59], a[180:183], v[104:107], v[56:59]\n"
        "s_cmp_eq_u32 s44, 63\n"
        "s_cbranch_scc1 L_end%=\n"
        "v_mfma_f32_16x16x32_bf16 v[60:63], a[188:191], v[104:107], v[60:63]\n"
        "s_xor_b32 s86, s83, s46\n"
        "ds_read_b128 v[32:35], v125 offset:20480\n"
        "s_waitcnt lgkmcnt(9)\n"
        "v_mfma_f32_16x16x32_bf16 v[48:51], v[4:7], v[104:107], v[48:51]\n"
        "s_and_b32 s86, s86, s53\n"
        "ds_read_b128 v[36:39], v125 offset:21504\n"
        "s_waitcnt lgkmcnt(9)\n"
        "v_mfma_f32_16x16x32_bf16 v[52:55], v[12:15], v[104:107], v[52:55]\n"
        "ds_read_b128 v[40:43], v125 offset:22528\n"
        "s_waitcnt lgkmcnt(9)\n"
        "v_mfma_f32_16x16x32_bf16 v[84:87], v[96:99], a[48:51], v[84:87]\n"
        "s_lshr_b32 s86, s86, 8\n"
        "ds_read_b128 v[104:107], v124 offset:9216\n"
        "v_mfma_f32_16x16x32_bf16 v[88:91], v[96:99], a[56:59], v[88:91]\n"
        "s_xor_b32 s86, s86, s34\n"
        "ds_read_b128 v[44:47], v125 offset:23552\n"
        "v_mfma_f32_16x16x32_bf16 v[56:59], a[192:195], v[96:99], v[56:59]\n"
        "s_xor_b32 s34, s34, s86\n"
        "ds_read_b128 v[0:3], v125 offset:0\n"
        "v_mfma_f32_16x16x32_bf16 v[60:63], a[200:203], v[96:99], v[60:63]\n"
        "ds_read_b128 v[4:7], v125 offset:1024\n"
        "s_waitcnt vmcnt(6)\n"
        "v_cmp_eq_u32 vcc, 0, v140\n"
        "s_waitcnt lgkmcnt(11)\n"
        "v_mfma_f32_16x16x32_bf16 v[48:51], v[16:19], v[96:99], v[48:51]\n"
        "s_cbranch_vccnz L_sw0_0%=\n"
        "L_sok_0%=:\n"
        "s_waitcnt lgkmcnt(9)\n"
        "v_mfma_f32_16x16x32_bf16 v[52:55], v[24:27], v[96:99], v[52:55]\n"
        "v_readlane_b32 s81, v140, s99\n"
        "v_max_u32_dpp v140, v140, v140 row_shr:1 row_mask:0xf bank_mask:0xf\n"
        "v_mfma_f32_16x16x32_bf16 v[84:87], v[100:103], a[52:55], v[84:87]\n"
        "ds_read_b128 v[96:99], v124 offset:10240\n"
        "v_max_u32_dpp v140, v140, v140 row_shr:2 row_mask:0xf bank_mask:0xf\n"
        "v_mfma_f32_16x16x32_bf16 v[88:91], v[100:103], a[60:63], v[88:91]\n"
        "buffer_load_dwordx4 v[108:111], v132, s[36:39], s56 offen offset:0 sc1\n"
        "v_max_u32_dpp v140, v140, v140 row_shr:4 row_mask:0xf bank_mask:0xf\n"
        "v_mfma_f32_16x16x32_bf16 v[56:59], a[196:199], v[100:103], v[56:59]\n"
        "ds_read_b128 v[8:11], v125 offset:2048\n"
        "v_max_u32_dpp v140, v140, v140 row_shr:8 row_mask:0xf bank_mask:0xf\n"
        "v_mfma_f32_16x16x32_bf16 v[60:63], a[204:207], v[100:103], v[60:63]\n"
        "buffer_load_dwordx4 v[112:115], v132, s[36:39], s64 offen offset:0 sc1\n"
        "v_max_u32_dpp v140, v140, v140 row_bcast:15 row_mask:0xa bank_mask:0xf\n"
        "v_mfma_f32_16x16x32_bf16 v[48:51], v[20:23], v[100:103], v[48:51]\n"
        "ds_read_b128 v[12:15], v125 offset:3072\n"
        "v_max_u32_dpp v140, v140, v140 row_bcast:31 row_mask:0xc bank_mask:0xf\n"
        "s_waitcnt lgkmcnt(11)\n"
        "v_mfma_f32_16x16x32_bf16 v[52:55], v[28:31], v[100:103], v[52:55]\n"
        "v_readlane_b32 s33, v140, 63\n"
        "s_lshl1_add_u32 s28, s28, s81\n"
        "s_waitcnt lgkmcnt(10)\n"
        "v_mfma_f32_16x16x32_bf16 v[84:87], v[92:95], a[64:67], v[84:87]\n"
        "ds_read_b128 v[100:103], v124 offset:11264\n"
        "s_add_u32 s28, s28, 36\n"
        "s_sub_i32 s33, s33, s28\n"
        "v_mfma_f32_16x16x32_bf16 v[88:91], v[92:95], a[72:75], v[88:91]\n"
        "s_min_i32 s33, s33, 500\n"
        "s_mov_b32 s28, 0\n"
        "L_dl_0%=:\n"
        "s_sub_i32 s33, s33, 3\n"
        "s_cmp_gt_i32 s33, 0\n"
        "s_cbranch_scc1 L_dl_0%=\n"
        "v_mfma_f32_16x16x32_bf16 v[56:59], a[208:211], v[92:95], v[56:59]\n"
        "s_xor_b32 s51, s51, s86\n"
        "buffer_load_dwordx4 v[80:83], v138, s[36:39], s50 offen offset:0 sc1\n"
        "v_mfma_f32_16x16x32_bf16 v[60:63], a[216:219], v[92:95], v[60:63]\n"
        "s_add_u32 s55, s55, 786432\n"
        "ds_read_b128 v[16:19], v125 offset:4096\n"
        "s_waitcnt lgkmcnt(11)\n"
        "v_mfma_f32_16x16x32_bf16 v[48:51], v[32:35], v[92:95], v[48:51]\n"
        "s_add_u32 s54, s54, 786432\n"
        "buffer_load_dwordx4 v[64:67], v138, s[36:39], s49 offen offset:0 sc1\n"
        "s_waitcnt lgkmcnt(9)\n"
        "v_mfma_f32_16x16x32_bf16 v[52:55], v[40:43], v[92:95], v[52:55]\n"
        "ds_read_b128 v[20:23], v125 offset:5120\n"
        "s_waitcnt lgkmcnt(9)\n"
        "v_mfma_f32_16x16x32_bf16 v[84:87], v[104:107], a[68:71], v[84:87]\n"
        "s_add_u32 s60, s60, 131072\n"
        "ds_read_b128 v[92:95], v124 offset:12288\n"
        "v_mfma_f32_16x16x32_bf16 v[88:91], v[104:107], a[76:79], v[88:91]\n"
        "s_add_u32 s58, s58, 36864\n"
        "buffer_load_dwordx4 v[68:71], v138, s[36:39], s49 offen offset:256 sc1\n"
        "v_mfma_f32_16x16x32_bf16 v[56:59], a[212:215], v[104:107], v[56:59]\n"
        "s_xor_b32 s66, s51, 1024\n"
        "ds_read_b128 v[24:27], v125 offset:6144\n"
        "v_mfma_f32_16x16x32_bf16 v[60:63], a[220:223], v[104:107], v[60:63]\n"
        "s_xor_b32 s67, s51, 8192\n"
        "buffer_load_dwordx4 v[72:75], v138, s[36:39], s65 offen offset:0 sc1\n"
        "v_mfma_f32_16x16x32_bf16 v[48:51], v[36:39], v[104:107], v[48:51]\n"
        "s_xor_b32 s68, s51, 9216\n"
        "ds_read_b128 v[28:31], v125 offset:7168\n"
        "s_waitcnt lgkmcnt(11)\n"
        "v_mfma_f32_16x16x32_bf16 v[52:55], v[44:47], v[104:107], v[52:55]\n"
        "buffer_load_dwordx4 v[76:79], v138, s[36:39], s65 offen offset:256 sc1\n"
        "s_waitcnt lgkmcnt(8)\n"
        "v_mfma_f32_16x16x32_bf16 v[84:87], v[96:99], a[80:83], v[84:87]\n"
        "s_sub_u32 s75, 127, s84\n"
        "ds_read_b128 v[104:107], v124 offset:13312\n"
        "v_mfma_f32_16x16x32_bf16 v[88:91], v[96:99], a[88:91], v[88:91]\n"
        "s_sub_u32 s101, s77, s75\n"
        "ds_read_b128 v[32:35], v125 offset:8192\n"
        "v_mfma_f32_16x16x32_bf16 v[56:59], a[224:227], v[96:99], v[56:59]\n"
        "s_mul_i32 s35, s75, 0x800080\n"
        "ds_read_b128 v[36:39], v125 offset:9216\n"
        "v_mfma_f32_16x16x32_bf16 v[60:63], a[232:235], v[96:99], v[60:63]\n"
        "s_lshl_b32 s80, s75, 23\n"
        "ds_read_b128 v[40:43], v125 offset:10240\n"
        "v_mfma_f32_16x16x32_bf16 v[48:51], v[192:195], v[96:99], v[48:51]\n"
        "s_sub_u32 s73, 0x3e0293ee, s80\n"
        "ds_read_b128 v[44:47], v125 offset:11264\n"
        "v_mfma_f32_16x16x32_bf16 v[52:55], v[200:203], v[96:99], v[52:55]\n"
        "s_sub_u32 s74, 0x3f800000, s80\n"
        "s_add_u32 s80, s75, 128\n"
        "s_waitcnt lgkmcnt(10)\n"
        "v_mfma_f32_16x16x32_bf16 v[84:87], v[100:103], a[84:87], v[84:87]\n"
        "s_lshl_b32 s80, s80, 8\n"
        "ds_read_b128 v[96:99], v124 offset:14336\n"
        "v_mfma_f32_16x16x32_bf16 v[88:91], v[100:103], a[92:95], v[88:91]\n"
        "s_add_u32 s44, s44, 1\n"
        "s_add_u32 s61, s61, 32\n"
        "s_add_u32 s62, s80, 1\n"
        "v_mfma_f32_16x16x32_bf16 v[56:59], a[228:231], v[100:103], v[56:59]\n"
        "s_branch L_rot_0%=\n"
        "L_light_1%=:\n"
        "v_mfma_f32_16x16x32_bf16 v[52:55], v[252:255], v[104:107], v[52:55]\n"
        "v_mfma_f32_16x16x32_bf16 v[84:87], v[96:99], a[112:115], v[84:87]\n"
        "v_mfma_f32_16x16x32_bf16 v[88:91], v[96:99], a[120:123], v[88:91]\n"
        "v_cvt_pk_bf16_f32 v48, v48, v52\n"
        "v_cvt_pk_bf16_f32 v49, v49, v53\n"
        "v_mfma_f32_16x16x32_bf16 v[84:87], v[100:103], a[116:119], v[84:87]\n"
        "v_cvt_pk_bf16_f32 v50, v50, v54\n"
        "v_cvt_pk_bf16_f32 v51, v51, v55\n"
        "v_mfma_f32_16x16x32_bf16 v[88:91], v[100:103], a[124:127], v[88:91]\n"
        "v_mfma_f32_16x16x32_bf16 v[84:87], v[92:95], a[128:131], v[84:87]\n"
        "buffer_store_dwordx4 v[48:51], v124, s[36:39], s54 offen offset:1024 sc1\n"
        "v_mfma_f32_16x16x32_bf16 v[88:91], v[92:95], a[136:139], v[88:91]\n"
        "v_mfma_f32_16x16x32_bf16 v[84:87], v[104:107], a[132:135], v[84:87]\n"
        "v_mfma_f32_16x16x32_bf16 v[88:91], v[104:107], a[140:143], v[88:91]\n"
        "v_mfma_f32_16x16x32_bf16 v[56:59], v[32:35], v[92:95], v[56:59]\n"
        "v_mfma_f32_16x16x32_bf16 v[60:63], v[40:43], v[92:95], v[60:63]\n"
        "v_cvt_pk_bf16_f32 v84, v84, v85\n"
        "v_cvt_pk_bf16_f32 v85, v86, v87\n"
        "v_mfma_f32_16x16x32_bf16 v[56:59], v[16:19], v[96:99], v[56:59]\n"
        "v_cvt_pk_bf16_f32 v86, v88, v89\n"
        "v_cvt_pk_bf16_f32 v87, v90, v91\n"
        "v_mfma_f32_16x16x32_bf16 v[60:63], v[24:27], v[96:99], v[60:63]\n"
        "v_mfma_f32_16x16x32_bf16 v[56:59], v[20:23], v[100:103], v[56:59]\n"
        "buffer_store_dwordx4 v[84:87], v124, s[36:39], s54 offen offset:2048 sc1\n"
        "v_mov_b64 v[92:93], 0\n"
        "v_mfma_f32_16x16x32_bf16 v[60:63], v[28:31], v[100:103], v[60:63]\n"
        "v_mov_b64 v[94:95], 0\n"
        "v_mov_b64 v[96:97], 0\n"
        "v_mfma_f32_16x16x32_bf16 v[56:59], v[36:39], v[104:107], v[56:59]\n"
        "v_mov_b64 v[98:99], 0\n"
        "v_mfma_f32_16x16x32_bf16 v[60:63], v[44:47], v[104:107], v[60:63]\n"
        "s_nop 7\n"
        "v_cvt_pk_bf16_f32 v100, v56, v60\n"
        "v_cvt_pk_bf16_f32 v101, v57, v61\n"
        "v_cvt_pk_bf16_f32 v102, v58, v62\n"
        "v_cvt_pk_bf16_f32 v103, v59, v63\n"
        "buffer_store_dwordx4 v[100:103], v124, s[36:39], s54 offen offset:0 sc1\n"
        "s_barrier\n"
        "s_add_u32 s69, s58, s82\n"
        "s_sub_u32 s72, s58, s82\n"
        "s_waitcnt vmcnt(7)\n"
        "buffer_load_dwordx4 v[64:67], v124, s[36:39], s69 offen offset:0 sc1\n"
        "s_waitcnt vmcnt(7)\n"
        "buffer_load_dwordx4 v[68:71], v124, s[36:39], s69 offen offset:1024 sc1\n"
        "s_waitcnt vmcnt(7)\n"
        "buffer_load_dwordx4 v[72:75], v124, s[36:39], s72 offen offset:2048 sc1\n"
        "s_waitcnt vmcnt(7)\n"
        "buffer_load_dwordx4 v[76:79], v124, s[36:39], s72 offen offset:3072 sc1\n"
        "s_mov_b32 s81, 64\n"
        "buffer_load_dword v107, off, s[36:39], s81 sc1\n"
        "s_memrealtime s[26:27]\n"
        "s_waitcnt vmcnt(5)\n"
        "s_waitcnt lgkmcnt(0)\n"
        "s_barrier\n"
        "s_add_u32 s29, s29, 1024\n"
        "s_waitcnt vmcnt(0)\n"
        "L_par_1%=:\n"
        "v_readfirstlane_b32 s80, v107\n"
        "s_nop 0\n"
        "s_cmp_lt_i32 s80, 0\n"
        "s_cbranch_scc1 L_par2_1%=\n"
        "s_add_u32 s63, s63, 1\n"
        "s_cmp_gt_u32 s63, 0x40000\n"
        "s_cbranch_scc1 L_abort%=\n"
        "s_sleep 4\n"
        "buffer_load_dword v107, off, s[36:39], s81 sc1\n"
        "s_waitcnt vmcnt(0)\n"
        "s_branch L_par_1%=\n"
        "L_par2_1%=:\n"
        "s_bfe_u32 s46, s80, 0x10018\n"
        "s_lshl_b32 s46, s46, 21\n"
        "s_mov_b32 s53, 0x200000\n"
        "s_bfe_u32 s84, s80, 0x80010\n"
        "s_sub_u32 s84, 255, s84\n"
        "s_bfe_u32 s76, s80, 0x80008\n"
        "s_sub_u32 s76, s76, 32\n"
        "s_max_i32 s76, s76, 66\n"
        "s_bfe_u32 s77, s80, 0x80000\n"
        "s_sub_u32 s77, s77, 158\n"
        "s_bitset0_b32 s49, 30\n"
        "s_bitset0_b32 s50, 30\n"
        "s_bitset0_b32 s56, 30\n"
        "s_bitset0_b32 s64, 30\n"
        "v_mov_b32 v140, 0\n"
        "s_mov_b32 s28, 1000\n"
        "s_branch L_xok_1%=\n"
        "L_x2_1%=:\n"
        "s_add_u32 s69, s58, s82\n"
        "s_sub_u32 s72, s58, s82\n"
        "buffer_load_dwordx4 v[64:67], v124, s[36:39], s69 offen offset:0 sc1\n"
        "buffer_load_dwordx4 v[68:71], v124, s[36:39], s69 offen offset:1024 sc1\n"
        "buffer_load_dwordx4 v[72:75], v124, s[36:39], s72 offen offset:2048 sc1\n"
        "buffer_load_dwordx4 v[76:79], v124, s[36:39], s72 offen offset:3072 sc1\n"
        "s_waitcnt vmcnt(0)\n"
        "s_memrealtime s[26:27]\n"
        "s_waitcnt lgkmcnt(0)\n"
        "s_add_u32 s28, s28, s26\n"
        "L_xok_1%=:\n"
        "v_mfma_f32_16x16x32_bf16 v[84:87], v[64:67], a[0:3], 0\n"
        "v_mfma_f32_16x16x32_bf16 v[88:91], v[64:67], a[8:11], 0\n"
        "v_mfma_f32_16x16x32_bf16 v[56:59], a[144:147], v[64:67], 0\n"
        "s_branch L_win_1%=\n"
        "L_retry_1%=:\n"
        "s_add_u32 s63, s63, 1\n"
        "s_cmp_gt_u32 s63, 0x40000\n"
        "s_cbranch_scc1 L_abort%=\n"
        "s_sleep 2\n"
        "s_waitcnt vmcnt(0)\n"
        "s_waitcnt lgkmcnt(0)\n"
        "buffer_load_dwordx4 v[80:83], v138, s[36:39], s50 offen offset:0 sc1\n"
        "buffer_load_dwordx4 v[64:67], v138, s[36:39], s49 offen offset:0 sc1\n"
        "buffer_load_dwordx4 v[68:71], v138, s[36:39], s49 offen offset:256 sc1\n"
        "buffer_load_dwordx4 v[72:75], v138, s[36:39], s65 offen offset:0 sc1\n"
        "buffer_load_dwordx4 v[76:79], v138, s[36:39], s65 offen offset:256 sc1\n"
        "s_waitcnt vmcnt(4)\n"
        "v_bitop3_b32 v123, 32, v80, v82 bitop3:0x80\n"
        "v_add_u32_dpp v80, v81, v80 row_half_mirror row_mask:0xf bank_mask:0xf\n"
        "v_add_u32_dpp v82, v83, v82 row_half_mirror row_mask:0xf bank_mask:0xf\n"
        "v_cvt_f32_i32 v80, v80\n"
        "v_cvt_f32_i32 v82, v82\n"
        "v_cvt_pk_bf16_f32 v52, v80, v82\n"
        "ds_write_b32 v137, v52\n"
        "s_waitcnt vmcnt(3)\n"
        "v_bitop3_b32 v123, v123, v64, v66 bitop3:0x80\n"
        "v_cvt_f32_i32 v64, v64\n"
        "v_cvt_f32_i32 v65, v65\n"
        "v_cvt_f32_i32 v66, v66\n"
        "v_cvt_f32_i32 v67, v67\n"
        "v_cvt_pk_bf16_f32 v52, v64, v65\n"
        "v_cvt_pk_bf16_f32 v53, v66, v67\n"
        "ds_write_b64 v126, v[52:53] offset:0\n"
        "s_waitcnt vmcnt(2)\n"
        "v_bitop3_b32 v123, v123, v68, v70 bitop3:0x80\n"
        "v_cvt_f32_i32 v68, v68\n"
        "v_cvt_f32_i32 v69, v69\n"
        "v_cvt_f32_i32 v70, v70\n"
        "v_cvt_f32_i32 v71, v71\n"
        "v_cvt_pk_bf16_f32 v54, v68, v69\n"
        "v_cvt_pk_bf16_f32 v55, v70, v71\n"
        "ds_write_b64 v126, v[54:55] offset:512\n"
        "s_waitcnt vmcnt(1)\n"
        "v_bitop3_b32 v123, v123, v72, v74 bitop3:0x80\n"
        "v_cvt_f32_i32 v72, v72\n"
        "v_cvt_f32_i32 v73, v73\n"
        "ds_read_b128 v[48:51], v124 offset:2048\n"
        "v_cvt_f32_i32 v74, v74\n"
        "v_cvt_f32_i32 v75, v75\n"
        "v_cvt_pk_bf16_f32 v52, v72, v73\n"
        "v_cvt_pk_bf16_f32 v53, v74, v75\n"
        "ds_write_b64 v126, v[52:53] offset:1024\n"
        "s_waitcnt vmcnt(0)\n"
        "v_bitop3_b32 v123, v123, v76, v78 bitop3:0x80\n"
        "v_cmp_eq_u32 vcc, 0, v123\n"
        "v_mov_b64 v[122:123], 0\n"
        "v_cvt_f32_i32 v76, v76\n"
        "v_cvt_f32_i32 v77, v77\n"
        "v_cvt_f32_i32 v78, v78\n"
        "v_cvt_f32_i32 v79, v79\n"
        "v_cvt_pk_bf16_f32 v54, v76, v77\n"
        "v_cvt_pk_bf16_f32 v55, v78, v79\n"
        "ds_write_b64 v126, v[54:55] offset:1536\n"
        "v_mov_b64 v[92:93], 0\n"
        "v_mov_b64 v[94:95], 0\n"
        "v_mov_b64 v[96:97], 0\n"
        "v_mov_b64 v[98:99], 0\n"
        "s_waitcnt lgkmcnt(0)\n"
        "s_branch L_dec_1%=\n"
        "L_sw0_1%=:\n"
        "s_memrealtime s[26:27]\n"
        "s_waitcnt lgkmcnt(0)\n"
        "s_sub_u32 s28, s28, s26\n"
        "L_sw_1%=:\n"
        "s_add_u32 s63, s63, 1\n"
        "s_cmp_gt_u32 s63, 0x40000\n"
        "s_cbranch_scc1 L_abort%=\n"
        "s_sleep 2\n"
        "buffer_load_dword v140, v124, s[36:39], s29 offen sc1\n"
        "s_waitcnt vmcnt(0)\n"
        "v_cmp_eq_u32 vcc, 0, v140\n"
        "s_cbranch_vccnz L_sw_1%=\n"
        "s_memrealtime s[26:27]\n"
        "s_waitcnt lgkmcnt(0)\n"
        "s_add_u32 s28, s28, s26\n"
        "s_branch L_sok_1%=\n"
        ".p2align 6\n"
        "L_rot_1%=:\n"
        "v_mfma_f32_16x16x32_bf16 v[60:63], a[236:239], v[100:103], v[60:63]\n"
        "v_mov_b64 v[118:119], 0\n"
        "v_mov_b64 v[120:121], 0\n"
        "v_mfma_f32_16x16x32_bf16 v[48:51], v[196:199], v[100:103], v[48:51]\n"
        "v_and_b32 v122, 0x70, v124\n"
        "v_lshrrev_b32 v123, 7, v124\n"
        "v_mfma_f32_16x16x32_bf16 v[52:55], v[204:207], v[100:103], v[52:55]\n"
        "v_lshl_or_b32 v122, v123, 8, v122\n"
        "s_waitcnt lgkmcnt(9)\n"
        "v_mfma_f32_16x16x32_bf16 v[84:87], v[92:95], a[96:99], v[84:87]\n"
        "ds_read_b128 v[100:103], v124 offset:11264\n"
        "s_cmp_gt_i32 s44, 2\n"
        "s_cselect_b32 s48, s47, 0x40000000\n"
        "v_mfma_f32_16x16x32_bf16 v[88:91], v[92:95], a[104:107], v[88:91]\n"
        "s_bfm_b64 exec, 40, 0\n"
        "buffer_store_dwordx4 v[118:121], v122, s[36:39], s48 offen sc1\n"
        "s_mov_b64 exec, -1\n"
        "v_mfma_f32_16x16x32_bf16 v[56:59], v[0:3], v[92:95], v[56:59]\n"
        "s_add_u32 s47, s47, 163840\n"
        "v_mfma_f32_16x16x32_bf16 v[60:63], v[8:11], v[92:95], v[60:63]\n"
        "v_mfma_f32_16x16x32_bf16 v[48:51], v[208:211], v[92:95], v[48:51]\n"
        "s_waitcnt lgkmcnt(0)\n"
        "s_barrier\n"
        "v_mfma_f32_16x16x32_bf16 v[52:55], v[216:219], v[92:95], v[52:55]\n"
        "v_mov_b64 v[116:117], 0\n"
        "v_mfma_f32_16x16x32_bf16 v[84:87], v[104:107], a[100:103], v[84:87]\n"
        "ds_read_b128 v[92:95], v124 offset:16384\n"
        "v_mfma_f32_16x16x32_bf16 v[88:91], v[104:107], a[108:111], v[88:91]\n"
        "v_mfma_f32_16x16x32_bf16 v[56:59], v[4:7], v[104:107], v[56:59]\n"
        "v_mfma_f32_16x16x32_bf16 v[60:63], v[12:15], v[104:107], v[60:63]\n"
        "v_mfma_f32_16x16x32_bf16 v[48:51], v[212:215], v[104:107], v[48:51]\n"
        "v_mfma_f32_16x16x32_bf16 v[52:55], v[220:223], v[104:107], v[52:55]\n"
        "v_mfma_f32_16x16x32_bf16 v[48:51], v[224:227], v[96:99], v[48:51]\n"
        "ds_read_b128 v[104:107], v124 offset:17408\n"
        "v_mfma_f32_16x16x32_bf16 v[52:55], v[232:235], v[96:99], v[52:55]\n"
        "v_mfma_f32_16x16x32_bf16 v[48:51], v[228:231], v[100:103], v[48:51]\n"
        "v_mfma_f32_16x16x32_bf16 v[52:55], v[236:239], v[100:103], v[52:55]\n"
        "s_waitcnt lgkmcnt(1)\n"
        "v_mfma_f32_16x16x32_bf16 v[48:51], v[240:243], v[92:95], v[48:51]\n"
        "v_mfma_f32_16x16x32_bf16 v[52:55], v[248:251], v[92:95], v[52:55]\n"
        "s_waitcnt lgkmcnt(0)\n"
        "v_mfma_f32_16x16x32_bf16 v[48:51], v[244:247], v[104:107], v[48:51]\n"
        "s_cmp_lt_i32 s44, 0\n"
        "s_cbranch_scc1 L_light_1%=\n"
        "v_mfma_f32_16x16x32_bf16 v[52:55], v[252:255], v[104:107], v[52:55]\n"
        "s_waitcnt vmcnt(5)\n"
        "v_bitop3_b32 v123, 32, v80, v82 bitop3:0x80\n"
        "s_nop 0\n"
        "v_add_u32_dpp v80, v81, v80 row_half_mirror row_mask:0xf bank_mask:0xf\n"
        "v_mfma_f32_16x16x32_bf16 v[84:87], v[96:99], a[112:115], v[84:87]\n"
        "s_nop 0\n"
        "v_add_u32_dpp v82, v83, v82 row_half_mirror row_mask:0xf bank_mask:0xf\n"
        "v_mfma_f32_16x16x32_bf16 v[88:91], v[96:99], a[120:123], v[88:91]\n"
        "v_cvt_pk_bf16_f32 v48, v48, v52\n"
        "v_cvt_pk_bf16_f32 v49, v49, v53\n"
        "s_nop 0\n"
        "v_mfma_f32_16x16x32_bf16 v[84:87], v[100:103], a[116:119], v[84:87]\n"
        "v_cvt_pk_bf16_f32 v50, v50, v54\n"
        "v_cvt_pk_bf16_f32 v51, v51, v55\n"
        "v_mfma_f32_16x16x32_bf16 v[88:91], v[100:103], a[124:127], v[88:91]\n"
        "v_cvt_f32_i32 v80, v80\n"
        "s_nop 0\n"
        "v_cvt_f32_i32 v82, v82\n"
        "v_mfma_f32_16x16x32_bf16 v[84:87], v[92:95], a[128:131], v[84:87]\n"
        "buffer_store_dwordx4 v[48:51], v124, s[36:39], s54 offen offset:1024 sc1\n"
        "v_mfma_f32_16x16x32_bf16 v[88:91], v[92:95], a[136:139], v[88:91]\n"
        "s_nop 0\n"
        "v_cvt_pk_bf16_f32 v52, v80, v82\n"
        "v_mfma_f32_16x16x32_bf16 v[84:87], v[104:107], a[132:135], v[84:87]\n"
        "s_nop 0\n"
        "v_mfma_f32_16x16x32_bf16 v[88:91], v[104:107], a[140:143], v[88:91]\n"
        "ds_write_b32 v137, v52\n"
        "s_waitcnt vmcnt(5)\n"
        "v_bitop3_b32 v123, v123, v64, v66 bitop3:0x80\n"
        "v_cvt_f32_i32 v64, v64\n"
        "v_mfma_f32_16x16x32_bf16 v[56:59], v[32:35], v[92:95], v[56:59]\n"
        "v_cvt_f32_i32 v65, v65\n"
        "v_cvt_f32_i32 v66, v66\n"
        "v_mfma_f32_16x16x32_bf16 v[60:63], v[40:43], v[92:95], v[60:63]\n"
        "v_cvt_pk_bf16_f32 v84, v84, v85\n"
        "v_cvt_pk_bf16_f32 v85, v86, v87\n"
        "v_mfma_f32_16x16x32_bf16 v[56:59], v[16:19], v[96:99], v[56:59]\n"
        "v_cvt_pk_bf16_f32 v86, v88, v89\n"
        "v_cvt_pk_bf16_f32 v87, v90, v91\n"
        "v_mfma_f32_16x16x32_bf16 v[60:63], v[24:27], v[96:99], v[60:63]\n"
        "v_cvt_f32_i32 v67, v67\n"
        "v_cvt_pk_bf16_f32 v52, v64, v65\n"
        "v_mfma_f32_16x16x32_bf16 v[56:59], v[20:23], v[100:103], v[56:59]\n"
        "buffer_store_dwordx4 v[84:87], v124, s[36:39], s54 offen offset:2048 sc1\n"
        "v_mov_b64 v[92:93], 0\n"
        "v_cvt_pk_bf16_f32 v53, v66, v67\n"
        "v_mfma_f32_16x16x32_bf16 v[60:63], v[28:31], v[100:103], v[60:63]\n"
        "v_mov_b64 v[94:95], 0\n"
        "v_mov_b64 v[96:97], 0\n"
        "ds_write_b64 v126, v[52:53] offset:0\n"
        "v_mfma_f32_16x16x32_bf16 v[56:59], v[36:39], v[104:107], v[56:59]\n"
        "v_mov_b64 v[98:99], 0\n"
        "s_waitcnt vmcnt(5)\n"
        "v_bitop3_b32 v123, v123, v68, v70 bitop3:0x80\n"
        "v_mfma_f32_16x16x32_bf16 v[60:63], v[44:47], v[104:107], v[60:63]\n"
        "v_cvt_f32_i32 v68, v68\n"
        "v_cvt_f32_i32 v69, v69\n"
        "v_cvt_f32_i32 v70, v70\n"
        "v_cvt_f32_i32 v71, v71\n"
        "v_cvt_pk_bf16_f32 v54, v68, v69\n"
        "v_cvt_pk_bf16_f32 v55, v70, v71\n"
        "ds_write_b64 v126, v[54:55] offset:512\n"
        "s_waitcnt vmcnt(4)\n"
        "v_bitop3_b32 v123, v123, v72, v74 bitop3:0x80\n"
        "v_cvt_f32_i32 v72, v72\n"
        "v_cvt_pk_bf16_f32 v100, v56, v60\n"
        "v_cvt_pk_bf16_f32 v101, v57, v61\n"
        "v_cvt_pk_bf16_f32 v102, v58, v62\n"
        "v_cvt_pk_bf16_f32 v103, v59, v63\n"
        "v_cvt_f32_i32 v73, v73\n"
        "ds_read_b128 v[48:51], v124 offset:2048\n"
        "v_cvt_f32_i32 v74, v74\n"
        "v_cvt_f32_i32 v75, v75\n"
        "v_cvt_pk_bf16_f32 v52, v72, v73\n"
        "v_cvt_pk_bf16_f32 v53, v74, v75\n"
        "ds_write_b64 v126, v[52:53] offset:1024\n"
        "s_waitcnt vmcnt(3)\n"
        "v_bitop3_b32 v123, v123, v76, v78 bitop3:0x80\n"
        "v_cmp_eq_u32 vcc, 0, v123\n"
        "v_mov_b64 v[122:123], 0\n"
        "v_cvt_f32_i32 v76, v76\n"
        "v_cvt_f32_i32 v77, v77\n"
        "v_cvt_f32_i32 v78, v78\n"
        "v_cvt_f32_i32 v79, v79\n"
        "v_cvt_pk_bf16_f32 v54, v76, v77\n"
        "v_cvt_pk_bf16_f32 v55, v78, v79\n"
        "ds_write_b64 v126, v[54:55] offset:1536\n"
        "L_dec_1%=:\n"
        "s_waitcnt lgkmcnt(2)\n"
        "v_smfmac_f32_16x16x64_bf16 v[92:95], v[48:51], a[144:151], v134\n"
        "ds_read_b128 v[52:55], v124 offset:3072\n"
        "v_max3_f32 v104, |v80|, |v82|, |v64|\n"
        "v_smfmac_f32_16x16x64_bf16 v[96:99], v[48:51], a[152:159], v134\n"
        "s_cbranch_vccnz L_retry_1%=\n"
        "v_max3_f32 v104, |v65|, |v66|, v104\n"
        "v_smfmac_f32_16x16x64_bf16 v[116:119], v[48:51], a[240:247], v134\n"
        "s_add_u32 s29, s29, 1024\n"
        "v_max3_f32 v104, |v67|, |v68|, v104\n"
        "v_smfmac_f32_16x16x64_bf16 v[120:123], v[48:51], a[248:255], v134\n"
        "s_waitcnt lgkmcnt(0)\n"
        "v_max3_f32 v104, |v69|, |v70|, v104\n"
        "s_barrier\n"
        "ds_read_b128 v[56:59], v124 offset:4096\n"
        "v_smfmac_f32_16x16x64_bf16 v[92:95], v[52:55], a[160:167], v134\n"
        "buffer_load_dwordx4 v[88:91], v124, s[36:39], s55 offen offset:3072 sc1\n"
        "ds_read_b128 v[60:63], v124 offset:7168\n"
        "v_max3_f32 v104, |v71|, |v72|, v104\n"
        "v_smfmac_f32_16x16x64_bf16 v[96:99], v[52:55], a[168:175], v134\n"
        "ds_read_b128 v[80:83], v124 offset:5120\n"
        "v_max3_f32 v104, |v73|, |v74|, v104\n"
        "v_smfmac_f32_16x16x64_bf16 v[116:119], v[52:55], v[176:183], v134\n"
        "buffer_load_dwordx4 v[84:87], v124, s[36:39], s55 offen offset:0 sc1\n"
        "ds_read_b128 v[64:67], v124 offset:8192\n"
        "v_max3_f32 v104, |v75|, |v76|, v104\n"
        "v_smfmac_f32_16x16x64_bf16 v[120:123], v[52:55], v[184:191], v134\n"
        "ds_read_b128 v[68:71], v124 offset:0\n"
        "v_max3_f32 v104, |v77|, |v78|, v104\n"
        "s_waitcnt lgkmcnt(4)\n"
        "v_smfmac_f32_16x16x64_bf16 v[92:95], v[56:59], v[0:7], v134\n"
        "ds_read_b128 v[72:75], v124 offset:1024\n"
        "v_max_f32_e64 v104, |v79|, v104\n"
        "v_smfmac_f32_16x16x64_bf16 v[96:99], v[56:59], v[8:15], v134\n"
        "ds_read_b128 v[0:3], v125 offset:12288\n"
        "v_max_f32_dpp v104, v104, v104 row_shr:1 row_mask:0xf bank_mask:0xf\n"
        "s_waitcnt lgkmcnt(3)\n"
        "v_smfmac_f32_16x16x64_bf16 v[92:95], v[60:63], a[224:231], v134\n"
        "ds_read_b128 v[76:79], v124 offset:6144\n"
        "v_max_f32_dpp v104, v104, v104 row_shr:2 row_mask:0xf bank_mask:0xf\n"
        "v_smfmac_f32_16x16x64_bf16 v[96:99], v[60:63], a[232:239], v134\n"
        "ds_read_b128 v[4:7], v125 offset:13312\n"
        "v_max_f32_dpp v104, v104, v104 row_shr:4 row_mask:0xf bank_mask:0xf\n"
        "v_smfmac_f32_16x16x64_bf16 v[92:95], v[80:83], v[16:23], v134\n"
        "ds_read_b128 v[8:11], v125 offset:14336\n"
        "v_max_f32_dpp v104, v104, v104 row_shr:8 row_mask:0xf bank_mask:0xf\n"
        "v_smfmac_f32_16x16x64_bf16 v[96:99], v[80:83], v[24:31], v134\n"
        "ds_read_b128 v[12:15], v125 offset:15360\n"
        "v_max_f32_dpp v104, v104, v104 row_bcast:15 row_mask:0xa bank_mask:0xf\n"
        "v_smfmac_f32_16x16x64_bf16 v[92:95], v[64:67], v[32:39], v134\n"
        "ds_read_b128 v[16:19], v125 offset:16384\n"
        "v_max_f32_dpp v104, v104, v104 row_bcast:31 row_mask:0xc bank_mask:0xf\n"
        "v_smfmac_f32_16x16x64_bf16 v[96:99], v[64:67], v[40:47], v134\n"
        "ds_read_b128 v[20:23], v125 offset:17408\n"
        "v_readlane_b32 s85, v104, 63\n"
        "s_waitcnt lgkmcnt(5)\n"
        "v_smfmac_f32_16x16x64_bf16 v[92:95], v[68:71], a[176:183], v134\n"
        "ds_read_b128 v[24:27], v125 offset:18432\n"
        "v_smfmac_f32_16x16x64_bf16 v[96:99], v[68:71], a[184:191], v134\n"
        "ds_read_b128 v[28:31], v125 offset:19456\n"
        "v_mov_b32 v104, s85\n"
        "v_smfmac_f32_16x16x64_bf16 v[92:95], v[72:75], a[192:199], v134\n"
        "ds_read_b128 v[32:35], v125 offset:20480\n"
        "v_smfmac_f32_16x16x64_bf16 v[96:99], v[72:75], a[200:207], v134\n"
        "ds_read_b128 v[36:39], v125 offset:21504\n"
        "s_add_u32 s69, s58, s82\n"
        "v_smfmac_f32_16x16x64_bf16 v[92:95], v[76:79], a[208:215], v134\n"
        "ds_read_b128 v[40:43], v125 offset:22528\n"
        "s_sub_u32 s72, s58, s82\n"
        "v_smfmac_f32_16x16x64_bf16 v[96:99], v[76:79], a[216:223], v134\n"
        "ds_read_b128 v[44:47], v125 offset:23552\n"
        "v_smfmac_f32_16x16x64_bf16 v[116:119], v[64:67], v[240:247], v134\n"
        "ds_write_b32 v142, v104\n"
        "v_add_f32 v92, v92, v94\n"
        "v_add_f32 v93, v93, v95\n"
        "v_smfmac_f32_16x16x64_bf16 v[120:123], v[64:67], v[248:255], v134\n"
        "v_add_f32 v96, v96, v98\n"
        "v_add_f32 v97, v97, v99\n"
        "v_cvt_pk_bf16_f32 v92, v92, v96\n"
        "v_cvt_pk_bf16_f32 v93, v93, v97\n"
        "s_waitcnt lgkmcnt(3)\n"
        "v_smfmac_f32_16x16x64_bf16 v[116:119], v[68:71], v[0:7], v134\n"
        "ds_write2_b32 v127, v92, v93 offset1:72\n"
        "v_smfmac_f32_16x16x64_bf16 v[120:123], v[68:71], v[8:15], v134\n"
        "v_mov_b64 v[98:99], 0\n"
        "v_smfmac_f32_16x16x64_bf16 v[116:119], v[72:75], v[16:23], v134\n"
        "buffer_store_dwordx4 v[100:103], v124, s[36:39], s54 offen offset:0 sc1\n"
        "v_mov_b64 v[96:97], 0\n"
        "v_smfmac_f32_16x16x64_bf16 v[120:123], v[72:75], v[24:31], v134\n"
        "s_waitcnt vmcnt(4)\n"
        "s_waitcnt lgkmcnt(0)\n"
        "v_mov_b64 v[106:107], 0\n"
        "s_barrier\n"
        "ds_read_b128 v[0:3], v128 offset:0\n"
        "v_smfmac_f32_16x16x64_bf16 v[116:119], v[76:79], v[32:39], v134\n"
        "ds_read_b128 v[24:27], v124 offset:20480\n"
        "v_mov_b64 v[104:105], 0\n"
        "v_smfmac_f32_16x16x64_bf16 v[120:123], v[76:79], v[40:47], v134\n"
        "ds_read_b128 v[4:7], v128 offset:64\n"
        "v_mov_b64 v[94:95], 0\n"
        "v_smfmac_f32_16x16x64_bf16 v[116:119], v[60:63], v[192:199], v134\n"
        "ds_read_b128 v[8:11], v128 offset:128\n"
        "v_mov_b64 v[92:93], 0\n"
        "v_smfmac_f32_16x16x64_bf16 v[120:123], v[60:63], v[200:207], v134\n"
        "ds_read_b128 v[12:15], v128 offset:192\n"
        "v_mov_b64 v[20:21], 0\n"
        "v_smfmac_f32_16x16x64_bf16 v[116:119], v[56:59], v[208:215], v134\n"
        "s_waitcnt lgkmcnt(3)\n"
        "v_max3_f32 v24, v24, v25, v26\n"
        "s_waitcnt lgkmcnt(2)\n"
        "v_smfmac_f32_16x16x64_bf16 v[104:107], v[108:111], v[0:7], v134\n"
        "v_max_f32 v24, v24, v27\n"
        "v_smfmac_f32_16x16x64_bf16 v[120:123], v[56:59], v[216:223], v134\n"
        "v_readfirstlane_b32 s84, v24\n"
        "s_waitcnt lgkmcnt(0)\n"
        "v_smfmac_f32_16x16x64_bf16 v[104:107], v[112:115], v[8:15], v134\n"
        "v_mov_b64 v[22:23], 0\n"
        "v_smfmac_f32_16x16x64_bf16 v[116:119], v[80:83], v[224:231], v134\n"
        "s_lshr_b32 s84, s84, 23\n"
        "s_add_u32 s84, s84, s101\n"
        "v_smfmac_f32_16x16x64_bf16 v[120:123], v[80:83], v[232:239], v134\n"
        "s_max_i32 s84, s84, s76\n"
        "s_min_i32 s84, s84, 186\n"
        "v_add_f32 v104, v104, v106\n"
        "v_add_f32 v105, v105, v107\n"
        "v_smfmac_f32_16x16x64_bf16 v[92:95], v[64:67], a[128:135], v134\n"
        "ds_write_b64 v131, v[104:105]\n"
        "v_add_f32 v116, v116, v118\n"
        "v_add_f32 v117, v117, v119\n"
        "v_pk_add_f32 v[120:121], v[120:121], v[122:123]\n"
        "v_cvt_pk_bf16_f32 v116, v116, v120\n"
        "v_cvt_pk_bf16_f32 v117, v117, v121\n"
        "ds_write2_b32 v129, v116, v117 offset1:8\n"
        "v_smfmac_f32_16x16x64_bf16 v[96:99], v[64:67], a[136:143], v134\n"
        "s_mul_i32 s88, s84, 0x800080\n"
        "s_mov_b32 s89, s88\n"
        "v_smfmac_f32_16x16x64_bf16 v[92:95], v[68:71], a[32:39], v134\n"
        "s_add_u32 s90, s88, s35\n"
        "s_mov_b32 s91, s90\n"
        "v_mov_b64 v[104:105], s[88:89]\n"
        "v_smfmac_f32_16x16x64_bf16 v[96:99], v[68:71], a[40:47], v134\n"
        "buffer_load_dwordx4 v[64:67], v124, s[36:39], s69 offen offset:0 sc1\n"
        "v_mov_b64 v[106:107], s[90:91]\n"
        "v_smfmac_f32_16x16x64_bf16 v[92:95], v[48:51], a[0:7], v134\n"
        "s_waitcnt lgkmcnt(0)\n"
        "v_mov_b64 v[100:101], s[88:89]\n"
        "s_barrier\n"
        "ds_read_b128 v[24:27], v130\n"
        "v_smfmac_f32_16x16x64_bf16 v[96:99], v[48:51], a[8:15], v134\n"
        "ds_read_b128 v[28:31], v130 offset:1024\n"
        "v_mov_b64 v[102:103], s[90:91]\n"
        "v_smfmac_f32_16x16x64_bf16 v[92:95], v[52:55], a[16:23], v134\n"
        "ds_read_b128 v[116:119], v124 offset:13568\n"
        "v_mov_b64 v[48:49], 0\n"
        "v_smfmac_f32_16x16x64_bf16 v[96:99], v[52:55], a[24:31], v134\n"
        "ds_read_b128 v[32:35], v141 offset:0\n"
        "v_mov_b64 v[50:51], 0\n"
        "v_smfmac_f32_16x16x64_bf16 v[92:95], v[56:59], a[96:103], v134\n"
        "ds_read_b128 v[36:39], v141 offset:1024\n"
        "v_mov_b64 v[52:53], 0\n"
        "v_smfmac_f32_16x16x64_bf16 v[96:99], v[56:59], a[104:111], v134\n"
        "ds_read_b128 v[40:43], v141 offset:2048\n"
        "v_mov_b64 v[54:55], 0\n"
        "s_waitcnt lgkmcnt(5)\n"
        "v_smfmac_f32_16x16x64_bf16 v[20:23], v[24:27], v[0:7], v134\n"
        "ds_read_b128 v[44:47], v141 offset:3072\n"
        "v_mov_b64 v[56:57], 0\n"
        "s_waitcnt lgkmcnt(4)\n"
        "v_smfmac_f32_16x16x64_bf16 v[20:23], v[28:31], v[8:15], v134\n"
        "ds_read_b128 v[0:3], v125 offset:24576\n"
        "v_max3_f32 v121, v116, v117, v118\n"
        "v_smfmac_f32_16x16x64_bf16 v[92:95], v[72:75], a[48:55], v134\n"
        "buffer_load_dwordx4 v[68:71], v124, s[36:39], s69 offen offset:1024 sc1\n"
        "v_mov_b64 v[58:59], 0\n"
        "v_smfmac_f32_16x16x64_bf16 v[96:99], v[72:75], a[56:63], v134\n"
        "ds_read_b128 v[4:7], v125 offset:25600\n"
        "v_add_f32 v20, v20, v22\n"
        "v_add_f32 v21, v21, v23\n"
        "v_cndmask_b32 v120, v20, v21, s[92:93]\n"
        "v_mul_f32 v120, s74, v120\n"
        "v_max3_f32 v121, v121, v119, v120\n"
        "v_smfmac_f32_16x16x64_bf16 v[92:95], v[76:79], a[64:71], v134\n"
        "buffer_load_dwordx4 v[72:75], v124, s[36:39], s72 offen offset:2048 sc1\n"
        "v_max_f32_dpp v121, v121, v121 quad_perm:[2,3,0,1] row_mask:0xf bank_mask:0xf\n"
        "v_mov_b32 v122, v121\n"
        "v_smfmac_f32_16x16x64_bf16 v[96:99], v[76:79], a[72:79], v134\n"
        "ds_read_b128 v[8:11], v125 offset:26624\n"
        "v_permlane16_swap_b32 v121, v122\n"
        "v_max_f32 v121, v121, v122\n"
        "v_mov_b32 v122, v121\n"
        "v_smfmac_f32_16x16x64_bf16 v[92:95], v[80:83], a[112:119], v134\n"
        "buffer_load_dwordx4 v[76:79], v124, s[36:39], s72 offen offset:3072 sc1\n"
        "v_permlane32_swap_b32 v121, v122\n"
        "v_smfmac_f32_16x16x64_bf16 v[96:99], v[80:83], a[120:127], v134\n"
        "ds_read_b128 v[12:15], v125 offset:27648\n"
        "v_max_f32 v121, v121, v122\n"
        "v_mul_f32_e64 v123, -s73, v121\n"
        "v_fma_f32 v120, v120, s73, v123\n"
        "v_fma_f32 v116, v116, s73, v123\n"
        "v_smfmac_f32_16x16x64_bf16 v[92:95], v[60:63], a[80:87], v134\n"
        "v_fma_f32 v117, v117, s73, v123\n"
        "v_fma_f32 v118, v118, s73, v123\n"
        "v_fma_f32 v119, v119, s73, v123\n"
        "v_exp_f32 v120, v120\n"
        "v_smfmac_f32_16x16x64_bf16 v[96:99], v[60:63], a[88:95], v134\n"
        "ds_read_b128 v[16:19], v125 offset:28672\n"
        "v_exp_f32 v116, v116\n"
        "v_mul_f32 v120, s74, v120\n"
        "v_exp_f32 v117, v117\n"
        "v_exp_f32 v118, v118\n"
        "v_exp_f32 v119, v119\n"
        "s_waitcnt vmcnt(5)\n"
        "v_mov_b64 v[108:109], v[86:87]\n"
        "v_mov_b64 v[112:113], v[90:91]\n"
        "v_cvt_pk_bf16_f32 v120, v120, v120\n"
        "v_cvt_pk_bf16_f32 v60, v116, v117\n"
        "v_cvt_pk_bf16_f32 v62, v118, v119\n"
        "v_and_b32 v61, v120, v135\n"
        "v_and_b32 v63, v120, v136\n"
        "v_pk_add_f32 v[92:93], v[92:93], v[94:95]\n"
        "v_pk_add_f32 v[96:97], v[96:97], v[98:99]\n"
        "v_smfmac_f32_16x16x64_bf16 v[56:59], v[60:63], v[100:107], v134\n"
        "ds_read_b128 v[20:23], v125 offset:29696\n"
        "v_cvt_pk_bf16_f32 v86, v92, v93\n"
        "v_cvt_pk_bf16_f32 v87, v92, v93\n"
        "v_mov_b64 v[90:91], v[86:87]\n"
        "v_cvt_pk_bf16_f32 v110, v96, v97\n"
        "v_cvt_pk_bf16_f32 v111, v96, v97\n"
        "v_smfmac_f32_16x16x64_bf16 v[48:51], v[60:63], v[84:91], v134\n"
        "ds_read_b128 v[24:27], v125 offset:30720\n"
        "v_mov_b64 v[114:115], v[110:111]\n"
        "v_add_f32 v56, v56, v58\n"
        "v_add_f32 v57, v57, v59\n"
        "v_smfmac_f32_16x16x64_bf16 v[52:55], v[60:63], v[108:115], v134\n"
        "ds_read_b128 v[28:31], v125 offset:31744\n"
        "s_nop 0\n"
        "v_rcp_f32 v56, v56\n"
        "v_rcp_f32 v57, v57\n"
        "v_pk_add_f32 v[48:49], v[48:49], v[50:51]\n"
        "v_pk_mul_f32 v[48:49], v[48:49], v[56:57]\n"
        "v_mov_b32 v110, v86\n"
        "v_mov_b64 v[92:93], s[70:71]\n"
        "v_pk_add_f32 v[52:53], v[52:53], v[54:55]\n"
        "v_pk_mul_f32 v[52:53], v[52:53], v[56:57]\n"
        "v_cvt_pk_bf16_f32 v48, v48, v52\n"
        "v_cvt_pk_bf16_f32 v49, v49, v53\n"
        "ds_write2_b32 v139, v48, v49 offset1:4\n"
        "v_mov_b64 v[94:95], 0\n"
        "v_mov_b64 v[96:97], 0\n"
        "v_mov_b64 v[98:99], 0\n"
        "buffer_store_dwordx2 v[110:111], v126, s[36:39], s60 offen sc1\n"
        "v_mov_b64 v[100:101], s[70:71]\n"
        "v_mov_b64 v[102:103], 0\n"
        "v_mov_b64 v[104:105], 0\n"
        "s_waitcnt lgkmcnt(0)\n"
        "v_mov_b64 v[106:107], 0\n"
        "s_barrier\n"
        "ds_read_b128 v[108:111], v124 offset:18432\n"
        "v_mov_b64 v[118:119], 0\n"
        "ds_read_b128 v[112:115], v124 offset:19456\n"
        "v_mov_b64 v[118:119], 0\n"
        "s_nop 0\n"
        "v_mov_b64 v[116:117], s[78:79]\n"
        "s_nop 0\n"
        "v_mov_b64 v[118:119], 0\n"
        "s_nop 0\n"
        "v_mov_b64 v[116:117], s[78:79]\n"
        "s_nop 0\n"
        "s_waitcnt lgkmcnt(1)\n"
        "v_smfmac_f32_16x16x64_bf16 v[116:119], v[108:111], v[32:39], v134\n"
        "s_waitcnt lgkmcnt(0)\n"
        "v_smfmac_f32_16x16x64_bf16 v[116:119], v[112:115], v[40:47], v134\n"
        "v_smfmac_f32_16x16x64_bf16 v[92:95], v[108:111], v[144:151], v134\n"
        "v_smfmac_f32_16x16x64_bf16 v[96:99], v[108:111], v[152:159], v134\n"
        "v_add_f32 v116, v116, v118\n"
        "v_add_f32 v117, v117, v119\n"
        "v_smfmac_f32_16x16x64_bf16 v[92:95], v[112:115], v[160:167], v134\n"
        "buffer_load_dword v140, v124, s[36:39], s29 offen sc1\n"
        "v_cvt_rpi_i32_f32 v118, v116\n"
        "v_bfi_b32 v118, 63, 2, v118\n"
        "v_cvt_rpi_i32_f32 v119, v117\n"
        "v_smfmac_f32_16x16x64_bf16 v[96:99], v[112:115], v[168:175], v134\n"
        "buffer_atomic_add_x2 v[118:119], v133, s[36:39], s52 offen\n"
        "v_smfmac_f32_16x16x64_bf16 v[100:103], v[108:111], v[0:7], v134\n"
        "v_add_f32 v92, v92, v94\n"
        "v_add_f32 v93, v93, v95\n"
        "v_cvt_rpi_i32_f32 v94, v92\n"
        "v_smfmac_f32_16x16x64_bf16 v[104:107], v[108:111], v[8:15], v134\n"
        "v_bfi_b32 v94, 63, 1, v94\n"
        "v_add_f32 v96, v96, v98\n"
        "v_smfmac_f32_16x16x64_bf16 v[100:103], v[112:115], v[16:23], v134\n"
        "v_cvt_rpi_i32_f32 v95, v96\n"
        "v_smfmac_f32_16x16x64_bf16 v[104:107], v[112:115], v[24:31], v134\n"
        "buffer_atomic_add_x2 v[94:95], v133, s[36:39], s51 offen\n"
        "v_add_f32 v97, v97, v99\n"
        "v_cvt_rpi_i32_f32 v98, v93\n"
        "v_bfi_b32 v98, 63, 1, v98\n"
        "v_cvt_rpi_i32_f32 v99, v97\n"
        "s_waitcnt vmcnt(7)\n"
        "v_mfma_f32_16x16x32_bf16 v[84:87], v[64:67], a[0:3], 0\n"
        "buffer_atomic_add_x2 v[98:99], v133, s[36:39], s66 offen\n"
        "v_add_f32 v100, v100, v102\n"
        "v_add_f32 v101, v101, v103\n"
        "v_add_f32 v104, v104, v106\n"
        "v_cvt_rpi_i32_f32 v102, v100\n"
        "v_bfi_b32 v102, 63, 1, v102\n"
        "v_cvt_rpi_i32_f32 v103, v104\n"
        "v_mfma_f32_16x16x32_bf16 v[88:91], v[64:67], a[8:11], 0\n"
        "buffer_atomic_add_x2 v[102:103], v133, s[36:39], s67 offen\n"
        "v_add_f32 v105, v105, v107\n"
        "v_cvt_rpi_i32_f32 v106, v101\n"
        "v_bfi_b32 v106, 63, 1, v106\n"
        "v_cvt_rpi_i32_f32 v107, v105\n"
        "v_mfma_f32_16x16x32_bf16 v[56:59], a[144:147], v[64:67], 0\n"
        "buffer_atomic_add_x2 v[106:107], v133, s[36:39], s68 offen\n"
        "L_win_1%=:\n"
        "v_mfma_f32_16x16x32_bf16 v[60:63], a[152:155], v[64:67], 0\n"
        "v_mfma_f32_16x16x32_bf16 v[48:51], a[240:243], v[64:67], 0\n"
        "v_mfma_f32_16x16x32_bf16 v[52:55], a[248:251], v[64:67], 0\n"
        "ds_write_b128 v124, v[64:67] offset:4096\n"
        "s_waitcnt vmcnt(9)\n"
        "v_mfma_f32_16x16x32_bf16 v[84:87], v[68:71], a[4:7], v[84:87]\n"
        "v_mfma_f32_16x16x32_bf16 v[88:91], v[68:71], a[12:15], v[88:91]\n"
        "ds_write_b128 v124, v[68:71] offset:5120\n"
        "v_mfma_f32_16x16x32_bf16 v[56:59], a[148:151], v[68:71], v[56:59]\n"
        "v_mfma_f32_16x16x32_bf16 v[60:63], a[156:159], v[68:71], v[60:63]\n"
        "s_waitcnt vmcnt(8)\n"
        "ds_write_b128 v124, v[72:75] offset:6144\n"
        "v_mfma_f32_16x16x32_bf16 v[48:51], a[244:247], v[68:71], v[48:51]\n"
        "v_mfma_f32_16x16x32_bf16 v[52:55], a[252:255], v[68:71], v[52:55]\n"
        "s_waitcnt vmcnt(7)\n"
        "ds_write_b128 v124, v[76:79] offset:7168\n"
        "v_mfma_f32_16x16x32_bf16 v[84:87], v[72:75], a[16:19], v[84:87]\n"
        "v_mfma_f32_16x16x32_bf16 v[88:91], v[72:75], a[24:27], v[88:91]\n"
        "v_mfma_f32_16x16x32_bf16 v[56:59], a[160:163], v[72:75], v[56:59]\n"
        "v_mfma_f32_16x16x32_bf16 v[60:63], a[168:171], v[72:75], v[60:63]\n"
        "v_mfma_f32_16x16x32_bf16 v[48:51], v[176:179], v[72:75], v[48:51]\n"
        "ds_read_b128 v[0:3], v125 offset:12288\n"
        "v_mfma_f32_16x16x32_bf16 v[52:55], v[184:187], v[72:75], v[52:55]\n"
        "ds_read_b128 v[8:11], v125 offset:14336\n"
        "v_mfma_f32_16x16x32_bf16 v[84:87], v[76:79], a[20:23], v[84:87]\n"
        "s_waitcnt lgkmcnt(2)\n"
        "s_barrier\n"
        "v_mfma_f32_16x16x32_bf16 v[88:91], v[76:79], a[28:31], v[88:91]\n"
        "ds_read_b128 v[92:95], v124 offset:0\n"
        "v_mov_b32 v116, s62\n"
        "v_mfma_f32_16x16x32_bf16 v[56:59], a[164:167], v[76:79], v[56:59]\n"
        "s_add_u32 s49, s49, 163840\n"
        "ds_read_b128 v[104:107], v124 offset:1024\n"
        "v_mfma_f32_16x16x32_bf16 v[60:63], a[172:175], v[76:79], v[60:63]\n"
        "s_add_u32 s50, s50, 163840\n"
        "ds_read_b128 v[4:7], v125 offset:13312\n"
        "v_mfma_f32_16x16x32_bf16 v[48:51], v[180:183], v[76:79], v[48:51]\n"
        "s_xor_b32 s49, s49, s86\n"
        "ds_read_b128 v[12:15], v125 offset:15360\n"
        "v_mfma_f32_16x16x32_bf16 v[52:55], v[188:191], v[76:79], v[52:55]\n"
        "s_xor_b32 s65, s49, 8192\n"
        "s_add_u32 s56, s56, 786432\n"
        "s_waitcnt lgkmcnt(3)\n"
        "v_mfma_f32_16x16x32_bf16 v[84:87], v[92:95], a[32:35], v[84:87]\n"
        "s_add_u32 s64, s64, 786432\n"
        "ds_read_b128 v[96:99], v124 offset:2048\n"
        "v_mfma_f32_16x16x32_bf16 v[88:91], v[92:95], a[40:43], v[88:91]\n"
        "ds_read_b128 v[100:103], v124 offset:3072\n"
        "v_mfma_f32_16x16x32_bf16 v[56:59], a[176:179], v[92:95], v[56:59]\n"
        "ds_read_b128 v[16:19], v125 offset:16384\n"
        "v_mfma_f32_16x16x32_bf16 v[60:63], a[184:187], v[92:95], v[60:63]\n"
        "s_add_u32 s51, s51, 163840\n"
        "ds_read_b128 v[20:23], v125 offset:17408\n"
        "v_mfma_f32_16x16x32_bf16 v[48:51], v[0:3], v[92:95], v[48:51]\n"
        "s_add_u32 s52, s52, 163840\n"
        "ds_read_b128 v[24:27], v125 offset:18432\n"
        "v_mfma_f32_16x16x32_bf16 v[52:55], v[8:11], v[92:95], v[52:55]\n"
        "ds_read_b128 v[28:31], v125 offset:19456\n"
        "s_waitcnt lgkmcnt(8)\n"
        "v_mfma_f32_16x16x32_bf16 v[84:87], v[104:107], a[36:39], v[84:87]\n"
        "s_add_u32 s83, s83, 163840\n"
        "ds_read_b128 v[92:95], v124 offset:12288\n"
        "v_mfma_f32_16x16x32_bf16 v[88:91], v[104:107], a[44:47], v[88:91]\n"
        "s_xor_b32 s86, s83, s46\n"
        "s_and_b32 s86, s86, s53\n"
        "s_lshr_b32 s86, s86, 8\n"
        "v_mfma_f32_16x16x32_bf16 v[56:59], a[180:183], v[104:107], v[56:59]\n"
        "s_waitcnt vmcnt(6)\n"
        "s_mov_b64 exec, 1\n"
        "buffer_atomic_umax v116, off, s[36:39], s61\n"
        "s_mov_b64 exec, -1\n"
        "v_mfma_f32_16x16x32_bf16 v[60:63], a[188:191], v[104:107], v[60:63]\n"
        "s_cmp_eq_u32 s44, 63\n"
        "s_cbranch_scc1 L_end%=\n"
        "s_waitcnt lgkmcnt(8)\n"
        "v_mfma_f32_16x16x32_bf16 v[48:51], v[4:7], v[104:107], v[48:51]\n"
        "s_xor_b32 s86, s86, s34\n"
        "ds_read_b128 v[32:35], v125 offset:20480\n"
        "s_waitcnt lgkmcnt(8)\n"
        "v_mfma_f32_16x16x32_bf16 v[52:55], v[12:15], v[104:107], v[52:55]\n"
        "ds_read_b128 v[36:39], v125 offset:21504\n"
        "s_waitcnt lgkmcnt(8)\n"
        "v_mfma_f32_16x16x32_bf16 v[84:87], v[96:99], a[48:51], v[84:87]\n"
        "s_xor_b32 s34, s34, s86\n"
        "ds_read_b128 v[104:107], v124 offset:13312\n"
        "v_mfma_f32_16x16x32_bf16 v[88:91], v[96:99], a[56:59], v[88:91]\n"
        "s_xor_b32 s51, s51, s86\n"
        "ds_read_b128 v[40:43], v125 offset:22528\n"
        "v_mfma_f32_16x16x32_bf16 v[56:59], a[192:195], v[96:99], v[56:59]\n"
        "s_add_u32 s55, s55, 786432\n"
        "ds_read_b128 v[44:47], v125 offset:23552\n"
        "v_mfma_f32_16x16x32_bf16 v[60:63], a[200:203], v[96:99], v[60:63]\n"
        "ds_read_b128 v[0:3], v125 offset:0\n"
        "s_waitcnt vmcnt(6)\n"
        "v_cmp_eq_u32 vcc, 0, v140\n"
        "s_waitcnt lgkmcnt(10)\n"
        "v_mfma_f32_16x16x32_bf16 v[48:51], v[16:19], v[96:99], v[48:51]\n"
        "s_cbranch_vccnz L_sw0_1%=\n"
        "L_sok_1%=:\n"
        "s_waitcnt lgkmcnt(8)\n"
        "v_mfma_f32_16x16x32_bf16 v[52:55], v[24:27], v[96:99], v[52:55]\n"
        "v_readlane_b32 s81, v140, s99\n"
        "v_max_u32_dpp v140, v140, v140 row_shr:1 row_mask:0xf bank_mask:0xf\n"
        "v_mfma_f32_16x16x32_bf16 v[84:87], v[100:103], a[52:55], v[84:87]\n"
        "ds_read_b128 v[96:99], v124 offset:14336\n"
        "v_max_u32_dpp v140, v140, v140 row_shr:2 row_mask:0xf bank_mask:0xf\n"
        "v_mfma_f32_16x16x32_bf16 v[88:91], v[100:103], a[60:63], v[88:91]\n"
        "ds_read_b128 v[4:7], v125 offset:1024\n"
        "v_max_u32_dpp v140, v140, v140 row_shr:4 row_mask:0xf bank_mask:0xf\n"
        "v_mfma_f32_16x16x32_bf16 v[56:59], a[196:199], v[100:103], v[56:59]\n"
        "buffer_load_dwordx4 v[108:111], v132, s[36:39], s56 offen offset:0 sc1\n"
        "v_max_u32_dpp v140, v140, v140 row_shr:8 row_mask:0xf bank_mask:0xf\n"
        "v_mfma_f32_16x16x32_bf16 v[60:63], a[204:207], v[100:103], v[60:63]\n"
        "ds_read_b128 v[8:11], v125 offset:2048\n"
        "v_max_u32_dpp v140, v140, v140 row_bcast:15 row_mask:0xa bank_mask:0xf\n"
        "v_mfma_f32_16x16x32_bf16 v[48:51], v[20:23], v[100:103], v[48:51]\n"
        "buffer_load_dwordx4 v[112:115], v132, s[36:39], s64 offen offset:0 sc1\n"
        "v_max_u32_dpp v140, v140, v140 row_bcast:31 row_mask:0xc bank_mask:0xf\n"
        "s_waitcnt lgkmcnt(10)\n"
        "v_mfma_f32_16x16x32_bf16 v[52:55], v[28:31], v[100:103], v[52:55]\n"
        "v_readlane_b32 s33, v140, 63\n"
        "s_lshl1_add_u32 s28, s28, s81\n"
        "s_waitcnt lgkmcnt(9)\n"
        "v_mfma_f32_16x16x32_bf16 v[84:87], v[92:95], a[64:67], v[84:87]\n"
        "ds_read_b128 v[100:103], v124 offset:15360\n"
        "s_add_u32 s28, s28, 36\n"
        "s_sub_i32 s33, s33, s28\n"
        "v_mfma_f32_16x16x32_bf16 v[88:91], v[92:95], a[72:75], v[88:91]\n"
        "s_min_i32 s33, s33, 500\n"
        "s_mov_b32 s28, 0\n"
        "L_dl_1%=:\n"
        "s_sub_i32 s33, s33, 3\n"
        "s_cmp_gt_i32 s33, 0\n"
        "s_cbranch_scc1 L_dl_1%=\n"
        "v_mfma_f32_16x16x32_bf16 v[56:59], a[208:211], v[92:95], v[56:59]\n"
        "s_add_u32 s54, s54, 786432\n"
        "ds_read_b128 v[12:15], v125 offset:3072\n"
        "v_mfma_f32_16x16x32_bf16 v[60:63], a[216:219], v[92:95], v[60:63]\n"
        "s_add_u32 s60, s60, 131072\n"
        "buffer_load_dwordx4 v[80:83], v138, s[36:39], s50 offen offset:0 sc1\n"
        "s_waitcnt lgkmcnt(10)\n"
        "v_mfma_f32_16x16x32_bf16 v[48:51], v[32:35], v[92:95], v[48:51]\n"
        "s_add_u32 s58, s58, 36864\n"
        "ds_read_b128 v[16:19], v125 offset:4096\n"
        "s_waitcnt lgkmcnt(8)\n"
        "v_mfma_f32_16x16x32_bf16 v[52:55], v[40:43], v[92:95], v[52:55]\n"
        "buffer_load_dwordx4 v[64:67], v138, s[36:39], s49 offen offset:0 sc1\n"
        "v_mfma_f32_16x16x32_bf16 v[84:87], v[104:107], a[68:71], v[84:87]\n"
        "s_xor_b32 s66, s51, 1024\n"
        "ds_read_b128 v[92:95], v124 offset:8192\n"
        "v_mfma_f32_16x16x32_bf16 v[88:91], v[104:107], a[76:79], v[88:91]\n"
        "s_xor_b32 s67, s51, 8192\n"
        "buffer_load_dwordx4 v[68:71], v138, s[36:39], s49 offen offset:256 sc1\n"
        "v_mfma_f32_16x16x32_bf16 v[56:59], a[212:215], v[104:107], v[56:59]\n"
        "s_xor_b32 s68, s51, 9216\n"
        "ds_read_b128 v[20:23], v125 offset:5120\n"
        "v_mfma_f32_16x16x32_bf16 v[60:63], a[220:223], v[104:107], v[60:63]\n"
        "s_sub_u32 s75, 127, s84\n"
        "buffer_load_dwordx4 v[72:75], v138, s[36:39], s65 offen offset:0 sc1\n"
        "v_mfma_f32_16x16x32_bf16 v[48:51], v[36:39], v[104:107], v[48:51]\n"
        "s_sub_u32 s101, s77, s75\n"
        "ds_read_b128 v[24:27], v125 offset:6144\n"
        "s_waitcnt lgkmcnt(10)\n"
        "v_mfma_f32_16x16x32_bf16 v[52:55], v[44:47], v[104:107], v[52:55]\n"
        "buffer_load_dwordx4 v[76:79], v138, s[36:39], s65 offen offset:256 sc1\n"
        "s_waitcnt lgkmcnt(8)\n"
        "v_mfma_f32_16x16x32_bf16 v[84:87], v[96:99], a[80:83], v[84:87]\n"
        "s_mul_i32 s35, s75, 0x800080\n"
        "ds_read_b128 v[104:107], v124 offset:9216\n"
        "v_mfma_f32_16x16x32_bf16 v[88:91], v[96:99], a[88:91], v[88:91]\n"
        "s_lshl_b32 s80, s75, 23\n"
        "ds_read_b128 v[28:31], v125 offset:7168\n"
        "v_mfma_f32_16x16x32_bf16 v[56:59], a[224:227], v[96:99], v[56:59]\n"
        "s_sub_u32 s73, 0x3e0293ee, s80\n"
        "ds_read_b128 v[32:35], v125 offset:8192\n"
        "v_mfma_f32_16x16x32_bf16 v[60:63], a[232:235], v[96:99], v[60:63]\n"
        "s_sub_u32 s74, 0x3f800000, s80\n"
        "ds_read_b128 v[36:39], v125 offset:9216\n"
        "v_mfma_f32_16x16x32_bf16 v[48:51], v[192:195], v[96:99], v[48:51]\n"
        "s_add_u32 s80, s75, 128\n"
        "ds_read_b128 v[40:43], v125 offset:10240\n"
        "v_mfma_f32_16x16x32_bf16 v[52:55], v[200:203], v[96:99], v[52:55]\n"
        "ds_read_b128 v[44:47], v125 offset:11264\n"
        "s_waitcnt lgkmcnt(11)\n"
        "v_mfma_f32_16x16x32_bf16 v[84:87], v[100:103], a[84:87], v[84:87]\n"
        "s_lshl_b32 s80, s80, 8\n"
        "ds_read_b128 v[96:99], v124 offset:10240\n"
        "v_mfma_f32_16x16x32_bf16 v[88:91], v[100:103], a[92:95], v[88:91]\n"
        "s_add_u32 s44, s44, 1\n"
        "s_add_u32 s61, s61, 32\n"
        "s_add_u32 s62, s80, 1\n"
        "v_mfma_f32_16x16x32_bf16 v[56:59], a[228:231], v[100:103], v[56:59]\n"
        "s_branch L_rot_1%=\n"
        "L_light_2%=:\n"
        "v_mfma_f32_16x16x32_bf16 v[52:55], v[252:255], v[104:107], v[52:55]\n"
        "v_mfma_f32_16x16x32_bf16 v[84:87], v[96:99], a[112:115], v[84:87]\n"
        "v_mfma_f32_16x16x32_bf16 v[88:91], v[96:99], a[120:123], v[88:91]\n"
        "v_cvt_pk_bf16_f32 v48, v48, v52\n"
        "v_cvt_pk_bf16_f32 v49, v49, v53\n"
        "v_mfma_f32_16x16x32_bf16 v[84:87], v[100:103], a[116:119], v[84:87]\n"
        "v_cvt_pk_bf16_f32 v50, v50, v54\n"
        "v_cvt_pk_bf16_f32 v51, v51, v55\n"
        "v_mfma_f32_16x16x32_bf16 v[88:91], v[100:103], a[124:127], v[88:91]\n"
        "v_mfma_f32_16x16x32_bf16 v[84:87], v[92:95], a[128:131], v[84:87]\n"
        "v_mfma_f32_16x16x32_bf16 v[88:91], v[92:95], a[136:139], v[88:91]\n"
        "buffer_store_dwordx4 v[48:51], v124, s[36:39], s54 offen offset:1024 sc1\n"
        "v_mfma_f32_16x16x32_bf16 v[84:87], v[104:107], a[132:135], v[84:87]\n"
        "v_mfma_f32_16x16x32_bf16 v[88:91], v[104:107], a[140:143], v[88:91]\n"
        "v_mfma_f32_16x16x32_bf16 v[56:59], v[32:35], v[92:95], v[56:59]\n"
        "v_mfma_f32_16x16x32_bf16 v[60:63], v[40:43], v[92:95], v[60:63]\n"
        "v_cvt_pk_bf16_f32 v84, v84, v85\n"
        "v_cvt_pk_bf16_f32 v85, v86, v87\n"
        "v_mfma_f32_16x16x32_bf16 v[56:59], v[16:19], v[96:99], v[56:59]\n"
        "v_cvt_pk_bf16_f32 v86, v88, v89\n"
        "v_cvt_pk_bf16_f32 v87, v90, v91\n"
        "v_mfma_f32_16x16x32_bf16 v[60:63], v[24:27], v[96:99], v[60:63]\n"
        "v_mfma_f32_16x16x32_bf16 v[56:59], v[20:23], v[100:103], v[56:59]\n"
        "v_mov_b64 v[92:93], 0\n"
        "v_mov_b64 v[94:95], 0\n"
        "v_mfma_f32_16x16x32_bf16 v[60:63], v[28:31], v[100:103], v[60:63]\n"
        "buffer_store_dwordx4 v[84:87], v124, s[36:39], s54 offen offset:2048 sc1\n"
        "v_mov_b64 v[96:97], 0\n"
        "v_mfma_f32_16x16x32_bf16 v[56:59], v[36:39], v[104:107], v[56:59]\n"
        "v_mov_b64 v[98:99], 0\n"
        "v_mfma_f32_16x16x32_bf16 v[60:63], v[44:47], v[104:107], v[60:63]\n"
        "s_nop 7\n"
        "v_cvt_pk_bf16_f32 v100, v56, v60\n"
        "v_cvt_pk_bf16_f32 v101, v57, v61\n"
        "v_cvt_pk_bf16_f32 v102, v58, v62\n"
        "v_cvt_pk_bf16_f32 v103, v59, v63\n"
        "buffer_store_dwordx4 v[100:103], v124, s[36:39], s54 offen offset:0 sc1\n"
        "s_barrier\n"
        "s_add_u32 s69, s58, s82\n"
        "s_sub_u32 s72, s58, s82\n"
        "s_add_u32 s87, s58, 8192\n"
        "s_waitcnt vmcnt(6)\n"
        "buffer_load_dwordx4 v[64:67], v124, s[36:39], s69 offen offset:0 sc1\n"
        "s_waitcnt vmcnt(6)\n"
        "buffer_load_dwordx4 v[68:71], v124, s[36:39], s69 offen offset:1024 sc1\n"
        "s_waitcnt vmcnt(6)\n"
        "buffer_load_dwordx4 v[72:75], v124, s[36:39], s72 offen offset:2048 sc1\n"
        "s_waitcnt vmcnt(6)\n"
        "buffer_load_dwordx4 v[76:79], v124, s[36:39], s72 offen offset:3072 sc1\n"
        "buffer_load_dwordx4 v[80:83], v124, s[36:39], s87 offen offset:0 sc1\n"
        "s_mov_b32 s81, 64\n"
        "buffer_load_dword v107, off, s[36:39], s81 sc1\n"
        "s_memrealtime s[26:27]\n"
        "s_waitcnt vmcnt(6)\n"
        "s_waitcnt lgkmcnt(0)\n"
        "s_barrier\n"
        "s_add_u32 s29, s29, 1024\n"
        "s_waitcnt vmcnt(0)\n"
        "L_par_2%=:\n"
        "v_readfirstlane_b32 s80, v107\n"
        "s_nop 0\n"
        "s_cmp_lt_i32 s80, 0\n"
        "s_cbranch_scc1 L_par2_2%=\n"
        "s_add_u32 s63, s63, 1\n"
        "s_cmp_gt_u32 s63, 0x40000\n"
        "s_cbranch_scc1 L_abort%=\n"
        "s_sleep 4\n"
        "buffer_load_dword v107, off, s[36:39], s81 sc1\n"
        "s_waitcnt vmcnt(0)\n"
        "s_branch L_par_2%=\n"
        "L_par2_2%=:\n"
        "s_bfe_u32 s46, s80, 0x10018\n"
        "s_lshl_b32 s46, s46, 21\n"
        "s_mov_b32 s53, 0x200000\n"
        "s_bfe_u32 s84, s80, 0x80010\n"
        "s_sub_u32 s84, 255, s84\n"
        "s_bfe_u32 s76, s80, 0x80008\n"
        "s_sub_u32 s76, s76, 32\n"
        "s_max_i32 s76, s76, 66\n"
        "s_bfe_u32 s77, s80, 0x80000\n"
        "s_sub_u32 s77, s77, 158\n"
        "s_bitset0_b32 s49, 30\n"
        "s_bitset0_b32 s50, 30\n"
        "s_bitset0_b32 s56, 30\n"
        "s_bitset0_b32 s64, 30\n"
        "v_mov_b32 v140, 0\n"
        "s_mov_b32 s28, 1000\n"
        "s_branch L_xok_2%=\n"
        "L_x2_2%=:\n"
        "s_add_u32 s69, s58, s82\n"
        "s_sub_u32 s72, s58, s82\n"
        "s_add_u32 s87, s58, 8192\n"
        "buffer_load_dwordx4 v[64:67], v124, s[36:39], s69 offen offset:0 sc1\n"
        "buffer_load_dwordx4 v[68:71], v124, s[36:39], s69 offen offset:1024 sc1\n"
        "buffer_load_dwordx4 v[72:75], v124, s[36:39], s72 offen offset:2048 sc1\n"
        "buffer_load_dwordx4 v[76:79], v124, s[36:39], s72 offen offset:3072 sc1\n"
        "buffer_load_dwordx4 v[80:83], v124, s[36:39], s87 offen offset:0 sc1\n"
        "s_waitcnt vmcnt(0)\n"
        "s_memrealtime s[26:27]\n"
        "s_waitcnt lgkmcnt(0)\n"
        "s_add_u32 s28, s28, s26\n"
        "L_xok_2%=:\n"
        "v_mfma_f32_16x16x32_bf16 v[84:87], v[64:67], a[0:3], 0\n"
        "v_mfma_f32_16x16x32_bf16 v[88:91], v[64:67], a[8:11], 0\n"
        "v_mfma_f32_16x16x32_bf16 v[56:59], a[144:147], v[64:67], 0\n"
        "s_branch L_win_2%=\n"
        "L_retry_2%=:\n"
        "s_add_u32 s63, s63, 1\n"
        "s_cmp_gt_u32 s63, 0x40000\n"
        "s_cbranch_scc1 L_abort%=\n"
        "s_sleep 2\n"
        "s_waitcnt vmcnt(0)\n"
        "s_waitcnt lgkmcnt(0)\n"
        "buffer_load_dwordx4 v[80:83], v138, s[36:39], s50 offen offset:0 sc1\n"
        "buffer_load_dwordx4 v[64:67], v138, s[36:39], s49 offen offset:0 sc1\n"
        "buffer_load_dwordx4 v[68:71], v138, s[36:39], s49 offen offset:256 sc1\n"
        "buffer_load_dwordx4 v[72:75], v138, s[36:39], s65 offen offset:0 sc1\n"
        "buffer_load_dwordx4 v[76:79], v138, s[36:39], s65 offen offset:256 sc1\n"
        "s_waitcnt vmcnt(4)\n"
        "v_bitop3_b32 v123, 32, v80, v82 bitop3:0x80\n"
        "v_add_u32_dpp v80, v81, v80 row_half_mirror row_mask:0xf bank_mask:0xf\n"
        "v_add_u32_dpp v82, v83, v82 row_half_mirror row_mask:0xf bank_mask:0xf\n"
        "v_cvt_f32_i32 v80, v80\n"
        "v_cvt_f32_i32 v82, v82\n"
        "v_cvt_pk_bf16_f32 v52, v80, v82\n"
        "ds_write_b32 v137, v52\n"
        "s_waitcnt vmcnt(3)\n"
        "v_bitop3_b32 v123, v123, v64, v66 bitop3:0x80\n"
        "v_cvt_f32_i32 v64, v64\n"
        "v_cvt_f32_i32 v65, v65\n"
        "v_cvt_f32_i32 v66, v66\n"
        "v_cvt_f32_i32 v67, v67\n"
        "v_cvt_pk_bf16_f32 v52, v64, v65\n"
        "v_cvt_pk_bf16_f32 v53, v66, v67\n"
        "ds_write_b64 v126, v[52:53] offset:0\n"
        "s_waitcnt vmcnt(2)\n"
        "v_bitop3_b32 v123, v123, v68, v70 bitop3:0x80\n"
        "v_cvt_f32_i32 v68, v68\n"
        "v_cvt_f32_i32 v69, v69\n"
        "v_cvt_f32_i32 v70, v70\n"
        "v_cvt_f32_i32 v71, v71\n"
        "v_cvt_pk_bf16_f32 v54, v68, v69\n"
        "v_cvt_pk_bf16_f32 v55, v70, v71\n"
        "ds_write_b64 v126, v[54:55] offset:512\n"
        "s_waitcnt vmcnt(1)\n"
        "v_bitop3_b32 v123, v123, v72, v74 bitop3:0x80\n"
        "v_cvt_f32_i32 v72, v72\n"
        "v_cvt_f32_i32 v73, v73\n"
        "ds_read_b128 v[48:51], v124 offset:4096\n"
        "v_cvt_f32_i32 v74, v74\n"
        "v_cvt_f32_i32 v75, v75\n"
        "v_cvt_pk_bf16_f32 v52, v72, v73\n"
        "v_cvt_pk_bf16_f32 v53, v74, v75\n"
        "ds_write_b64 v126, v[52:53] offset:1024\n"
        "s_waitcnt vmcnt(0)\n"
        "v_bitop3_b32 v123, v123, v76, v78 bitop3:0x80\n"
        "v_cmp_eq_u32 vcc, 0, v123\n"
        "v_mov_b64 v[122:123], 0\n"
        "v_cvt_f32_i32 v76, v76\n"
        "v_cvt_f32_i32 v77, v77\n"
        "v_cvt_f32_i32 v78, v78\n"
        "v_cvt_f32_i32 v79, v79\n"
        "v_cvt_pk_bf16_f32 v54, v76, v77\n"
        "v_cvt_pk_bf16_f32 v55, v78, v79\n"
        "ds_write_b64 v126, v[54:55] offset:1536\n"
        "v_mov_b64 v[92:93], 0\n"
        "v_mov_b64 v[94:95], 0\n"
        "v_mov_b64 v[96:97], 0\n"
        "v_mov_b64 v[98:99], 0\n"
        "s_waitcnt lgkmcnt(0)\n"
        "s_branch L_dec_2%=\n"
        "L_sw0_2%=:\n"
        "s_memrealtime s[26:27]\n"
        "s_waitcnt lgkmcnt(0)\n"
        "s_sub_u32 s28, s28, s26\n"
        "L_sw_2%=:\n"
        "s_add_u32 s63, s63, 1\n"
        "s_cmp_gt_u32 s63, 0x40000\n"
        "s_cbranch_scc1 L_abort%=\n"
        "s_sleep 2\n"
        "buffer_load_dword v140, v124, s[36:39], s29 offen sc1\n"
        "s_waitcnt vmcnt(0)\n"
        "v_cmp_eq_u32 vcc, 0, v140\n"
        "s_cbranch_vccnz L_sw_2%=\n"
        "s_memrealtime s[26:27]\n"
        "s_waitcnt lgkmcnt(0)\n"
        "s_add_u32 s28, s28, s26\n"
        "s_branch L_sok_2%=\n"
        ".p2align 6\n"
        "L_rot_2%=:\n"
        "v_mfma_f32_16x16x32_bf16 v[60:63], a[236:239], v[100:103], v[60:63]\n"
        "v_mfma_f32_16x16x32_bf16 v[48:51], v[196:199], v[100:103], v[48:51]\n"
        "v_mfma_f32_16x16x32_bf16 v[52:55], v[204:207], v[100:103], v[52:55]\n"
        "s_waitcnt lgkmcnt(10)\n"
        "v_mfma_f32_16x16x32_bf16 v[84:87], v[92:95], a[96:99], v[84:87]\n"
        "ds_read_b128 v[100:103], v124 offset:7168\n"
        "v_mfma_f32_16x16x32_bf16 v[88:91], v[92:95], a[104:107], v[88:91]\n"
        "v_mfma_f32_16x16x32_bf16 v[56:59], v[0:3], v[92:95], v[56:59]\n"
        "v_mfma_f32_16x16x32_bf16 v[60:63], v[8:11], v[92:95], v[60:63]\n"
        "v_mfma_f32_16x16x32_bf16 v[48:51], v[208:211], v[92:95], v[48:51]\n"
        "s_waitcnt lgkmcnt(0)\n"
        "s_barrier\n"
        "v_mfma_f32_16x16x32_bf16 v[52:55], v[216:219], v[92:95], v[52:55]\n"
        "v_mov_b64 v[116:117], 0\n"
        "v_mfma_f32_16x16x32_bf16 v[84:87], v[104:107], a[100:103], v[84:87]\n"
        "ds_read_b128 v[92:95], v124 offset:16384\n"
        "v_mov_b64 v[118:119], 0\n"
        "v_mfma_f32_16x16x32_bf16 v[88:91], v[104:107], a[108:111], v[88:91]\n"
        "v_mov_b64 v[120:121], 0\n"
        "v_mfma_f32_16x16x32_bf16 v[56:59], v[4:7], v[104:107], v[56:59]\n"
        "v_mfma_f32_16x16x32_bf16 v[60:63], v[12:15], v[104:107], v[60:63]\n"
        "v_mfma_f32_16x16x32_bf16 v[48:51], v[212:215], v[104:107], v[48:51]\n"
        "v_mfma_f32_16x16x32_bf16 v[52:55], v[220:223], v[104:107], v[52:55]\n"
        "v_mfma_f32_16x16x32_bf16 v[48:51], v[224:227], v[96:99], v[48:51]\n"
        "ds_read_b128 v[104:107], v124 offset:17408\n"
        "v_mfma_f32_16x16x32_bf16 v[52:55], v[232:235], v[96:99], v[52:55]\n"
        "v_mfma_f32_16x16x32_bf16 v[48:51], v[228:231], v[100:103], v[48:51]\n"
        "v_mfma_f32_16x16x32_bf16 v[52:55], v[236:239], v[100:103], v[52:55]\n"
        "s_waitcnt lgkmcnt(1)\n"
        "v_mfma_f32_16x16x32_bf16 v[48:51], v[240:243], v[92:95], v[48:51]\n"
        "v_mfma_f32_16x16x32_bf16 v[52:55], v[248:251], v[92:95], v[52:55]\n"
        "s_waitcnt lgkmcnt(0)\n"
        "v_mfma_f32_16x16x32_bf16 v[48:51], v[244:247], v[104:107], v[48:51]\n"
        "s_cmp_lt_i32 s44, 0\n"
        "s_cbranch_scc1 L_light_2%=\n"
        "v_mfma_f32_16x16x32_bf16 v[52:55], v[252:255], v[104:107], v[52:55]\n"
        "s_waitcnt vmcnt(4)\n"
        "v_bitop3_b32 v123, 32, v80, v82 bitop3:0x80\n"
        "s_nop 0\n"
        "v_add_u32_dpp v80, v81, v80 row_half_mirror row_mask:0xf bank_mask:0xf\n"
        "v_mfma_f32_16x16x32_bf16 v[84:87], v[96:99], a[112:115], v[84:87]\n"
        "s_nop 0\n"
        "v_add_u32_dpp v82, v83, v82 row_half_mirror row_mask:0xf bank_mask:0xf\n"
        "v_mfma_f32_16x16x32_bf16 v[88:91], v[96:99], a[120:123], v[88:91]\n"
        "v_cvt_pk_bf16_f32 v48, v48, v52\n"
        "v_cvt_pk_bf16_f32 v49, v49, v53\n"
        "s_nop 0\n"
        "v_mfma_f32_16x16x32_bf16 v[84:87], v[100:103], a[116:119], v[84:87]\n"
        "v_cvt_pk_bf16_f32 v50, v50, v54\n"
        "v_cvt_pk_bf16_f32 v51, v51, v55\n"
        "v_mfma_f32_16x16x32_bf16 v[88:91], v[100:103], a[124:127], v[88:91]\n"
        "v_cvt_f32_i32 v80, v80\n"
        "s_nop 0\n"
        "v_cvt_f32_i32 v82, v82\n"
        "v_mfma_f32_16x16x32_bf16 v[84:87], v[92:95], a[128:131], v[84:87]\n"
        "s_nop 0\n"
        "v_cvt_pk_bf16_f32 v52, v80, v82\n"
        "v_mfma_f32_16x16x32_bf16 v[88:91], v[92:95], a[136:139], v[88:91]\n"
        "buffer_store_dwordx4 v[48:51], v124, s[36:39], s54 offen offset:1024 sc1\n"
        "v_mfma_f32_16x16x32_bf16 v[84:87], v[104:107], a[132:135], v[84:87]\n"
        "s_nop 0\n"
        "v_mfma_f32_16x16x32_bf16 v[88:91], v[104:107], a[140:143], v[88:91]\n"
        "ds_write_b32 v137, v52\n"
        "s_waitcnt vmcnt(4)\n"
        "v_bitop3_b32 v123, v123, v64, v66 bitop3:0x80\n"
        "v_cvt_f32_i32 v64, v64\n"
        "v_mfma_f32_16x16x32_bf16 v[56:59], v[32:35], v[92:95], v[56:59]\n"
        "v_cvt_f32_i32 v65, v65\n"
        "v_cvt_f32_i32 v66, v66\n"
        "v_mfma_f32_16x16x32_bf16 v[60:63], v[40:43], v[92:95], v[60:63]\n"
        "v_cvt_pk_bf16_f32 v84, v84, v85\n"
        "v_cvt_pk_bf16_f32 v85, v86, v87\n"
        "v_mfma_f32_16x16x32_bf16 v[56:59], v[16:19], v[96:99], v[56:59]\n"
        "v_cvt_pk_bf16_f32 v86, v88, v89\n"
        "v_cvt_pk_bf16_f32 v87, v90, v91\n"
        "v_mfma_f32_16x16x32_bf16 v[60:63], v[24:27], v[96:99], v[60:63]\n"
        "v_cvt_f32_i32 v67, v67\n"
        "v_cvt_pk_bf16_f32 v52, v64, v65\n"
        "v_mfma_f32_16x16x32_bf16 v[56:59], v[20:23], v[100:103], v[56:59]\n"
        "v_mov_b64 v[92:93], 0\n"
        "v_mov_b64 v[94:95], 0\n"
        "v_mfma_f32_16x16x32_bf16 v[60:63], v[28:31], v[100:103], v[60:63]\n"
        "buffer_store_dwordx4 v[84:87], v124, s[36:39], s54 offen offset:2048 sc1\n"
        "v_mov_b64 v[96:97], 0\n"
        "v_cvt_pk_bf16_f32 v53, v66, v67\n"
        "v_mfma_f32_16x16x32_bf16 v[56:59], v[36:39], v[104:107], v[56:59]\n"
        "v_mov_b64 v[98:99], 0\n"
        "ds_write_b64 v126, v[52:53] offset:0\n"
        "s_waitcnt vmcnt(4)\n"
        "v_bitop3_b32 v123, v123, v68, v70 bitop3:0x80\n"
        "v_mfma_f32_16x16x32_bf16 v[60:63], v[44:47], v[104:107], v[60:63]\n"
        "v_cvt_f32_i32 v68, v68\n"
        "v_cvt_f32_i32 v69, v69\n"
        "v_cvt_f32_i32 v70, v70\n"
        "v_cvt_f32_i32 v71, v71\n"
        "v_cvt_pk_bf16_f32 v54, v68, v69\n"
        "v_cvt_pk_bf16_f32 v55, v70, v71\n"
        "ds_write_b64 v126, v[54:55] offset:512\n"
        "s_waitcnt vmcnt(3)\n"
        "v_bitop3_b32 v123, v123, v72, v74 bitop3:0x80\n"
        "v_cvt_f32_i32 v72, v72\n"
        "v_cvt_pk_bf16_f32 v100, v56, v60\n"
        "v_cvt_pk_bf16_f32 v101, v57, v61\n"
        "v_cvt_pk_bf16_f32 v102, v58, v62\n"
        "v_cvt_pk_bf16_f32 v103, v59, v63\n"
        "v_cvt_f32_i32 v73, v73\n"
        "ds_read_b128 v[48:51], v124 offset:4096\n"
        "v_cvt_f32_i32 v74, v74\n"
        "v_cvt_f32_i32 v75, v75\n"
        "v_cvt_pk_bf16_f32 v52, v72, v73\n"
        "v_cvt_pk_bf16_f32 v53, v74, v75\n"
        "ds_write_b64 v126, v[52:53] offset:1024\n"
        "s_waitcnt vmcnt(2)\n"
        "v_bitop3_b32 v123, v123, v76, v78 bitop3:0x80\n"
        "v_cmp_eq_u32 vcc, 0, v123\n"
        "v_mov_b64 v[122:123], 0\n"
        "v_cvt_f32_i32 v76, v76\n"
        "v_cvt_f32_i32 v77, v77\n"
        "v_cvt_f32_i32 v78, v78\n"
        "v_cvt_f32_i32 v79, v79\n"
        "v_cvt_pk_bf16_f32 v54, v76, v77\n"
        "v_cvt_pk_bf16_f32 v55, v78, v79\n"
        "ds_write_b64 v126, v[54:55] offset:1536\n"
        "L_dec_2%=:\n"
        "s_waitcnt lgkmcnt(2)\n"
        "v_smfmac_f32_16x16x64_bf16 v[92:95], v[48:51], a[144:151], v134\n"
        "ds_read_b128 v[52:55], v124 offset:5120\n"
        "v_max3_f32 v104, |v80|, |v82|, |v64|\n"
        "v_smfmac_f32_16x16x64_bf16 v[96:99], v[48:51], a[152:159], v134\n"
        "s_cbranch_vccnz L_retry_2%=\n"
        "v_max3_f32 v104, |v65|, |v66|, v104\n"
        "v_smfmac_f32_16x16x64_bf16 v[116:119], v[48:51], a[240:247], v134\n"
        "s_add_u32 s29, s29, 1024\n"
        "v_max3_f32 v104, |v67|, |v68|, v104\n"
        "v_smfmac_f32_16x16x64_bf16 v[120:123], v[48:51], a[248:255], v134\n"
        "s_waitcnt lgkmcnt(0)\n"
        "v_max3_f32 v104, |v69|, |v70|, v104\n"
        "s_barrier\n"
        "ds_read_b128 v[56:59], v124 offset:2048\n"
        "v_smfmac_f32_16x16x64_bf16 v[92:95], v[52:55], a[160:167], v134\n"
        "ds_read_b128 v[60:63], v124 offset:1024\n"
        "v_max3_f32 v104, |v71|, |v72|, v104\n"
        "v_smfmac_f32_16x16x64_bf16 v[96:99], v[52:55], a[168:175], v134\n"
        "buffer_load_dwordx4 v[88:91], v124, s[36:39], s55 offen offset:3072 sc1\n"
        "ds_read_b128 v[80:83], v124 offset:3072\n"
        "v_max3_f32 v104, |v73|, |v74|, v104\n"
        "v_smfmac_f32_16x16x64_bf16 v[116:119], v[52:55], v[176:183], v134\n"
        "ds_read_b128 v[64:67], v124 offset:8192\n"
        "v_max3_f32 v104, |v75|, |v76|, v104\n"
        "v_smfmac_f32_16x16x64_bf16 v[120:123], v[52:55], v[184:191], v134\n"
        "buffer_load_dwordx4 v[84:87], v124, s[36:39], s55 offen offset:0 sc1\n"
        "ds_read_b128 v[68:71], v124 offset:6144\n"
        "v_max3_f32 v104, |v77|, |v78|, v104\n"
        "s_waitcnt lgkmcnt(4)\n"
        "v_smfmac_f32_16x16x64_bf16 v[92:95], v[56:59], v[0:7], v134\n"
        "ds_read_b128 v[72:75], v124 offset:7168\n"
        "v_max_f32_e64 v104, |v79|, v104\n"
        "v_smfmac_f32_16x16x64_bf16 v[96:99], v[56:59], v[8:15], v134\n"
        "ds_read_b128 v[0:3], v125 offset:12288\n"
        "v_max_f32_dpp v104, v104, v104 row_shr:1 row_mask:0xf bank_mask:0xf\n"
        "s_waitcnt lgkmcnt(3)\n"
        "v_smfmac_f32_16x16x64_bf16 v[92:95], v[60:63], a[224:231], v134\n"
        "ds_read_b128 v[76:79], v124 offset:0\n"
        "v_max_f32_dpp v104, v104, v104 row_shr:2 row_mask:0xf bank_mask:0xf\n"
        "v_smfmac_f32_16x16x64_bf16 v[96:99], v[60:63], a[232:239], v134\n"
        "ds_read_b128 v[4:7], v125 offset:13312\n"
        "v_max_f32_dpp v104, v104, v104 row_shr:4 row_mask:0xf bank_mask:0xf\n"
        "v_smfmac_f32_16x16x64_bf16 v[92:95], v[80:83], v[16:23], v134\n"
        "ds_read_b128 v[8:11], v125 offset:14336\n"
        "v_max_f32_dpp v104, v104, v104 row_shr:8 row_mask:0xf bank_mask:0xf\n"
        "v_smfmac_f32_16x16x64_bf16 v[96:99], v[80:83], v[24:31], v134\n"
        "ds_read_b128 v[12:15], v125 offset:15360\n"
        "v_max_f32_dpp v104, v104, v104 row_bcast:15 row_mask:0xa bank_mask:0xf\n"
        "v_smfmac_f32_16x16x64_bf16 v[92:95], v[64:67], v[32:39], v134\n"
        "ds_read_b128 v[16:19], v125 offset:16384\n"
        "v_max_f32_dpp v104, v104, v104 row_bcast:31 row_mask:0xc bank_mask:0xf\n"
        "v_smfmac_f32_16x16x64_bf16 v[96:99], v[64:67], v[40:47], v134\n"
        "ds_read_b128 v[20:23], v125 offset:17408\n"
        "v_readlane_b32 s85, v104, 63\n"
        "s_waitcnt lgkmcnt(5)\n"
        "v_smfmac_f32_16x16x64_bf16 v[92:95], v[68:71], a[176:183], v134\n"
        "ds_read_b128 v[24:27], v125 offset:18432\n"
        "v_smfmac_f32_16x16x64_bf16 v[96:99], v[68:71], a[184:191], v134\n"
        "ds_read_b128 v[28:31], v125 offset:19456\n"
        "v_mov_b32 v104, s85\n"
        "v_smfmac_f32_16x16x64_bf16 v[92:95], v[72:75], a[192:199], v134\n"
        "ds_read_b128 v[32:35], v125 offset:20480\n"
        "v_smfmac_f32_16x16x64_bf16 v[96:99], v[72:75], a[200:207], v134\n"
        "ds_read_b128 v[36:39], v125 offset:21504\n"
        "s_add_u32 s69, s58, s82\n"
        "v_smfmac_f32_16x16x64_bf16 v[92:95], v[76:79], a[208:215], v134\n"
        "ds_read_b128 v[40:43], v125 offset:22528\n"
        "s_sub_u32 s72, s58, s82\n"
        "v_smfmac_f32_16x16x64_bf16 v[96:99], v[76:79], a[216:223], v134\n"
        "ds_read_b128 v[44:47], v125 offset:23552\n"
        "s_add_u32 s87, s58, 8192\n"
        "v_smfmac_f32_16x16x64_bf16 v[116:119], v[64:67], v[240:247], v134\n"
        "ds_write_b32 v142, v104\n"
        "v_add_f32 v92, v92, v94\n"
        "v_add_f32 v93, v93, v95\n"
        "v_smfmac_f32_16x16x64_bf16 v[120:123], v[64:67], v[248:255], v134\n"
        "v_add_f32 v96, v96, v98\n"
        "v_add_f32 v97, v97, v99\n"
        "v_cvt_pk_bf16_f32 v92, v92, v96\n"
        "v_cvt_pk_bf16_f32 v93, v93, v97\n"
        "s_waitcnt lgkmcnt(3)\n"
        "v_smfmac_f32_16x16x64_bf16 v[116:119], v[68:71], v[0:7], v134\n"
        "ds_write2_b32 v127, v92, v93 offset1:72\n"
        "v_smfmac_f32_16x16x64_bf16 v[120:123], v[68:71], v[8:15], v134\n"
        "v_mov_b64 v[98:99], 0\n"
        "v_smfmac_f32_16x16x64_bf16 v[116:119], v[72:75], v[16:23], v134\n"
        "v_mov_b64 v[96:97], 0\n"
        "v_smfmac_f32_16x16x64_bf16 v[120:123], v[72:75], v[24:31], v134\n"
        "s_waitcnt vmcnt(3)\n"
        "s_waitcnt lgkmcnt(0)\n"
        "v_mov_b64 v[106:107], 0\n"
        "s_barrier\n"
        "ds_read_b128 v[0:3], v128 offset:0\n"
        "v_smfmac_f32_16x16x64_bf16 v[116:119], v[76:79], v[32:39], v134\n"
        "ds_read_b128 v[24:27], v124 offset:20480\n"
        "v_mov_b64 v[104:105], 0\n"
        "v_smfmac_f32_16x16x64_bf16 v[120:123], v[76:79], v[40:47], v134\n"
        "ds_read_b128 v[4:7], v128 offset:64\n"
        "v_mov_b64 v[94:95], 0\n"
        "v_smfmac_f32_16x16x64_bf16 v[116:119], v[60:63], v[192:199], v134\n"
        "ds_read_b128 v[8:11], v128 offset:128\n"
        "v_mov_b64 v[92:93], 0\n"
        "v_smfmac_f32_16x16x64_bf16 v[120:123], v[60:63], v[200:207], v134\n"
        "ds_read_b128 v[12:15], v128 offset:192\n"
        "v_mov_b64 v[20:21], 0\n"
        "v_smfmac_f32_16x16x64_bf16 v[116:119], v[56:59], v[208:215], v134\n"
        "s_waitcnt lgkmcnt(3)\n"
        "v_max3_f32 v24, v24, v25, v26\n"
        "s_waitcnt lgkmcnt(2)\n"
        "v_smfmac_f32_16x16x64_bf16 v[104:107], v[108:111], v[0:7], v134\n"
        "v_max_f32 v24, v24, v27\n"
        "v_smfmac_f32_16x16x64_bf16 v[120:123], v[56:59], v[216:223], v134\n"
        "v_readfirstlane_b32 s84, v24\n"
        "s_waitcnt lgkmcnt(0)\n"
        "v_smfmac_f32_16x16x64_bf16 v[104:107], v[112:115], v[8:15], v134\n"
        "v_mov_b64 v[22:23], 0\n"
        "v_smfmac_f32_16x16x64_bf16 v[116:119], v[80:83], v[224:231], v134\n"
        "s_lshr_b32 s84, s84, 23\n"
        "s_add_u32 s84, s84, s101\n"
        "v_smfmac_f32_16x16x64_bf16 v[120:123], v[80:83], v[232:239], v134\n"
        "s_max_i32 s84, s84, s76\n"
        "s_min_i32 s84, s84, 186\n"
        "v_add_f32 v104, v104, v106\n"
        "v_add_f32 v105, v105, v107\n"
        "v_smfmac_f32_16x16x64_bf16 v[92:95], v[64:67], a[128:135], v134\n"
        "ds_write_b64 v131, v[104:105]\n"
        "v_add_f32 v116, v116, v118\n"
        "v_add_f32 v117, v117, v119\n"
        "v_pk_add_f32 v[120:121], v[120:121], v[122:123]\n"
        "v_cvt_pk_bf16_f32 v116, v116, v120\n"
        "v_cvt_pk_bf16_f32 v117, v117, v121\n"
        "ds_write2_b32 v129, v116, v117 offset1:8\n"
        "v_smfmac_f32_16x16x64_bf16 v[96:99], v[64:67], a[136:143], v134\n"
        "s_mul_i32 s88, s84, 0x800080\n"
        "s_mov_b32 s89, s88\n"
        "v_smfmac_f32_16x16x64_bf16 v[92:95], v[68:71], a[32:39], v134\n"
        "buffer_store_dwordx4 v[100:103], v124, s[36:39], s54 offen offset:0 sc1\n"
        "v_mov_b64 v[104:105], s[88:89]\n"
        "v_smfmac_f32_16x16x64_bf16 v[96:99], v[68:71], a[40:47], v134\n"
        "s_add_u32 s90, s88, s35\n"
        "s_mov_b32 s91, s90\n"
        "v_mov_b64 v[106:107], s[90:91]\n"
        "v_smfmac_f32_16x16x64_bf16 v[92:95], v[48:51], a[0:7], v134\n"
        "s_waitcnt lgkmcnt(0)\n"
        "v_mov_b64 v[100:101], s[88:89]\n"
        "s_barrier\n"
        "ds_read_b128 v[24:27], v130\n"
        "v_smfmac_f32_16x16x64_bf16 v[96:99], v[48:51], a[8:15], v134\n"
        "ds_read_b128 v[28:31], v130 offset:1024\n"
        "v_mov_b64 v[102:103], s[90:91]\n"
        "v_smfmac_f32_16x16x64_bf16 v[92:95], v[52:55], a[16:23], v134\n"
        "ds_read_b128 v[116:119], v124 offset:13568\n"
        "v_mov_b64 v[48:49], 0\n"
        "v_smfmac_f32_16x16x64_bf16 v[96:99], v[52:55], a[24:31], v134\n"
        "buffer_load_dwordx4 v[64:67], v124, s[36:39], s69 offen offset:0 sc1\n"
        "v_mov_b64 v[50:51], 0\n"
        "v_smfmac_f32_16x16x64_bf16 v[92:95], v[56:59], a[96:103], v134\n"
        "v_mov_b64 v[52:53], 0\n"
        "v_smfmac_f32_16x16x64_bf16 v[96:99], v[56:59], a[104:111], v134\n"
        "buffer_load_dwordx4 v[68:71], v124, s[36:39], s69 offen offset:1024 sc1\n"
        "v_mov_b64 v[54:55], 0\n"
        "s_waitcnt lgkmcnt(2)\n"
        "v_smfmac_f32_16x16x64_bf16 v[20:23], v[24:27], v[0:7], v134\n"
        "v_mov_b64 v[56:57], 0\n"
        "s_waitcnt lgkmcnt(0)\n"
        "v_smfmac_f32_16x16x64_bf16 v[20:23], v[28:31], v[8:15], v134\n"
        "v_max3_f32 v121, v116, v117, v118\n"
        "v_smfmac_f32_16x16x64_bf16 v[92:95], v[72:75], a[48:55], v134\n"
        "ds_read_b128 v[0:3], v125 offset:24576\n"
        "v_mov_b64 v[58:59], 0\n"
        "v_smfmac_f32_16x16x64_bf16 v[96:99], v[72:75], a[56:63], v134\n"
        "buffer_load_dwordx4 v[72:75], v124, s[36:39], s72 offen offset:2048 sc1\n"
        "v_add_f32 v20, v20, v22\n"
        "v_add_f32 v21, v21, v23\n"
        "v_cndmask_b32 v120, v20, v21, s[92:93]\n"
        "v_mul_f32 v120, s74, v120\n"
        "v_max3_f32 v121, v121, v119, v120\n"
        "v_smfmac_f32_16x16x64_bf16 v[92:95], v[76:79], a[64:71], v134\n"
        "ds_read_b128 v[4:7], v125 offset:25600\n"
        "v_max_f32_dpp v121, v121, v121 quad_perm:[2,3,0,1] row_mask:0xf bank_mask:0xf\n"
        "v_mov_b32 v122, v121\n"
        "v_smfmac_f32_16x16x64_bf16 v[96:99], v[76:79], a[72:79], v134\n"
        "buffer_load_dwordx4 v[76:79], v124, s[36:39], s72 offen offset:3072 sc1\n"
        "v_permlane16_swap_b32 v121, v122\n"
        "v_max_f32 v121, v121, v122\n"
        "v_mov_b32 v122, v121\n"
        "v_smfmac_f32_16x16x64_bf16 v[92:95], v[80:83], a[112:119], v134\n"
        "ds_read_b128 v[8:11], v125 offset:26624\n"
        "v_permlane32_swap_b32 v121, v122\n"
        "v_smfmac_f32_16x16x64_bf16 v[96:99], v[80:83], a[120:127], v134\n"
        "buffer_load_dwordx4 v[80:83], v124, s[36:39], s87 offen offset:0 sc1\n"
        "v_max_f32 v121, v121, v122\n"
        "v_mul_f32_e64 v123, -s73, v121\n"
        "v_fma_f32 v120, v120, s73, v123\n"
        "v_fma_f32 v116, v116, s73, v123\n"
        "v_smfmac_f32_16x16x64_bf16 v[92:95], v[60:63], a[80:87], v134\n"
        "ds_read_b128 v[12:15], v125 offset:27648\n"
        "v_fma_f32 v117, v117, s73, v123\n"
        "v_fma_f32 v118, v118, s73, v123\n"
        "v_fma_f32 v119, v119, s73, v123\n"
        "v_exp_f32 v120, v120\n"
        "v_smfmac_f32_16x16x64_bf16 v[96:99], v[60:63], a[88:95], v134\n"
        "ds_read_b128 v[16:19], v125 offset:28672\n"
        "v_exp_f32 v116, v116\n"
        "v_mul_f32 v120, s74, v120\n"
        "v_exp_f32 v117, v117\n"
        "v_exp_f32 v118, v118\n"
        "v_exp_f32 v119, v119\n"
        "s_waitcnt vmcnt(6)\n"
        "v_mov_b64 v[108:109], v[86:87]\n"
        "v_mov_b64 v[112:113], v[90:91]\n"
        "v_cvt_pk_bf16_f32 v120, v120, v120\n"
        "v_cvt_pk_bf16_f32 v60, v116, v117\n"
        "v_cvt_pk_bf16_f32 v62, v118, v119\n"
        "v_and_b32 v61, v120, v135\n"
        "v_and_b32 v63, v120, v136\n"
        "v_pk_add_f32 v[92:93], v[92:93], v[94:95]\n"
        "v_pk_add_f32 v[96:97], v[96:97], v[98:99]\n"
        "v_smfmac_f32_16x16x64_bf16 v[56:59], v[60:63], v[100:107], v134\n"
        "ds_read_b128 v[20:23], v125 offset:29696\n"
        "v_cvt_pk_bf16_f32 v86, v92, v93\n"
        "v_cvt_pk_bf16_f32 v87, v92, v93\n"
        "v_mov_b64 v[90:91], v[86:87]\n"
        "v_cvt_pk_bf16_f32 v110, v96, v97\n"
        "v_cvt_pk_bf16_f32 v111, v96, v97\n"
        "v_smfmac_f32_16x16x64_bf16 v[48:51], v[60:63], v[84:91], v134\n"
        "ds_read_b128 v[28:31], v125 offset:31744\n"
        "v_mov_b64 v[114:115], v[110:111]\n"
        "v_add_f32 v56, v56, v58\n"
        "v_add_f32 v57, v57, v59\n"
        "v_smfmac_f32_16x16x64_bf16 v[52:55], v[60:63], v[108:115], v134\n"
        "s_nop 1\n"
        "v_rcp_f32 v56, v56\n"
        "v_rcp_f32 v57, v57\n"
        "v_pk_add_f32 v[48:49], v[48:49], v[50:51]\n"
        "v_pk_mul_f32 v[48:49], v[48:49], v[56:57]\n"
        "v_mov_b32 v110, v86\n"
        "v_mov_b64 v[92:93], s[70:71]\n"
        "v_pk_add_f32 v[52:53], v[52:53], v[54:55]\n"
        "v_pk_mul_f32 v[52:53], v[52:53], v[56:57]\n"
        "v_cvt_pk_bf16_f32 v48, v48, v52\n"
        "v_cvt_pk_bf16_f32 v49, v49, v53\n"
        "ds_write2_b32 v139, v48, v49 offset1:4\n"
        "v_mov_b64 v[94:95], 0\n"
        "v_mov_b64 v[96:97], 0\n"
        "v_mov_b64 v[98:99], 0\n"
        "v_mov_b64 v[100:101], s[70:71]\n"
        "v_mov_b64 v[102:103], 0\n"
        "buffer_store_dwordx2 v[110:111], v126, s[36:39], s60 offen sc1\n"
        "v_mov_b64 v[104:105], 0\n"
        "s_waitcnt lgkmcnt(0)\n"
        "v_mov_b64 v[106:107], 0\n"
        "s_barrier\n"
        "ds_read_b128 v[108:111], v124 offset:18432\n"
        "v_mov_b64 v[106:107], 0\n"
        "ds_read_b128 v[112:115], v124 offset:19456\n"
        "v_mov_b64 v[106:107], 0\n"
        "s_bitcmp1_b32 s59, 30\n"
        "s_cbranch_scc1 L_s1_2%=\n"
        "buffer_store_dwordx4 v[24:27], v124, s[36:39], s59 offen offset:0 sc1\n"
        "L_s1_2%=:\n"
        "v_mov_b64 v[106:107], 0\n"
        "v_mov_b64 v[106:107], 0\n"
        "ds_read_b128 v[24:27], v125 offset:30720\n"
        "v_mov_b64 v[106:107], 0\n"
        "s_waitcnt lgkmcnt(2)\n"
        "v_smfmac_f32_16x16x64_bf16 v[92:95], v[108:111], v[144:151], v134\n"
        "v_smfmac_f32_16x16x64_bf16 v[96:99], v[108:111], v[152:159], v134\n"
        "s_waitcnt lgkmcnt(1)\n"
        "v_smfmac_f32_16x16x64_bf16 v[92:95], v[112:115], v[160:167], v134\n"
        "v_smfmac_f32_16x16x64_bf16 v[96:99], v[112:115], v[168:175], v134\n"
        "v_smfmac_f32_16x16x64_bf16 v[100:103], v[108:111], v[0:7], v134\n"
        "v_add_f32 v92, v92, v94\n"
        "v_add_f32 v93, v93, v95\n"
        "v_cvt_rpi_i32_f32 v94, v92\n"
        "v_smfmac_f32_16x16x64_bf16 v[104:107], v[108:111], v[8:15], v134\n"
        "buffer_load_dword v140, v124, s[36:39], s29 offen sc1\n"
        "v_bfi_b32 v94, 63, 1, v94\n"
        "v_add_f32 v96, v96, v98\n"
        "v_smfmac_f32_16x16x64_bf16 v[100:103], v[112:115], v[16:23], v134\n"
        "v_cvt_rpi_i32_f32 v95, v96\n"
        "s_waitcnt lgkmcnt(0)\n"
        "v_smfmac_f32_16x16x64_bf16 v[104:107], v[112:115], v[24:31], v134\n"
        "buffer_atomic_add_x2 v[94:95], v133, s[36:39], s51 offen\n"
        "v_add_f32 v97, v97, v99\n"
        "v_cvt_rpi_i32_f32 v98, v93\n"
        "v_bfi_b32 v98, 63, 1, v98\n"
        "v_cvt_rpi_i32_f32 v99, v97\n"
        "s_waitcnt vmcnt(7)\n"
        "v_mfma_f32_16x16x32_bf16 v[84:87], v[64:67], a[0:3], 0\n"
        "buffer_atomic_add_x2 v[98:99], v133, s[36:39], s66 offen\n"
        "v_add_f32 v100, v100, v102\n"
        "v_add_f32 v101, v101, v103\n"
        "v_add_f32 v104, v104, v106\n"
        "v_cvt_rpi_i32_f32 v102, v100\n"
        "v_bfi_b32 v102, 63, 1, v102\n"
        "v_cvt_rpi_i32_f32 v103, v104\n"
        "v_mfma_f32_16x16x32_bf16 v[88:91], v[64:67], a[8:11], 0\n"
        "buffer_atomic_add_x2 v[102:103], v133, s[36:39], s67 offen\n"
        "v_add_f32 v105, v105, v107\n"
        "v_cvt_rpi_i32_f32 v106, v101\n"
        "v_bfi_b32 v106, 63, 1, v106\n"
        "v_cvt_rpi_i32_f32 v107, v105\n"
        "v_mfma_f32_16x16x32_bf16 v[56:59], a[144:147], v[64:67], 0\n"
        "buffer_atomic_add_x2 v[106:107], v133, s[36:39], s68 offen\n"
        "L_win_2%=:\n"
        "v_mfma_f32_16x16x32_bf16 v[60:63], a[152:155], v[64:67], 0\n"
        "v_mfma_f32_16x16x32_bf16 v[48:51], a[240:243], v[64:67], 0\n"
        "ds_write_b128 v124, v[64:67] offset:8192\n"
        "v_mfma_f32_16x16x32_bf16 v[52:55], a[248:251], v[64:67], 0\n"
        "s_waitcnt vmcnt(9)\n"
        "v_mfma_f32_16x16x32_bf16 v[84:87], v[68:71], a[4:7], v[84:87]\n"
        "ds_write_b128 v124, v[68:71] offset:9216\n"
        "v_mfma_f32_16x16x32_bf16 v[88:91], v[68:71], a[12:15], v[88:91]\n"
        "v_mfma_f32_16x16x32_bf16 v[56:59], a[148:151], v[68:71], v[56:59]\n"
        "s_waitcnt vmcnt(8)\n"
        "ds_write_b128 v124, v[72:75] offset:10240\n"
        "v_mfma_f32_16x16x32_bf16 v[60:63], a[156:159], v[68:71], v[60:63]\n"
        "v_mfma_f32_16x16x32_bf16 v[48:51], a[244:247], v[68:71], v[48:51]\n"
        "s_waitcnt vmcnt(7)\n"
        "ds_write_b128 v124, v[76:79] offset:11264\n"
        "v_mfma_f32_16x16x32_bf16 v[52:55], a[252:255], v[68:71], v[52:55]\n"
        "v_mfma_f32_16x16x32_bf16 v[84:87], v[72:75], a[16:19], v[84:87]\n"
        "s_waitcnt vmcnt(6)\n"
        "ds_write_b128 v124, v[80:83] offset:16384\n"
        "v_mfma_f32_16x16x32_bf16 v[88:91], v[72:75], a[24:27], v[88:91]\n"
        "v_mfma_f32_16x16x32_bf16 v[56:59], a[160:163], v[72:75], v[56:59]\n"
        "v_mfma_f32_16x16x32_bf16 v[60:63], a[168:171], v[72:75], v[60:63]\n"
        "v_mfma_f32_16x16x32_bf16 v[48:51], v[176:179], v[72:75], v[48:51]\n"
        "ds_read_b128 v[0:3], v125 offset:12288\n"
        "v_mfma_f32_16x16x32_bf16 v[52:55], v[184:187], v[72:75], v[52:55]\n"
        "ds_read_b128 v[8:11], v125 offset:14336\n"
        "v_mfma_f32_16x16x32_bf16 v[84:87], v[76:79], a[20:23], v[84:87]\n"
        "s_waitcnt lgkmcnt(2)\n"
        "s_barrier\n"
        "v_mfma_f32_16x16x32_bf16 v[88:91], v[76:79], a[28:31], v[88:91]\n"
        "ds_read_b128 v[92:95], v124 offset:12288\n"
        "v_mov_b32 v116, s62\n"
        "v_mfma_f32_16x16x32_bf16 v[56:59], a[164:167], v[76:79], v[56:59]\n"
        "s_add_u32 s49, s49, 163840\n"
        "ds_read_b128 v[104:107], v124 offset:13312\n"
        "v_mfma_f32_16x16x32_bf16 v[60:63], a[172:175], v[76:79], v[60:63]\n"
        "s_add_u32 s50, s50, 163840\n"
        "ds_read_b128 v[4:7], v125 offset:13312\n"
        "v_mfma_f32_16x16x32_bf16 v[48:51], v[180:183], v[76:79], v[48:51]\n"
        "s_xor_b32 s49, s49, s86\n"
        "ds_read_b128 v[12:15], v125 offset:15360\n"
        "v_mfma_f32_16x16x32_bf16 v[52:55], v[188:191], v[76:79], v[52:55]\n"
        "s_xor_b32 s65, s49, 8192\n"
        "s_add_u32 s56, s56, 786432\n"
        "s_waitcnt lgkmcnt(3)\n"
        "v_mfma_f32_16x16x32_bf16 v[84:87], v[92:95], a[32:35], v[84:87]\n"
        "s_add_u32 s64, s64, 786432\n"
        "ds_read_b128 v[96:99], v124 offset:14336\n"
        "v_mfma_f32_16x16x32_bf16 v[88:91], v[92:95], a[40:43], v[88:91]\n"
        "ds_read_b128 v[100:103], v124 offset:15360\n"
        "v_mfma_f32_16x16x32_bf16 v[56:59], a[176:179], v[92:95], v[56:59]\n"
        "ds_read_b128 v[16:19], v125 offset:16384\n"
        "v_mfma_f32_16x16x32_bf16 v[60:63], a[184:187], v[92:95], v[60:63]\n"
        "s_add_u32 s51, s51, 163840\n"
        "ds_read_b128 v[20:23], v125 offset:17408\n"
        "v_mfma_f32_16x16x32_bf16 v[48:51], v[0:3], v[92:95], v[48:51]\n"
        "s_add_u32 s83, s83, 163840\n"
        "ds_read_b128 v[24:27], v125 offset:18432\n"
        "v_mfma_f32_16x16x32_bf16 v[52:55], v[8:11], v[92:95], v[52:55]\n"
        "ds_read_b128 v[28:31], v125 offset:19456\n"
        "s_waitcnt lgkmcnt(8)\n"
        "v_mfma_f32_16x16x32_bf16 v[84:87], v[104:107], a[36:39], v[84:87]\n"
        "s_xor_b32 s86, s83, s46\n"
        "ds_read_b128 v[92:95], v124 offset:0\n"
        "v_mfma_f32_16x16x32_bf16 v[88:91], v[104:107], a[44:47], v[88:91]\n"
        "s_and_b32 s86, s86, s53\n"
        "s_lshr_b32 s86, s86, 8\n"
        "s_xor_b32 s86, s86, s34\n"
        "v_mfma_f32_16x16x32_bf16 v[56:59], a[180:183], v[104:107], v[56:59]\n"
        "s_xor_b32 s34, s34, s86\n"
        "s_xor_b32 s51, s51, s86\n"
        "s_add_u32 s55, s55, 786432\n"
        "v_mfma_f32_16x16x32_bf16 v[60:63], a[188:191], v[104:107], v[60:63]\n"
        "s_waitcnt vmcnt(5)\n"
        "s_mov_b64 exec, 1\n"
        "buffer_atomic_umax v116, off, s[36:39], s61\n"
        "s_mov_b64 exec, -1\n"
        "s_waitcnt lgkmcnt(8)\n"
        "v_mfma_f32_16x16x32_bf16 v[48:51], v[4:7], v[104:107], v[48:51]\n"
        "s_cmp_eq_u32 s44, 63\n"
        "s_cbranch_scc1 L_end%=\n"
        "s_waitcnt lgkmcnt(7)\n"
        "v_mfma_f32_16x16x32_bf16 v[52:55], v[12:15], v[104:107], v[52:55]\n"
        "ds_read_b128 v[32:35], v125 offset:20480\n"
        "s_waitcnt lgkmcnt(7)\n"
        "v_mfma_f32_16x16x32_bf16 v[84:87], v[96:99], a[48:51], v[84:87]\n"
        "s_add_u32 s54, s54, 786432\n"
        "ds_read_b128 v[104:107], v124 offset:1024\n"
        "v_mfma_f32_16x16x32_bf16 v[88:91], v[96:99], a[56:59], v[88:91]\n"
        "s_add_u32 s59, s59, 131072\n"
        "ds_read_b128 v[36:39], v125 offset:21504\n"
        "v_mfma_f32_16x16x32_bf16 v[56:59], a[192:195], v[96:99], v[56:59]\n"
        "s_add_u32 s60, s60, 131072\n"
        "ds_read_b128 v[40:43], v125 offset:22528\n"
        "v_mfma_f32_16x16x32_bf16 v[60:63], a[200:203], v[96:99], v[60:63]\n"
        "ds_read_b128 v[44:47], v125 offset:23552\n"
        "s_waitcnt vmcnt(5)\n"
        "v_cmp_eq_u32 vcc, 0, v140\n"
        "s_waitcnt lgkmcnt(9)\n"
        "v_mfma_f32_16x16x32_bf16 v[48:51], v[16:19], v[96:99], v[48:51]\n"
        "s_cbranch_vccnz L_sw0_2%=\n"
        "L_sok_2%=:\n"
        "s_waitcnt lgkmcnt(7)\n"
        "v_mfma_f32_16x16x32_bf16 v[52:55], v[24:27], v[96:99], v[52:55]\n"
        "v_readlane_b32 s81, v140, s99\n"
        "v_max_u32_dpp v140, v140, v140 row_shr:1 row_mask:0xf bank_mask:0xf\n"
        "v_mfma_f32_16x16x32_bf16 v[84:87], v[100:103], a[52:55], v[84:87]\n"
        "ds_read_b128 v[96:99], v124 offset:2048\n"
        "v_max_u32_dpp v140, v140, v140 row_shr:2 row_mask:0xf bank_mask:0xf\n"
        "v_mfma_f32_16x16x32_bf16 v[88:91], v[100:103], a[60:63], v[88:91]\n"
        "buffer_load_dwordx4 v[108:111], v132, s[36:39], s56 offen offset:0 sc1\n"
        "v_max_u32_dpp v140, v140, v140 row_shr:4 row_mask:0xf bank_mask:0xf\n"
        "v_mfma_f32_16x16x32_bf16 v[56:59], a[196:199], v[100:103], v[56:59]\n"
        "ds_read_b128 v[0:3], v125 offset:0\n"
        "v_max_u32_dpp v140, v140, v140 row_shr:8 row_mask:0xf bank_mask:0xf\n"
        "v_mfma_f32_16x16x32_bf16 v[60:63], a[204:207], v[100:103], v[60:63]\n"
        "buffer_load_dwordx4 v[112:115], v132, s[36:39], s64 offen offset:0 sc1\n"
        "v_max_u32_dpp v140, v140, v140 row_bcast:15 row_mask:0xa bank_mask:0xf\n"
        "v_mfma_f32_16x16x32_bf16 v[48:51], v[20:23], v[100:103], v[48:51]\n"
        "ds_read_b128 v[4:7], v125 offset:1024\n"
        "v_max_u32_dpp v140, v140, v140 row_bcast:31 row_mask:0xc bank_mask:0xf\n"
        "s_waitcnt lgkmcnt(9)\n"
        "v_mfma_f32_16x16x32_bf16 v[52:55], v[28:31], v[100:103], v[52:55]\n"
        "v_readlane_b32 s33, v140, 63\n"
        "s_lshl1_add_u32 s28, s28, s81\n"
        "s_waitcnt lgkmcnt(8)\n"
        "v_mfma_f32_16x16x32_bf16 v[84:87], v[92:95], a[64:67], v[84:87]\n"
        "ds_read_b128 v[100:103], v124 offset:3072\n"
        "s_add_u32 s28, s28, 36\n"
        "s_sub_i32 s33, s33, s28\n"
        "v_mfma_f32_16x16x32_bf16 v[88:91], v[92:95], a[72:75], v[88:91]\n"
        "s_min_i32 s33, s33, 500\n"
        "s_mov_b32 s28, 0\n"
        "L_dl_2%=:\n"
        "s_sub_i32 s33, s33, 3\n"
        "s_cmp_gt_i32 s33, 0\n"
        "s_cbranch_scc1 L_dl_2%=\n"
        "v_mfma_f32_16x16x32_bf16 v[56:59], a[208:211], v[92:95], v[56:59]\n"
        "s_add_u32 s58, s58, 36864\n"
        "buffer_load_dwordx4 v[80:83], v138, s[36:39], s50 offen offset:0 sc1\n"
        "v_mfma_f32_16x16x32_bf16 v[60:63], a[216:219], v[92:95], v[60:63]\n"
        "s_xor_b32 s66, s51, 1024\n"
        "ds_read_b128 v[8:11], v125 offset:2048\n"
        "s_waitcnt lgkmcnt(9)\n"
        "v_mfma_f32_16x16x32_bf16 v[48:51], v[32:35], v[92:95], v[48:51]\n"
        "s_xor_b32 s67, s51, 8192\n"
        "buffer_load_dwordx4 v[64:67], v138, s[36:39], s49 offen offset:0 sc1\n"
        "s_waitcnt lgkmcnt(6)\n"
        "v_mfma_f32_16x16x32_bf16 v[52:55], v[40:43], v[92:95], v[52:55]\n"
        "ds_read_b128 v[12:15], v125 offset:3072\n"
        "v_mfma_f32_16x16x32_bf16 v[84:87], v[104:107], a[68:71], v[84:87]\n"
        "s_xor_b32 s68, s51, 9216\n"
        "ds_read_b128 v[92:95], v124 offset:4096\n"
        "v_mfma_f32_16x16x32_bf16 v[88:91], v[104:107], a[76:79], v[88:91]\n"
        "s_sub_u32 s75, 127, s84\n"
        "buffer_load_dwordx4 v[68:71], v138, s[36:39], s49 offen offset:256 sc1\n"
        "v_mfma_f32_16x16x32_bf16 v[56:59], a[212:215], v[104:107], v[56:59]\n"
        "s_sub_u32 s101, s77, s75\n"
        "ds_read_b128 v[16:19], v125 offset:4096\n"
        "v_mfma_f32_16x16x32_bf16 v[60:63], a[220:223], v[104:107], v[60:63]\n"
        "s_mul_i32 s35, s75, 0x800080\n"
        "buffer_load_dwordx4 v[72:75], v138, s[36:39], s65 offen offset:0 sc1\n"
        "v_mfma_f32_16x16x32_bf16 v[48:51], v[36:39], v[104:107], v[48:51]\n"
        "s_lshl_b32 s80, s75, 23\n"
        "ds_read_b128 v[20:23], v125 offset:5120\n"
        "s_waitcnt lgkmcnt(9)\n"
        "v_mfma_f32_16x16x32_bf16 v[52:55], v[44:47], v[104:107], v[52:55]\n"
        "buffer_load_dwordx4 v[76:79], v138, s[36:39], s65 offen offset:256 sc1\n"
        "s_waitcnt lgkmcnt(8)\n"
        "v_mfma_f32_16x16x32_bf16 v[84:87], v[96:99], a[80:83], v[84:87]\n"
        "s_sub_u32 s73, 0x3e0293ee, s80\n"
        "ds_read_b128 v[104:107], v124 offset:5120\n"
        "v_mfma_f32_16x16x32_bf16 v[88:91], v[96:99], a[88:91], v[88:91]\n"
        "s_sub_u32 s74, 0x3f800000, s80\n"
        "ds_read_b128 v[24:27], v125 offset:6144\n"
        "v_mfma_f32_16x16x32_bf16 v[56:59], a[224:227], v[96:99], v[56:59]\n"
        "s_add_u32 s80, s75, 128\n"
        "ds_read_b128 v[28:31], v125 offset:7168\n"
        "v_mfma_f32_16x16x32_bf16 v[60:63], a[232:235], v[96:99], v[60:63]\n"
        "s_lshl_b32 s80, s80, 8\n"
        "ds_read_b128 v[32:35], v125 offset:8192\n"
        "v_mfma_f32_16x16x32_bf16 v[48:51], v[192:195], v[96:99], v[48:51]\n"
        "s_add_u32 s62, s80, 1\n"
        "ds_read_b128 v[36:39], v125 offset:9216\n"
        "v_mfma_f32_16x16x32_bf16 v[52:55], v[200:203], v[96:99], v[52:55]\n"
        "ds_read_b128 v[40:43], v125 offset:10240\n"
        "s_waitcnt lgkmcnt(11)\n"
        "v_mfma_f32_16x16x32_bf16 v[84:87], v[100:103], a[84:87], v[84:87]\n"
        "s_add_u32 s44, s44, 1\n"
        "ds_read_b128 v[96:99], v124 offset:6144\n"
        "v_mfma_f32_16x16x32_bf16 v[88:91], v[100:103], a[92:95], v[88:91]\n"
        "s_add_u32 s61, s61, 32\n"
        "ds_read_b128 v[44:47], v125 offset:11264\n"
        "v_mfma_f32_16x16x32_bf16 v[56:59], a[228:231], v[100:103], v[56:59]\n"
        "s_branch L_rot_2%=\n"
        "L_light_3%=:\n"
        "v_mfma_f32_16x16x32_bf16 v[52:55], v[252:255], v[104:107], v[52:55]\n"
        "v_mfma_f32_16x16x32_bf16 v[84:87], v[96:99], a[112:115], v[84:87]\n"
        "v_mfma_f32_16x16x32_bf16 v[88:91], v[96:99], a[120:123], v[88:91]\n"
        "v_cvt_pk_bf16_f32 v48, v48, v52\n"
        "v_cvt_pk_bf16_f32 v49, v49, v53\n"
        "v_mfma_f32_16x16x32_bf16 v[84:87], v[100:103], a[116:119], v[84:87]\n"
        "v_cvt_pk_bf16_f32 v50, v50, v54\n"
        "v_cvt_pk_bf16_f32 v51, v51, v55\n"
        "v_mfma_f32_16x16x32_bf16 v[88:91], v[100:103], a[124:127], v[88:91]\n"
        "v_mfma_f32_16x16x32_bf16 v[84:87], v[92:95], a[128:131], v[84:87]\n"
        "v_mfma_f32_16x16x32_bf16 v[88:91], v[92:95], a[136:139], v[88:91]\n"
        "v_mfma_f32_16x16x32_bf16 v[84:87], v[104:107], a[132:135], v[84:87]\n"
        "buffer_store_dwordx4 v[48:51], v124, s[36:39], s54 offen offset:1024 sc1\n"
        "v_mfma_f32_16x16x32_bf16 v[88:91], v[104:107], a[140:143], v[88:91]\n"
        "v_mfma_f32_16x16x32_bf16 v[56:59], v[32:35], v[92:95], v[56:59]\n"
        "v_mfma_f32_16x16x32_bf16 v[60:63], v[40:43], v[92:95], v[60:63]\n"
        "v_cvt_pk_bf16_f32 v84, v84, v85\n"
        "v_cvt_pk_bf16_f32 v85, v86, v87\n"
        "v_mfma_f32_16x16x32_bf16 v[56:59], v[16:19], v[96:99], v[56:59]\n"
        "v_cvt_pk_bf16_f32 v86, v88, v89\n"
        "v_cvt_pk_bf16_f32 v87, v90, v91\n"
        "v_mfma_f32_16x16x32_bf16 v[60:63], v[24:27], v[96:99], v[60:63]\n"
        "v_mfma_f32_16x16x32_bf16 v[56:59], v[20:23], v[100:103], v[56:59]\n"
        "v_mov_b64 v[92:93], 0\n"
        "v_mov_b64 v[94:95], 0\n"
        "v_mfma_f32_16x16x32_bf16 v[60:63], v[28:31], v[100:103], v[60:63]\n"
        "v_mov_b64 v[96:97], 0\n"
        "v_mov_b64 v[98:99], 0\n"
        "v_mfma_f32_16x16x32_bf16 v[56:59], v[36:39], v[104:107], v[56:59]\n"
        "buffer_store_dwordx4 v[84:87], v124, s[36:39], s54 offen offset:2048 sc1\n"
        "v_mfma_f32_16x16x32_bf16 v[60:63], v[44:47], v[104:107], v[60:63]\n"
        "s_nop 7\n"
        "v_cvt_pk_bf16_f32 v100, v56, v60\n"
        "v_cvt_pk_bf16_f32 v101, v57, v61\n"
        "v_cvt_pk_bf16_f32 v102, v58, v62\n"
        "v_cvt_pk_bf16_f32 v103, v59, v63\n"
        "buffer_store_dwordx4 v[100:103], v124, s[36:39], s54 offen offset:0 sc1\n"
        "s_waitcnt vmcnt(3)\n"
        "v_cmp_ge_u32 vcc, s31, v140\n"
        "s_and_b32 vcc_lo, vcc_lo, 0x1ff\n"
        "s_cbranch_scc0 L_xl_3%=\n"
        "s_branch L_xl2_3%=\n"
        "L_xl1_3%=:\n"
        "s_add_u32 s63, s63, 1\n"
        "s_cmp_gt_u32 s63, 0x40000\n"
        "s_cbranch_scc1 L_abort%=\n"
        "s_sleep 4\n"
        "L_xl2_3%=:\n"
        "v_mov_b32 v140, 0\n"
        "buffer_atomic_add v140, v143, s[36:39], s57 offen sc0\n"
        "s_waitcnt vmcnt(0)\n"
        "v_cmp_ge_u32 vcc, s31, v140\n"
        "s_and_b32 vcc_lo, vcc_lo, 0x1ff\n"
        "s_cbranch_scc1 L_xl1_3%=\n"
        "L_xl_3%=:\n"
        "s_barrier\n"
        "s_add_u32 s69, s58, s82\n"
        "s_sub_u32 s72, s58, s82\n"
        "s_add_u32 s87, s58, 5120\n"
        "buffer_load_dwordx4 v[64:67], v124, s[36:39], s69 offen offset:0 sc1\n"
        "buffer_load_dwordx4 v[68:71], v124, s[36:39], s69 offen offset:1024 sc1\n"
        "buffer_load_dwordx4 v[72:75], v124, s[36:39], s72 offen offset:2048 sc1\n"
        "buffer_load_dwordx4 v[76:79], v124, s[36:39], s72 offen offset:3072 sc1\n"
        "buffer_load_dwordx4 v[80:83], v124, s[36:39], s87 offen offset:0 sc1\n"
        "s_mov_b32 s81, 64\n"
        "buffer_load_dword v107, off, s[36:39], s81 sc1\n"
        "s_memrealtime s[26:27]\n"
        "s_waitcnt vmcnt(6)\n"
        "s_waitcnt lgkmcnt(0)\n"
        "s_barrier\n"
        "v_lshl_or_b32 v20, s26, 1, 1\n"
        "s_add_u32 s29, s29, 1024\n"
        "s_add_u32 s80, s29, s30\n"
        "s_mov_b64 exec, 1\n"
        "buffer_atomic_umax v20, off, s[36:39], s80\n"
        "s_mov_b64 exec, -1\n"
        "s_waitcnt vmcnt(1)\n"
        "L_par_3%=:\n"
        "v_readfirstlane_b32 s80, v107\n"
        "s_nop 0\n"
        "s_cmp_lt_i32 s80, 0\n"
        "s_cbranch_scc1 L_par2_3%=\n"
        "s_add_u32 s63, s63, 1\n"
        "s_cmp_gt_u32 s63, 0x40000\n"
        "s_cbranch_scc1 L_abort%=\n"
        "s_sleep 4\n"
        "buffer_load_dword v107, off, s[36:39], s81 sc1\n"
        "s_waitcnt vmcnt(0)\n"
        "s_branch L_par_3%=\n"
        "L_par2_3%=:\n"
        "s_bfe_u32 s46, s80, 0x10018\n"
        "s_lshl_b32 s46, s46, 21\n"
        "s_mov_b32 s53, 0x200000\n"
        "s_bfe_u32 s84, s80, 0x80010\n"
        "s_sub_u32 s84, 255, s84\n"
        "s_bfe_u32 s76, s80, 0x80008\n"
        "s_sub_u32 s76, s76, 32\n"
        "s_max_i32 s76, s76, 66\n"
        "s_bfe_u32 s77, s80, 0x80000\n"
        "s_sub_u32 s77, s77, 158\n"
        "s_bitset0_b32 s49, 30\n"
        "s_bitset0_b32 s50, 30\n"
        "s_bitset0_b32 s56, 30\n"
        "s_bitset0_b32 s64, 30\n"
        "v_mov_b32 v140, 0\n"
        "s_mov_b32 s28, 1000\n"
        "s_branch L_xok_3%=\n"
        "L_x2_3%=:\n"
        "s_add_u32 s69, s58, s82\n"
        "s_sub_u32 s72, s58, s82\n"
        "s_add_u32 s87, s58, 5120\n"
        "buffer_load_dwordx4 v[64:67], v124, s[36:39], s69 offen offset:0 sc1\n"
        "buffer_load_dwordx4 v[68:71], v124, s[36:39], s69 offen offset:1024 sc1\n"
        "buffer_load_dwordx4 v[72:75], v124, s[36:39], s72 offen offset:2048 sc1\n"
        "buffer_load_dwordx4 v[76:79], v124, s[36:39], s72 offen offset:3072 sc1\n"
        "buffer_load_dwordx4 v[80:83], v124, s[36:39], s87 offen offset:0 sc1\n"
        "s_waitcnt vmcnt(0)\n"
        "s_memrealtime s[26:27]\n"
        "s_waitcnt lgkmcnt(0)\n"
        "s_add_u32 s28, s28, s26\n"
        "L_xok_3%=:\n"
        "v_mfma_f32_16x16x32_bf16 v[84:87], v[64:67], a[0:3], 0\n"
        "v_mfma_f32_16x16x32_bf16 v[88:91], v[64:67], a[8:11], 0\n"
        "v_mfma_f32_16x16x32_bf16 v[56:59], a[144:147], v[64:67], 0\n"
        "s_branch L_win_3%=\n"
        "L_retry_3%=:\n"
        "s_add_u32 s63, s63, 1\n"
        "s_cmp_gt_u32 s63, 0x40000\n"
        "s_cbranch_scc1 L_abort%=\n"
        "s_sleep 2\n"
        "s_waitcnt vmcnt(0)\n"
        "s_waitcnt lgkmcnt(0)\n"
        "buffer_load_dwordx4 v[80:83], v138, s[36:39], s50 offen offset:0 sc1\n"
        "buffer_load_dwordx4 v[64:67], v138, s[36:39], s49 offen offset:0 sc1\n"
        "buffer_load_dwordx4 v[68:71], v138, s[36:39], s49 offen offset:256 sc1\n"
        "buffer_load_dwordx4 v[72:75], v138, s[36:39], s65 offen offset:0 sc1\n"
        "buffer_load_dwordx4 v[76:79], v138, s[36:39], s65 offen offset:256 sc1\n"
        "s_waitcnt vmcnt(4)\n"
        "v_bitop3_b32 v123, 32, v80, v82 bitop3:0x80\n"
        "v_add_u32_dpp v80, v81, v80 row_half_mirror row_mask:0xf bank_mask:0xf\n"
        "v_add_u32_dpp v82, v83, v82 row_half_mirror row_mask:0xf bank_mask:0xf\n"
        "v_cvt_f32_i32 v80, v80\n"
        "v_cvt_f32_i32 v82, v82\n"
        "v_cvt_pk_bf16_f32 v52, v80, v82\n"
        "ds_write_b32 v137, v52\n"
        "s_waitcnt vmcnt(3)\n"
        "v_bitop3_b32 v123, v123, v64, v66 bitop3:0x80\n"
        "v_cvt_f32_i32 v64, v64\n"
        "v_cvt_f32_i32 v65, v65\n"
        "v_cvt_f32_i32 v66, v66\n"
        "v_cvt_f32_i32 v67, v67\n"
        "v_cvt_pk_bf16_f32 v52, v64, v65\n"
        "v_cvt_pk_bf16_f32 v53, v66, v67\n"
        "ds_write_b64 v126, v[52:53] offset:0\n"
        "s_waitcnt vmcnt(2)\n"
        "v_bitop3_b32 v123, v123, v68, v70 bitop3:0x80\n"
        "v_cvt_f32_i32 v68, v68\n"
        "v_cvt_f32_i32 v69, v69\n"
        "v_cvt_f32_i32 v70, v70\n"
        "v_cvt_f32_i32 v71, v71\n"
        "v_cvt_pk_bf16_f32 v54, v68, v69\n"
        "v_cvt_pk_bf16_f32 v55, v70, v71\n"
        "ds_write_b64 v126, v[54:55] offset:512\n"
        "s_waitcnt vmcnt(1)\n"
        "v_bitop3_b32 v123, v123, v72, v74 bitop3:0x80\n"
        "v_cvt_f32_i32 v72, v72\n"
        "v_cvt_f32_i32 v73, v73\n"
        "ds_read_b128 v[48:51], v124 offset:6144\n"
        "v_cvt_f32_i32 v74, v74\n"
        "v_cvt_f32_i32 v75, v75\n"
        "v_cvt_pk_bf16_f32 v52, v72, v73\n"
        "v_cvt_pk_bf16_f32 v53, v74, v75\n"
        "ds_write_b64 v126, v[52:53] offset:1024\n"
        "s_waitcnt vmcnt(0)\n"
        "v_bitop3_b32 v123, v123, v76, v78 bitop3:0x80\n"
        "v_cmp_eq_u32 vcc, 0, v123\n"
        "v_mov_b64 v[122:123], 0\n"
        "v_cvt_f32_i32 v76, v76\n"
        "v_cvt_f32_i32 v77, v77\n"
        "v_cvt_f32_i32 v78, v78\n"
        "v_cvt_f32_i32 v79, v79\n"
        "v_cvt_pk_bf16_f32 v54, v76, v77\n"
        "v_cvt_pk_bf16_f32 v55, v78, v79\n"
        "ds_write_b64 v126, v[54:55] offset:1536\n"
        "v_mov_b64 v[92:93], 0\n"
        "v_mov_b64 v[94:95], 0\n"
        "v_mov_b64 v[96:97], 0\n"
        "v_mov_b64 v[98:99], 0\n"
        "s_waitcnt lgkmcnt(0)\n"
        "s_branch L_dec_3%=\n"
        "L_sw0_3%=:\n"
        "s_memrealtime s[26:27]\n"
        "s_waitcnt lgkmcnt(0)\n"
        "s_sub_u32 s28, s28, s26\n"
        "L_sw_3%=:\n"
        "s_add_u32 s63, s63, 1\n"
        "s_cmp_gt_u32 s63, 0x40000\n"
        "s_cbranch_scc1 L_abort%=\n"
        "s_sleep 2\n"
        "buffer_load_dword v140, v124, s[36:39], s29 offen sc1\n"
        "s_waitcnt vmcnt(0)\n"
        "v_cmp_eq_u32 vcc, 0, v140\n"
        "s_cbranch_vccnz L_sw_3%=\n"
        "s_memrealtime s[26:27]\n"
        "s_waitcnt lgkmcnt(0)\n"
        "s_add_u32 s28, s28, s26\n"
        "s_branch L_sok_3%=\n"
        "L_xs_3%=:\n"
        "s_branch L_xs2_3%=\n"
        "L_xs1_3%=:\n"
        "s_add_u32 s63, s63, 1\n"
        "s_cmp_gt_u32 s63, 0x40000\n"
        "s_cbranch_scc1 L_abort%=\n"
        "s_sleep 4\n"
        "L_xs2_3%=:\n"
        "v_mov_b32 v140, 0\n"
        "buffer_atomic_add v140, v143, s[36:39], s57 offen sc0\n"
        "s_waitcnt vmcnt(0)\n"
        "v_cmp_ge_u32 vcc, s31, v140\n"
        "s_and_b32 vcc_lo, vcc_lo, 0x1ff\n"
        "s_cbranch_scc1 L_xs1_3%=\n"
        "s_branch L_xr_3%=\n"
        ".p2align 6\n"
        "L_rot_3%=:\n"
        "v_mfma_f32_16x16x32_bf16 v[48:51], v[196:199], v[100:103], v[48:51]\n"
        "v_mfma_f32_16x16x32_bf16 v[52:55], v[204:207], v[100:103], v[52:55]\n"
        "s_waitcnt lgkmcnt(10)\n"
        "v_mfma_f32_16x16x32_bf16 v[84:87], v[92:95], a[96:99], v[84:87]\n"
        "ds_read_b128 v[100:103], v124 offset:3072\n"
        "v_mfma_f32_16x16x32_bf16 v[88:91], v[92:95], a[104:107], v[88:91]\n"
        "v_mfma_f32_16x16x32_bf16 v[56:59], v[0:3], v[92:95], v[56:59]\n"
        "v_mfma_f32_16x16x32_bf16 v[60:63], v[8:11], v[92:95], v[60:63]\n"
        "v_mfma_f32_16x16x32_bf16 v[48:51], v[208:211], v[92:95], v[48:51]\n"
        "s_waitcnt lgkmcnt(0)\n"
        "s_barrier\n"
        "v_mfma_f32_16x16x32_bf16 v[52:55], v[216:219], v[92:95], v[52:55]\n"
        "v_mov_b64 v[116:117], 0\n"
        "v_mfma_f32_16x16x32_bf16 v[84:87], v[104:107], a[100:103], v[84:87]\n"
        "ds_read_b128 v[92:95], v124 offset:16384\n"
        "v_mov_b32 v140, 0\n"
        "v_mfma_f32_16x16x32_bf16 v[88:91], v[104:107], a[108:111], v[88:91]\n"
        "buffer_atomic_add v140, v143, s[36:39], s57 offen sc0\n"
        "v_mov_b64 v[118:119], 0\n"
        "v_mfma_f32_16x16x32_bf16 v[56:59], v[4:7], v[104:107], v[56:59]\n"
        "v_mov_b64 v[120:121], 0\n"
        "v_mfma_f32_16x16x32_bf16 v[60:63], v[12:15], v[104:107], v[60:63]\n"
        "v_mfma_f32_16x16x32_bf16 v[48:51], v[212:215], v[104:107], v[48:51]\n"
        "v_mfma_f32_16x16x32_bf16 v[52:55], v[220:223], v[104:107], v[52:55]\n"
        "v_mfma_f32_16x16x32_bf16 v[48:51], v[224:227], v[96:99], v[48:51]\n"
        "ds_read_b128 v[104:107], v124 offset:17408\n"
        "v_mfma_f32_16x16x32_bf16 v[52:55], v[232:235], v[96:99], v[52:55]\n"
        "v_mfma_f32_16x16x32_bf16 v[48:51], v[228:231], v[100:103], v[48:51]\n"
        "v_mfma_f32_16x16x32_bf16 v[52:55], v[236:239], v[100:103], v[52:55]\n"
        "s_waitcnt lgkmcnt(1)\n"
        "v_mfma_f32_16x16x32_bf16 v[48:51], v[240:243], v[92:95], v[48:51]\n"
        "v_mfma_f32_16x16x32_bf16 v[52:55], v[248:251], v[92:95], v[52:55]\n"
        "s_waitcnt lgkmcnt(0)\n"
        "v_mfma_f32_16x16x32_bf16 v[48:51], v[244:247], v[104:107], v[48:51]\n"
        "s_cmp_lt_i32 s44, 0\n"
        "s_cbranch_scc1 L_light_3%=\n"
        "v_mfma_f32_16x16x32_bf16 v[52:55], v[252:255], v[104:107], v[52:55]\n"
        "s_waitcnt vmcnt(5)\n"
        "v_bitop3_b32 v123, 32, v80, v82 bitop3:0x80\n"
        "s_nop 0\n"
        "v_add_u32_dpp v80, v81, v80 row_half_mirror row_mask:0xf bank_mask:0xf\n"
        "v_mfma_f32_16x16x32_bf16 v[84:87], v[96:99], a[112:115], v[84:87]\n"
        "s_nop 0\n"
        "v_add_u32_dpp v82, v83, v82 row_half_mirror row_mask:0xf bank_mask:0xf\n"
        "v_mfma_f32_16x16x32_bf16 v[88:91], v[96:99], a[120:123], v[88:91]\n"
        "v_cvt_pk_bf16_f32 v48, v48, v52\n"
        "v_cvt_pk_bf16_f32 v49, v49, v53\n"
        "s_nop 0\n"
        "v_mfma_f32_16x16x32_bf16 v[84:87], v[100:103], a[116:119], v[84:87]\n"
        "v_cvt_pk_bf16_f32 v50, v50, v54\n"
        "v_cvt_pk_bf16_f32 v51, v51, v55\n"
        "v_mfma_f32_16x16x32_bf16 v[88:91], v[100:103], a[124:127], v[88:91]\n"
        "v_cvt_f32_i32 v80, v80\n"
        "s_nop 0\n"
        "v_cvt_f32_i32 v82, v82\n"
        "v_mfma_f32_16x16x32_bf16 v[84:87], v[92:95], a[128:131], v[84:87]\n"
        "s_nop 0\n"
        "v_cvt_pk_bf16_f32 v52, v80, v82\n"
        "v_mfma_f32_16x16x32_bf16 v[88:91], v[92:95], a[136:139], v[88:91]\n"
        "s_nop 0\n"
        "v_mfma_f32_16x16x32_bf16 v[84:87], v[104:107], a[132:135], v[84:87]\n"
        "buffer_store_dwordx4 v[48:51], v124, s[36:39], s54 offen offset:1024 sc1\n"
        "v_mfma_f32_16x16x32_bf16 v[88:91], v[104:107], a[140:143], v[88:91]\n"
        "ds_write_b32 v137, v52\n"
        "s_waitcnt vmcnt(5)\n"
        "v_bitop3_b32 v123, v123, v64, v66 bitop3:0x80\n"
        "v_cvt_f32_i32 v64, v64\n"
        "v_mfma_f32_16x16x32_bf16 v[56:59], v[32:35], v[92:95], v[56:59]\n"
        "v_cvt_f32_i32 v65, v65\n"
        "v_cvt_f32_i32 v66, v66\n"
        "v_mfma_f32_16x16x32_bf16 v[60:63], v[40:43], v[92:95], v[60:63]\n"
        "v_cvt_pk_bf16_f32 v84, v84, v85\n"
        "v_cvt_pk_bf16_f32 v85, v86, v87\n"
        "v_mfma_f32_16x16x32_bf16 v[56:59], v[16:19], v[96:99], v[56:59]\n"
        "v_cvt_pk_bf16_f32 v86, v88, v89\n"
        "v_cvt_pk_bf16_f32 v87, v90, v91\n"
        "v_mfma_f32_16x16x32_bf16 v[60:63], v[24:27], v[96:99], v[60:63]\n"
        "v_cvt_f32_i32 v67, v67\n"
        "v_cvt_pk_bf16_f32 v52, v64, v65\n"
        "v_mfma_f32_16x16x32_bf16 v[56:59], v[20:23], v[100:103], v[56:59]\n"
        "v_mov_b64 v[92:93], 0\n"
        "v_mov_b64 v[94:95], 0\n"
        "v_mfma_f32_16x16x32_bf16 v[60:63], v[28:31], v[100:103], v[60:63]\n"
        "v_mov_b64 v[96:97], 0\n"
        "v_mov_b64 v[98:99], 0\n"
        "v_mfma_f32_16x16x32_bf16 v[56:59], v[36:39], v[104:107], v[56:59]\n"
        "buffer_store_dwordx4 v[84:87], v124, s[36:39], s54 offen offset:2048 sc1\n"
        "v_cvt_pk_bf16_f32 v53, v66, v67\n"
        "v_mfma_f32_16x16x32_bf16 v[60:63], v[44:47], v[104:107], v[60:63]\n"
        "ds_write_b64 v126, v[52:53] offset:0\n"
        "s_waitcnt vmcnt(5)\n"
        "v_bitop3_b32 v123, v123, v68, v70 bitop3:0x80\n"
        "v_cvt_f32_i32 v68, v68\n"
        "v_cvt_f32_i32 v69, v69\n"
        "v_cvt_f32_i32 v70, v70\n"
        "v_cvt_f32_i32 v71, v71\n"
        "v_cvt_pk_bf16_f32 v54, v68, v69\n"
        "v_cvt_pk_bf16_f32 v55, v70, v71\n"
        "ds_write_b64 v126, v[54:55] offset:512\n"
        "s_waitcnt vmcnt(4)\n"
        "v_bitop3_b32 v123, v123, v72, v74 bitop3:0x80\n"
        "v_cvt_pk_bf16_f32 v100, v56, v60\n"
        "v_cvt_pk_bf16_f32 v101, v57, v61\n"
        "v_cvt_pk_bf16_f32 v102, v58, v62\n"
        "v_cvt_pk_bf16_f32 v103, v59, v63\n"
        "v_cvt_f32_i32 v72, v72\n"
        "v_cvt_f32_i32 v73, v73\n"
        "ds_read_b128 v[48:51], v124 offset:6144\n"
        "v_cvt_f32_i32 v74, v74\n"
        "v_cvt_f32_i32 v75, v75\n"
        "v_cvt_pk_bf16_f32 v52, v72, v73\n"
        "v_cvt_pk_bf16_f32 v53, v74, v75\n"
        "ds_write_b64 v126, v[52:53] offset:1024\n"
        "s_waitcnt vmcnt(3)\n"
        "v_bitop3_b32 v123, v123, v76, v78 bitop3:0x80\n"
        "v_cmp_eq_u32 vcc, 0, v123\n"
        "v_mov_b64 v[122:123], 0\n"
        "v_cvt_f32_i32 v76, v76\n"
        "v_cvt_f32_i32 v77, v77\n"
        "v_cvt_f32_i32 v78, v78\n"
        "v_cvt_f32_i32 v79, v79\n"
        "v_cvt_pk_bf16_f32 v54, v76, v77\n"
        "v_cvt_pk_bf16_f32 v55, v78, v79\n"
        "ds_write_b64 v126, v[54:55] offset:1536\n"
        "L_dec_3%=:\n"
        "s_waitcnt lgkmcnt(2)\n"
        "v_smfmac_f32_16x16x64_bf16 v[92:95], v[48:51], a[144:151], v134\n"
        "ds_read_b128 v[52:55], v124 offset:7168\n"
        "v_max3_f32 v104, |v80|, |v82|, |v64|\n"
        "v_smfmac_f32_16x16x64_bf16 v[96:99], v[48:51], a[152:159], v134\n"
        "s_cbranch_vccnz L_retry_3%=\n"
        "v_max3_f32 v104, |v65|, |v66|, v104\n"
        "v_smfmac_f32_16x16x64_bf16 v[116:119], v[48:51], a[240:247], v134\n"
        "s_add_u32 s29, s29, 1024\n"
        "s_add_u32 s80, s29, s30\n"
        "v_max3_f32 v104, |v67|, |v68|, v104\n"
        "v_smfmac_f32_16x16x64_bf16 v[120:123], v[48:51], a[248:255], v134\n"
        "s_waitcnt lgkmcnt(0)\n"
        "v_max3_f32 v104, |v69|, |v70|, v104\n"
        "s_barrier\n"
        "ds_read_b128 v[56:59], v124 offset:0\n"
        "v_smfmac_f32_16x16x64_bf16 v[92:95], v[52:55], a[160:167], v134\n"
        "ds_read_b128 v[60:63], v124 offset:3072\n"
        "v_max3_f32 v104, |v71|, |v72|, v104\n"
        "v_smfmac_f32_16x16x64_bf16 v[96:99], v[52:55], a[168:175], v134\n"
        "ds_read_b128 v[80:83], v124 offset:1024\n"
        "v_max3_f32 v104, |v73|, |v74|, v104\n"
        "v_smfmac_f32_16x16x64_bf16 v[116:119], v[52:55], v[176:183], v134\n"
        "buffer_load_dwordx4 v[88:91], v124, s[36:39], s55 offen offset:3072 sc1\n"
        "ds_read_b128 v[64:67], v124 offset:8192\n"
        "v_max3_f32 v104, |v75|, |v76|, v104\n"
        "v_smfmac_f32_16x16x64_bf16 v[120:123], v[52:55], v[184:191], v134\n"
        "ds_read_b128 v[68:71], v124 offset:4096\n"
        "v_max3_f32 v104, |v77|, |v78|, v104\n"
        "s_waitcnt lgkmcnt(4)\n"
        "v_smfmac_f32_16x16x64_bf16 v[92:95], v[56:59], v[0:7], v134\n"
        "buffer_load_dwordx4 v[84:87], v124, s[36:39], s55 offen offset:0 sc1\n"
        "ds_read_b128 v[72:75], v124 offset:5120\n"
        "v_max_f32_e64 v104, |v79|, v104\n"
        "v_smfmac_f32_16x16x64_bf16 v[96:99], v[56:59], v[8:15], v134\n"
        "ds_read_b128 v[0:3], v125 offset:12288\n"
        "v_max_f32_dpp v104, v104, v104 row_shr:1 row_mask:0xf bank_mask:0xf\n"
        "s_waitcnt lgkmcnt(3)\n"
        "v_smfmac_f32_16x16x64_bf16 v[92:95], v[60:63], a[224:231], v134\n"
        "ds_read_b128 v[76:79], v124 offset:2048\n"
        "v_max_f32_dpp v104, v104, v104 row_shr:2 row_mask:0xf bank_mask:0xf\n"
        "v_smfmac_f32_16x16x64_bf16 v[96:99], v[60:63], a[232:239], v134\n"
        "ds_read_b128 v[4:7], v125 offset:13312\n"
        "v_max_f32_dpp v104, v104, v104 row_shr:4 row_mask:0xf bank_mask:0xf\n"
        "v_smfmac_f32_16x16x64_bf16 v[92:95], v[80:83], v[16:23], v134\n"
        "ds_read_b128 v[8:11], v125 offset:14336\n"
        "v_max_f32_dpp v104, v104, v104 row_shr:8 row_mask:0xf bank_mask:0xf\n"
        "v_smfmac_f32_16x16x64_bf16 v[96:99], v[80:83], v[24:31], v134\n"
        "ds_read_b128 v[12:15], v125 offset:15360\n"
        "v_max_f32_dpp v104, v104, v104 row_bcast:15 row_mask:0xa bank_mask:0xf\n"
        "v_smfmac_f32_16x16x64_bf16 v[92:95], v[64:67], v[32:39], v134\n"
        "ds_read_b128 v[16:19], v125 offset:16384\n"
        "v_max_f32_dpp v104, v104, v104 row_bcast:31 row_mask:0xc bank_mask:0xf\n"
        "v_smfmac_f32_16x16x64_bf16 v[96:99], v[64:67], v[40:47], v134\n"
        "ds_read_b128 v[20:23], v125 offset:17408\n"
        "v_readlane_b32 s85, v104, 63\n"
        "s_waitcnt lgkmcnt(5)\n"
        "v_smfmac_f32_16x16x64_bf16 v[92:95], v[68:71], a[176:183], v134\n"
        "ds_read_b128 v[24:27], v125 offset:18432\n"
        "v_smfmac_f32_16x16x64_bf16 v[96:99], v[68:71], a[184:191], v134\n"
        "ds_read_b128 v[28:31], v125 offset:19456\n"
        "v_mov_b32 v104, s85\n"
        "v_smfmac_f32_16x16x64_bf16 v[92:95], v[72:75], a[192:199], v134\n"
        "ds_read_b128 v[32:35], v125 offset:20480\n"
        "v_smfmac_f32_16x16x64_bf16 v[96:99], v[72:75], a[200:207], v134\n"
        "ds_read_b128 v[36:39], v125 offset:21504\n"
        "s_add_u32 s69, s58, s82\n"
        "v_smfmac_f32_16x16x64_bf16 v[92:95], v[76:79], a[208:215], v134\n"
        "ds_read_b128 v[40:43], v125 offset:22528\n"
        "s_sub_u32 s72, s58, s82\n"
        "v_smfmac_f32_16x16x64_bf16 v[96:99], v[76:79], a[216:223], v134\n"
        "ds_read_b128 v[44:47], v125 offset:23552\n"
        "s_add_u32 s87, s58, 5120\n"
        "v_smfmac_f32_16x16x64_bf16 v[116:119], v[64:67], v[240:247], v134\n"
        "ds_write_b32 v142, v104\n"
        "v_add_f32 v92, v92, v94\n"
        "v_add_f32 v93, v93, v95\n"
        "v_smfmac_f32_16x16x64_bf16 v[120:123], v[64:67], v[248:255], v134\n"
        "v_add_f32 v96, v96, v98\n"
        "v_add_f32 v97, v97, v99\n"
        "v_cvt_pk_bf16_f32 v92, v92, v96\n"
        "v_cvt_pk_bf16_f32 v93, v93, v97\n"
        "s_waitcnt lgkmcnt(3)\n"
        "v_smfmac_f32_16x16x64_bf16 v[116:119], v[68:71], v[0:7], v134\n"
        "ds_write2_b32 v127, v92, v93 offset1:72\n"
        "s_waitcnt vmcnt(4)\n"
        "v_cmp_ge_u32 vcc, s31, v140\n"
        "v_smfmac_f32_16x16x64_bf16 v[120:123], v[68:71], v[8:15], v134\n"
        "s_and_b32 vcc_lo, vcc_lo, 0x1ff\n"
        "s_cbranch_scc1 L_xs_3%=\n"
        "L_xr_3%=:\n"
        "s_memrealtime s[26:27]\n"
        "v_smfmac_f32_16x16x64_bf16 v[116:119], v[72:75], v[16:23], v134\n"
        "v_mov_b64 v[96:97], 0\n"
        "v_mov_b64 v[106:107], 0\n"
        "v_smfmac_f32_16x16x64_bf16 v[120:123], v[72:75], v[24:31], v134\n"
        "s_waitcnt vmcnt(3)\n"
        "s_waitcnt lgkmcnt(0)\n"
        "v_lshl_or_b32 v20, s26, 1, 1\n"
        "s_barrier\n"
        "ds_read_b128 v[0:3], v128 offset:0\n"
        "v_smfmac_f32_16x16x64_bf16 v[116:119], v[76:79], v[32:39], v134\n"
        "s_mov_b64 exec, 1\n"
        "buffer_atomic_umax v20, off, s[36:39], s80\n"
        "s_mov_b64 exec, -1\n"
        "ds_read_b128 v[24:27], v124 offset:20480\n"
        "v_mov_b64 v[104:105], 0\n"
        "v_smfmac_f32_16x16x64_bf16 v[120:123], v[76:79], v[40:47], v134\n"
        "ds_read_b128 v[4:7], v128 offset:64\n"
        "v_mov_b64 v[94:95], 0\n"
        "v_smfmac_f32_16x16x64_bf16 v[116:119], v[60:63], v[192:199], v134\n"
        "ds_read_b128 v[8:11], v128 offset:128\n"
        "v_mov_b64 v[92:93], 0\n"
        "v_smfmac_f32_16x16x64_bf16 v[120:123], v[60:63], v[200:207], v134\n"
        "ds_read_b128 v[12:15], v128 offset:192\n"
        "v_mov_b64 v[20:21], 0\n"
        "v_smfmac_f32_16x16x64_bf16 v[116:119], v[56:59], v[208:215], v134\n"
        "s_waitcnt lgkmcnt(3)\n"
        "v_max3_f32 v24, v24, v25, v26\n"
        "v_mov_b64 v[98:99], 0\n"
        "s_waitcnt lgkmcnt(2)\n"
        "v_smfmac_f32_16x16x64_bf16 v[104:107], v[108:111], v[0:7], v134\n"
        "v_max_f32 v24, v24, v27\n"
        "v_smfmac_f32_16x16x64_bf16 v[120:123], v[56:59], v[216:223], v134\n"
        "v_readfirstlane_b32 s84, v24\n"
        "s_waitcnt lgkmcnt(0)\n"
        "v_smfmac_f32_16x16x64_bf16 v[104:107], v[112:115], v[8:15], v134\n"
        "v_mov_b64 v[22:23], 0\n"
        "v_smfmac_f32_16x16x64_bf16 v[116:119], v[80:83], v[224:231], v134\n"
        "s_lshr_b32 s84, s84, 23\n"
        "s_add_u32 s84, s84, s101\n"
        "v_smfmac_f32_16x16x64_bf16 v[120:123], v[80:83], v[232:239], v134\n"
        "s_max_i32 s84, s84, s76\n"
        "s_min_i32 s84, s84, 186\n"
        "v_add_f32 v104, v104, v106\n"
        "v_add_f32 v105, v105, v107\n"
        "v_smfmac_f32_16x16x64_bf16 v[92:95], v[64:67], a[128:135], v134\n"
        "ds_write_b64 v131, v[104:105]\n"
        "v_add_f32 v116, v116, v118\n"
        "v_add_f32 v117, v117, v119\n"
        "v_pk_add_f32 v[120:121], v[120:121], v[122:123]\n"
        "v_cvt_pk_bf16_f32 v116, v116, v120\n"
        "v_cvt_pk_bf16_f32 v117, v117, v121\n"
        "ds_write2_b32 v129, v116, v117 offset1:8\n"
        "v_smfmac_f32_16x16x64_bf16 v[96:99], v[64:67], a[136:143], v134\n"
        "s_mul_i32 s88, s84, 0x800080\n"
        "s_mov_b32 s89, s88\n"
        "v_smfmac_f32_16x16x64_bf16 v[92:95], v[68:71], a[32:39], v134\n"
        "s_add_u32 s90, s88, s35\n"
        "s_mov_b32 s91, s90\n"
        "v_mov_b64 v[104:105], s[88:89]\n"
        "v_smfmac_f32_16x16x64_bf16 v[96:99], v[68:71], a[40:47], v134\n"
        "buffer_store_dwordx4 v[100:103], v124, s[36:39], s54 offen offset:0 sc1\n"
        "v_mov_b64 v[106:107], s[90:91]\n"
        "v_smfmac_f32_16x16x64_bf16 v[92:95], v[48:51], a[0:7], v134\n"
        "s_waitcnt lgkmcnt(0)\n"
        "v_mov_b64 v[100:101], s[88:89]\n"
        "s_barrier\n"
        "ds_read_b128 v[24:27], v130\n"
        "v_smfmac_f32_16x16x64_bf16 v[96:99], v[48:51], a[8:15], v134\n"
        "ds_read_b128 v[28:31], v130 offset:1024\n"
        "v_mov_b64 v[102:103], s[90:91]\n"
        "v_smfmac_f32_16x16x64_bf16 v[92:95], v[52:55], a[16:23], v134\n"
        "ds_read_b128 v[116:119], v124 offset:13568\n"
        "v_mov_b64 v[48:49], 0\n"
        "v_smfmac_f32_16x16x64_bf16 v[96:99], v[52:55], a[24:31], v134\n"
        "v_mov_b64 v[50:51], 0\n"
        "v_smfmac_f32_16x16x64_bf16 v[92:95], v[56:59], a[96:103], v134\n"
        "buffer_load_dwordx4 v[64:67], v124, s[36:39], s69 offen offset:0 sc1\n"
        "v_mov_b64 v[52:53], 0\n"
        "v_smfmac_f32_16x16x64_bf16 v[96:99], v[56:59], a[104:111], v134\n"
        "v_mov_b64 v[54:55], 0\n"
        "s_waitcnt lgkmcnt(2)\n"
        "v_smfmac_f32_16x16x64_bf16 v[20:23], v[24:27], v[0:7], v134\n"
        "buffer_load_dwordx4 v[68:71], v124, s[36:39], s69 offen offset:1024 sc1\n"
        "v_mov_b64 v[56:57], 0\n"
        "s_waitcnt lgkmcnt(0)\n"
        "v_smfmac_f32_16x16x64_bf16 v[20:23], v[28:31], v[8:15], v134\n"
        "ds_read_b128 v[0:3], v125 offset:24576\n"
        "v_max3_f32 v121, v116, v117, v118\n"
        "v_smfmac_f32_16x16x64_bf16 v[92:95], v[72:75], a[48:55], v134\n"
        "v_mov_b64 v[58:59], 0\n"
        "v_smfmac_f32_16x16x64_bf16 v[96:99], v[72:75], a[56:63], v134\n"
        "ds_read_b128 v[4:7], v125 offset:25600\n"
        "v_add_f32 v20, v20, v22\n"
        "v_add_f32 v21, v21, v23\n"
        "v_cndmask_b32 v120, v20, v21, s[92:93]\n"
        "v_mul_f32 v120, s74, v120\n"
        "v_max3_f32 v121, v121, v119, v120\n"
        "v_smfmac_f32_16x16x64_bf16 v[92:95], v[76:79], a[64:71], v134\n"
        "buffer_load_dwordx4 v[72:75], v124, s[36:39], s72 offen offset:2048 sc1\n"
        "v_max_f32_dpp v121, v121, v121 quad_perm:[2,3,0,1] row_mask:0xf bank_mask:0xf\n"
        "v_mov_b32 v122, v121\n"
        "v_smfmac_f32_16x16x64_bf16 v[96:99], v[76:79], a[72:79], v134\n"
        "ds_read_b128 v[8:11], v125 offset:26624\n"
        "v_permlane16_swap_b32 v121, v122\n"
        "v_max_f32 v121, v121, v122\n"
        "v_mov_b32 v122, v121\n"
        "v_smfmac_f32_16x16x64_bf16 v[92:95], v[80:83], a[112:119], v134\n"
        "buffer_load_dwordx4 v[76:79], v124, s[36:39], s72 offen offset:3072 sc1\n"
        "v_permlane32_swap_b32 v121, v122\n"
        "v_smfmac_f32_16x16x64_bf16 v[96:99], v[80:83], a[120:127], v134\n"
        "ds_read_b128 v[12:15], v125 offset:27648\n"
        "v_max_f32 v121, v121, v122\n"
        "v_mul_f32_e64 v123, -s73, v121\n"
        "v_fma_f32 v120, v120, s73, v123\n"
        "v_fma_f32 v116, v116, s73, v123\n"
        "v_smfmac_f32_16x16x64_bf16 v[92:95], v[60:63], a[80:87], v134\n"
        "buffer_load_dwordx4 v[80:83], v124, s[36:39], s87 offen offset:0 sc1\n"
        "v_fma_f32 v117, v117, s73, v123\n"
        "v_fma_f32 v118, v118, s73, v123\n"
        "v_fma_f32 v119, v119, s73, v123\n"
        "v_exp_f32 v120, v120\n"
        "v_smfmac_f32_16x16x64_bf16 v[96:99], v[60:63], a[88:95], v134\n"
        "ds_read_b128 v[16:19], v125 offset:28672\n"
        "v_exp_f32 v116, v116\n"
        "v_mul_f32 v120, s74, v120\n"
        "v_exp_f32 v117, v117\n"
        "v_exp_f32 v118, v118\n"
        "v_exp_f32 v119, v119\n"
        "s_waitcnt vmcnt(7)\n"
        "v_mov_b64 v[108:109], v[86:87]\n"
        "v_mov_b64 v[112:113], v[90:91]\n"
        "v_cvt_pk_bf16_f32 v120, v120, v120\n"
        "v_cvt_pk_bf16_f32 v60, v116, v117\n"
        "v_cvt_pk_bf16_f32 v62, v118, v119\n"
        "v_and_b32 v61, v120, v135\n"
        "v_and_b32 v63, v120, v136\n"
        "v_pk_add_f32 v[92:93], v[92:93], v[94:95]\n"
        "v_pk_add_f32 v[96:97], v[96:97], v[98:99]\n"
        "v_smfmac_f32_16x16x64_bf16 v[56:59], v[60:63], v[100:107], v134\n"
        "ds_read_b128 v[20:23], v125 offset:29696\n"
        "v_cvt_pk_bf16_f32 v86, v92, v93\n"
        "v_cvt_pk_bf16_f32 v87, v92, v93\n"
        "v_mov_b64 v[90:91], v[86:87]\n"
        "v_cvt_pk_bf16_f32 v110, v96, v97\n"
        "v_cvt_pk_bf16_f32 v111, v96, v97\n"
        "v_smfmac_f32_16x16x64_bf16 v[48:51], v[60:63], v[84:91], v134\n"
        "ds_read_b128 v[24:27], v125 offset:30720\n"
        "v_mov_b64 v[114:115], v[110:111]\n"
        "v_add_f32 v56, v56, v58\n"
        "v_add_f32 v57, v57, v59\n"
        "v_smfmac_f32_16x16x64_bf16 v[52:55], v[60:63], v[108:115], v134\n"
        "s_nop 1\n"
        "v_rcp_f32 v56, v56\n"
        "v_rcp_f32 v57, v57\n"
        "v_pk_add_f32 v[48:49], v[48:49], v[50:51]\n"
        "v_pk_mul_f32 v[48:49], v[48:49], v[56:57]\n"
        "v_mov_b32 v110, v86\n"
        "v_mov_b64 v[92:93], s[70:71]\n"
        "v_pk_add_f32 v[52:53], v[52:53], v[54:55]\n"
        "v_pk_mul_f32 v[52:53], v[52:53], v[56:57]\n"
        "v_cvt_pk_bf16_f32 v48, v48, v52\n"
        "v_cvt_pk_bf16_f32 v49, v49, v53\n"
        "ds_write2_b32 v139, v48, v49 offset1:4\n"
        "v_mov_b64 v[94:95], 0\n"
        "v_mov_b64 v[96:97], 0\n"
        "v_mov_b64 v[98:99], 0\n"
        "v_mov_b64 v[100:101], s[70:71]\n"
        "v_mov_b64 v[102:103], 0\n"
        "v_mov_b64 v[104:105], 0\n"
        "buffer_store_dwordx2 v[110:111], v126, s[36:39], s60 offen sc1\n"
        "s_waitcnt lgkmcnt(0)\n"
        "v_mov_b64 v[106:107], 0\n"
        "s_barrier\n"
        "ds_read_b128 v[108:111], v124 offset:18432\n"
        "v_mov_b64 v[106:107], 0\n"
        "ds_read_b128 v[112:115], v124 offset:19456\n"
        "v_mov_b64 v[106:107], 0\n"
        "s_bitcmp1_b32 s59, 30\n"
        "s_cbranch_scc1 L_s1_3%=\n"
        "buffer_store_dwordx4 v[28:31], v124, s[36:39], s59 offen offset:1024 sc1\n"
        "L_s1_3%=:\n"
        "v_mov_b64 v[106:107], 0\n"
        "v_mov_b64 v[106:107], 0\n"
        "ds_read_b128 v[28:31], v125 offset:31744\n"
        "v_mov_b64 v[106:107], 0\n"
        "s_waitcnt lgkmcnt(2)\n"
        "v_smfmac_f32_16x16x64_bf16 v[92:95], v[108:111], v[144:151], v134\n"
        "v_smfmac_f32_16x16x64_bf16 v[96:99], v[108:111], v[152:159], v134\n"
        "s_waitcnt lgkmcnt(1)\n"
        "v_smfmac_f32_16x16x64_bf16 v[92:95], v[112:115], v[160:167], v134\n"
        "v_smfmac_f32_16x16x64_bf16 v[96:99], v[112:115], v[168:175], v134\n"
        "v_smfmac_f32_16x16x64_bf16 v[100:103], v[108:111], v[0:7], v134\n"
        "v_add_f32 v92, v92, v94\n"
        "v_add_f32 v93, v93, v95\n"
        "v_cvt_rpi_i32_f32 v94, v92\n"
        "v_smfmac_f32_16x16x64_bf16 v[104:107], v[108:111], v[8:15], v134\n"
        "v_bfi_b32 v94, 63, 1, v94\n"
        "v_add_f32 v96, v96, v98\n"
        "v_smfmac_f32_16x16x64_bf16 v[100:103], v[112:115], v[16:23], v134\n"
        "buffer_load_dword v140, v124, s[36:39], s29 offen sc1\n"
        "v_cvt_rpi_i32_f32 v95, v96\n"
        "s_waitcnt lgkmcnt(0)\n"
        "v_smfmac_f32_16x16x64_bf16 v[104:107], v[112:115], v[24:31], v134\n"
        "buffer_atomic_add_x2 v[94:95], v133, s[36:39], s51 offen\n"
        "v_add_f32 v97, v97, v99\n"
        "v_cvt_rpi_i32_f32 v98, v93\n"
        "v_bfi_b32 v98, 63, 1, v98\n"
        "v_cvt_rpi_i32_f32 v99, v97\n"
        "s_waitcnt vmcnt(7)\n"
        "v_mfma_f32_16x16x32_bf16 v[84:87], v[64:67], a[0:3], 0\n"
        "buffer_atomic_add_x2 v[98:99], v133, s[36:39], s66 offen\n"
        "v_add_f32 v100, v100, v102\n"
        "v_add_f32 v101, v101, v103\n"
        "v_add_f32 v104, v104, v106\n"
        "v_cvt_rpi_i32_f32 v102, v100\n"
        "v_bfi_b32 v102, 63, 1, v102\n"
        "v_cvt_rpi_i32_f32 v103, v104\n"
        "v_mfma_f32_16x16x32_bf16 v[88:91], v[64:67], a[8:11], 0\n"
        "buffer_atomic_add_x2 v[102:103], v133, s[36:39], s67 offen\n"
        "v_add_f32 v105, v105, v107\n"
        "v_cvt_rpi_i32_f32 v106, v101\n"
        "v_bfi_b32 v106, 63, 1, v106\n"
        "v_cvt_rpi_i32_f32 v107, v105\n"
        "v_mfma_f32_16x16x32_bf16 v[56:59], a[144:147], v[64:67], 0\n"
        "buffer_atomic_add_x2 v[106:107], v133, s[36:39], s68 offen\n"
        "L_win_3%=:\n"
        "v_mfma_f32_16x16x32_bf16 v[60:63], a[152:155], v[64:67], 0\n"
        "v_mfma_f32_16x16x32_bf16 v[48:51], a[240:243], v[64:67], 0\n"
        "v_mfma_f32_16x16x32_bf16 v[52:55], a[248:251], v[64:67], 0\n"
        "ds_write_b128 v124, v[64:67] offset:12288\n"
        "s_waitcnt vmcnt(9)\n"
        "v_mfma_f32_16x16x32_bf16 v[84:87], v[68:71], a[4:7], v[84:87]\n"
        "v_mfma_f32_16x16x32_bf16 v[88:91], v[68:71], a[12:15], v[88:91]\n"
        "ds_write_b128 v124, v[68:71] offset:13312\n"
        "v_mfma_f32_16x16x32_bf16 v[56:59], a[148:151], v[68:71], v[56:59]\n"
        "v_mfma_f32_16x16x32_bf16 v[60:63], a[156:159], v[68:71], v[60:63]\n"
        "s_waitcnt vmcnt(8)\n"
        "ds_write_b128 v124, v[72:75] offset:14336\n"
        "v_mfma_f32_16x16x32_bf16 v[48:51], a[244:247], v[68:71], v[48:51]\n"
        "v_mfma_f32_16x16x32_bf16 v[52:55], a[252:255], v[68:71], v[52:55]\n"
        "s_waitcnt vmcnt(7)\n"
        "ds_write_b128 v124, v[76:79] offset:15360\n"
        "v_mfma_f32_16x16x32_bf16 v[84:87], v[72:75], a[16:19], v[84:87]\n"
        "v_mfma_f32_16x16x32_bf16 v[88:91], v[72:75], a[24:27], v[88:91]\n"
        "s_waitcnt vmcnt(6)\n"
        "ds_write_b128 v124, v[80:83] offset:17408\n"
        "v_mfma_f32_16x16x32_bf16 v[56:59], a[160:163], v[72:75], v[56:59]\n"
        "v_mfma_f32_16x16x32_bf16 v[60:63], a[168:171], v[72:75], v[60:63]\n"
        "v_mfma_f32_16x16x32_bf16 v[48:51], v[176:179], v[72:75], v[48:51]\n"
        "ds_read_b128 v[0:3], v125 offset:12288\n"
        "v_mfma_f32_16x16x32_bf16 v[52:55], v[184:187], v[72:75], v[52:55]\n"
        "ds_read_b128 v[8:11], v125 offset:14336\n"
        "v_mfma_f32_16x16x32_bf16 v[84:87], v[76:79], a[20:23], v[84:87]\n"
        "s_waitcnt lgkmcnt(2)\n"
        "s_barrier\n"
        "v_mfma_f32_16x16x32_bf16 v[88:91], v[76:79], a[28:31], v[88:91]\n"
        "ds_read_b128 v[92:95], v124 offset:8192\n"
        "v_mov_b32 v116, s62\n"
        "v_mfma_f32_16x16x32_bf16 v[56:59], a[164:167], v[76:79], v[56:59]\n"
        "s_add_u32 s49, s49, 163840\n"
        "ds_read_b128 v[104:107], v124 offset:9216\n"
        "v_mfma_f32_16x16x32_bf16 v[60:63], a[172:175], v[76:79], v[60:63]\n"
        "s_add_u32 s50, s50, 163840\n"
        "ds_read_b128 v[4:7], v125 offset:13312\n"
        "v_mfma_f32_16x16x32_bf16 v[48:51], v[180:183], v[76:79], v[48:51]\n"
        "s_xor_b32 s49, s49, s86\n"
        "ds_read_b128 v[12:15], v125 offset:15360\n"
        "v_mfma_f32_16x16x32_bf16 v[52:55], v[188:191], v[76:79], v[52:55]\n"
        "s_xor_b32 s65, s49, 8192\n"
        "s_add_u32 s56, s56, 786432\n"
        "s_waitcnt lgkmcnt(3)\n"
        "v_mfma_f32_16x16x32_bf16 v[84:87], v[92:95], a[32:35], v[84:87]\n"
        "s_add_u32 s64, s64, 786432\n"
        "ds_read_b128 v[96:99], v124 offset:10240\n"
        "v_mfma_f32_16x16x32_bf16 v[88:91], v[92:95], a[40:43], v[88:91]\n"
        "ds_read_b128 v[100:103], v124 offset:11264\n"
        "v_mfma_f32_16x16x32_bf16 v[56:59], a[176:179], v[92:95], v[56:59]\n"
        "ds_read_b128 v[16:19], v125 offset:16384\n"
        "v_mfma_f32_16x16x32_bf16 v[60:63], a[184:187], v[92:95], v[60:63]\n"
        "s_add_u32 s51, s51, 163840\n"
        "ds_read_b128 v[20:23], v125 offset:17408\n"
        "v_mfma_f32_16x16x32_bf16 v[48:51], v[0:3], v[92:95], v[48:51]\n"
        "s_add_u32 s83, s83, 163840\n"
        "ds_read_b128 v[24:27], v125 offset:18432\n"
        "v_mfma_f32_16x16x32_bf16 v[52:55], v[8:11], v[92:95], v[52:55]\n"
        "ds_read_b128 v[28:31], v125 offset:19456\n"
        "s_waitcnt lgkmcnt(8)\n"
        "v_mfma_f32_16x16x32_bf16 v[84:87], v[104:107], a[36:39], v[84:87]\n"
        "s_xor_b32 s86, s83, s46\n"
        "ds_read_b128 v[92:95], v124 offset:4096\n"
        "v_mfma_f32_16x16x32_bf16 v[88:91], v[104:107], a[44:47], v[88:91]\n"
        "s_and_b32 s86, s86, s53\n"
        "s_lshr_b32 s86, s86, 8\n"
        "s_xor_b32 s86, s86, s34\n"
        "v_mfma_f32_16x16x32_bf16 v[56:59], a[180:183], v[104:107], v[56:59]\n"
        "s_xor_b32 s34, s34, s86\n"
        "s_xor_b32 s51, s51, s86\n"
        "s_add_u32 s55, s55, 786432\n"
        "v_mfma_f32_16x16x32_bf16 v[60:63], a[188:191], v[104:107], v[60:63]\n"
        "s_add_u32 s54, s54, 786432\n"
        "ds_read_b128 v[32:35], v125 offset:20480\n"
        "s_waitcnt lgkmcnt(9)\n"
        "v_mfma_f32_16x16x32_bf16 v[48:51], v[4:7], v[104:107], v[48:51]\n"
        "s_waitcnt vmcnt(5)\n"
        "s_mov_b64 exec, 1\n"
        "buffer_atomic_umax v116, off, s[36:39], s61\n"
        "s_mov_b64 exec, -1\n"
        "s_waitcnt lgkmcnt(8)\n"
        "v_mfma_f32_16x16x32_bf16 v[52:55], v[12:15], v[104:107], v[52:55]\n"
        "ds_read_b128 v[36:39], v125 offset:21504\n"
        "s_waitcnt lgkmcnt(8)\n"
        "v_mfma_f32_16x16x32_bf16 v[84:87], v[96:99], a[48:51], v[84:87]\n"
        "s_add_u32 s59, s59, 131072\n"
        "ds_read_b128 v[104:107], v124 offset:5120\n"
        "v_mfma_f32_16x16x32_bf16 v[88:91], v[96:99], a[56:59], v[88:91]\n"
        "s_cmp_eq_u32 s44, 63\n"
        "s_cbranch_scc1 L_end%=\n"
        "v_mfma_f32_16x16x32_bf16 v[56:59], a[192:195], v[96:99], v[56:59]\n"
        "s_add_u32 s60, s60, 131072\n"
        "ds_read_b128 v[40:43], v125 offset:22528\n"
        "v_mfma_f32_16x16x32_bf16 v[60:63], a[200:203], v[96:99], v[60:63]\n"
        "ds_read_b128 v[44:47], v125 offset:23552\n"
        "s_waitcnt vmcnt(5)\n"
        "v_cmp_eq_u32 vcc, 0, v140\n"
        "s_waitcnt lgkmcnt(9)\n"
        "v_mfma_f32_16x16x32_bf16 v[48:51], v[16:19], v[96:99], v[48:51]\n"
        "s_cbranch_vccnz L_sw0_3%=\n"
        "L_sok_3%=:\n"
        "s_waitcnt lgkmcnt(7)\n"
        "v_mfma_f32_16x16x32_bf16 v[52:55], v[24:27], v[96:99], v[52:55]\n"
        "v_readlane_b32 s81, v140, s99\n"
        "v_max_u32_dpp v140, v140, v140 row_shr:1 row_mask:0xf bank_mask:0xf\n"
        "v_mfma_f32_16x16x32_bf16 v[84:87], v[100:103], a[52:55], v[84:87]\n"
        "ds_read_b128 v[96:99], v124 offset:6144\n"
        "v_max_u32_dpp v140, v140, v140 row_shr:2 row_mask:0xf bank_mask:0xf\n"
        "v_mfma_f32_16x16x32_bf16 v[88:91], v[100:103], a[60:63], v[88:91]\n"
        "ds_read_b128 v[0:3], v125 offset:0\n"
        "v_max_u32_dpp v140, v140, v140 row_shr:4 row_mask:0xf bank_mask:0xf\n"
        "v_mfma_f32_16x16x32_bf16 v[56:59], a[196:199], v[100:103], v[56:59]\n"
        "buffer_load_dwordx4 v[108:111], v132, s[36:39], s56 offen offset:0 sc1\n"
        "v_max_u32_dpp v140, v140, v140 row_shr:8 row_mask:0xf bank_mask:0xf\n"
        "v_mfma_f32_16x16x32_bf16 v[60:63], a[204:207], v[100:103], v[60:63]\n"
        "ds_read_b128 v[4:7], v125 offset:1024\n"
        "v_max_u32_dpp v140, v140, v140 row_bcast:15 row_mask:0xa bank_mask:0xf\n"
        "v_mfma_f32_16x16x32_bf16 v[48:51], v[20:23], v[100:103], v[48:51]\n"
        "buffer_load_dwordx4 v[112:115], v132, s[36:39], s64 offen offset:0 sc1\n"
        "v_max_u32_dpp v140, v140, v140 row_bcast:31 row_mask:0xc bank_mask:0xf\n"
        "s_waitcnt lgkmcnt(9)\n"
        "v_mfma_f32_16x16x32_bf16 v[52:55], v[28:31], v[100:103], v[52:55]\n"
        "v_readlane_b32 s33, v140, 63\n"
        "s_lshl1_add_u32 s28, s28, s81\n"
        "s_waitcnt lgkmcnt(8)\n"
        "v_mfma_f32_16x16x32_bf16 v[84:87], v[92:95], a[64:67], v[84:87]\n"
        "ds_read_b128 v[100:103], v124 offset:7168\n"
        "s_add_u32 s28, s28, 36\n"
        "s_sub_i32 s33, s33, s28\n"
        "v_mfma_f32_16x16x32_bf16 v[88:91], v[92:95], a[72:75], v[88:91]\n"
        "s_min_i32 s33, s33, 500\n"
        "s_mov_b32 s28, 0\n"
        "L_dl_3%=:\n"
        "s_sub_i32 s33, s33, 3\n"
        "s_cmp_gt_i32 s33, 0\n"
        "s_cbranch_scc1 L_dl_3%=\n"
        "v_mfma_f32_16x16x32_bf16 v[56:59], a[208:211], v[92:95], v[56:59]\n"
        "s_add_u32 s58, s58, 36864\n"
        "ds_read_b128 v[8:11], v125 offset:2048\n"
        "v_mfma_f32_16x16x32_bf16 v[60:63], a[216:219], v[92:95], v[60:63]\n"
        "s_xor_b32 s66, s51, 1024\n"
        "buffer_load_dwordx4 v[80:83], v138, s[36:39], s50 offen offset:0 sc1\n"
        "s_waitcnt lgkmcnt(9)\n"
        "v_mfma_f32_16x16x32_bf16 v[48:51], v[32:35], v[92:95], v[48:51]\n"
        "s_xor_b32 s67, s51, 8192\n"
        "ds_read_b128 v[12:15], v125 offset:3072\n"
        "s_waitcnt lgkmcnt(7)\n"
        "v_mfma_f32_16x16x32_bf16 v[52:55], v[40:43], v[92:95], v[52:55]\n"
        "buffer_load_dwordx4 v[64:67], v138, s[36:39], s49 offen offset:0 sc1\n"
        "v_mfma_f32_16x16x32_bf16 v[84:87], v[104:107], a[68:71], v[84:87]\n"
        "s_xor_b32 s68, s51, 9216\n"
        "ds_read_b128 v[92:95], v124 offset:0\n"
        "v_mfma_f32_16x16x32_bf16 v[88:91], v[104:107], a[76:79], v[88:91]\n"
        "s_sub_u32 s75, 127, s84\n"
        "buffer_load_dwordx4 v[68:71], v138, s[36:39], s49 offen offset:256 sc1\n"
        "v_mfma_f32_16x16x32_bf16 v[56:59], a[212:215], v[104:107], v[56:59]\n"
        "s_sub_u32 s101, s77, s75\n"
        "ds_read_b128 v[16:19], v125 offset:4096\n"
        "v_mfma_f32_16x16x32_bf16 v[60:63], a[220:223], v[104:107], v[60:63]\n"
        "s_mul_i32 s35, s75, 0x800080\n"
        "buffer_load_dwordx4 v[72:75], v138, s[36:39], s65 offen offset:0 sc1\n"
        "v_mfma_f32_16x16x32_bf16 v[48:51], v[36:39], v[104:107], v[48:51]\n"
        "s_lshl_b32 s80, s75, 23\n"
        "ds_read_b128 v[20:23], v125 offset:5120\n"
        "s_waitcnt lgkmcnt(9)\n"
        "v_mfma_f32_16x16x32_bf16 v[52:55], v[44:47], v[104:107], v[52:55]\n"
        "buffer_load_dwordx4 v[76:79], v138, s[36:39], s65 offen offset:256 sc1\n"
        "s_waitcnt lgkmcnt(8)\n"
        "v_mfma_f32_16x16x32_bf16 v[84:87], v[96:99], a[80:83], v[84:87]\n"
        "s_sub_u32 s73, 0x3e0293ee, s80\n"
        "ds_read_b128 v[104:107], v124 offset:1024\n"
        "v_mfma_f32_16x16x32_bf16 v[88:91], v[96:99], a[88:91], v[88:91]\n"
        "s_sub_u32 s74, 0x3f800000, s80\n"
        "ds_read_b128 v[24:27], v125 offset:6144\n"
        "v_mfma_f32_16x16x32_bf16 v[56:59], a[224:227], v[96:99], v[56:59]\n"
        "s_add_u32 s80, s75, 128\n"
        "ds_read_b128 v[28:31], v125 offset:7168\n"
        "v_mfma_f32_16x16x32_bf16 v[60:63], a[232:235], v[96:99], v[60:63]\n"
        "s_lshl_b32 s80, s80, 8\n"
        "ds_read_b128 v[32:35], v125 offset:8192\n"
        "v_mfma_f32_16x16x32_bf16 v[48:51], v[192:195], v[96:99], v[48:51]\n"
        "s_add_u32 s62, s80, 1\n"
        "ds_read_b128 v[36:39], v125 offset:9216\n"
        "v_mfma_f32_16x16x32_bf16 v[52:55], v[200:203], v[96:99], v[52:55]\n"
        "ds_read_b128 v[40:43], v125 offset:10240\n"
        "s_waitcnt lgkmcnt(11)\n"
        "v_mfma_f32_16x16x32_bf16 v[84:87], v[100:103], a[84:87], v[84:87]\n"
        "s_add_u32 s44, s44, 1\n"
        "ds_read_b128 v[96:99], v124 offset:2048\n"
        "v_mfma_f32_16x16x32_bf16 v[88:91], v[100:103], a[92:95], v[88:91]\n"
        "s_add_u32 s61, s61, 32\n"
        "ds_read_b128 v[44:47], v125 offset:11264\n"
        "v_mfma_f32_16x16x32_bf16 v[56:59], a[228:231], v[100:103], v[56:59]\n"
        "s_xor_b32 s57, s57, 4096\n"
        "s_add_u32 s31, s31, 1\n"
        "s_and_b32 s31, s31, 63\n"
        "v_mfma_f32_16x16x32_bf16 v[60:63], a[236:239], v[100:103], v[60:63]\n"
        "s_branch L_rot_3%=\n"
        "L_abort%=:\n"
        "v_mov_b32 v0, 1\n"
        "s_mov_b32 s80, 0\n"
        "buffer_store_dword v0, off, s[36:39], s80 sc1\n"
        "L_end%=:\n"
        "s_waitcnt vmcnt(0)\n"
        "s_waitcnt lgkmcnt(0)\n"
        :: "s"(ws), "s"(Wq), "s"(Wk), "s"(Wv), "s"(Wo), "s"(id), "s"(ack)
        : "v0", "v1", "v2", "v3", "v4", "v5", "v6", "v7", "v8", "v9", "v10", "v11", "v12", "v13", "v14", "v15", "v16", "v17", "v18", "v19", "v20", "v21", "v22", "v23",
          "v24", "v25", "v26", "v27", "v28", "v29", "v30", "v31", "v32", "v33", "v34", "v35", "v36", "v37", "v38", "v39", "v40", "v41", "v42", "v43", "v44", "v45", "v46", "v47",
          "v48", "v49", "v50", "v51", "v52", "v53", "v54", "v55", "v56", "v57", "v58", "v59", "v60", "v61", "v62", "v63", "v64", "v65", "v66", "v67", "v68", "v69", "v70", "v71",
          "v72", "v73", "v74", "v75", "v76", "v77", "v78", "v79", "v80", "v81", "v82", "v83", "v84", "v85", "v86", "v87", "v88", "v89", "v90", "v91", "v92", "v93", "v94", "v95",
          "v96", "v97", "v98", "v99", "v100", "v101", "v102", "v103", "v104", "v105", "v106", "v107", "v108", "v109", "v110", "v111", "v112", "v113", "v114", "v115", "v116", "v117", "v118", "v119",
          "v120", "v121", "v122", "v123", "v124", "v125", "v126", "v127", "v128", "v129", "v130", "v131", "v132", "v133", "v134", "v135", "v136", "v137", "v138", "v139", "v140", "v141", "v142", "v143",
          "v144", "v145", "v146", "v147", "v148", "v149", "v150", "v151", "v152", "v153", "v154", "v155", "v156", "v157", "v158", "v159", "v160", "v161", "v162", "v163", "v164", "v165", "v166", "v167",
          "v168", "v169", "v170", "v171", "v172", "v173", "v174", "v175", "v176", "v177", "v178", "v179", "v180", "v181", "v182", "v183", "v184", "v185", "v186", "v187", "v188", "v189", "v190", "v191",
          "v192", "v193", "v194", "v195", "v196", "v197", "v198", "v199", "v200", "v201", "v202", "v203", "v204", "v205", "v206", "v207", "v208", "v209", "v210", "v211", "v212", "v213", "v214", "v215",
          "v216", "v217", "v218", "v219", "v220", "v221", "v222", "v223", "v224", "v225", "v226", "v227", "v228", "v229", "v230", "v231", "v232", "v233", "v234", "v235", "v236", "v237", "v238", "v239",
          "v240", "v241", "v242", "v243", "v244", "v245", "v246", "v247", "v248", "v249", "v250", "v251", "v252", "v253", "v254", "v255", "a0", "a1", "a2", "a3", "a4", "a5", "a6", "a7",
          "a8", "a9", "a10", "a11", "a12", "a13", "a14", "a15", "a16", "a17", "a18", "a19", "a20", "a21", "a22", "a23", "a24", "a25", "a26", "a27", "a28", "a29", "a30", "a31",
          "a32", "a33", "a34", "a35", "a36", "a37", "a38", "a39", "a40", "a41", "a42", "a43", "a44", "a45", "a46", "a47", "a48", "a49", "a50", "a51", "a52", "a53", "a54", "a55",
          "a56", "a57", "a58", "a59", "a60", "a61", "a62", "a63", "a64", "a65", "a66", "a67", "a68", "a69", "a70", "a71", "a72", "a73", "a74", "a75", "a76", "a77", "a78", "a79",
          "a80", "a81", "a82", "a83", "a84", "a85", "a86", "a87", "a88", "a89", "a90", "a91", "a92", "a93", "a94", "a95", "a96", "a97", "a98", "a99", "a100", "a101", "a102", "a103",
          "a104", "a105", "a106", "a107", "a108", "a109", "a110", "a111", "a112", "a113", "a114", "a115", "a116", "a117", "a118", "a119", "a120", "a121", "a122", "a123", "a124", "a125", "a126", "a127",
          "a128", "a129", "a130", "a131", "a132", "a133", "a134", "a135", "a136", "a137", "a138", "a139", "a140", "a141", "a142", "a143", "a144", "a145", "a146", "a147", "a148", "a149", "a150", "a151",
          "a152", "a153", "a154", "a155", "a156", "a157", "a158", "a159", "a160", "a161", "a162", "a163", "a164", "a165", "a166", "a167", "a168", "a169", "a170", "a171", "a172", "a173", "a174", "a175",
          "a176", "a177", "a178", "a179", "a180", "a181", "a182", "a183", "a184", "a185", "a186", "a187", "a188", "a189", "a190", "a191", "a192", "a193", "a194", "a195", "a196", "a197", "a198", "a199",
          "a200", "a201", "a202", "a203", "a204", "a205", "a206", "a207", "a208", "a209", "a210", "a211", "a212", "a213", "a214", "a215", "a216", "a217", "a218", "a219", "a220", "a221", "a222", "a223",
          "a224", "a225", "a226", "a227", "a228", "a229", "a230", "a231", "a232", "a233", "a234", "a235", "a236", "a237", "a238", "a239", "a240", "a241", "a242", "a243", "a244", "a245", "a246", "a247",
          "a248", "a249", "a250", "a251", "a252", "a253", "a254", "a255", "s20", "s21", "s22", "s23", "s24", "s25", "s26", "s27", "s28", "s29", "s30", "s31", "s32", "s33", "s34", "s35",
          "s36", "s37", "s38", "s39", "s40", "s41", "s42", "s43", "s44", "s45", "s46", "s47", "s48", "s49", "s50", "s51", "s52", "s53", "s54", "s55", "s56", "s57", "s58", "s59",
          "s60", "s61", "s62", "s63", "s64", "s65", "s66", "s67", "s68", "s69", "s70", "s71", "s72", "s73", "s74", "s75", "s76", "s77", "s78", "s79", "s80", "s81", "s82", "s83",
          "s84", "s85", "s86", "s87", "s88", "s89", "s90", "s91", "s92", "s93", "s94", "s95", "s96", "s97", "s98", "s99", "s100", "s101", "vcc", "scc", "m0", "memory");
}
constexpr u32 OOB = 0x40000000u;
#define BID (blockIdx.x ^ 248u)

constexpr u32 XNB_W = OFF_XNB, FX_W = 0;
__device__ __forceinline__ float bf2f(u16 v) { return __uint_as_float(((u32)v) << 16); }
__device__ __forceinline__ float bflo(u32 v) { return __uint_as_float(v << 16); }
__device__ __forceinline__ float bfhi(u32 v) { return __uint_as_float(v & 0xffff0000u); }
__device__ __forceinline__ u32 pack2(float a, float b) { bfv2 v = {(__bf16)a, (__bf16)b}; return __builtin_bit_cast(u32, v); }
__device__ __forceinline__ float rbf(float f) { return (float)(__bf16)f; }
__device__ __forceinline__ v4u ld16(rsrc_t r, u32 off) { return __builtin_amdgcn_raw_buffer_load_b128(r, off, 0, 16); }
__device__ __forceinline__ v2u ld8(rsrc_t r, u32 off) { return __builtin_amdgcn_raw_buffer_load_b64(r, off, 0, 16); }
__device__ __forceinline__ void st16(rsrc_t r, u32 off, v4u v) { __builtin_amdgcn_raw_buffer_store_b128(v, r, off, 0, 16); }
__device__ __forceinline__ void st8(rsrc_t r, u32 off, v2u v) { __builtin_amdgcn_raw_buffer_store_b64(v, r, off, 0, 16); }
__device__ __forceinline__ void st4(rsrc_t r, u32 off, u32 v) { __builtin_amdgcn_raw_buffer_store_b32(v, r, off, 0, 16); }
#define DRAIN() asm volatile("s_waitcnt vmcnt(0)" ::: "memory")
#define CBAR() asm volatile("" ::: "memory")
#define SCHED() __builtin_amdgcn_sched_barrier(0)
#define VMW(n) asm volatile("s_waitcnt vmcnt(%0)" :: "n"(n) : "memory")
#define LBAR() asm volatile("s_waitcnt lgkmcnt(0)\n\ts_barrier" ::: "memory")
__device__ __forceinline__ u32 ldw(char* ws, u32 off) { return __hip_atomic_load((gu32*)(ws + off), RLX_AGENT); }
constexpr u32 SPIN_CAP = 1u << 20;
#define SLP_A 2
#define SLP_S 6
__device__ __forceinline__ bool spin_fail(char* ws, u32& spins) {
    ++spins;
    if ((spins & 63u) == 0u) {
        if (ldw(ws, OFF_ABORT)) return true;
        if (spins > SPIN_CAP) { __hip_atomic_store((gu32*)(ws + OFF_ABORT), 1u, RLX_AGENT); return true; }
    }
    return false;
}
#define NAP_A 2
#define NAP_W 2
#define NAP_C 1
__device__ __forceinline__ void nap(int n) { for (int i = 0; i < n; ++i) __builtin_amdgcn_s_sleep(127); }
__device__ __forceinline__ f32x4 mfma32(bf16x8 a, bf16x8 b, f32x4 c) { return __builtin_amdgcn_mfma_f32_16x16x32_bf16(a, b, c, 0, 0, 0); }
__device__ __forceinline__ f32x4 mfma16(bf16x4 a, bf16x4 b, f32x4 c) { return __builtin_amdgcn_mfma_f32_16x16x16bf16_1k(a, b, c, 0, 0, 0); }
template <bool AG, bool SW, bool FIRST>
__device__ __forceinline__ void mmaw(f32x4& acc, const bf16x8& w, const bf16x8& x) {
    if constexpr (FIRST) {
        if constexpr (AG && !SW) asm volatile("v_mfma_f32_16x16x32_bf16 %0, %1, %2, 0" : "=&v"(acc) : "a"(w), "v"(x));
        else if constexpr (AG && SW) asm volatile("v_mfma_f32_16x16x32_bf16 %0, %2, %1, 0" : "=&v"(acc) : "a"(w), "v"(x));
        else if constexpr (!SW) asm volatile("v_mfma_f32_16x16x32_bf16 %0, %1, %2, 0" : "=&v"(acc) : "v"(w), "v"(x));
        else asm volatile("v_mfma_f32_16x16x32_bf16 %0, %2, %1, 0" : "=&v"(acc) : "v"(w), "v"(x));
    } else {
        if constexpr (AG && !SW) asm volatile("v_mfma_f32_16x16x32_bf16 %0, %1, %2, %0" : "+v"(acc) : "a"(w), "v"(x));
        else if constexpr (AG && SW) asm volatile("v_mfma_f32_16x16x32_bf16 %0, %2, %1, %0" : "+v"(acc) : "a"(w), "v"(x));
        else if constexpr (!SW) asm volatile("v_mfma_f32_16x16x32_bf16 %0, %1, %2, %0" : "+v"(acc) : "v"(w), "v"(x));
        else asm volatile("v_mfma_f32_16x16x32_bf16 %0, %2, %1, %0" : "+v"(acc) : "v"(w), "v"(x));
    }
}
#define NOP2() asm volatile("s_nop 1")
#define NOP8() asm volatile("s_nop 7")
#define PIN(x) asm volatile("" : "+v"(x))
#define PINA(x) asm volatile("" : "+a"(x))
template <bool PERM = false>
__device__ __forceinline__ void load_pair(const u16* W, int ld, int krow, int ncol, bf16x8& f0, bf16x8& f1) {
    u32 d[8];
#pragma unroll
    for (int j = 0; j < 8; ++j) d[j] = *(const u32*)(W + (size_t)(PERM ? krow + 16 * (j & 1) + (j >> 1) : krow + j) * ld + ncol);
    v4u a, b;
#pragma unroll
    for (int q = 0; q < 4; ++q) {
        a[q] = __builtin_amdgcn_perm(d[2 * q + 1], d[2 * q], 0x05040100u);
        b[q] = __builtin_amdgcn_perm(d[2 * q + 1], d[2 * q], 0x07060302u);
    }
    f0 = __builtin_bit_cast(bf16x8, a);
    f1 = __builtin_bit_cast(bf16x8, b);
}
template <bool PERM = false>
__device__ __forceinline__ void load_quad(const u16* W, int ld, int krow, int ncol, bf16x8& f0, bf16x8& f1, bf16x8& f2, bf16x8& f3) {
    v2u d[8];
#pragma unroll
    for (int j = 0; j < 8; ++j) d[j] = *(const v2u*)(W + (size_t)(PERM ? krow + 16 * (j & 1) + (j >> 1) : krow + j) * ld + ncol);
    v4u a, b, c, e;
#pragma unroll
    for (int q = 0; q < 4; ++q) {
        a[q] = __builtin_amdgcn_perm(d[2 * q + 1][0], d[2 * q][0], 0x05040100u);
        b[q] = __builtin_amdgcn_perm(d[2 * q + 1][0], d[2 * q][0], 0x07060302u);
        c[q] = __builtin_amdgcn_perm(d[2 * q + 1][1], d[2 * q][1], 0x05040100u);
        e[q] = __builtin_amdgcn_perm(d[2 * q + 1][1], d[2 * q][1], 0x07060302u);
    }
    f0 = __builtin_bit_cast(bf16x8, a); f1 = __builtin_bit_cast(bf16x8, b);
    f2 = __builtin_bit_cast(bf16x8, c); f3 = __builtin_bit_cast(bf16x8, e);
}
__device__ __forceinline__ v4u pack_quad(f32x4 t0, f32x4 t1, f32x4 t2, f32x4 t3, bool hf) {
    return v4u{pack2(hf ? t0[2] : t0[0], hf ? t1[2] : t1[0]), pack2(hf ? t2[2] : t2[0], hf ? t3[2] : t3[0]),
               pack2(hf ? t0[3] : t0[1], hf ? t1[3] : t1[1]), pack2(hf ? t2[3] : t2[1], hf ? t3[3] : t3[1])};
}
__device__ __forceinline__ v4u pack_pair(f32x4 t0, f32x4 t1) {
    v4u o;
#pragma unroll
    for (int r = 0; r < 4; ++r) o[r] = pack2(t0[r], t1[r]);
    return o;
}
__device__ __forceinline__ float vmax(float a, float b) { float r; asm("v_max_f32 %0, %1, %2" : "=v"(r) : "v"(a), "v"(b)); return r; }
__device__ __forceinline__ float xmax(float v) {
    auto a = __builtin_amdgcn_permlane16_swap(__float_as_uint(v), __float_as_uint(v), false, false);
    v = vmax(__uint_as_float(a[0]), __uint_as_float(a[1]));
    auto b = __builtin_amdgcn_permlane32_swap(__float_as_uint(v), __float_as_uint(v), false, false);
    return vmax(__uint_as_float(b[0]), __uint_as_float(b[1]));
}
__device__ __forceinline__ float xsum(float v) {
    auto a = __builtin_amdgcn_permlane16_swap(__float_as_uint(v), __float_as_uint(v), false, false);
    v = __uint_as_float(a[0]) + __uint_as_float(a[1]);
    auto b = __builtin_amdgcn_permlane32_swap(__float_as_uint(v), __float_as_uint(v), false, false);
    return __uint_as_float(b[0]) + __uint_as_float(b[1]);
}
template <int CTRL> __device__ __forceinline__ float dpp(float v) {
    return __uint_as_float(__builtin_amdgcn_update_dpp(0u, __float_as_uint(v), CTRL, 0xf, 0xf, false));
}
__device__ __forceinline__ float wave_sum(float v) {
    v += dpp<0xB1>(v);
    v += dpp<0x4E>(v);
    v += dpp<0x141>(v);
    v += dpp<0x140>(v);
    return xsum(v);
}
__device__ __forceinline__ f32x2 un2(u32 w) { return f32x2{bflo(w), bfhi(w)}; }
__device__ __forceinline__ u32 pack2(f32x2 v) { return pack2(v[0], v[1]); }
__device__ __forceinline__ void ln_par(float s, float q, f32x2& a, f32x2& b) {
    const float mean = s * (1.f / D_), inv = __builtin_amdgcn_rcpf(rbf(__builtin_amdgcn_sqrtf(rbf(rbf(fmaxf(q * (1.f / D_) - mean * mean, 0.f)) + 1e-5f))));
    a = f32x2{inv, inv};
    b = -rbf(mean) * a;
}
__device__ __forceinline__ void ln_row(const f32x2 (&v)[6], v2u (&y)[3]) {
    f32x2 s = v[0], q = v[0] * v[0], a, b;
#pragma unroll
    for (int i = 1; i < 6; ++i) { s += v[i]; q += v[i] * v[i]; }
    ln_par(wave_sum(s[0] + s[1]), wave_sum(q[0] + q[1]), a, b);
#pragma unroll
    for (int i = 0; i < 6; ++i) y[i >> 1][i & 1] = pack2(v[i] * a + b);
}

__device__ __forceinline__ u32 prog(u32 k) { return OFF_PROG + (k & 7) * 4096 + (k >> 3) * 256; }
#define CR 2
#define CA 1
#define CP 1
#define CB 1
constexpr u32 FOA = 8 * CX, FR = FOA + 8 * CA, FPB = FR + 2 * CR, FOB = FPB + 16 * CP, NPROG = FOB + 8 * CB;
static_assert(NPROG <= 128);
__device__ __forceinline__ u32 rdw(rsrc_t rs, u32 off) { return (u32)__builtin_amdgcn_raw_ptr_buffer_atomic_add_i32(0, rs, off, 0, 0); }
__device__ int buf_umax(int, rsrc_t, int, int, int) __asm("llvm.amdgcn.raw.ptr.buffer.atomic.umax.i32");
__device__ int buf_swap(int, rsrc_t, int, int, int) __asm("llvm.amdgcn.raw.ptr.buffer.atomic.swap.i32");
__device__ __forceinline__ void put(rsrc_t rs, u32 off, u32 v) { buf_swap(v, rs, off, 0, 0); }
__device__ __forceinline__ void tell(rsrc_t rs, u32 mine, u32 done) { buf_umax(done, rs, mine, 0, 0); }
template <int N> __device__ __forceinline__ u32 words(u32 off) { const u32 lane = threadIdx.x & 63; return lane < N ? off + lane * 4 : OOB; }
template <int N> __device__ __forceinline__ bool reached(u32 f, u32 need) { return ((u32)__ballot(f < need) & (u32)((1ull << N) - 1)) == 0u; }
template <int N> __device__ __forceinline__ u32 wait_prog(char* ws, rsrc_t rs, u32 off, u32 need) {
    u32 spins = 0, f;
    while (!reached<N>(f = rdw(rs, off), need)) {
        if (spin_fail(ws, spins)) break;
        __builtin_amdgcn_s_sleep(SLP_A);
    }
    CBAR();
    return __builtin_amdgcn_readfirstlane(f);
}
#define PS 8
#define AS 0
#define BS 2
template <int N, bool ANN, int P> __device__ __forceinline__ u32 watch(char* ws, rsrc_t rs, u32 mine, u32 done, u32 off, u32 need) {
    u32 f = rdw(rs, off), spins = 0;
    if (ANN) { VMW(1); tell(rs, mine, done); PIN(f); }
    if (!reached<N>(f, need))
        do {
            if (P) __builtin_amdgcn_s_sleep(P);
            f = rdw(rs, off);
        } while (!reached<N>(f, need) && !spin_fail(ws, spins));
    CBAR();
    return __builtin_amdgcn_readfirstlane(f);
}
template <int N> __device__ __forceinline__ void meet(char* ws, rsrc_t rs, bool me, u32 mine, u32 done, u32 off, u32 need) {
    if (me) watch<N, true, BS>(ws, rs, mine, done, off, need);
    else { DRAIN(); tell(rs, mine, done); }
    asm volatile("s_barrier" ::: "memory");
}
__device__ __forceinline__ bool told(const char* p) { return __builtin_amdgcn_readfirstlane(*(const u32*)p); }
__device__ __forceinline__ u32 my_word() { return words<1>(OFF_ACK + (BID * 4 + (threadIdx.x >> 6)) * 4); }
__device__ __forceinline__ u32 ask(rsrc_t rs) { DRAIN(); return rdw(rs, my_word()); }
__device__ __forceinline__ void go(rsrc_t rs, u32 a) {
    for (u32 spins = 0; !reached<1>(a, 1u) && spins < SPIN_CAP; ++spins) { __builtin_amdgcn_s_sleep(SLP_A); a = rdw(rs, my_word()); }
    CBAR();
    put(rs, my_word(), 2u);
}
#define CLR 16
template <int E = 1> __device__ __forceinline__ void clear(rsrc_t rs, u32 off, int n) {
#pragma unroll 5
    for (int i = 0; i < n; ++i) __builtin_amdgcn_raw_buffer_store_b128(v4u{0u, 0u, 0u, 0u}, rs, off + (i * 32 + (threadIdx.x >> 3)) * (128 * E) + (threadIdx.x & 7) * 16, 0, CLR);
}
__device__ __forceinline__ void role_lead(char* ws, rsrc_t rs) {
    const u32 tid = threadIdx.x;
    clear(rs, 0, 3);
#pragma unroll
    for (u32 i = FX_W ? CX / 4 : 0; i < (NPROG + 31) / 32; ++i) st16(rs, prog(i * 32 + (tid >> 3)) + (tid & 7) * 16, v4u{0u, 0u, 0u, 0u});
    DRAIN();
    __syncthreads();
#pragma unroll
    for (int k = 0; k < 4; ++k) put(rs, OFF_ACK + (k * 256 + tid) * 4, 1u);
    DRAIN();
    u32 pend = tid < 248 || tid >= 252 ? 15u : 7u, spins = 0;
    bool z = tid < 128;
    while (__any(pend)) {
        u32 old[4];
#pragma unroll
        for (int k = 0; k < 4; ++k) old[k] = __hip_atomic_fetch_max((gu32*)(ws + OFF_ACK) + k * 256 + tid, 1u, RLX_AGENT);
#pragma unroll
        for (int k = 0; k < 4; ++k) if (old[k] == 2u || (old[k] == 0u && !(k == 2 && tid < 128))) pend &= ~(1u << k);
        if (z && !__any(pend & 4u)) { tell(rs, words<1>(OFF_Z + (tid >> 6) * 4), 1u); z = false; }
        if (spin_fail(ws, spins)) break;
        __builtin_amdgcn_s_sleep(SLP_A);
    }
}
typedef short s16x4 __attribute__((ext_vector_type(4)));
__device__ __forceinline__ v2u tr_read(const char* p) {
    return __builtin_bit_cast(v2u, __builtin_amdgcn_ds_read_tr16_b64_v4i16((__attribute__((address_space(3))) s16x4*)p));
}
__device__ __forceinline__ bf16x8 asf(v4u v) { return __builtin_bit_cast(bf16x8, v); }

constexpr u32 TILE = 18432;
__device__ __forceinline__ u32 quad(int lane) { return ((lane >> 2) & 3) * 4608 + (lane >> 4) * 64 + (lane & 3) * 16; }
__device__ __forceinline__ v4u ldt(rsrc_t r, u32 vo, u32 so) { return __builtin_amdgcn_raw_buffer_load_b128(r, vo, so, 16); }
__device__ __forceinline__ void stt(rsrc_t r, u32 vo, u32 so, v4u v) {
    __builtin_amdgcn_raw_buffer_store_b128(v, r, vo, so, 16);
    asm volatile("s_nop 1" :: "v"(v) : "memory");
}

__device__ __forceinline__ void ld_tile(int tile, const u16* x0, v4u (&t)[18]) {
    const int lane = threadIdx.x & 63;
#pragma unroll
    for (int ks = 0; ks < 18; ++ks) t[ks] = *(const v4u*)(x0 + (size_t)(tile * 16 + (lane & 15)) * D_ + ks * 32 + (lane >> 4) * 8);
}
__device__ __forceinline__ void ln_tile(v4u (&t)[18]) {
    f32x2 s = {0.f, 0.f}, q = {0.f, 0.f}, a, b;
#pragma unroll
    for (int i = 0; i < 72; ++i) { const f32x2 v = un2(t[i >> 2][i & 3]); s += v; q += v * v; }
    ln_par(xsum(s[0] + s[1]), xsum(q[0] + q[1]), a, b);
#pragma unroll
    for (int i = 0; i < 72; ++i) t[i >> 2][i & 3] = pack2(un2(t[i >> 2][i & 3]) * a + b);
}
__device__ __forceinline__ void role_t0(int tile, const u16* x0, char* ws, rsrc_t rs) {
    v4u t[18];
    ld_tile(tile, x0, t);
    const u32 a = ask(rs);
    ln_tile(t);
    go(rs, a);
#pragma unroll
    for (int ks = 0; ks < 18; ++ks) stt(rs, quad(threadIdx.x & 63) + (ks & 15) * 256, OFF_XNA + tile * TILE + (ks >> 4) * 4096, t[ks]);
    DRAIN();
    tell(rs, words<16>(OFF_CXNA + tile * 64), NSEG);
}

constexpr int SL = 56;
template <int NR, bool FIN, int S0 = 0, int S1 = NSEG>
__device__ __forceinline__ void role_r(int idx, const u16* x0, u16* out, char* ws, rsrc_t rs, char* smem) {
    const int tid = threadIdx.x, lane = tid & 63, wave = __builtin_amdgcn_readfirstlane(tid >> 6);
    const bool act = lane < 48;
    const int lc = act ? lane : 0;
    const int rl = (idx * 4 + wave) * NR;
    const u32 po = act ? lane * 16 : OOB, xo = act ? (lane >> 1) * 64 + (lane & 1) * 8 : OOB;
    const u32 fp = words<24>(prog(FPB + idx % CP * 16 + (FIN ? 4 : 0) + (rl >> 3) + (lane < 12 ? 0 : 8)) + 16), fo = lane < CR ? prog(FR + lane * 2 + (rl >> 4)) + (rl & 15) * 4 : OOB;
    if (!FIN) {
        v2u xr[2][3];
#pragma unroll
        for (int i = 0; i < 6; ++i) xr[i / 3][i % 3] = *(const v2u*)(x0 + ((i / 3) * SEG + rl) * D_ + ((i % 3) * 48 + lc) * 4);
        const u32 a = ask(rs);
#pragma unroll
        for (int u = 0; u < 2; ++u) {
            f32x2 v[6];
#pragma unroll
            for (int i = 0; i < 6; ++i) v[i] = un2(act ? xr[u][i >> 1][i & 1] : 0u);
            v2u y[3];
            ln_row(v, y);
#pragma unroll
            for (int j = 0; j < 3; ++j) if (act) *(v2u*)(smem + 9216 + u * 4608 + (j * 24 + (lc >> 1)) * 64 + wave * 16 + (lc & 1) * 8) = y[j];
        }
        LBAR();
#pragma unroll
        for (int i = 0; i < 3; ++i) {
            const int c = tid + 256 * i;
            if (c < 576) st16(rs, OFF_XNA + (idx >> 2) * TILE + (idx & 3) * 4608 + (c < 288 ? c * 16 : 2 * TILE - 4608 + c * 16), *(const v4u*)(smem + 9216 + c * 16));
        }
        DRAIN();
        go(rs, a);
        tell(rs, lane < 2 ? OFF_CXNA + lane * 128 + rl * 4 : OOB, NSEG);
    }
    bool owe = false;
    const u32 f1 = wave ? OOB : fp;
    meet<24>(ws, rs, !wave, OOB, 0, fp, S0 + 1);
    const rsrc_t ro = __builtin_amdgcn_make_buffer_rsrc(out, 0, XB, 0x00020000);
    v2u xn[NR][3];
    auto ldx = [&](int s) {
#pragma unroll
        for (int i = 0; i < 3 * NR; ++i)
            xn[i / 3][i % 3] = __builtin_amdgcn_raw_buffer_load_b64(rs, xo + (i % 3) * 1536, OFF_X + (FIN ? 2 : 0) * XB + (s * 2 + (rl >> 4)) * TILE + ((rl >> 2) & 3) * 4608 + ((rl + i / 3) & 3) * 16, 16);
    };
    ldx(S0);
#pragma unroll 1
    for (int s = S0; s < S1; ++s) {
        v2u xr[NR][3];
        v4u pr[NR][4][3];
#pragma unroll
        for (int u = 0; u < NR; ++u) {
#pragma unroll
            for (int p = 0; p < 4; ++p)
#pragma unroll
                for (int j = 0; j < 3; ++j) pr[u][p][j] = ldt(rs, po + j * 768, OFF_PB + (((FIN ? 64 + s : s) * 4 + p) * SEG + rl + u) * D_ * 4);
        }
        SCHED();
#pragma unroll
        for (int i = 0; i < 3 * NR; ++i) xr[i / 3][i % 3] = xn[i / 3][i % 3];
        ldx(min(s + 1, NSEG - 1));
        SCHED();
        if (!FIN && owe) { VMW(15 * NR); tell(rs, fo, s); }
        u32 fn;
        if (FIN) { CBAR(); fn = rdw(rs, f1); }
        char* sm = smem + (s & 1) * 9216;
#pragma unroll
        for (int u = 0; u < NR; ++u) {
            f32x2 v[6];
#pragma unroll
            for (int i = 0; i < 6; ++i) {
                auto P = [&](int p) { return f32x2{__uint_as_float(pr[u][p][i >> 1][2 * (i & 1)]), __uint_as_float(pr[u][p][i >> 1][2 * (i & 1) + 1])}; };
                v[i] = ((P(0) + P(1)) + (P(2) + P(3))) + un2(xr[u][i >> 1][i & 1]);
            }
            v2u y[3];
            if (!FIN) { PIN(v[5]); CBAR(); fn = rdw(rs, f1); }
            if (!FIN) ln_row(v, y);
            if (FIN) {
#pragma unroll
                for (int j = 0; j < 3; ++j) __builtin_amdgcn_raw_buffer_store_b64(v2u{pack2(v[2 * j]), pack2(v[2 * j + 1])}, ro, (po >> 1) + j * 384, (s * SEG + rl + u) * (D_ * 2), 0);
            } else if (act) {
#pragma unroll
                for (int j = 0; j < 3; ++j) {
                    const v2u xw = {pack2(v[2 * j]), pack2(v[2 * j + 1])};
                    {
                        *(v2u*)(sm + (j * 24 + (lc >> 1)) * 64 + wave * 16 + (lc & 1) * 8) = y[j];
                        *(v2u*)(sm + 4608 + (j * 24 + (lc >> 1)) * 64 + wave * 16 + (lc & 1) * 8) = xw;
                    }
                }
            }
        }
        SCHED();
        PIN(fn);
        if (!wave) *(u32*)(smem + 32768 + (s & 1) * 4) = reached<24>(fn, s + 2);
        LBAR();
        owe = told(smem + 32768 + (s & 1) * 4);
        if (!FIN) {
#pragma unroll
            for (int i = 0; i < 3; ++i) {
                const int c = tid + 256 * i, c2 = c < 288 ? c : c - 288;
                if (c < 576) st16(rs, (c < 288 ? OFF_XNA : OFF_X) + XB + (s * 2 + (idx >> 2)) * TILE + (idx & 3) * 4608 + c2 * 16, *(const v4u*)(sm + c * 16));
            }
        }
        CBAR();
        if (!owe) {
            if (!FIN) meet<24>(ws, rs, !wave, fo, s + 1, fp, min(s + 2, NSEG));
            else if (s + 1 < S1) { if (!wave) watch<24, false, BS>(ws, rs, 0, 0, fp, s + 2); asm volatile("s_barrier" ::: "memory"); }
        }
    }
}

__device__ __forceinline__ void role_a1(int layer, int hp, const u16* wqkv, char* ws, rsrc_t rs, char* smem) {
    const int tid = threadIdx.x, lane = tid & 63, l15 = lane & 15, g = lane >> 4;
    const int wave = __builtin_amdgcn_readfirstlane(tid >> 6), hd = wave >> 1, m = wave & 1;
    bf16x8 w[18][4];
    v4u x[18];
    u32 a0;
    const u32 fx = wave < 3 ? OOB : words<32>(layer ? prog(FR + hp % CR * 2 + (g & 1)) - (g & 1) * 64 : OFF_CXNA), fs = layer ? 0 : 128, xb = OFF_XNA + layer * XB + m * TILE;
    const u32 fo = lane < CA ? prog(FOA + lane * 8 + (layer * 2 + m) * 2) + hp * 4 : OOB, ql = quad(lane);
    {
        const rsrc_t wr = __builtin_amdgcn_make_buffer_rsrc((void*)wqkv, 0, D_ * NQA * 2, 0x00020000);
        const int c = l15 ^ (2 * (g | hd << 2));
        const u32 vo = c < 15 ? (4 * wave + g) * (NQA * 2) + (c / 5) * (D_ * 2) + ((hp * 72) & ~15) + (c % 5) * 16 : OOB;
#pragma unroll
        for (int i = 0; i < 36; ++i) __builtin_amdgcn_raw_ptr_buffer_load_lds(wr, (__attribute__((address_space(3))) void*)(smem + (4 * i + wave) * 1024), 16, vo, i * 16 * NQA * 2, 0, 0);
        const char* rd[4];
#pragma unroll
        for (int t = 0; t < 4; ++t) {
            const int by = (hp & 1) * 8 + (t == 2 ? 32 + 80 * min(l15 & 3, 2) : hd * 40 + (l15 & 3) * 8 + 80 * min(t, 2));
            rd[t] = smem + (8 * g + (l15 >> 2)) * 256 + (by ^ (32 * ((l15 >> 2) | (g & 1) << 2)));
        }
        a0 = ask(rs);
        LBAR();
#pragma unroll
        for (int ks = 0; ks < 18; ++ks)
#pragma unroll
            for (int t = 0; t < 4; ++t) {
                const v2u a = tr_read(rd[t] + ks * 8192), b = tr_read(rd[t] + ks * 8192 + 1024);
                w[ks][t] = asf(v4u{a[0], a[1], b[0], b[1]});
            }
#pragma unroll
        for (int i = 0; i < 72; ++i) if (i < 60) PINA(w[i >> 2][i & 3]); else PIN(w[i >> 2][i & 3]);
        LBAR();
    }
    char* sK = smem + hd * 8192;
    char* sV = smem + 16384 + hd * 8192;
#pragma unroll
    for (int i = 0; i < 8; ++i) *(v4u*)(smem + (i * 256 + tid) * 16) = v4u{0u, 0u, 0u, 0u};
    LBAR();
    const bool odd = l15 & 1;
    const u32 pm = odd ? 0xffff0000u : 0xffffu;
    bool ok[4];
#pragma unroll
    for (int i = 0; i < 4; ++i) { const int d = 16 + l15 - (16 * (i >> 1) + 4 * g + 2 * (i & 1) + (l15 & 1)); ok[i] = d >= 0 && d <= 14; }
    const u32 vb = (4 * g + (l15 >> 2)) * 48 + (l15 & 3) * 8, vb2 = (4 * g + (l15 >> 2)) * 48 + ((l15 & 3) == 0 ? 32 : 40);
    char* sO = smem + 32768 + m * 1024;
    bool owe = false;
    auto body = [&](int s, auto own) {
        auto ldx = [&](int ks) { if constexpr (!own) x[ks] = ldt(rs, ql + (ks & 15) * 256, xb + s * 2 * TILE + (ks >> 4) * 4096); SCHED(); };
#pragma unroll
        for (int ks = 0; ks < 12; ++ks) ldx(ks);
        if (owe) { VMW(12); tell(rs, fo + (~s & 1) * 4096, s); }
        f32x4 acc[4];
#pragma unroll
        for (int ks = 0; ks < 18; ++ks) {
#pragma unroll
            for (int t = 0; t < 4; ++t) {
                if (ks == 0) mmaw<true, false, true>(acc[t], w[ks][t], asf(x[ks]));
                else if (ks < 15) mmaw<true, false, false>(acc[t], w[ks][t], asf(x[ks]));
                else mmaw<false, false, false>(acc[t], w[ks][t], asf(x[ks]));
            }
            SCHED();
            if (ks < 6) ldx(ks + 12);
        }
        CBAR();
        NOP8();
#pragma unroll
        for (int t = 0; t < 4; ++t) PIN(acc[t]);
        const int T = (2 * s + m) & 7, Tp = (T + 7) & 7;
        const u32 e01 = pack2(hd ? acc[2][2] : acc[2][0], hd ? acc[2][3] : acc[2][1]);
        const v4u qf = {pack2(acc[0][0], acc[0][1]), pack2(acc[0][2], acc[0][3]), g ? 0u : e01, 0u};
        const v4u kf = {pack2(acc[1][0], acc[1][1]), pack2(acc[1][2], acc[1][3]), __builtin_amdgcn_permlane16_swap(e01, e01, false, false)[1], 0u};
        *(v4u*)(sK + T * 1024 + lane * 16) = kf;
        *(v2u*)(sV + T * 1024 + l15 * 48 + g * 8) = v2u{pack2(acc[3][0], acc[3][1]), pack2(acc[3][2], acc[3][3])};
        if (g == 2) *(u32*)(sV + T * 1024 + l15 * 48 + 32) = e01;
        const f32x4 s1 = mfma32(asf(kf), asf(qf), f32x4{0.f, 0.f, 0.f, 0.f});
        LBAR();
        const u32 fn = rdw(rs, fx + (s + 1 < NSEG ? s + 1 : s) * fs);
        CBAR();
        const v4u kp = *(const v4u*)(sK + Tp * 1024 + lane * 16);
        const v2u va0 = tr_read(sV + Tp * 1024 + vb), va1 = tr_read(sV + T * 1024 + vb), vc0 = tr_read(sV + Tp * 1024 + vb2), vc1 = tr_read(sV + T * 1024 + vb2);
        const f32x4 s0 = mfma32(asf(kp), asf(qf), f32x4{0.f, 0.f, 0.f, 0.f});
        float l[4] = {odd ? s0[1] : s0[0], odd ? s0[3] : s0[2], odd ? s1[1] : s1[0], odd ? s1[3] : s1[2]};
        constexpr float SC = 0.23570226039551584f * 1.4426950408889634f;
#pragma unroll
        for (int i = 0; i < 4; ++i) l[i] = ok[i] && (i > 1 || s + m) ? l[i] : -3.0e38f;
        const float mx = -SC * xmax(fmaxf(fmaxf(l[0], l[1]), fmaxf(l[2], l[3])));
        v4u p;
#pragma unroll
        for (int i = 0; i < 4; ++i) { l[i] = __builtin_amdgcn_exp2f(l[i] * SC + mx); p[i] = pack2(l[i], l[i]) & pm; }
        const f32x4 oa = mfma32(asf(v4u{va0[0], va0[1], va1[0], va1[1]}), asf(p), f32x4{0.f, 0.f, 0.f, 0.f});
        const f32x4 ob = mfma32(asf(v4u{vc0[0], vc0[1], vc1[0], vc1[1]}), asf(p), f32x4{0.f, 0.f, 0.f, 0.f});
        const float inv = __builtin_amdgcn_rcpf(xsum((l[0] + l[1]) + (l[2] + l[3])));
        const v2u o = {pack2(oa[0] * inv, oa[1] * inv), pack2(oa[2] * inv, oa[3] * inv)};
        const u32 oe = pack2(ob[0] * inv, ob[1] * inv);
        if (hd) { *(v2u*)(sO + lane * 8) = o; if (g == 0) *(u32*)(sO + 512 + lane * 4) = oe; }
        LBAR();
        if (!hd) {
            const v2u o2 = *(const v2u*)(sO + lane * 8);
            const u32 oe2 = *(const u32*)(sO + 512 + l15 * 4);
            const u32 dst = OFF_OA + layer * XB + (s * 2 + m) * TILE;
            stt(rs, lane * 16, dst + hp * 1024, v4u{o[0], o[1], o2[0], o2[1]});
            if (g == 0) st8(rs, dst + 16384 + hp * 128 + lane * 8, v2u{oe, oe2});
        }
        CBAR();
        if (wave == 3) *(u32*)(smem + 36864) = reached<32>(fn, s + 2);
        LBAR();
        const bool have = told(smem + 36864);
        owe = have && !hd;
        if (!have) meet<32>(ws, rs, wave == 3, hd ? OOB : fo + (s & 1) * 4096, s + 1, fx + (s + 1 < NSEG ? s + 1 : s) * fs, min(s + 2, NSEG));
    };
    go(rs, a0);
    meet<32>(ws, rs, wave == 3, OOB, 0, fx, 1);
    for (int s = 0; s < NSEG; ++s) body(s, std::false_type());
}

__device__ __forceinline__ void role_a2(int layer, int nb, const u16* x0, const u16* woA, char* ws, rsrc_t rs, char* smem) {
    const int tid = threadIdx.x, lane = tid & 63, l15 = lane & 15, g = lane >> 4;
    const int wave = __builtin_amdgcn_readfirstlane(tid >> 6), m = wave & 1;
    bf16x8 w[18][4];
    {
        v4u t[18];
#pragma unroll
        for (int i = 0; i < 18; ++i) t[i] = *(const v4u*)(woA + (size_t)(i * 32 + (tid >> 3)) * D_ + nb * 64 + (tid & 7) * 8);
#pragma unroll
        for (int i = 0; i < 18; ++i) *(v4u*)(smem + (i * 32 + (tid >> 3)) * 144 + (tid & 7) * 16) = t[i];
        __syncthreads();
        const int q = l15 >> 2;
        const char* ra = smem + (4 * g + q) * 144 + (l15 & 3) * 16;
        const char* rb = smem + (36 * g + 16 + q) * 144 + (l15 & 3) * 16;
#pragma unroll
        for (int ks = 0; ks < 18; ++ks)
#pragma unroll
            for (int T = 0; T < 4; ++T) {
                const int co = (T >> 1) * 64 + (T & 1) * 8;
                v2u a, b;
                if (ks < 16) { a = tr_read(ra + ks * 36 * 144 + co); b = tr_read(ra + (ks * 36 + 20) * 144 + co); }
                else { a = tr_read(rb + (ks - 16) * 288 * 144 + co); b = tr_read(rb + ((ks - 16) * 288 + 144) * 144 + co); }
                w[ks][T] = asf(v4u{a[0], a[1], b[0], b[1]});
            }
#pragma unroll
        for (int i = 0; i < 72; ++i) if (i < 60) PINA(w[i >> 2][i & 3]); else PIN(w[i >> 2][i & 3]);
    }
    go(rs, ask(rs));
    wait_prog<2>(ws, rs, words<2>(OFF_Z), 1);
    const u32 fi = words<16>(prog(FOA + nb % CA * 8 + (layer * 2 + m) * 2 + (wave >> 1))), fo = lane < CX ? FX_W + prog(lane * 8 + (layer * 2 + m) * 2 + (wave >> 1)) + nb * 4 : OOB;
    v4u xn[2];
    auto ldr = [&](int s) {
#pragma unroll
        for (int P = 0; P < 2; ++P) xn[P] = *(const v4u*)(x0 + (size_t)(s * SEG + m * 16 + l15) * D_ + nb * 64 + P * 32 + g * 8);
    };
    if (layer == 0) ldr(wave >> 1);
    watch<16, false, PS>(ws, rs, 0, 0, fi, (wave >> 1) + 1);
    for (int s = wave >> 1; s < NSEG; s += 2) {
        const u32 task = (layer * 64 + s) * 2 + m;
        const u32 ro = (s * 2 + m) * TILE + nb * 2048, rq = (s * 2 + m) * TILE + nb * 512;
        v4u x[18], xr[2];
#pragma unroll
        for (int ks = 0; ks < 16; ++ks) x[ks] = ldt(rs, lane * 16 + (ks & 3) * 1024, OFF_OA + layer * XB + (s * 2 + m) * TILE + (ks >> 2) * 4096);
#pragma unroll
        for (int e = 0; e < 2; ++e) {
            const v2u a = __builtin_amdgcn_raw_buffer_load_b64(rs, lane * 8 + e * 1024, OFF_OA + layer * XB + (s * 2 + m) * TILE + 16384, 16);
            const v2u b = __builtin_amdgcn_raw_buffer_load_b64(rs, lane * 8 + e * 1024 + 512, OFF_OA + layer * XB + (s * 2 + m) * TILE + 16384, 16);
            x[16 + e] = v4u{a[0], a[1], b[0], b[1]};
        }
        if (layer != 0) {
#pragma unroll
            for (int P = 0; P < 2; ++P) xr[P] = ldt(rs, quad(lane) + P * 256, OFF_X + XB + rq);
        } else {
            xr[0] = xn[0]; xr[1] = xn[1];
            ldr(min(s + 2, NSEG - 1));
        }
        f32x4 acc[4];
        f32x2 xf[8];
#pragma unroll
        for (int ks = 0; ks < 18; ++ks)
#pragma unroll
            for (int t = 0; t < 4; ++t) {
                if (ks == 0) mmaw<true, false, true>(acc[t], w[ks][t], asf(x[ks]));
                else if (ks < 15) mmaw<true, false, false>(acc[t], w[ks][t], asf(x[ks]));
                else mmaw<false, false, false>(acc[t], w[ks][t], asf(x[ks]));
                if (ks >= 16) { u32 xw = xr[ks & 1][t]; PIN(xw); xf[(ks & 1) * 4 + t] = un2(xw); PIN(xf[(ks & 1) * 4 + t]); }
            }
        NOP8();
        f32x2 v[8];
#pragma unroll
        for (int i = 0; i < 8; ++i) { PIN(acc[i >> 1]); v[i] = f32x2{acc[i >> 1][2 * (i & 1)], acc[i >> 1][2 * (i & 1) + 1]} + xf[i]; }
        f32x2 su = v[0], sq = v[0] * v[0], a, b;
#pragma unroll
        for (int i = 1; i < 8; ++i) { su += v[i]; sq += v[i] * v[i]; }
        const u32 so = OFF_STAT + task * 2304 + l15 * 8;
        asm volatile("buffer_atomic_or_x2 %0, %1, %2, 0 offen" :: "v"(v2u{__float_as_uint(xsum(su[0] + su[1])), __float_as_uint(xsum(sq[0] + sq[1])) | 1u}),
                     "v"(g ? OOB : so + nb * 256), "s"(rs) : "memory");
        u64 st[3] = {0, 0, g ? 1ull << 32 : 0};
        u32 spins = 0;
        __builtin_amdgcn_s_sleep(SLP_S);
        for (;;) {
            CBAR();
            u64 z = 0;
            PIN(z);
#pragma unroll
            for (int i = 0; i < 3; ++i) if ((st[i] >> 32) == 0) st[i] = __hip_atomic_fetch_add((GAS u64*)(ws + (so + (g + 4 * i) * 256)), z, RLX_AGENT);
            if (__all((st[0] >> 32) != 0 && (st[1] >> 32) != 0 && (st[2] >> 32) != 0)) break;
            if (spin_fail(ws, spins)) break;
        }
        ln_par(xsum(__uint_as_float((u32)st[0]) + __uint_as_float((u32)st[1]) + __uint_as_float((u32)st[2])),
               xsum(__uint_as_float(st[0] >> 32) + __uint_as_float(st[1] >> 32) + __uint_as_float(st[2] >> 32)), a, b);
#pragma unroll
        for (int P = 0; P < 2; ++P) stt(rs, lane * 16 + P * 1024, XNB_W + layer * XB + ro, v4u{pack2(v[4 * P] * a + b), pack2(v[4 * P + 1] * a + b), pack2(v[4 * P + 2] * a + b), pack2(v[4 * P + 3] * a + b)});
#pragma unroll
        for (int P = 0; P < 2; ++P) stt(rs, quad(lane) + P * 256, OFF_X + 2 * layer * XB + rq, v4u{pack2(v[4 * P]), pack2(v[4 * P + 1]), pack2(v[4 * P + 2]), pack2(v[4 * P + 3])});
        if ((s & ~1) == 8) wait_prog<1>(ws, rs, words<1>(OFF_Z2), 112);
        watch<16, true, PS>(ws, rs, fo, s + 1, fi, s + 2 < NSEG ? s + 3 : s + 1);
    }
}

__device__ __forceinline__ void role_w(int layer, int kb, int nb, const u16* Wo, char* ws, rsrc_t rs, char* smem) {
    const int tid = threadIdx.x, lane = tid & 63, l15 = lane & 15, g = lane >> 4;
    const int wave = __builtin_amdgcn_readfirstlane(tid >> 6);
    float* sR = (float*)smem;
    bf16x8 w[3][8][2];
#pragma unroll
    for (int i = 0; i < 3; ++i)
#pragma unroll
        for (int ks = 0; ks < 8; ++ks) load_pair<true>(Wo, D_, kb * 1024 + wave * 256 + ks * 32 + 4 * g, nb * 96 + i * 32 + 2 * l15, w[i][ks][0], w[i][ks][1]);
#pragma unroll
    for (int i = 0; i < 48; ++i) PINA(w[i >> 4][(i >> 1) & 7][i & 1]);
    const u32 fo = wave ? OOB : words<8>(prog(FOB + nb % CB * 8 + layer * 4 + kb)), ob = OFF_OB + (layer * 2048 + kb * 8 + wave * 2) * 8192;
    const u32 fp = lane < CP ? prog(FPB + lane * 16 + layer * 4 + wave + (kb >> 1) * 8) + 16 + (kb * 6 + nb) * 4 : OOB;
    bool owe = false;
    go(rs, ask(rs));
    meet<8>(ws, rs, !wave, OOB, 0, fo, 1);
    for (int s = 0; s < NSEG; ++s) {
        v4u tb[16];
        auto ldb = [&](int i) { tb[i] = ldt(rs, lane * 16 + (i & 3) * 1024, ob + s * 262144 + (i >> 2) * 4096); SCHED(); };
#pragma unroll
        for (int i = 0; i < 10; ++i) ldb(i);
        if (owe) { VMW(10); tell(rs, fp, s); }
        f32x4 acc[3][2][2];
        u32 fn;
#pragma unroll
        for (int ks = 0; ks < 8; ++ks) {
#pragma unroll
            for (int m = 0; m < 2; ++m) {
#pragma unroll
                for (int i = 0; i < 3; ++i)
#pragma unroll
                    for (int t = 0; t < 2; ++t) {
                        if (ks == 0) mmaw<true, false, true>(acc[i][t][m], w[i][ks][t], asf(tb[2 * ks + m]));
                        else mmaw<true, false, false>(acc[i][t][m], w[i][ks][t], asf(tb[2 * ks + m]));
                    }
                SCHED();
                if (ks < 3) ldb(2 * ks + m + 10);
            }
        }
        NOP8();
        float* sB = sR + (s & 1) * 12800;
#pragma unroll
        for (int m = 0; m < 2; ++m)
#pragma unroll
            for (int i = 0; i < 3; ++i) {
                if (m && i == 0) { CBAR(); fn = rdw(rs, fo); CBAR(); }
                PIN(acc[i][0][m]); PIN(acc[i][1][m]);
                const f32x4 a0 = acc[i][0][m], a1 = acc[i][1][m];
                float* dst = sB + (wave * 32 + m * 16 + l15) * 100 + i * 32 + 8 * g;
                *(f32x4*)dst = f32x4{a0[0], a1[0], a0[1], a1[1]};
                *(f32x4*)(dst + 4) = f32x4{a0[2], a1[2], a0[3], a1[3]};
            }
        LBAR();
#pragma unroll
        for (int i = 0; i < 3; ++i) {
            const int c = wave * 192 + lane + 64 * i, tok = c / 24, c4 = (c % 24) * 4;
            f32x4 a = *(const f32x4*)(sB + tok * 100 + c4);
#pragma unroll
            for (int wv = 1; wv < 4; ++wv) a += *(const f32x4*)(sB + (wv * 32 + tok) * 100 + c4);
            st16(rs, OFF_PB + ((((layer * 64 + s) * 4 + kb) * SEG + tok) * D_ + nb * 96 + c4) * 4, __builtin_bit_cast(v4u, a));
        }
        CBAR();
        if (!wave) *(u32*)(smem + 102400) = reached<8>(fn, s + 2);
        LBAR();
        owe = told(smem + 102400);
        if (!owe) meet<8>(ws, rs, !wave, fp, s + 1, fo, min(s + 2, NSEG));
    }
}

__device__ __forceinline__ int exp_above(float x) { return (int)((__float_as_uint(x) >> 23) & 255u) - 126; }
__device__ __forceinline__ float sq2(u32 w, float c) { return __builtin_amdgcn_fdot2_f32_bf16(__builtin_bit_cast(bfv2, w), __builtin_bit_cast(bfv2, w), c, false); }
__device__ __forceinline__ float wave_max(float v) {
    v = vmax(v, dpp<0xB1>(v)); v = vmax(v, dpp<0x4E>(v)); v = vmax(v, dpp<0x141>(v)); v = vmax(v, dpp<0x140>(v));
    return xmax(v);
}
__device__ __forceinline__ void par_done(char* ws, rsrc_t rs) {
    const int tid = threadIdx.x;
    DRAIN();
    __syncthreads();
    if (tid < 64) {
        u32 n = 0;
        if (tid == 0) n = __hip_atomic_fetch_add((gu32*)(ws + OFF_CC), 1u, RLX_AGENT);
        if (__builtin_amdgcn_readfirstlane(n) == 63) {
            const v2u fm = ld8(rs, OFF_CH + (tid & 31) * 8);
            const u32 b0 = ldw(ws, OFF_CH + 256);
            u32 ph, spins = 0;
            while ((ph = ldw(ws, OFF_CH + 384)) == 0u && !spin_fail(ws, spins)) __builtin_amdgcn_s_sleep(2);
            const float c = sqrtf(__uint_as_float(fm[0])) * sqrtf(__uint_as_float(fm[1]));
            const float cs = wave_sum(c) * 0.5f;
            const int e35 = min(max(exp_above(43.75f * cs), -100), 100), ek = min(max(exp_above(30.f * cs), -100), 100);
            if (tid == 0) st4(rs, OFF_PAR, 0x80000000u | (ph & 1u) << 24 | b0 << 16 | (u32)(e35 + 128) << 8 | (u32)(ek + 128));
        }
    }
}
#define PAGE_STEP 0
#define CODE_BYTES 83968
__device__ __forceinline__ void touch(int j, int xc, const u16* wqkv, const u16* Wq, const u16* Wk, const u16* Wv, const u16* Wo, rsrc_t rs) {
    const u32 L = j * 256 + threadIdx.x, o = (L >> 3) * NB + xc * 512 + (L & 7) * 64;
    auto get = [](const u16* p) { asm volatile("" :: "v"(*(const u32*)p) : "memory"); };

    get(wqkv + min(L >> 1, 1727u) * D_ + xc * 72 + (L & 1) * 70);
    get(Wv + o);
    if ((j & ~3) == 8) go(rs, ask(rs));
    get(Wo + (xc * 4608 + L) * 64);
    get(Wq + o);
    get(Wk + o);
}
__device__ __forceinline__ void role_par(int h, int p, const u16* mem0, const u16* Wv, const u16* Wo, char* ws, rsrc_t rs, char* smem) {
    const int tid = threadIdx.x;
    float* sR = (float*)smem;
    float* sW = sR + 1024;
    const u32 a0 = ask(rs);
    if (p == 0) {
        v4u w[36];
#pragma unroll
        for (int i = 0; i < 36; ++i) w[i] = *(const v4u*)(Wv + (size_t)(i * 16 + (tid >> 4)) * NB + h * 128 + (tid & 15) * 8);
        float f[4] = {0.f, 0.f, 0.f, 0.f};
#pragma unroll
        for (int i = 0; i < 144; ++i) f[i & 3] = sq2(w[i >> 2][i & 3], f[i & 3]);
        const float t = wave_sum((f[0] + f[1]) + (f[2] + f[3]));
        if ((tid & 63) == 0) sW[tid >> 6] = t;
        __syncthreads();
        go(rs, a0);
        if (tid == 0) st4(rs, OFF_CH + h * 8, __float_as_uint((sW[0] + sW[1]) + (sW[2] + sW[3])));
    } else {
        u32 me[9];
        v4u w[43];
        const int q = h * 72 + min(tid, 71);
        int i0, i1;
        if (q < 2048) { i0 = ((q >> 3) & 7) * D_ + (q >> 8) * 64 + ((q >> 6) & 3) * 16 + (q & 3) * 4 + ((q >> 2) & 1) * 2; i1 = i0 + 1; }
        else { i0 = ((q >> 4) & 3) * 2 * D_ + 512 + ((q >> 6) & 3) * 16 + (q & 15); i1 = i0 + D_; }
        const u16 m0 = mem0[i0], m1 = mem0[i1];
#pragma unroll
        for (int i = 0; i < 9; ++i) me[i] = ((const u32*)mem0)[i * 256 + tid];
#pragma unroll
        for (int i = 0; i < 43; ++i) w[i] = *(const v4u*)(Wo + (size_t)(h * 128 + min(3 * i + tid / 72, 127)) * D_ + (tid % 72) * 8);
        float mm = 0.f;
#pragma unroll
        for (int i = 0; i < 9; ++i) mm = fmaxf(mm, fmaxf(fabsf(bflo(me[i])), fabsf(bfhi(me[i]))));
        mm = wave_max(mm);
        if ((tid & 63) == 0) sW[tid >> 6] = mm;
        LBAR();
        const int b0 = 28 - min(max(exp_above(fmaxf(fmaxf(sW[0], sW[1]), fmaxf(sW[2], sW[3]))), -30), 90);
        f32x4 c = {0.f, 0.f, 0.f, 0.f};
#pragma unroll
        for (int i = 0; i < 172; ++i) c[i & 3] = sq2(w[i >> 2][i & 3], c[i & 3]);
        *(f32x4*)(sR + tid * 4) = c;
        if (tid < 72) {
            const v2u z = {(u32)(int)rintf(ldexpf(bf2f(m0), b0 - 3)) << 6 | 32u, (u32)(int)rintf(ldexpf(bf2f(m1), b0 + 3))};
            const u32 a = (q < 2048 ? (q >> 9) * 16384 + ((q >> 8) & 1) * 8192 + ((q >> 3) & 7) * 1024 + ((q >> 6) & 1) * 512 + ((q >> 7) & 1) * 256
                                    : 65536 + ((q >> 3) & 1) * 512 + ((q >> 4) & 3) * 2048 + ((q >> 6) & 1) * 256 + ((q >> 7) & 1) * 8192) + (q & 7) * 8;
            for (int i = 0; i < 4; ++i) {
                st8(rs, OFF_ACC + (2 + (i >> 1)) * (ACC_SLOT / 2) + (i & 1) * 64 + a, z);
                st8(rs, q < 2048 ? OOB : OFF_ACC + (2 + (i >> 1)) * (ACC_SLOT / 2) + 1024 + (i & 1) * 64 + a, v2u{32u, 0u});
            }
        }
        LBAR();
        go(rs, a0);
        if (tid < 64) {
            float m = 0.f;
#pragma unroll
            for (int i = 0; i < 5; ++i) { const int d = min(tid + 64 * i, 287); m = fmaxf(m, sR[d] + sR[288 + d] + sR[576 + d]); }
            m = wave_max(m);
            if (tid < 2) st4(rs, tid ? OFF_CH + 256 : OFF_CH + h * 8 + 4, tid ? (u32)(b0 + 3 + 128) : __float_as_uint(m));
        }
    }
    par_done(ws, rs);
}
__device__ __forceinline__ void role_side(char* ws, rsrc_t rs, char* smem) {
    const int lane = threadIdx.x & 63, wave = threadIdx.x >> 6;
    go(rs, ask(rs));
    const u32 base = ((OFF_JUNK + 16383u) & ~16383u) + wave * 32768;
    st16(rs, lane < 32 ? base + (lane >> 3) * 8192 + (lane & 7) * 16 : OOB, v4u{0u, 0u, 0u, 0u});
    int vote = 0;
    DRAIN();
#pragma unroll 1
    for (int r = 0; r < 2; ++r) {
        u32 t[2];
        __builtin_amdgcn_raw_ptr_buffer_atomic_add_i32(1, rs, lane < 2 ? base + r * 16384 + lane * 8192 : OOB, 0, 0);
        DRAIN();
#pragma unroll
        for (int e = 0; e < 2; ++e) {
            const u32 a = base + r * 16384 + e * 8192;
            v4u q;
            u64 t0, t1;
            asm volatile("s_memtime %0\n\ts_waitcnt lgkmcnt(0)\n\tbuffer_load_dwordx4 %2, %3, %4, 0 offen sc1\n\ts_waitcnt vmcnt(0)\n\ts_memtime %1\n\ts_waitcnt lgkmcnt(0)"
                         : "=&s"(t0), "=&s"(t1), "=&v"(q) : "v"(lane < 4 ? a + lane * 16 : OOB), "s"(rs) : "memory");
            t[e] = (u32)(t1 - t0);
        }
        const u32 near = t[1] < t[0], b21 = ((u32)(size_t)ws + base + r * 16384) >> 21 & 1;
        vote += (near ^ (BID >> 2 & 1) ^ b21) ? 1 : -1;
    }
    if (lane == 0) *(int*)(smem + wave * 4) = vote;
    __syncthreads();
    if (threadIdx.x == 0) { const int* v = (const int*)smem; st4(rs, OFF_CH + 384, v[0] + v[1] + v[2] + v[3] > 0 ? 3u : 2u); }
}
__device__ __forceinline__ void clear_late(int i, rsrc_t rs) {
    nap(NAP_A);
    clear<2>(rs, OFF_ACC + (10 + 2 * i) * ACC_SLOT, 40);
    go(rs, ask(rs));
    __builtin_amdgcn_raw_ptr_buffer_atomic_add_i32(1, rs, words<1>(OFF_Z2), 0, 0);
}

using v8u = __attribute__((ext_vector_type(8))) unsigned;
template <bool FIRST> __device__ __forceinline__ void mm16(f32x4& acc, v2u a, v2u b) {
    if constexpr (FIRST) asm volatile("v_mfma_f32_16x16x16_bf16 %0, %1, %2, 0" : "=&v"(acc) : "v"(a), "v"(b));
    else asm volatile("v_mfma_f32_16x16x16_bf16 %0, %1, %2, %0" : "+v"(acc) : "v"(a), "v"(b));
}
__device__ __forceinline__ v4u half(const v8u& v, int i) { return i ? v4u{v[4], v[5], v[6], v[7]} : v4u{v[0], v[1], v[2], v[3]}; }
__device__ __forceinline__ void role_att(int layer, int h, char* ws, rsrc_t rs) {
    const int lane = threadIdx.x & 63, l15 = lane & 15, g = lane >> 4;
    const u32 fl = words<6>(OFF_FKVM + ((layer * 32 + h) * 68 + 2) * 32);
    const u32 kvo = ((g >> 1) * 16 + ((l15 >> 3) | ((l15 >> 1) & 2)) * 4 + (l15 & 1) + (g & 1) * 2) * 16, vmo = (lane & 57) * 8 + (lane & 2) * 16 + (lane & 4) * 4;
    constexpr float SC = 0.08838834764831845f * 1.4426950408889634f;
    const u32 fo = lane < CB ? prog(FOB + lane * 8 + layer * 4 + (h >> 3)) + (h & 7) * 4 : OOB;
    v4u k[2][4], v[2][4];
    auto issue = [&](int s, v8u (&q)[2][2]) {
        const u32 blk = OFF_QKV + ((layer * 66 + s) * 32 + h) * 24576;
        v4u t[2][4];
#pragma unroll
        for (int w = 0; w < 4; ++w)
#pragma unroll
            for (int p = 0; p < 2; ++p) { t[p][w] = ldt(rs, lane * 16, blk + w * 6144 + p * 3072); k[p][w] = ldt(rs, lane * 16 + 1024, blk + w * 6144 + p * 3072); }
#pragma unroll
        for (int p = 0; p < 2; ++p)
#pragma unroll
            for (int i = 0; i < 2; ++i) q[p][i] = __builtin_shufflevector(t[p][2 * i], t[p][2 * i + 1], 0, 1, 2, 3, 4, 5, 6, 7);
    };
    auto issuev = [&](int s) {
        const u32 blk = OFF_QKV + ((layer * 66 + s) * 32 + h) * 24576;
#pragma unroll
        for (int p = 0; p < 2; ++p)
#pragma unroll
            for (int w = 0; w < 4; ++w) v[p][w] = ldt(rs, lane * 16 + 2048, blk + w * 6144 + p * 3072);
    };
    auto body = [&](int s, v8u (&q)[2][2], v8u (&qn)[2][2]) {
        const u32 mb = OFF_KVM + ((layer * 64 + s) * 32 + h) * 4096;
        f32x4 sc[3];
#pragma unroll
        for (int w = 0; w < 4; ++w)
#pragma unroll
            for (int u = 0; u < 3; ++u) {
                if (w == 0) mmaw<false, false, true>(sc[u], asf(k[u >> 1][w]), asf(half(q[u > 0][w >> 1], w & 1)));
                else mmaw<false, false, false>(sc[u], asf(k[u >> 1][w]), asf(half(q[u > 0][w >> 1], w & 1)));
            }
        NOP8();
        float mx[2] = {-3.0e38f, -3.0e38f}, sum[2] = {0.f, 0.f};
#pragma unroll
        for (int u = 0; u < 3; ++u) {
            PIN(sc[u]);
#pragma unroll
            for (int r = 0; r < 4; ++r) {
                if (u != 1) sc[u][r] = 4 * g + r <= l15 ? sc[u][r] : -3.0e38f;
                mx[u > 0] = fmaxf(mx[u > 0], sc[u][r]);
            }
        }
        SCHED();
        u32 f = rdw(rs, fl + s * 32);
        SCHED();
        v2u pt[3];
#pragma unroll
        for (int m = 0; m < 2; ++m) mx[m] = xmax(mx[m]) * SC;
#pragma unroll
        for (int u = 0; u < 3; ++u) {
#pragma unroll
            for (int r = 0; r < 4; ++r) { sc[u][r] = __builtin_amdgcn_exp2f(sc[u][r] * SC - mx[u > 0]); sum[u > 0] += sc[u][r]; }
            pt[u] = v2u{pack2(sc[u][0], sc[u][1]), pack2(sc[u][2], sc[u][3])};
            PIN(pt[u]);
        }
        VMW(9);
        tell(rs, fo, s);
        NOP2();
        f32x4 o[2][4][2], sm[2];
#pragma unroll
        for (int u = 0; u < 3; ++u)
#pragma unroll
            for (int w = 0; w < 4; ++w)
#pragma unroll
                for (int t = 0; t < 2; ++t) {
                    const v2u a = {v[u >> 1][w][2 * t], v[u >> 1][w][2 * t + 1]};
                    if (u < 2) mm16<true>(o[u][w][t], a, pt[u]); else mm16<false>(o[1][w][t], a, pt[u]);
                }
#pragma unroll
        for (int m = 0; m < 2; ++m) { sum[m] = xsum(sum[m]); PIN(sum[m]); PIN(mx[m]); }
        NOP8();
        SCHED();
        PIN(f);
        f = reached<6>(f, 1u) ? __builtin_amdgcn_readfirstlane(f) : watch<6, false, AS>(ws, rs, 0, 0, fl + s * 32, 1u);
        CBAR();
        v4u km[4];
        v2u vm[4];
#pragma unroll
        for (int w = 0; w < 4; ++w) km[w] = ldt(rs, kvo + w * 512, mb);
#pragma unroll
        for (int w = 0; w < 4; ++w) vm[w] = __builtin_amdgcn_raw_buffer_load_b64(rs, vmo, mb + 2048 + w * 512, 16);
        SCHED();
        issue(s + 1 < NSEG ? s + 1 : s, qn);
        SCHED();
        const float si = __uint_as_float((255u - ((f >> 8) & 255u)) << 23);
#pragma unroll
        for (int w = 0; w < 4; ++w)
#pragma unroll
            for (int m = 0; m < 2; ++m) {
                if (w == 0) mmaw<false, false, true>(sm[m], asf(km[w]), asf(half(q[m][w >> 1], w & 1)));
                else mmaw<false, false, false>(sm[m], asf(km[w]), asf(half(q[m][w >> 1], w & 1)));
            }
        NOP8();
        v2u pm[2][2];
        float a[2];
#pragma unroll
        for (int m = 0; m < 2; ++m) {
            PIN(sm[m]);
            float m0 = sm[m][0] * (si * SC), m1 = sm[m][1] * (si * SC);
            const float mm = fmaxf(xmax(fmaxf(m0, m1)), mx[m]);
            m0 = __builtin_amdgcn_exp2f(m0 - mm); m1 = __builtin_amdgcn_exp2f(m1 - mm);
            a[m] = __builtin_amdgcn_exp2f(mx[m] - mm);
            const float inv = __builtin_amdgcn_rcpf(xsum(m0 + m1) + a[m] * sum[m]);
            a[m] *= inv;
            const u32 pp = pack2(m0 * inv * si, m1 * inv * si);
            pm[m][0] = v2u{pp, 0u}; pm[m][1] = v2u{0u, pp};
            PIN(pm[m][0]); PIN(pm[m][1]);
        }
        auto scale = [&](int w) {
#pragma unroll
            for (int m = 0; m < 2; ++m)
#pragma unroll
                for (int t = 0; t < 2; ++t) { o[m][w][t] *= a[m]; PIN(o[m][w][t]); }
        };
        auto put = [&](int w) {
#pragma unroll
            for (int m = 0; m < 2; ++m) { PIN(o[m][w][0]); PIN(o[m][w][1]); stt(rs, lane * 16 + m * 1024, OFF_OB + (((layer * 64 + s) * 32 + h) * 4 + w) * 2048, pack_pair(o[m][w][0], o[m][w][1])); }
        };
        scale(0);
#pragma unroll
        for (int w = 0; w < 4; ++w) {
            if (w < 3) scale(w + 1); else NOP2();
#pragma unroll
            for (int m = 0; m < 2; ++m)
#pragma unroll
                for (int t = 0; t < 2; ++t) mm16<false>(o[m][w][t], vm[w], pm[m][t]);
            if (w) put(w - 1);
        }
        NOP8();
        put(3);
        issuev(s + 1 < NSEG ? s + 1 : s);
    };
    v8u A[2][2], B[2][2];
    go(rs, ask(rs));
    wait_prog<2>(ws, rs, words<2>(OFF_Z), 1);
    watch<6, false, PS>(ws, rs, 0, 0, fl - 32, 1u);
    issue(0, A);
    issuev(0);
    for (int s = 0; s < NSEG; s += 2) { body(s, A, B); body(s + 1, B, A); }
    VMW(8);
    tell(rs, fo, NSEG);
}

__global__ __launch_bounds__(256, 1) void mega_kernel(const u16* __restrict__ x0, const u16* __restrict__ wqkv, const u16* __restrict__ woA,
                                                     const u16* __restrict__ mem0, const u16* __restrict__ Wq, const u16* __restrict__ Wk,
                                                     const u16* __restrict__ Wv, const u16* __restrict__ Wo, u16* out, char* ws) {
    __shared__ __attribute__((aligned(16))) char smem[163840];
    const char* code;
    asm volatile("s_getpc_b64 %0" : "=s"(code));
    u32 ins = 0;
    {
        const u32 i = BID >> 3, L = ((i & 1) | (i >> 2) << 1) * 256 + threadIdx.x;
        if ((i & 2) && L * 128 < CODE_BYTES) ins = *(const GAS u32*)(code + L * 128);
    }
    const rsrc_t rs = __builtin_amdgcn_make_buffer_rsrc(ws, 0, WS_BYTES, 0x00020000);
    const int b = BID;
    const int xc = b & 7, ix = b >> 3;
    const int wave = __builtin_amdgcn_readfirstlane(threadIdx.x >> 6);
    put(rs, my_word(), 0u);
    if ((ix & ~3) == 16) {
#pragma unroll
        for (int i = 0; i < 5; ++i) {
            const int u = (b - 128) * 5 + i;
            if (u < 68) clear(rs, OFF_FKVM + u * 4096, 1); else clear<2>(rs, b < 156 ? OFF_STAT + (u - 68) * 8192 : OOB, 1);
        }
        clear<2>(rs, OFF_ACC + 2 * ACC_SLOT + (b - 128) * 40960, 5);
    }
    const u32 late = 0x4b3fccccu;
    if (late >> ix & 1) { touch(__popc(late << (31 - ix)) - 1, xc, wqkv, Wq, Wk, Wv, Wo, rs); asm volatile("" :: "v"(ins)); }
    if (ix == 24 || ix == 25 || ix == 30 || (b >= 250 && b < 254)) clear_late(ix < 26 ? b - 192 : ix == 30 ? b - 224 : b - 226, rs);
#define ROLE(m)
    if (ix < 16) {
        if (ix & 2) { role_par(4 * xc + (ix >> 2), ix & 1, mem0, Wv, Wo, ws, rs, smem); nap(NAP_C); }
        role_chain(ws, Wq, Wk, Wv, Wo, (u32)(((ix >> 1) & 1) << 8 | (4 * xc + (ix >> 2)) << 3 | (ix & 1) << 2 | wave), OFF_ACK + (b * 4 + wave) * 4);
    }
    else if (ix < 22) {
        if (xc & 1) {
            nap(NAP_W);
            ROLE(1) if (ix >= 20) for (int i = 0; i < 2; ++i) role_t0(64 + ((ix - 20) * 8 + xc - i) * 4 + wave, x0, ws, rs);
        }
        ROLE(4) role_w(xc & 1, xc >> 1, ix - 16, Wo, ws, rs, smem);
    }
    else if (ix < 26) { ROLE(ix < 24 ? 1 : 16) role_a1((ix - 22) >> 1, 2 * xc + (ix & 1), wqkv, ws, rs, smem);
        ROLE(8) if (ix == 22) { DRAIN(); role_r<1, true, SL, NSEG>(xc, x0, out, ws, rs, smem); }
    }
    else if (ix < 28) {
        if (ix == 27) nap(NAP_A);
        ROLE(1) if (b != 208) role_t0(((ix - 26) * 8 + xc) * 4 + wave, x0, ws, rs);
        ROLE(2) role_att(ix - 26, 4 * xc + wave, ws, rs);
    }
    else if (ix == 28) { ROLE(8) role_r<1, false>(xc, x0, out, ws, rs, smem); }
    else if (b < 250) { ROLE(b & 8 && b != 249 ? 64 : 32) role_a2(ix < 31 ? ix - 29 : xc, ix < 31 ? xc : 8, x0, woA, ws, rs, smem); }
    else if (b < 254) { ROLE(8) role_r<2, true, 0, SL>(xc - 2, x0, out, ws, rs, smem); }
    else if (b == 254) role_lead(ws, rs);
    else role_side(ws, rs, smem);
}

extern "C" void kernel_launch(void* const* d_in, const int* in_sizes, int n_in, void* d_out, int out_size, void* d_ws,
                              size_t ws_size, hipStream_t stream) {
    mega_kernel<<<256, 256, 0, stream>>>((const u16*)d_in[0], (const u16*)d_in[1], (const u16*)d_in[2], (const u16*)d_in[4],
                                         (const u16*)d_in[5], (const u16*)d_in[6], (const u16*)d_in[7], (const u16*)d_in[8],
                                         (u16*)d_out, (char*)d_ws);
}
```

```cpp
#include <hip/hip_runtime.h>
#include <hip/hip_bf16.h>
#include <stdint.h>
#include <type_traits>

typedef unsigned short u16;
typedef unsigned u32;
typedef unsigned long long u64;
using bf16x8 = __attribute__((ext_vector_type(8))) short;
using bf16x4 = __attribute__((ext_vector_type(4))) short;
using f32x4 = __attribute__((ext_vector_type(4))) float;
using f32x2 = __attribute__((ext_vector_type(2))) float;
using v4u = __attribute__((ext_vector_type(4))) unsigned;
using v2u = __attribute__((ext_vector_type(2))) unsigned;
typedef __bf16 bfv2 __attribute__((ext_vector_type(2)));
#define GAS __attribute__((address_space(1)))
typedef GAS unsigned gu32;
#define RLX_AGENT __ATOMIC_RELAXED, __HIP_MEMORY_SCOPE_AGENT
typedef __amdgpu_buffer_rsrc_t rsrc_t;

constexpr int S_ = 2048, D_ = 576, NSEG = 64, SEG = 32, NB = 4096, NQA = 1728;

constexpr u32 XB = S_ * D_ * 2;
constexpr u32 OFF_ABORT = 0u;
constexpr u32 OFF_PAR = 64u;
constexpr u32 OFF_CC = 128u;
constexpr u32 OFF_Z = 256u;
constexpr u32 OFF_Z2 = 384u;
constexpr u32 OFF_CH = 512u;
constexpr u32 OFF_CXNA = 1024u;
constexpr u32 OFF_FKVM = 12288u;
constexpr u32 OFF_SCT = 151552u;
constexpr u32 OFF_STAT = 290816u;
constexpr u32 SYNC_BYTES = 880640u;
constexpr u32 CX = 4u;
constexpr u32 OFF_PROG = 884736u;
constexpr u32 OFF_ACK = 1015808u;
constexpr u32 OFF_ACC = 1048576u;
constexpr u32 OFF_XNA = 12189696u;
constexpr u32 OFF_XNB = 16908288u;
constexpr u32 OFF_X = 21626880u;
constexpr u32 OFF_OA = 28704768u;
constexpr u32 OFF_PB = 33423360u;
constexpr u32 OFF_OB = 71172096u;
constexpr u32 OFF_QKV = 104726528u;
constexpr u32 OFF_KVM = 208535552u;
constexpr u32 OFF_DBG = 225312768u;
constexpr u32 OFF_JUNK = 225902592u;
constexpr u32 WS_BYTES = 230653952u;
constexpr u32 ACC_SLOT = 163840;
__device__ __forceinline__ void role_chain(char* ws, const u16* Wq, const u16* Wk, const u16* Wv, const u16* Wo, u32 id, u32 ack) {
    asm volatile(
        "s_mov_b64 s[36:37], %0\n"
        "s_mov_b32 s38, 0xdbf8000\n"
        "s_mov_b32 s39, 0x00020000\n"
        "s_mov_b32 s42, 0x480000\n"
        "s_mov_b32 s43, 0x00020000\n"
        "s_and_b32 s45, %5, 3\n"
        "s_bfe_u32 s46, %5, 0x10002\n"
        "s_bfe_u32 s47, %5, 0x50003\n"
        "s_bfe_u32 s48, %5, 0x10008\n"
        "s_mov_b32 s63, 0\n"
        "v_mbcnt_lo_u32_b32 v128, -1, 0\n"
        "v_mbcnt_hi_u32_b32 v128, -1, v128\n"
        "v_and_b32 v129, 15, v128\n"
        "v_lshrrev_b32 v130, 4, v128\n"
        "v_lshlrev_b32 v131, 1, v130\n"
        "v_xor_b32 v131, v131, v129\n"
        "v_lshlrev_b32 v131, 4, v131\n"
        "v_lshl_add_u32 v134, v130, 13, v131\n"
        "v_xor_b32 v135, 0x80, v134\n"
        "v_and_b32 v131, 0xc0, v131\n"
        "v_lshl_or_b32 v140, v131, 14, v134\n"
        "v_xor_b32 v141, 0x200080, v140\n"
        "v_lshrrev_b32 v131, 2, v129\n"
        "v_lshl_add_u32 v132, v130, 3, v131\n"
        "v_lshlrev_b32 v136, 8, v132\n"
        "v_and_b32 v132, 1, v130\n"
        "v_lshl_or_b32 v131, v132, 2, v131\n"
        "v_lshlrev_b32 v131, 1, v131\n"
        "v_bfe_u32 v132, v129, 1, 1\n"
        "s_lshl_b32 s80, s45, 2\n"
        "v_or_b32 v132, s80, v132\n"
        "v_xor_b32 v131, v131, v132\n"
        "v_lshl_add_u32 v136, v131, 4, v136\n"
        "v_and_b32 v132, 1, v129\n"
        "v_lshl_add_u32 v136, v132, 3, v136\n"
        "v_xor_b32 v137, 32, v136\n"
        "s_lshl_b32 s49, s45, 17\n"
        "s_lshl_b32 s80, s47, 8\n"
        "s_add_u32 s49, s49, s80\n"
        "s_lshl_b32 s50, s45, 12\n"
        "s_bfe_u32 s80, s47, 0x10004\n"
        "s_xor_b32 s83, s80, 1\n"
        "s_lshl_b32 s67, s83, 19\n"
        "s_lshl_b32 s68, s80, 19\n"
        "s_add_u32 s67, s67, s49\n"
        "s_add_u32 s68, s68, s49\n"
        "s_mov_b64 s[40:41], %3\n"
        "v_xor_b32 v142, 0x0, v140\n"
        "v_xor_b32 v143, 0x0, v141\n"
        "s_add_u32 m0, s50, 0\n"
        "s_add_u32 s80, s67, 0\n"
        "buffer_load_dwordx4 v142, s[40:43], s80 offen lds\n"
        "s_add_u32 m0, s50, 1024\n"
        "s_add_u32 s80, s67, 32768\n"
        "buffer_load_dwordx4 v142, s[40:43], s80 offen lds\n"
        "s_add_u32 m0, s50, 2048\n"
        "s_add_u32 s80, s67, 65536\n"
        "buffer_load_dwordx4 v143, s[40:43], s80 offen lds\n"
        "s_add_u32 m0, s50, 3072\n"
        "s_add_u32 s80, s67, 98304\n"
        "buffer_load_dwordx4 v143, s[40:43], s80 offen lds\n"
        "v_xor_b32 v142, 0x0, v140\n"
        "v_xor_b32 v143, 0x0, v141\n"
        "s_add_u32 m0, s50, 16384\n"
        "s_add_u32 s80, s68, 0\n"
        "buffer_load_dwordx4 v142, s[40:43], s80 offen lds\n"
        "s_add_u32 m0, s50, 17408\n"
        "s_add_u32 s80, s68, 32768\n"
        "buffer_load_dwordx4 v142, s[40:43], s80 offen lds\n"
        "s_add_u32 m0, s50, 18432\n"
        "s_add_u32 s80, s68, 65536\n"
        "buffer_load_dwordx4 v143, s[40:43], s80 offen lds\n"
        "s_add_u32 m0, s50, 19456\n"
        "s_add_u32 s80, s68, 98304\n"
        "buffer_load_dwordx4 v143, s[40:43], s80 offen lds\n"
        "v_xor_b32 v142, 0x100000, v140\n"
        "v_xor_b32 v143, 0x100000, v141\n"
        "s_add_u32 m0, s50, 32768\n"
        "s_add_u32 s80, s67, 0\n"
        "buffer_load_dwordx4 v142, s[40:43], s80 offen lds\n"
        "s_add_u32 m0, s50, 33792\n"
        "s_add_u32 s80, s67, 32768\n"
        "buffer_load_dwordx4 v142, s[40:43], s80 offen lds\n"
        "s_add_u32 m0, s50, 34816\n"
        "s_add_u32 s80, s67, 65536\n"
        "buffer_load_dwordx4 v143, s[40:43], s80 offen lds\n"
        "s_add_u32 m0, s50, 35840\n"
        "s_add_u32 s80, s67, 98304\n"
        "buffer_load_dwordx4 v143, s[40:43], s80 offen lds\n"
        "s_mov_b64 s[40:41], %3\n"
        "v_xor_b32 v142, 0x100000, v140\n"
        "v_xor_b32 v143, 0x100000, v141\n"
        "s_add_u32 m0, s50, 49152\n"
        "s_add_u32 s80, s68, 0\n"
        "buffer_load_dwordx4 v142, s[40:43], s80 offen lds\n"
        "s_add_u32 m0, s50, 50176\n"
        "s_add_u32 s80, s68, 32768\n"
        "buffer_load_dwordx4 v142, s[40:43], s80 offen lds\n"
        "s_add_u32 m0, s50, 51200\n"
        "s_add_u32 s80, s68, 65536\n"
        "buffer_load_dwordx4 v143, s[40:43], s80 offen lds\n"
        "s_add_u32 m0, s50, 52224\n"
        "s_add_u32 s80, s68, 98304\n"
        "buffer_load_dwordx4 v143, s[40:43], s80 offen lds\n"
        "v_xor_b32 v142, 0x200000, v140\n"
        "v_xor_b32 v143, 0x200000, v141\n"
        "s_add_u32 m0, s50, 65536\n"
        "s_add_u32 s80, s67, 0\n"
        "buffer_load_dwordx4 v142, s[40:43], s80 offen lds\n"
        "s_add_u32 m0, s50, 66560\n"
        "s_add_u32 s80, s67, 32768\n"
        "buffer_load_dwordx4 v142, s[40:43], s80 offen lds\n"
        "s_add_u32 m0, s50, 67584\n"
        "s_add_u32 s80, s67, 65536\n"
        "buffer_load_dwordx4 v143, s[40:43], s80 offen lds\n"
        "s_add_u32 m0, s50, 68608\n"
        "s_add_u32 s80, s67, 98304\n"
        "buffer_load_dwordx4 v143, s[40:43], s80 offen lds\n"
        "v_xor_b32 v142, 0x200000, v140\n"
        "v_xor_b32 v143, 0x200000, v141\n"
        "s_add_u32 m0, s50, 81920\n"
        "s_add_u32 s80, s68, 0\n"
        "buffer_load_dwordx4 v142, s[40:43], s80 offen lds\n"
        "s_add_u32 m0, s50, 82944\n"
        "s_add_u32 s80, s68, 32768\n"
        "buffer_load_dwordx4 v142, s[40:43], s80 offen lds\n"
        "s_add_u32 m0, s50, 83968\n"
        "s_add_u32 s80, s68, 65536\n"
        "buffer_load_dwordx4 v143, s[40:43], s80 offen lds\n"
        "s_add_u32 m0, s50, 84992\n"
        "s_add_u32 s80, s68, 98304\n"
        "buffer_load_dwordx4 v143, s[40:43], s80 offen lds\n"
        "s_mov_b64 s[40:41], %3\n"
        "v_xor_b32 v142, 0x300000, v140\n"
        "v_xor_b32 v143, 0x300000, v141\n"
        "s_add_u32 m0, s50, 98304\n"
        "s_add_u32 s80, s67, 0\n"
        "buffer_load_dwordx4 v142, s[40:43], s80 offen lds\n"
        "s_add_u32 m0, s50, 99328\n"
        "s_add_u32 s80, s67, 32768\n"
        "buffer_load_dwordx4 v142, s[40:43], s80 offen lds\n"
        "s_add_u32 m0, s50, 100352\n"
        "s_add_u32 s80, s67, 65536\n"
        "buffer_load_dwordx4 v143, s[40:43], s80 offen lds\n"
        "s_add_u32 m0, s50, 101376\n"
        "s_add_u32 s80, s67, 98304\n"
        "buffer_load_dwordx4 v143, s[40:43], s80 offen lds\n"
        "v_xor_b32 v142, 0x300000, v140\n"
        "v_xor_b32 v143, 0x300000, v141\n"
        "s_add_u32 m0, s50, 114688\n"
        "s_add_u32 s80, s68, 0\n"
        "buffer_load_dwordx4 v142, s[40:43], s80 offen lds\n"
        "s_add_u32 m0, s50, 115712\n"
        "s_add_u32 s80, s68, 32768\n"
        "buffer_load_dwordx4 v142, s[40:43], s80 offen lds\n"
        "s_add_u32 m0, s50, 116736\n"
        "s_add_u32 s80, s68, 65536\n"
        "buffer_load_dwordx4 v143, s[40:43], s80 offen lds\n"
        "s_add_u32 m0, s50, 117760\n"
        "s_add_u32 s80, s68, 98304\n"
        "buffer_load_dwordx4 v143, s[40:43], s80 offen lds\n"
        "s_add_u32 m0, s50, 131072\n"
        "s_add_u32 s80, s49, 4194304\n"
        "buffer_load_dwordx4 v134, s[40:43], s80 offen lds\n"
        "s_add_u32 m0, s50, 132096\n"
        "s_add_u32 s80, s49, 4227072\n"
        "buffer_load_dwordx4 v134, s[40:43], s80 offen lds\n"
        "s_add_u32 m0, s50, 133120\n"
        "s_add_u32 s80, s49, 4259840\n"
        "buffer_load_dwordx4 v135, s[40:43], s80 offen lds\n"
        "s_add_u32 m0, s50, 134144\n"
        "s_add_u32 s80, s49, 4292608\n"
        "buffer_load_dwordx4 v135, s[40:43], s80 offen lds\n"
        "v_lshrrev_b32 v131, 3, v129\n"
        "v_lshlrev_b32 v131, 5, v131\n"
        "v_and_b32 v132, 3, v129\n"
        "v_lshl_add_u32 v131, v132, 3, v131\n"
        "v_and_b32 v132, 4, v129\n"
        "v_add_u32 v131, v131, v132\n"
        "s_movk_i32 s80, 4608\n"
        "v_mad_u32_u24 v138, v130, s80, v131\n"
        "s_and_b32 s81, s47, 1\n"
        "s_mul_i32 s81, s81, 6\n"
        "v_xor_b32 v131, s81, v129\n"
        "v_and_b32 v131, 14, v131\n"
        "v_lshlrev_b32 v131, 1, v131\n"
        "v_mad_u32_u24 v127, v130, s80, v131\n"
        "s_mul_i32 s51, s47, 147456\n"
        "s_lshl2_add_u32 s80, s46, s45\n"
        "s_lshl_b32 s80, s80, 7\n"
        "s_add_u32 s52, s51, s80\n"
        "s_and_b32 s80, s45, 1\n"
        "s_lshl_b32 s81, s80, 6\n"
        "s_sub_u32 s52, s52, s81\n"
        "s_lshl1_add_u32 s80, s46, s80\n"
        "s_lshl_b32 s80, s80, 5\n"
        "s_add_u32 s51, s51, s80\n"
        "s_add_u32 s51, s51, 1024\n"
        "s_and_b32 s80, s47, 2\n"
        "s_lshl_b32 s80, s80, 6\n"
        "s_add_u32 s52, s52, s80\n"
        "s_xor_b32 s66, s52, 128\n"
        "s_mov_b32 s53, 0x05040100\n"
        "s_mov_b32 s54, 0x07060302\n"
        "v_and_b32 v131, 1, v129\n"
        "s_mov_b32 s80, 0x02020202\n"
        "v_mul_lo_u32 v131, v131, s80\n"
        "v_add_u32 v139, s53, v131\n"
        "v_lshlrev_b32 v131, 4, v128\n"
        "s_and_b32 s80, s45, 1\n"
        "s_lshl_b32 s80, s80, 12\n"
        "s_add_u32 s84, s80, 152576\n"
        "s_mov_b64 s[40:41], %4\n"
        "s_add_u32 s80, s51, 0\n"
        "buffer_load_dword v64, v127, s[40:43], s80 offen offset:0\n"
        "buffer_load_dword v66, v127, s[40:43], s80 offen offset:1152\n"
        "buffer_load_dword v68, v127, s[40:43], s80 offen offset:2304\n"
        "buffer_load_dword v70, v127, s[40:43], s80 offen offset:3456\n"
        "s_add_u32 s80, s51, 18432\n"
        "buffer_load_dword v65, v127, s[40:43], s80 offen offset:0\n"
        "buffer_load_dword v67, v127, s[40:43], s80 offen offset:1152\n"
        "buffer_load_dword v69, v127, s[40:43], s80 offen offset:2304\n"
        "buffer_load_dword v71, v127, s[40:43], s80 offen offset:3456\n"
        "s_add_u32 s80, s51, 36864\n"
        "buffer_load_dword v72, v127, s[40:43], s80 offen offset:0\n"
        "buffer_load_dword v74, v127, s[40:43], s80 offen offset:1152\n"
        "buffer_load_dword v76, v127, s[40:43], s80 offen offset:2304\n"
        "buffer_load_dword v78, v127, s[40:43], s80 offen offset:3456\n"
        "s_add_u32 s80, s51, 55296\n"
        "buffer_load_dword v73, v127, s[40:43], s80 offen offset:0\n"
        "buffer_load_dword v75, v127, s[40:43], s80 offen offset:1152\n"
        "buffer_load_dword v77, v127, s[40:43], s80 offen offset:2304\n"
        "buffer_load_dword v79, v127, s[40:43], s80 offen offset:3456\n"
        "s_add_u32 s80, s51, 73728\n"
        "buffer_load_dword v80, v127, s[40:43], s80 offen offset:0\n"
        "buffer_load_dword v82, v127, s[40:43], s80 offen offset:1152\n"
        "buffer_load_dword v84, v127, s[40:43], s80 offen offset:2304\n"
        "buffer_load_dword v86, v127, s[40:43], s80 offen offset:3456\n"
        "s_add_u32 s80, s51, 92160\n"
        "buffer_load_dword v81, v127, s[40:43], s80 offen offset:0\n"
        "buffer_load_dword v83, v127, s[40:43], s80 offen offset:1152\n"
        "buffer_load_dword v85, v127, s[40:43], s80 offen offset:2304\n"
        "buffer_load_dword v87, v127, s[40:43], s80 offen offset:3456\n"
        "s_add_u32 s80, s51, 110592\n"
        "buffer_load_dword v88, v127, s[40:43], s80 offen offset:0\n"
        "buffer_load_dword v90, v127, s[40:43], s80 offen offset:1152\n"
        "buffer_load_dword v92, v127, s[40:43], s80 offen offset:2304\n"
        "buffer_load_dword v94, v127, s[40:43], s80 offen offset:3456\n"
        "s_add_u32 s80, s51, 129024\n"
        "buffer_load_dword v89, v127, s[40:43], s80 offen offset:0\n"
        "buffer_load_dword v91, v127, s[40:43], s80 offen offset:1152\n"
        "buffer_load_dword v93, v127, s[40:43], s80 offen offset:2304\n"
        "buffer_load_dword v95, v127, s[40:43], s80 offen offset:3456\n"
        "v_add_u32 v132, 0, v136\n"
        "v_add_u32 v133, 0, v137\n"
        "s_waitcnt vmcnt(56)\n"
        "s_barrier\n"
        "ds_read_b64_tr_b16 a[0:1], v132 offset:0\n"
        "ds_read_b64_tr_b16 a[2:3], v132 offset:1024\n"
        "ds_read_b64_tr_b16 a[4:5], v132 offset:8192\n"
        "ds_read_b64_tr_b16 a[6:7], v132 offset:9216\n"
        "ds_read_b64_tr_b16 a[8:9], v133 offset:0\n"
        "ds_read_b64_tr_b16 a[10:11], v133 offset:1024\n"
        "ds_read_b64_tr_b16 a[12:13], v133 offset:8192\n"
        "ds_read_b64_tr_b16 a[14:15], v133 offset:9216\n"
        "ds_read_b64_tr_b16 a[16:17], v132 offset:16384\n"
        "ds_read_b64_tr_b16 a[18:19], v132 offset:17408\n"
        "ds_read_b64_tr_b16 a[20:21], v132 offset:24576\n"
        "ds_read_b64_tr_b16 a[22:23], v132 offset:25600\n"
        "ds_read_b64_tr_b16 a[24:25], v133 offset:16384\n"
        "ds_read_b64_tr_b16 a[26:27], v133 offset:17408\n"
        "ds_read_b64_tr_b16 a[28:29], v133 offset:24576\n"
        "ds_read_b64_tr_b16 a[30:31], v133 offset:25600\n"
        "ds_read_b64_tr_b16 a[32:33], v132 offset:32768\n"
        "ds_read_b64_tr_b16 a[34:35], v132 offset:33792\n"
        "ds_read_b64_tr_b16 a[36:37], v132 offset:40960\n"
        "ds_read_b64_tr_b16 a[38:39], v132 offset:41984\n"
        "ds_read_b64_tr_b16 a[40:41], v133 offset:32768\n"
        "ds_read_b64_tr_b16 a[42:43], v133 offset:33792\n"
        "ds_read_b64_tr_b16 a[44:45], v133 offset:40960\n"
        "ds_read_b64_tr_b16 a[46:47], v133 offset:41984\n"
        "s_mov_b64 s[40:41], %4\n"
        "s_add_u32 s80, s52, 0\n"
        "buffer_load_dword v0, v138, s[40:43], s80 offen offset:0\n"
        "buffer_load_dword v2, v138, s[40:43], s80 offen offset:1152\n"
        "buffer_load_dword v4, v138, s[40:43], s80 offen offset:2304\n"
        "buffer_load_dword v6, v138, s[40:43], s80 offen offset:3456\n"
        "s_add_u32 s80, s52, 18432\n"
        "buffer_load_dword v1, v138, s[40:43], s80 offen offset:0\n"
        "buffer_load_dword v3, v138, s[40:43], s80 offen offset:1152\n"
        "buffer_load_dword v5, v138, s[40:43], s80 offen offset:2304\n"
        "buffer_load_dword v7, v138, s[40:43], s80 offen offset:3456\n"
        "s_add_u32 s80, s52, 36864\n"
        "buffer_load_dword v8, v138, s[40:43], s80 offen offset:0\n"
        "buffer_load_dword v10, v138, s[40:43], s80 offen offset:1152\n"
        "buffer_load_dword v12, v138, s[40:43], s80 offen offset:2304\n"
        "buffer_load_dword v14, v138, s[40:43], s80 offen offset:3456\n"
        "s_add_u32 s80, s52, 55296\n"
        "buffer_load_dword v9, v138, s[40:43], s80 offen offset:0\n"
        "buffer_load_dword v11, v138, s[40:43], s80 offen offset:1152\n"
        "buffer_load_dword v13, v138, s[40:43], s80 offen offset:2304\n"
        "buffer_load_dword v15, v138, s[40:43], s80 offen offset:3456\n"
        "s_add_u32 s80, s52, 73728\n"
        "buffer_load_dword v16, v138, s[40:43], s80 offen offset:0\n"
        "buffer_load_dword v18, v138, s[40:43], s80 offen offset:1152\n"
        "buffer_load_dword v20, v138, s[40:43], s80 offen offset:2304\n"
        "buffer_load_dword v22, v138, s[40:43], s80 offen offset:3456\n"
        "s_add_u32 s80, s52, 92160\n"
        "buffer_load_dword v17, v138, s[40:43], s80 offen offset:0\n"
        "buffer_load_dword v19, v138, s[40:43], s80 offen offset:1152\n"
        "buffer_load_dword v21, v138, s[40:43], s80 offen offset:2304\n"
        "buffer_load_dword v23, v138, s[40:43], s80 offen offset:3456\n"
        "s_add_u32 s80, s52, 110592\n"
        "buffer_load_dword v24, v138, s[40:43], s80 offen offset:0\n"
        "buffer_load_dword v26, v138, s[40:43], s80 offen offset:1152\n"
        "buffer_load_dword v28, v138, s[40:43], s80 offen offset:2304\n"
        "buffer_load_dword v30, v138, s[40:43], s80 offen offset:3456\n"
        "s_add_u32 s80, s52, 129024\n"
        "buffer_load_dword v25, v138, s[40:43], s80 offen offset:0\n"
        "buffer_load_dword v27, v138, s[40:43], s80 offen offset:1152\n"
        "buffer_load_dword v29, v138, s[40:43], s80 offen offset:2304\n"
        "buffer_load_dword v31, v138, s[40:43], s80 offen offset:3456\n"
        "v_add_u32 v132, 49152, v136\n"
        "v_add_u32 v133, 49152, v137\n"
        "s_waitcnt vmcnt(60)\n"
        "s_barrier\n"
        "ds_read_b64_tr_b16 a[48:49], v132 offset:0\n"
        "ds_read_b64_tr_b16 a[50:51], v132 offset:1024\n"
        "ds_read_b64_tr_b16 a[52:53], v132 offset:8192\n"
        "ds_read_b64_tr_b16 a[54:55], v132 offset:9216\n"
        "ds_read_b64_tr_b16 a[56:57], v133 offset:0\n"
        "ds_read_b64_tr_b16 a[58:59], v133 offset:1024\n"
        "ds_read_b64_tr_b16 a[60:61], v133 offset:8192\n"
        "ds_read_b64_tr_b16 a[62:63], v133 offset:9216\n"
        "ds_read_b64_tr_b16 a[64:65], v132 offset:16384\n"
        "ds_read_b64_tr_b16 a[66:67], v132 offset:17408\n"
        "ds_read_b64_tr_b16 a[68:69], v132 offset:24576\n"
        "ds_read_b64_tr_b16 a[70:71], v132 offset:25600\n"
        "ds_read_b64_tr_b16 a[72:73], v133 offset:16384\n"
        "ds_read_b64_tr_b16 a[74:75], v133 offset:17408\n"
        "ds_read_b64_tr_b16 a[76:77], v133 offset:24576\n"
        "ds_read_b64_tr_b16 a[78:79], v133 offset:25600\n"
        "ds_read_b64_tr_b16 a[80:81], v132 offset:32768\n"
        "ds_read_b64_tr_b16 a[82:83], v132 offset:33792\n"
        "ds_read_b64_tr_b16 a[84:85], v132 offset:40960\n"
        "ds_read_b64_tr_b16 a[86:87], v132 offset:41984\n"
        "ds_read_b64_tr_b16 a[88:89], v133 offset:32768\n"
        "ds_read_b64_tr_b16 a[90:91], v133 offset:33792\n"
        "ds_read_b64_tr_b16 a[92:93], v133 offset:40960\n"
        "ds_read_b64_tr_b16 a[94:95], v133 offset:41984\n"
        "s_mov_b64 s[40:41], %4\n"
        "s_add_u32 s80, s66, 0\n"
        "buffer_load_dword v32, v138, s[40:43], s80 offen offset:0\n"
        "buffer_load_dword v34, v138, s[40:43], s80 offen offset:1152\n"
        "buffer_load_dword v36, v138, s[40:43], s80 offen offset:2304\n"
        "buffer_load_dword v38, v138, s[40:43], s80 offen offset:3456\n"
        "s_add_u32 s80, s66, 18432\n"
        "buffer_load_dword v33, v138, s[40:43], s80 offen offset:0\n"
        "buffer_load_dword v35, v138, s[40:43], s80 offen offset:1152\n"
        "buffer_load_dword v37, v138, s[40:43], s80 offen offset:2304\n"
        "buffer_load_dword v39, v138, s[40:43], s80 offen offset:3456\n"
        "s_add_u32 s80, s66, 36864\n"
        "buffer_load_dword v40, v138, s[40:43], s80 offen offset:0\n"
        "buffer_load_dword v42, v138, s[40:43], s80 offen offset:1152\n"
        "buffer_load_dword v44, v138, s[40:43], s80 offen offset:2304\n"
        "buffer_load_dword v46, v138, s[40:43], s80 offen offset:3456\n"
        "s_add_u32 s80, s66, 55296\n"
        "buffer_load_dword v41, v138, s[40:43], s80 offen offset:0\n"
        "buffer_load_dword v43, v138, s[40:43], s80 offen offset:1152\n"
        "buffer_load_dword v45, v138, s[40:43], s80 offen offset:2304\n"
        "buffer_load_dword v47, v138, s[40:43], s80 offen offset:3456\n"
        "s_add_u32 s80, s66, 73728\n"
        "buffer_load_dword v48, v138, s[40:43], s80 offen offset:0\n"
        "buffer_load_dword v50, v138, s[40:43], s80 offen offset:1152\n"
        "buffer_load_dword v52, v138, s[40:43], s80 offen offset:2304\n"
        "buffer_load_dword v54, v138, s[40:43], s80 offen offset:3456\n"
        "s_add_u32 s80, s66, 92160\n"
        "buffer_load_dword v49, v138, s[40:43], s80 offen offset:0\n"
        "buffer_load_dword v51, v138, s[40:43], s80 offen offset:1152\n"
        "buffer_load_dword v53, v138, s[40:43], s80 offen offset:2304\n"
        "buffer_load_dword v55, v138, s[40:43], s80 offen offset:3456\n"
        "s_add_u32 s80, s66, 110592\n"
        "buffer_load_dword v56, v138, s[40:43], s80 offen offset:0\n"
        "buffer_load_dword v58, v138, s[40:43], s80 offen offset:1152\n"
        "buffer_load_dword v60, v138, s[40:43], s80 offen offset:2304\n"
        "buffer_load_dword v62, v138, s[40:43], s80 offen offset:3456\n"
        "s_add_u32 s80, s66, 129024\n"
        "buffer_load_dword v57, v138, s[40:43], s80 offen offset:0\n"
        "buffer_load_dword v59, v138, s[40:43], s80 offen offset:1152\n"
        "buffer_load_dword v61, v138, s[40:43], s80 offen offset:2304\n"
        "buffer_load_dword v63, v138, s[40:43], s80 offen offset:3456\n"
        "v_add_u32 v132, 98304, v136\n"
        "v_add_u32 v133, 98304, v137\n"
        "s_waitcnt vmcnt(60)\n"
        "s_barrier\n"
        "ds_read_b64_tr_b16 a[96:97], v132 offset:0\n"
        "ds_read_b64_tr_b16 a[98:99], v132 offset:1024\n"
        "ds_read_b64_tr_b16 a[100:101], v132 offset:8192\n"
        "ds_read_b64_tr_b16 a[102:103], v132 offset:9216\n"
        "ds_read_b64_tr_b16 a[104:105], v133 offset:0\n"
        "ds_read_b64_tr_b16 a[106:107], v133 offset:1024\n"
        "ds_read_b64_tr_b16 a[108:109], v133 offset:8192\n"
        "ds_read_b64_tr_b16 a[110:111], v133 offset:9216\n"
        "ds_read_b64_tr_b16 a[112:113], v132 offset:16384\n"
        "ds_read_b64_tr_b16 a[114:115], v132 offset:17408\n"
        "ds_read_b64_tr_b16 a[116:117], v132 offset:24576\n"
        "ds_read_b64_tr_b16 a[118:119], v132 offset:25600\n"
        "ds_read_b64_tr_b16 a[120:121], v133 offset:16384\n"
        "ds_read_b64_tr_b16 a[122:123], v133 offset:17408\n"
        "ds_read_b64_tr_b16 a[124:125], v133 offset:24576\n"
        "ds_read_b64_tr_b16 a[126:127], v133 offset:25600\n"
        "ds_read_b64_tr_b16 a[128:129], v132 offset:32768\n"
        "ds_read_b64_tr_b16 a[130:131], v132 offset:33792\n"
        "ds_read_b64_tr_b16 a[132:133], v132 offset:40960\n"
        "ds_read_b64_tr_b16 a[134:135], v132 offset:41984\n"
        "ds_read_b64_tr_b16 a[136:137], v133 offset:32768\n"
        "ds_read_b64_tr_b16 a[138:139], v133 offset:33792\n"
        "ds_read_b64_tr_b16 a[140:141], v133 offset:40960\n"
        "ds_read_b64_tr_b16 a[142:143], v133 offset:41984\n"
        "s_waitcnt lgkmcnt(0)\n"
        "s_barrier\n"
        "s_mov_b64 s[40:41], %1\n"
        "v_xor_b32 v142, 0x0, v140\n"
        "v_xor_b32 v143, 0x0, v141\n"
        "s_add_u32 m0, s50, 0\n"
        "s_add_u32 s80, s67, 0\n"
        "buffer_load_dwordx4 v142, s[40:43], s80 offen lds\n"
        "s_add_u32 m0, s50, 1024\n"
        "s_add_u32 s80, s67, 32768\n"
        "buffer_load_dwordx4 v142, s[40:43], s80 offen lds\n"
        "s_add_u32 m0, s50, 2048\n"
        "s_add_u32 s80, s67, 65536\n"
        "buffer_load_dwordx4 v143, s[40:43], s80 offen lds\n"
        "s_add_u32 m0, s50, 3072\n"
        "s_add_u32 s80, s67, 98304\n"
        "buffer_load_dwordx4 v143, s[40:43], s80 offen lds\n"
        "v_xor_b32 v142, 0x0, v140\n"
        "v_xor_b32 v143, 0x0, v141\n"
        "s_add_u32 m0, s50, 16384\n"
        "s_add_u32 s80, s68, 0\n"
        "buffer_load_dwordx4 v142, s[40:43], s80 offen lds\n"
        "s_add_u32 m0, s50, 17408\n"
        "s_add_u32 s80, s68, 32768\n"
        "buffer_load_dwordx4 v142, s[40:43], s80 offen lds\n"
        "s_add_u32 m0, s50, 18432\n"
        "s_add_u32 s80, s68, 65536\n"
        "buffer_load_dwordx4 v143, s[40:43], s80 offen lds\n"
        "s_add_u32 m0, s50, 19456\n"
        "s_add_u32 s80, s68, 98304\n"
        "buffer_load_dwordx4 v143, s[40:43], s80 offen lds\n"
        "v_xor_b32 v142, 0x100000, v140\n"
        "v_xor_b32 v143, 0x100000, v141\n"
        "s_add_u32 m0, s50, 32768\n"
        "s_add_u32 s80, s67, 0\n"
        "buffer_load_dwordx4 v142, s[40:43], s80 offen lds\n"
        "s_add_u32 m0, s50, 33792\n"
        "s_add_u32 s80, s67, 32768\n"
        "buffer_load_dwordx4 v142, s[40:43], s80 offen lds\n"
        "s_add_u32 m0, s50, 34816\n"
        "s_add_u32 s80, s67, 65536\n"
        "buffer_load_dwordx4 v143, s[40:43], s80 offen lds\n"
        "s_add_u32 m0, s50, 35840\n"
        "s_add_u32 s80, s67, 98304\n"
        "buffer_load_dwordx4 v143, s[40:43], s80 offen lds\n"
        "s_mov_b64 s[40:41], %1\n"
        "v_xor_b32 v142, 0x100000, v140\n"
        "v_xor_b32 v143, 0x100000, v141\n"
        "s_add_u32 m0, s50, 49152\n"
        "s_add_u32 s80, s68, 0\n"
        "buffer_load_dwordx4 v142, s[40:43], s80 offen lds\n"
        "s_add_u32 m0, s50, 50176\n"
        "s_add_u32 s80, s68, 32768\n"
        "buffer_load_dwordx4 v142, s[40:43], s80 offen lds\n"
        "s_add_u32 m0, s50, 51200\n"
        "s_add_u32 s80, s68, 65536\n"
        "buffer_load_dwordx4 v143, s[40:43], s80 offen lds\n"
        "s_add_u32 m0, s50, 52224\n"
        "s_add_u32 s80, s68, 98304\n"
        "buffer_load_dwordx4 v143, s[40:43], s80 offen lds\n"
        "v_xor_b32 v142, 0x200000, v140\n"
        "v_xor_b32 v143, 0x200000, v141\n"
        "s_add_u32 m0, s50, 65536\n"
        "s_add_u32 s80, s67, 0\n"
        "buffer_load_dwordx4 v142, s[40:43], s80 offen lds\n"
        "s_add_u32 m0, s50, 66560\n"
        "s_add_u32 s80, s67, 32768\n"
        "buffer_load_dwordx4 v142, s[40:43], s80 offen lds\n"
        "s_add_u32 m0, s50, 67584\n"
        "s_add_u32 s80, s67, 65536\n"
        "buffer_load_dwordx4 v143, s[40:43], s80 offen lds\n"
        "s_add_u32 m0, s50, 68608\n"
        "s_add_u32 s80, s67, 98304\n"
        "buffer_load_dwordx4 v143, s[40:43], s80 offen lds\n"
        "v_xor_b32 v142, 0x200000, v140\n"
        "v_xor_b32 v143, 0x200000, v141\n"
        "s_add_u32 m0, s50, 81920\n"
        "s_add_u32 s80, s68, 0\n"
        "buffer_load_dwordx4 v142, s[40:43], s80 offen lds\n"
        "s_add_u32 m0, s50, 82944\n"
        "s_add_u32 s80, s68, 32768\n"
        "buffer_load_dwordx4 v142, s[40:43], s80 offen lds\n"
        "s_add_u32 m0, s50, 83968\n"
        "s_add_u32 s80, s68, 65536\n"
        "buffer_load_dwordx4 v143, s[40:43], s80 offen lds\n"
        "s_add_u32 m0, s50, 84992\n"
        "s_add_u32 s80, s68, 98304\n"
        "buffer_load_dwordx4 v143, s[40:43], s80 offen lds\n"
        "s_mov_b64 s[40:41], %2\n"
        "v_xor_b32 v142, 0x0, v140\n"
        "v_xor_b32 v143, 0x0, v141\n"
        "s_add_u32 m0, s50, 98304\n"
        "s_add_u32 s80, s67, 0\n"
        "buffer_load_dwordx4 v142, s[40:43], s80 offen lds\n"
        "s_add_u32 m0, s50, 99328\n"
        "s_add_u32 s80, s67, 32768\n"
        "buffer_load_dwordx4 v142, s[40:43], s80 offen lds\n"
        "s_add_u32 m0, s50, 100352\n"
        "s_add_u32 s80, s67, 65536\n"
        "buffer_load_dwordx4 v143, s[40:43], s80 offen lds\n"
        "s_add_u32 m0, s50, 101376\n"
        "s_add_u32 s80, s67, 98304\n"
        "buffer_load_dwordx4 v143, s[40:43], s80 offen lds\n"
        "v_xor_b32 v142, 0x0, v140\n"
        "v_xor_b32 v143, 0x0, v141\n"
        "s_add_u32 m0, s50, 114688\n"
        "s_add_u32 s80, s68, 0\n"
        "buffer_load_dwordx4 v142, s[40:43], s80 offen lds\n"
        "s_add_u32 m0, s50, 115712\n"
        "s_add_u32 s80, s68, 32768\n"
        "buffer_load_dwordx4 v142, s[40:43], s80 offen lds\n"
        "s_add_u32 m0, s50, 116736\n"
        "s_add_u32 s80, s68, 65536\n"
        "buffer_load_dwordx4 v143, s[40:43], s80 offen lds\n"
        "s_add_u32 m0, s50, 117760\n"
        "s_add_u32 s80, s68, 98304\n"
        "buffer_load_dwordx4 v143, s[40:43], s80 offen lds\n"
        "v_xor_b32 v142, 0x200000, v140\n"
        "v_xor_b32 v143, 0x200000, v141\n"
        "s_add_u32 m0, s50, 131072\n"
        "s_add_u32 s80, s68, 0\n"
        "buffer_load_dwordx4 v142, s[40:43], s80 offen lds\n"
        "s_add_u32 m0, s50, 132096\n"
        "s_add_u32 s80, s68, 32768\n"
        "buffer_load_dwordx4 v142, s[40:43], s80 offen lds\n"
        "s_add_u32 m0, s50, 133120\n"
        "s_add_u32 s80, s68, 65536\n"
        "buffer_load_dwordx4 v143, s[40:43], s80 offen lds\n"
        "s_add_u32 m0, s50, 134144\n"
        "s_add_u32 s80, s68, 98304\n"
        "buffer_load_dwordx4 v143, s[40:43], s80 offen lds\n"
        "s_waitcnt vmcnt(63)\n"
        "v_perm_b32 v96, v65, v64, v139\n"
        "s_waitcnt vmcnt(63)\n"
        "v_perm_b32 v97, v67, v66, v139\n"
        "s_waitcnt vmcnt(63)\n"
        "v_perm_b32 v98, v69, v68, v139\n"
        "s_waitcnt vmcnt(63)\n"
        "v_perm_b32 v99, v71, v70, v139\n"
        "s_waitcnt vmcnt(63)\n"
        "v_perm_b32 v100, v73, v72, v139\n"
        "s_waitcnt vmcnt(63)\n"
        "v_perm_b32 v101, v75, v74, v139\n"
        "s_waitcnt vmcnt(63)\n"
        "v_perm_b32 v102, v77, v76, v139\n"
        "s_waitcnt vmcnt(63)\n"
        "v_perm_b32 v103, v79, v78, v139\n"
        "s_waitcnt vmcnt(63)\n"
        "v_perm_b32 v104, v81, v80, v139\n"
        "s_waitcnt vmcnt(63)\n"
        "v_perm_b32 v105, v83, v82, v139\n"
        "s_waitcnt vmcnt(63)\n"
        "v_perm_b32 v106, v85, v84, v139\n"
        "s_waitcnt vmcnt(63)\n"
        "v_perm_b32 v107, v87, v86, v139\n"
        "s_waitcnt vmcnt(63)\n"
        "v_perm_b32 v108, v89, v88, v139\n"
        "s_waitcnt vmcnt(63)\n"
        "v_perm_b32 v109, v91, v90, v139\n"
        "s_waitcnt vmcnt(63)\n"
        "v_perm_b32 v110, v93, v92, v139\n"
        "s_waitcnt vmcnt(63)\n"
        "v_perm_b32 v111, v95, v94, v139\n"
        "v_add_u32 v127, s84, v131\n"
        "ds_write_b128 v127, v[96:99] offset:0\n"
        "ds_write_b128 v127, v[100:103] offset:1024\n"
        "ds_write_b128 v127, v[104:107] offset:2048\n"
        "ds_write_b128 v127, v[108:111] offset:3072\n"
        "s_waitcnt vmcnt(63)\n"
        "v_perm_b32 v144, v1, v0, s53\n"
        "v_perm_b32 v152, v1, v0, s54\n"
        "s_waitcnt vmcnt(63)\n"
        "v_perm_b32 v145, v3, v2, s53\n"
        "v_perm_b32 v153, v3, v2, s54\n"
        "s_waitcnt vmcnt(63)\n"
        "v_perm_b32 v146, v5, v4, s53\n"
        "v_perm_b32 v154, v5, v4, s54\n"
        "s_waitcnt vmcnt(63)\n"
        "v_perm_b32 v147, v7, v6, s53\n"
        "v_perm_b32 v155, v7, v6, s54\n"
        "s_waitcnt vmcnt(63)\n"
        "v_perm_b32 v148, v9, v8, s53\n"
        "v_perm_b32 v156, v9, v8, s54\n"
        "s_waitcnt vmcnt(63)\n"
        "v_perm_b32 v149, v11, v10, s53\n"
        "v_perm_b32 v157, v11, v10, s54\n"
        "s_waitcnt vmcnt(63)\n"
        "v_perm_b32 v150, v13, v12, s53\n"
        "v_perm_b32 v158, v13, v12, s54\n"
        "s_waitcnt vmcnt(63)\n"
        "v_perm_b32 v151, v15, v14, s53\n"
        "v_perm_b32 v159, v15, v14, s54\n"
        "s_waitcnt vmcnt(63)\n"
        "v_perm_b32 v160, v17, v16, s53\n"
        "v_perm_b32 v168, v17, v16, s54\n"
        "s_waitcnt vmcnt(63)\n"
        "v_perm_b32 v161, v19, v18, s53\n"
        "v_perm_b32 v169, v19, v18, s54\n"
        "s_waitcnt vmcnt(63)\n"
        "v_perm_b32 v162, v21, v20, s53\n"
        "v_perm_b32 v170, v21, v20, s54\n"
        "s_waitcnt vmcnt(63)\n"
        "v_perm_b32 v163, v23, v22, s53\n"
        "v_perm_b32 v171, v23, v22, s54\n"
        "s_waitcnt vmcnt(63)\n"
        "v_perm_b32 v164, v25, v24, s53\n"
        "v_perm_b32 v172, v25, v24, s54\n"
        "s_waitcnt vmcnt(63)\n"
        "v_perm_b32 v165, v27, v26, s53\n"
        "v_perm_b32 v173, v27, v26, s54\n"
        "s_waitcnt vmcnt(63)\n"
        "v_perm_b32 v166, v29, v28, s53\n"
        "v_perm_b32 v174, v29, v28, s54\n"
        "s_waitcnt vmcnt(63)\n"
        "v_perm_b32 v167, v31, v30, s53\n"
        "v_perm_b32 v175, v31, v30, s54\n"
        "s_waitcnt lgkmcnt(0)\n"
        "s_waitcnt vmcnt(63)\n"
        "v_perm_b32 v96, v33, v32, s53\n"
        "v_perm_b32 v104, v33, v32, s54\n"
        "s_waitcnt vmcnt(62)\n"
        "v_perm_b32 v97, v35, v34, s53\n"
        "v_perm_b32 v105, v35, v34, s54\n"
        "s_waitcnt vmcnt(61)\n"
        "v_perm_b32 v98, v37, v36, s53\n"
        "v_perm_b32 v106, v37, v36, s54\n"
        "s_waitcnt vmcnt(60)\n"
        "v_perm_b32 v99, v39, v38, s53\n"
        "v_perm_b32 v107, v39, v38, s54\n"
        "s_waitcnt vmcnt(55)\n"
        "v_perm_b32 v100, v41, v40, s53\n"
        "v_perm_b32 v108, v41, v40, s54\n"
        "s_waitcnt vmcnt(54)\n"
        "v_perm_b32 v101, v43, v42, s53\n"
        "v_perm_b32 v109, v43, v42, s54\n"
        "s_waitcnt vmcnt(53)\n"
        "v_perm_b32 v102, v45, v44, s53\n"
        "v_perm_b32 v110, v45, v44, s54\n"
        "s_waitcnt vmcnt(52)\n"
        "v_perm_b32 v103, v47, v46, s53\n"
        "v_perm_b32 v111, v47, v46, s54\n"
        "s_waitcnt vmcnt(47)\n"
        "v_perm_b32 v112, v49, v48, s53\n"
        "v_perm_b32 v120, v49, v48, s54\n"
        "s_waitcnt vmcnt(46)\n"
        "v_perm_b32 v113, v51, v50, s53\n"
        "v_perm_b32 v121, v51, v50, s54\n"
        "s_waitcnt vmcnt(45)\n"
        "v_perm_b32 v114, v53, v52, s53\n"
        "v_perm_b32 v122, v53, v52, s54\n"
        "s_waitcnt vmcnt(44)\n"
        "v_perm_b32 v115, v55, v54, s53\n"
        "v_perm_b32 v123, v55, v54, s54\n"
        "s_waitcnt vmcnt(39)\n"
        "v_perm_b32 v116, v57, v56, s53\n"
        "v_perm_b32 v124, v57, v56, s54\n"
        "s_waitcnt vmcnt(38)\n"
        "v_perm_b32 v117, v59, v58, s53\n"
        "v_perm_b32 v125, v59, v58, s54\n"
        "s_waitcnt vmcnt(37)\n"
        "v_perm_b32 v118, v61, v60, s53\n"
        "v_perm_b32 v126, v61, v60, s54\n"
        "s_waitcnt vmcnt(36)\n"
        "v_perm_b32 v119, v63, v62, s53\n"
        "v_perm_b32 v127, v63, v62, s54\n"
        "v_add_u32 v132, 0, v136\n"
        "v_add_u32 v133, 0, v137\n"
        "s_waitcnt vmcnt(24)\n"
        "s_barrier\n"
        "ds_read_b64_tr_b16 a[144:145], v132 offset:0\n"
        "ds_read_b64_tr_b16 a[146:147], v132 offset:1024\n"
        "ds_read_b64_tr_b16 a[148:149], v132 offset:8192\n"
        "ds_read_b64_tr_b16 a[150:151], v132 offset:9216\n"
        "ds_read_b64_tr_b16 a[152:153], v133 offset:0\n"
        "ds_read_b64_tr_b16 a[154:155], v133 offset:1024\n"
        "ds_read_b64_tr_b16 a[156:157], v133 offset:8192\n"
        "ds_read_b64_tr_b16 a[158:159], v133 offset:9216\n"
        "ds_read_b64_tr_b16 a[160:161], v132 offset:16384\n"
        "ds_read_b64_tr_b16 a[162:163], v132 offset:17408\n"
        "ds_read_b64_tr_b16 a[164:165], v132 offset:24576\n"
        "ds_read_b64_tr_b16 a[166:167], v132 offset:25600\n"
        "ds_read_b64_tr_b16 a[168:169], v133 offset:16384\n"
        "ds_read_b64_tr_b16 a[170:171], v133 offset:17408\n"
        "ds_read_b64_tr_b16 a[172:173], v133 offset:24576\n"
        "ds_read_b64_tr_b16 a[174:175], v133 offset:25600\n"
        "ds_read_b64_tr_b16 a[176:177], v132 offset:32768\n"
        "ds_read_b64_tr_b16 a[178:179], v132 offset:33792\n"
        "ds_read_b64_tr_b16 a[180:181], v132 offset:40960\n"
        "ds_read_b64_tr_b16 a[182:183], v132 offset:41984\n"
        "ds_read_b64_tr_b16 a[184:185], v133 offset:32768\n"
        "ds_read_b64_tr_b16 a[186:187], v133 offset:33792\n"
        "ds_read_b64_tr_b16 a[188:189], v133 offset:40960\n"
        "ds_read_b64_tr_b16 a[190:191], v133 offset:41984\n"
        "s_waitcnt lgkmcnt(0)\n"
        "s_barrier\n"
        "s_mov_b64 s[40:41], %2\n"
        "v_xor_b32 v142, 0x300000, v140\n"
        "v_xor_b32 v143, 0x300000, v141\n"
        "s_add_u32 m0, s50, 0\n"
        "s_add_u32 s80, s67, 0\n"
        "buffer_load_dwordx4 v142, s[40:43], s80 offen lds\n"
        "s_add_u32 m0, s50, 1024\n"
        "s_add_u32 s80, s67, 32768\n"
        "buffer_load_dwordx4 v142, s[40:43], s80 offen lds\n"
        "s_add_u32 m0, s50, 2048\n"
        "s_add_u32 s80, s67, 65536\n"
        "buffer_load_dwordx4 v143, s[40:43], s80 offen lds\n"
        "s_add_u32 m0, s50, 3072\n"
        "s_add_u32 s80, s67, 98304\n"
        "buffer_load_dwordx4 v143, s[40:43], s80 offen lds\n"
        "v_xor_b32 v142, 0x300000, v140\n"
        "v_xor_b32 v143, 0x300000, v141\n"
        "s_add_u32 m0, s50, 16384\n"
        "s_add_u32 s80, s68, 0\n"
        "buffer_load_dwordx4 v142, s[40:43], s80 offen lds\n"
        "s_add_u32 m0, s50, 17408\n"
        "s_add_u32 s80, s68, 32768\n"
        "buffer_load_dwordx4 v142, s[40:43], s80 offen lds\n"
        "s_add_u32 m0, s50, 18432\n"
        "s_add_u32 s80, s68, 65536\n"
        "buffer_load_dwordx4 v143, s[40:43], s80 offen lds\n"
        "s_add_u32 m0, s50, 19456\n"
        "s_add_u32 s80, s68, 98304\n"
        "buffer_load_dwordx4 v143, s[40:43], s80 offen lds\n"
        "s_add_u32 m0, s50, 32768\n"
        "s_add_u32 s80, s49, 4194304\n"
        "buffer_load_dwordx4 v134, s[40:43], s80 offen lds\n"
        "s_add_u32 m0, s50, 33792\n"
        "s_add_u32 s80, s49, 4227072\n"
        "buffer_load_dwordx4 v134, s[40:43], s80 offen lds\n"
        "s_add_u32 m0, s50, 34816\n"
        "s_add_u32 s80, s49, 4259840\n"
        "buffer_load_dwordx4 v135, s[40:43], s80 offen lds\n"
        "s_add_u32 m0, s50, 35840\n"
        "s_add_u32 s80, s49, 4292608\n"
        "buffer_load_dwordx4 v135, s[40:43], s80 offen lds\n"
        "v_add_u32 v132, 49152, v136\n"
        "v_add_u32 v133, 49152, v137\n"
        "s_waitcnt vmcnt(24)\n"
        "s_barrier\n"
        "ds_read_b64_tr_b16 a[192:193], v132 offset:0\n"
        "ds_read_b64_tr_b16 a[194:195], v132 offset:1024\n"
        "ds_read_b64_tr_b16 a[196:197], v132 offset:8192\n"
        "ds_read_b64_tr_b16 a[198:199], v132 offset:9216\n"
        "ds_read_b64_tr_b16 a[200:201], v133 offset:0\n"
        "ds_read_b64_tr_b16 a[202:203], v133 offset:1024\n"
        "ds_read_b64_tr_b16 a[204:205], v133 offset:8192\n"
        "ds_read_b64_tr_b16 a[206:207], v133 offset:9216\n"
        "ds_read_b64_tr_b16 a[208:209], v132 offset:16384\n"
        "ds_read_b64_tr_b16 a[210:211], v132 offset:17408\n"
        "ds_read_b64_tr_b16 a[212:213], v132 offset:24576\n"
        "ds_read_b64_tr_b16 a[214:215], v132 offset:25600\n"
        "ds_read_b64_tr_b16 a[216:217], v133 offset:16384\n"
        "ds_read_b64_tr_b16 a[218:219], v133 offset:17408\n"
        "ds_read_b64_tr_b16 a[220:221], v133 offset:24576\n"
        "ds_read_b64_tr_b16 a[222:223], v133 offset:25600\n"
        "ds_read_b64_tr_b16 a[224:225], v132 offset:32768\n"
        "ds_read_b64_tr_b16 a[226:227], v132 offset:33792\n"
        "ds_read_b64_tr_b16 a[228:229], v132 offset:40960\n"
        "ds_read_b64_tr_b16 a[230:231], v132 offset:41984\n"
        "ds_read_b64_tr_b16 a[232:233], v133 offset:32768\n"
        "ds_read_b64_tr_b16 a[234:235], v133 offset:33792\n"
        "ds_read_b64_tr_b16 a[236:237], v133 offset:40960\n"
        "ds_read_b64_tr_b16 a[238:239], v133 offset:41984\n"
        "s_waitcnt lgkmcnt(0)\n"
        "s_barrier\n"
        "s_mov_b64 s[40:41], %1\n"
        "v_xor_b32 v142, 0x300000, v140\n"
        "v_xor_b32 v143, 0x300000, v141\n"
        "s_add_u32 m0, s50, 49152\n"
        "s_add_u32 s80, s67, 0\n"
        "buffer_load_dwordx4 v142, s[40:43], s80 offen lds\n"
        "s_add_u32 m0, s50, 50176\n"
        "s_add_u32 s80, s67, 32768\n"
        "buffer_load_dwordx4 v142, s[40:43], s80 offen lds\n"
        "s_add_u32 m0, s50, 51200\n"
        "s_add_u32 s80, s67, 65536\n"
        "buffer_load_dwordx4 v143, s[40:43], s80 offen lds\n"
        "s_add_u32 m0, s50, 52224\n"
        "s_add_u32 s80, s67, 98304\n"
        "buffer_load_dwordx4 v143, s[40:43], s80 offen lds\n"
        "v_xor_b32 v142, 0x300000, v140\n"
        "v_xor_b32 v143, 0x300000, v141\n"
        "s_add_u32 m0, s50, 65536\n"
        "s_add_u32 s80, s68, 0\n"
        "buffer_load_dwordx4 v142, s[40:43], s80 offen lds\n"
        "s_add_u32 m0, s50, 66560\n"
        "s_add_u32 s80, s68, 32768\n"
        "buffer_load_dwordx4 v142, s[40:43], s80 offen lds\n"
        "s_add_u32 m0, s50, 67584\n"
        "s_add_u32 s80, s68, 65536\n"
        "buffer_load_dwordx4 v143, s[40:43], s80 offen lds\n"
        "s_add_u32 m0, s50, 68608\n"
        "s_add_u32 s80, s68, 98304\n"
        "buffer_load_dwordx4 v143, s[40:43], s80 offen lds\n"
        "s_add_u32 m0, s50, 81920\n"
        "s_add_u32 s80, s49, 4194304\n"
        "buffer_load_dwordx4 v134, s[40:43], s80 offen lds\n"
        "s_add_u32 m0, s50, 82944\n"
        "s_add_u32 s80, s49, 4227072\n"
        "buffer_load_dwordx4 v134, s[40:43], s80 offen lds\n"
        "s_add_u32 m0, s50, 83968\n"
        "s_add_u32 s80, s49, 4259840\n"
        "buffer_load_dwordx4 v135, s[40:43], s80 offen lds\n"
        "s_add_u32 m0, s50, 84992\n"
        "s_add_u32 s80, s49, 4292608\n"
        "buffer_load_dwordx4 v135, s[40:43], s80 offen lds\n"
        "v_add_u32 v132, 98304, v136\n"
        "v_add_u32 v133, 98304, v137\n"
        "s_waitcnt vmcnt(24)\n"
        "s_barrier\n"
        "ds_read_b64_tr_b16 a[240:241], v132 offset:0\n"
        "ds_read_b64_tr_b16 a[242:243], v132 offset:1024\n"
        "ds_read_b64_tr_b16 a[244:245], v132 offset:8192\n"
        "ds_read_b64_tr_b16 a[246:247], v132 offset:9216\n"
        "ds_read_b64_tr_b16 a[248:249], v133 offset:0\n"
        "ds_read_b64_tr_b16 a[250:251], v133 offset:1024\n"
        "ds_read_b64_tr_b16 a[252:253], v133 offset:8192\n"
        "ds_read_b64_tr_b16 a[254:255], v133 offset:9216\n"
        "ds_read_b64_tr_b16 v[176:177], v132 offset:16384\n"
        "ds_read_b64_tr_b16 v[178:179], v132 offset:17408\n"
        "ds_read_b64_tr_b16 v[180:181], v132 offset:24576\n"
        "ds_read_b64_tr_b16 v[182:183], v132 offset:25600\n"
        "ds_read_b64_tr_b16 v[184:185], v133 offset:16384\n"
        "ds_read_b64_tr_b16 v[186:187], v133 offset:17408\n"
        "ds_read_b64_tr_b16 v[188:189], v133 offset:24576\n"
        "ds_read_b64_tr_b16 v[190:191], v133 offset:25600\n"
        "ds_read_b64_tr_b16 v[192:193], v132 offset:32768\n"
        "ds_read_b64_tr_b16 v[194:195], v132 offset:33792\n"
        "ds_read_b64_tr_b16 v[196:197], v132 offset:40960\n"
        "ds_read_b64_tr_b16 v[198:199], v132 offset:41984\n"
        "ds_read_b64_tr_b16 v[200:201], v133 offset:32768\n"
        "ds_read_b64_tr_b16 v[202:203], v133 offset:33792\n"
        "ds_read_b64_tr_b16 v[204:205], v133 offset:40960\n"
        "ds_read_b64_tr_b16 v[206:207], v133 offset:41984\n"
        "s_waitcnt lgkmcnt(0)\n"
        "s_barrier\n"
        "s_mov_b64 s[40:41], %2\n"
        "v_xor_b32 v142, 0x100000, v140\n"
        "v_xor_b32 v143, 0x100000, v141\n"
        "s_add_u32 m0, s50, 98304\n"
        "s_add_u32 s80, s67, 0\n"
        "buffer_load_dwordx4 v142, s[40:43], s80 offen lds\n"
        "s_add_u32 m0, s50, 99328\n"
        "s_add_u32 s80, s67, 32768\n"
        "buffer_load_dwordx4 v142, s[40:43], s80 offen lds\n"
        "s_add_u32 m0, s50, 100352\n"
        "s_add_u32 s80, s67, 65536\n"
        "buffer_load_dwordx4 v143, s[40:43], s80 offen lds\n"
        "s_add_u32 m0, s50, 101376\n"
        "s_add_u32 s80, s67, 98304\n"
        "buffer_load_dwordx4 v143, s[40:43], s80 offen lds\n"
        "v_xor_b32 v142, 0x100000, v140\n"
        "v_xor_b32 v143, 0x100000, v141\n"
        "s_add_u32 m0, s50, 114688\n"
        "s_add_u32 s80, s68, 0\n"
        "buffer_load_dwordx4 v142, s[40:43], s80 offen lds\n"
        "s_add_u32 m0, s50, 115712\n"
        "s_add_u32 s80, s68, 32768\n"
        "buffer_load_dwordx4 v142, s[40:43], s80 offen lds\n"
        "s_add_u32 m0, s50, 116736\n"
        "s_add_u32 s80, s68, 65536\n"
        "buffer_load_dwordx4 v143, s[40:43], s80 offen lds\n"
        "s_add_u32 m0, s50, 117760\n"
        "s_add_u32 s80, s68, 98304\n"
        "buffer_load_dwordx4 v143, s[40:43], s80 offen lds\n"
        "v_xor_b32 v142, 0x200000, v140\n"
        "v_xor_b32 v143, 0x200000, v141\n"
        "s_add_u32 m0, s50, 131072\n"
        "s_add_u32 s80, s67, 0\n"
        "buffer_load_dwordx4 v142, s[40:43], s80 offen lds\n"
        "s_add_u32 m0, s50, 132096\n"
        "s_add_u32 s80, s67, 32768\n"
        "buffer_load_dwordx4 v142, s[40:43], s80 offen lds\n"
        "s_add_u32 m0, s50, 133120\n"
        "s_add_u32 s80, s67, 65536\n"
        "buffer_load_dwordx4 v143, s[40:43], s80 offen lds\n"
        "s_add_u32 m0, s50, 134144\n"
        "s_add_u32 s80, s67, 98304\n"
        "buffer_load_dwordx4 v143, s[40:43], s80 offen lds\n"
        "v_add_u32 v132, 0, v136\n"
        "v_add_u32 v133, 0, v137\n"
        "s_waitcnt vmcnt(24)\n"
        "s_barrier\n"
        "ds_read_b64_tr_b16 v[208:209], v132 offset:0\n"
        "ds_read_b64_tr_b16 v[210:211], v132 offset:1024\n"
        "ds_read_b64_tr_b16 v[212:213], v132 offset:8192\n"
        "ds_read_b64_tr_b16 v[214:215], v132 offset:9216\n"
        "ds_read_b64_tr_b16 v[216:217], v133 offset:0\n"
        "ds_read_b64_tr_b16 v[218:219], v133 offset:1024\n"
        "ds_read_b64_tr_b16 v[220:221], v133 offset:8192\n"
        "ds_read_b64_tr_b16 v[222:223], v133 offset:9216\n"
        "ds_read_b64_tr_b16 v[224:225], v132 offset:16384\n"
        "ds_read_b64_tr_b16 v[226:227], v132 offset:17408\n"
        "ds_read_b64_tr_b16 v[228:229], v132 offset:24576\n"
        "ds_read_b64_tr_b16 v[230:231], v132 offset:25600\n"
        "ds_read_b64_tr_b16 v[232:233], v133 offset:16384\n"
        "ds_read_b64_tr_b16 v[234:235], v133 offset:17408\n"
        "ds_read_b64_tr_b16 v[236:237], v133 offset:24576\n"
        "ds_read_b64_tr_b16 v[238:239], v133 offset:25600\n"
        "ds_read_b64_tr_b16 v[240:241], v132 offset:32768\n"
        "ds_read_b64_tr_b16 v[242:243], v132 offset:33792\n"
        "ds_read_b64_tr_b16 v[244:245], v132 offset:40960\n"
        "ds_read_b64_tr_b16 v[246:247], v132 offset:41984\n"
        "ds_read_b64_tr_b16 v[248:249], v133 offset:32768\n"
        "ds_read_b64_tr_b16 v[250:251], v133 offset:33792\n"
        "ds_read_b64_tr_b16 v[252:253], v133 offset:40960\n"
        "ds_read_b64_tr_b16 v[254:255], v133 offset:41984\n"
        "v_add_u32 v132, 49152, v136\n"
        "v_add_u32 v133, 49152, v137\n"
        "s_waitcnt vmcnt(12)\n"
        "s_barrier\n"
        "ds_read_b64_tr_b16 v[0:1], v132 offset:0\n"
        "ds_read_b64_tr_b16 v[2:3], v132 offset:1024\n"
        "ds_read_b64_tr_b16 v[4:5], v132 offset:8192\n"
        "ds_read_b64_tr_b16 v[6:7], v132 offset:9216\n"
        "ds_read_b64_tr_b16 v[8:9], v133 offset:0\n"
        "ds_read_b64_tr_b16 v[10:11], v133 offset:1024\n"
        "ds_read_b64_tr_b16 v[12:13], v133 offset:8192\n"
        "ds_read_b64_tr_b16 v[14:15], v133 offset:9216\n"
        "ds_read_b64_tr_b16 v[16:17], v132 offset:16384\n"
        "ds_read_b64_tr_b16 v[18:19], v132 offset:17408\n"
        "ds_read_b64_tr_b16 v[20:21], v132 offset:24576\n"
        "ds_read_b64_tr_b16 v[22:23], v132 offset:25600\n"
        "ds_read_b64_tr_b16 v[24:25], v133 offset:16384\n"
        "ds_read_b64_tr_b16 v[26:27], v133 offset:17408\n"
        "ds_read_b64_tr_b16 v[28:29], v133 offset:24576\n"
        "ds_read_b64_tr_b16 v[30:31], v133 offset:25600\n"
        "ds_read_b64_tr_b16 v[32:33], v132 offset:32768\n"
        "ds_read_b64_tr_b16 v[34:35], v132 offset:33792\n"
        "ds_read_b64_tr_b16 v[36:37], v132 offset:40960\n"
        "ds_read_b64_tr_b16 v[38:39], v132 offset:41984\n"
        "ds_read_b64_tr_b16 v[40:41], v133 offset:32768\n"
        "ds_read_b64_tr_b16 v[42:43], v133 offset:33792\n"
        "ds_read_b64_tr_b16 v[44:45], v133 offset:40960\n"
        "ds_read_b64_tr_b16 v[46:47], v133 offset:41984\n"
        "v_add_u32 v132, 98304, v136\n"
        "v_add_u32 v133, 98304, v137\n"
        "s_waitcnt vmcnt(0)\n"
        "s_barrier\n"
        "ds_read_b64_tr_b16 v[48:49], v132 offset:0\n"
        "ds_read_b64_tr_b16 v[50:51], v132 offset:1024\n"
        "ds_read_b64_tr_b16 v[52:53], v132 offset:8192\n"
        "ds_read_b64_tr_b16 v[54:55], v132 offset:9216\n"
        "ds_read_b64_tr_b16 v[56:57], v133 offset:0\n"
        "ds_read_b64_tr_b16 v[58:59], v133 offset:1024\n"
        "ds_read_b64_tr_b16 v[60:61], v133 offset:8192\n"
        "ds_read_b64_tr_b16 v[62:63], v133 offset:9216\n"
        "ds_read_b64_tr_b16 v[64:65], v132 offset:16384\n"
        "ds_read_b64_tr_b16 v[66:67], v132 offset:17408\n"
        "ds_read_b64_tr_b16 v[68:69], v132 offset:24576\n"
        "ds_read_b64_tr_b16 v[70:71], v132 offset:25600\n"
        "ds_read_b64_tr_b16 v[72:73], v133 offset:16384\n"
        "ds_read_b64_tr_b16 v[74:75], v133 offset:17408\n"
        "ds_read_b64_tr_b16 v[76:77], v133 offset:24576\n"
        "ds_read_b64_tr_b16 v[78:79], v133 offset:25600\n"
        "ds_read_b64_tr_b16 v[80:81], v132 offset:32768\n"
        "ds_read_b64_tr_b16 v[82:83], v132 offset:33792\n"
        "ds_read_b64_tr_b16 v[84:85], v132 offset:40960\n"
        "ds_read_b64_tr_b16 v[86:87], v132 offset:41984\n"
        "ds_read_b64_tr_b16 v[88:89], v133 offset:32768\n"
        "ds_read_b64_tr_b16 v[90:91], v133 offset:33792\n"
        "ds_read_b64_tr_b16 v[92:93], v133 offset:40960\n"
        "ds_read_b64_tr_b16 v[94:95], v133 offset:41984\n"
        "s_waitcnt lgkmcnt(0)\n"
        "s_barrier\n"
        "s_lshl_b32 s80, s45, 15\n"
        "s_add_u32 s80, s80, 21504\n"
        "v_add_u32 v132, s80, v131\n"
        "v_mov_b32 v134, 0\n"
        "s_mov_b64 exec, 1\n"
        "buffer_atomic_add v134, off, s[36:39], %6 sc0\n"
        "s_mov_b64 exec, -1\n"
        "ds_write_b128 v132, v[0:3] offset:0\n"
        "ds_write_b128 v132, v[4:7] offset:1024\n"
        "ds_write_b128 v132, v[8:11] offset:2048\n"
        "ds_write_b128 v132, v[12:15] offset:3072\n"
        "ds_write_b128 v132, v[16:19] offset:4096\n"
        "ds_write_b128 v132, v[20:23] offset:5120\n"
        "ds_write_b128 v132, v[24:27] offset:6144\n"
        "ds_write_b128 v132, v[28:31] offset:7168\n"
        "ds_write_b128 v132, v[32:35] offset:8192\n"
        "ds_write_b128 v132, v[36:39] offset:9216\n"
        "ds_write_b128 v132, v[40:43] offset:10240\n"
        "ds_write_b128 v132, v[44:47] offset:11264\n"
        "ds_write_b128 v132, v[48:51] offset:12288\n"
        "ds_write_b128 v132, v[52:55] offset:13312\n"
        "ds_write_b128 v132, v[56:59] offset:14336\n"
        "ds_write_b128 v132, v[60:63] offset:15360\n"
        "ds_write_b128 v132, v[64:67] offset:16384\n"
        "ds_write_b128 v132, v[68:71] offset:17408\n"
        "ds_write_b128 v132, v[72:75] offset:18432\n"
        "ds_write_b128 v132, v[76:79] offset:19456\n"
        "ds_write_b128 v132, v[80:83] offset:20480\n"
        "ds_write_b128 v132, v[84:87] offset:21504\n"
        "ds_write_b128 v132, v[88:91] offset:22528\n"
        "ds_write_b128 v132, v[92:95] offset:23552\n"
        "ds_write_b128 v132, v[96:99] offset:24576\n"
        "ds_write_b128 v132, v[100:103] offset:25600\n"
        "ds_write_b128 v132, v[104:107] offset:26624\n"
        "ds_write_b128 v132, v[108:111] offset:27648\n"
        "ds_write_b128 v132, v[112:115] offset:28672\n"
        "ds_write_b128 v132, v[116:119] offset:29696\n"
        "ds_write_b128 v132, v[120:123] offset:30720\n"
        "ds_write_b128 v132, v[124:127] offset:31744\n"
        "s_waitcnt lgkmcnt(0)\n"
        "s_waitcnt lgkmcnt(0)\n"
        "L_hs%=:\n"
        "s_waitcnt vmcnt(0)\n"
        "v_readfirstlane_b32 s81, v134\n"
        "s_nop 0\n"
        "s_cmp_lg_u32 s81, 0\n"
        "s_cbranch_scc1 L_hs2%=\n"
        "s_add_u32 s63, s63, 1\n"
        "s_cmp_gt_u32 s63, 0x40000\n"
        "s_cbranch_scc1 L_abort%=\n"
        "s_sleep 4\n"
        "v_mov_b32 v134, 0\n"
        "s_mov_b64 exec, 1\n"
        "buffer_atomic_add v134, off, s[36:39], %6 sc0\n"
        "s_mov_b64 exec, -1\n"
        "s_branch L_hs%=\n"
        "L_hs2%=:\n"
        "v_mov_b32 v134, 2\n"
        "s_mov_b64 exec, 1\n"
        "buffer_atomic_swap v134, off, s[36:39], %6\n"
        "s_mov_b64 exec, -1\n"
        "v_mbcnt_lo_u32_b32 v0, -1, 0\n"
        "v_mbcnt_hi_u32_b32 v0, -1, v0\n"
        "v_and_b32 v1, 15, v0\n"
        "v_lshrrev_b32 v2, 4, v0\n"
        "v_lshlrev_b32 v124, 4, v0\n"
        "v_add_u32 v125, s80, v124\n"
        "s_mul_i32 s80, s45, 0\n"
        "s_add_u32 s80, s80, 0\n"
        "v_mov_b32 v138, s80\n"
        "v_bfe_u32 v3, v0, 0, 2\n"
        "s_mov_b32 s80, 16\n"
        "v_mad_u32_u24 v138, v3, s80, v138\n"
        "v_bfe_u32 v3, v0, 2, 3\n"
        "s_mov_b32 s80, 1024\n"
        "v_mad_u32_u24 v138, v3, s80, v138\n"
        "v_bfe_u32 v3, v0, 5, 1\n"
        "s_mov_b32 s80, 512\n"
        "v_mad_u32_u24 v138, v3, s80, v138\n"
        "s_mul_i32 s80, s45, 64\n"
        "s_add_u32 s80, s80, 9216\n"
        "v_mov_b32 v127, s80\n"
        "v_bfe_u32 v3, v0, 0, 2\n"
        "s_mov_b32 s80, 8\n"
        "v_mad_u32_u24 v127, v3, s80, v127\n"
        "v_bfe_u32 v3, v0, 2, 1\n"
        "s_mov_b32 s80, 4\n"
        "v_mad_u32_u24 v127, v3, s80, v127\n"
        "v_bfe_u32 v3, v0, 3, 1\n"
        "s_mov_b32 s80, 32\n"
        "v_mad_u32_u24 v127, v3, s80, v127\n"
        "v_bfe_u32 v3, v0, 4, 2\n"
        "s_mov_b32 s80, 576\n"
        "v_mad_u32_u24 v127, v3, s80, v127\n"
        "s_mul_i32 s80, s45, 2048\n"
        "s_add_u32 s80, s80, 0\n"
        "v_mov_b32 v126, s80\n"
        "v_bfe_u32 v3, v0, 0, 1\n"
        "s_mov_b32 s80, 8\n"
        "v_mad_u32_u24 v126, v3, s80, v126\n"
        "v_bfe_u32 v3, v0, 1, 1\n"
        "s_mov_b32 s80, 32\n"
        "v_mad_u32_u24 v126, v3, s80, v126\n"
        "v_bfe_u32 v3, v0, 2, 1\n"
        "s_mov_b32 s80, 16\n"
        "v_mad_u32_u24 v126, v3, s80, v126\n"
        "v_bfe_u32 v3, v0, 3, 3\n"
        "s_mov_b32 s80, 64\n"
        "v_mad_u32_u24 v126, v3, s80, v126\n"
        "s_mul_i32 s80, s45, 0\n"
        "s_add_u32 s80, s80, 9216\n"
        "v_mov_b32 v128, s80\n"
        "v_bfe_u32 v3, v0, 0, 1\n"
        "s_mov_b32 s80, 288\n"
        "v_mad_u32_u24 v128, v3, s80, v128\n"
        "v_bfe_u32 v3, v0, 2, 2\n"
        "s_mov_b32 s80, 576\n"
        "v_mad_u32_u24 v128, v3, s80, v128\n"
        "v_bfe_u32 v3, v0, 4, 2\n"
        "s_mov_b32 s80, 16\n"
        "v_mad_u32_u24 v128, v3, s80, v128\n"
        "s_mul_i32 s80, s45, 512\n"
        "s_add_u32 s80, s80, 11520\n"
        "v_mov_b32 v129, s80\n"
        "v_bfe_u32 v3, v0, 0, 2\n"
        "s_mov_b32 s80, 4\n"
        "v_mad_u32_u24 v129, v3, s80, v129\n"
        "v_bfe_u32 v3, v0, 2, 1\n"
        "s_mov_b32 s80, 16\n"
        "v_mad_u32_u24 v129, v3, s80, v129\n"
        "v_bfe_u32 v3, v0, 3, 1\n"
        "s_mov_b32 s80, 256\n"
        "v_mad_u32_u24 v129, v3, s80, v129\n"
        "v_bfe_u32 v3, v0, 4, 2\n"
        "s_mov_b32 s80, 64\n"
        "v_mad_u32_u24 v129, v3, s80, v129\n"
        "s_mul_i32 s80, s45, 512\n"
        "s_add_u32 s80, s80, 18432\n"
        "v_mov_b32 v139, s80\n"
        "v_bfe_u32 v3, v0, 0, 1\n"
        "s_mov_b32 s80, 32\n"
        "v_mad_u32_u24 v139, v3, s80, v139\n"
        "v_bfe_u32 v3, v0, 1, 2\n"
        "s_mov_b32 s80, 4\n"
        "v_mad_u32_u24 v139, v3, s80, v139\n"
        "v_bfe_u32 v3, v0, 3, 1\n"
        "s_mov_b32 s80, 256\n"
        "v_mad_u32_u24 v139, v3, s80, v139\n"
        "v_bfe_u32 v3, v0, 4, 2\n"
        "s_mov_b32 s80, 64\n"
        "v_mad_u32_u24 v139, v3, s80, v139\n"
        "s_mul_i32 s80, s45, 0\n"
        "s_add_u32 s80, s80, 11520\n"
        "v_mov_b32 v130, s80\n"
        "v_bfe_u32 v3, v0, 0, 1\n"
        "s_mov_b32 s80, 32\n"
        "v_mad_u32_u24 v130, v3, s80, v130\n"
        "v_bfe_u32 v3, v0, 1, 1\n"
        "s_mov_b32 s80, 16\n"
        "v_mad_u32_u24 v130, v3, s80, v130\n"
        "v_bfe_u32 v3, v0, 2, 1\n"
        "s_mov_b32 s80, 128\n"
        "v_mad_u32_u24 v130, v3, s80, v130\n"
        "v_bfe_u32 v3, v0, 3, 1\n"
        "s_mov_b32 s80, 64\n"
        "v_mad_u32_u24 v130, v3, s80, v130\n"
        "v_bfe_u32 v3, v0, 4, 2\n"
        "s_mov_b32 s80, 256\n"
        "v_mad_u32_u24 v130, v3, s80, v130\n"
        "s_mul_i32 s80, s45, 256\n"
        "s_add_u32 s80, s80, 13568\n"
        "v_mov_b32 v131, s80\n"
        "v_bfe_u32 v3, v0, 0, 1\n"
        "s_mov_b32 s80, 16\n"
        "v_mad_u32_u24 v131, v3, s80, v131\n"
        "v_bfe_u32 v3, v0, 2, 2\n"
        "s_mov_b32 s80, 64\n"
        "v_mad_u32_u24 v131, v3, s80, v131\n"
        "v_bfe_u32 v3, v0, 4, 1\n"
        "s_mov_b32 s80, 32\n"
        "v_mad_u32_u24 v131, v3, s80, v131\n"
        "v_bfe_u32 v3, v0, 5, 1\n"
        "s_mov_b32 s80, 8\n"
        "v_mad_u32_u24 v131, v3, s80, v131\n"
        "s_mul_i32 s80, s45, 0\n"
        "s_add_u32 s80, s80, 0\n"
        "v_mov_b32 v132, s80\n"
        "v_bfe_u32 v3, v0, 0, 1\n"
        "s_mov_b32 s80, 16\n"
        "v_mad_u32_u24 v132, v3, s80, v132\n"
        "v_bfe_u32 v3, v0, 1, 1\n"
        "s_mov_b32 s80, 256\n"
        "v_mad_u32_u24 v132, v3, s80, v132\n"
        "v_bfe_u32 v3, v0, 2, 2\n"
        "s_mov_b32 s80, 32\n"
        "v_mad_u32_u24 v132, v3, s80, v132\n"
        "v_bfe_u32 v3, v0, 4, 1\n"
        "s_mov_b32 s80, 512\n"
        "v_mad_u32_u24 v132, v3, s80, v132\n"
        "v_bfe_u32 v3, v0, 5, 1\n"
        "s_mov_b32 s80, 6144\n"
        "v_mad_u32_u24 v132, v3, s80, v132\n"
        "s_mul_i32 s80, s45, 0\n"
        "s_add_u32 s80, s80, 0\n"
        "v_mov_b32 v133, s80\n"
        "v_bfe_u32 v3, v0, 0, 3\n"
        "s_mov_b32 s80, 8\n"
        "v_mad_u32_u24 v133, v3, s80, v133\n"
        "v_bfe_u32 v3, v0, 3, 1\n"
        "s_mov_b32 s80, 512\n"
        "v_mad_u32_u24 v133, v3, s80, v133\n"
        "v_bfe_u32 v3, v0, 4, 2\n"
        "s_mov_b32 s80, 2048\n"
        "v_mad_u32_u24 v133, v3, s80, v133\n"
        "s_mul_i32 s80, s45, 256\n"
        "s_add_u32 s80, s80, 8192\n"
        "v_mov_b32 v137, s80\n"
        "v_bfe_u32 v3, v0, 0, 1\n"
        "s_mov_b32 s80, 32\n"
        "v_mad_u32_u24 v137, v3, s80, v137\n"
        "v_bfe_u32 v3, v0, 1, 1\n"
        "s_mov_b32 s80, 4\n"
        "v_mad_u32_u24 v137, v3, s80, v137\n"
        "v_bfe_u32 v3, v0, 2, 1\n"
        "s_mov_b32 s80, 16\n"
        "v_mad_u32_u24 v137, v3, s80, v137\n"
        "v_bfe_u32 v3, v0, 3, 2\n"
        "s_mov_b32 s80, 64\n"
        "v_mad_u32_u24 v137, v3, s80, v137\n"
        "v_bfe_u32 v3, v0, 5, 1\n"
        "s_mov_b32 s80, 8\n"
        "v_mad_u32_u24 v137, v3, s80, v137\n"
        "s_and_b32 s80, s47, 1\n"
        "v_bfe_u32 v3, v0, 2, 1\n"
        "v_xor_b32 v3, s80, v3\n"
        "v_mul_u32_u24 v3, 36, v3\n"
        "v_xor_b32 v137, v3, v137\n"
        "s_lshl_b32 s66, s80, 10\n"
        "v_xor_b32 v138, s66, v138\n"
        "s_mul_i32 s80, s45, 4\n"
        "s_add_u32 s80, s80, 20480\n"
        "v_mov_b32 v142, s80\n"
        "s_mul_i32 s80, s45, 0\n"
        "s_add_u32 s80, s80, 0\n"
        "v_mov_b32 v143, s80\n"
        "v_bfe_u32 v3, v0, 0, 6\n"
        "s_mov_b32 s80, 4\n"
        "v_mad_u32_u24 v143, v3, s80, v143\n"
        "v_cmp_gt_u32 vcc, 36, v143\n"
        "s_nop 1\n"
        "v_cndmask_b32 v143, 2.0, v143, vcc\n"
        "v_add_u32 v141, s84, v124\n"
        "s_mov_b32 s92, 0xcccccccc\n"
        "s_mov_b32 s93, 0xcccccccc\n"
        "s_lshl_b32 s82, s45, 12\n"
        "s_and_b32 s80, s45, 1\n"
        "s_mul_i32 s80, s80, -1408\n"
        "s_mul_i32 s100, s45, -4608\n"
        "s_add_u32 s100, s100, s80\n"
        "s_add_u32 s100, s100, 1024\n"
        "v_mov_b32 v3, 0x4444\n"
        "v_mov_b32 v4, 0xeeee\n"
        "v_cndmask_b32 v134, v3, v4, s[92:93]\n"
        "v_mov_b32 v3, 0xffff\n"
        "v_mov_b32 v4, 0xffff0000\n"
        "v_cndmask_b32 v3, v3, v4, s[92:93]\n"
        "v_cmp_gt_u32 vcc, 32, v0\n"
        "s_nop 1\n"
        "v_cndmask_b32 v135, 0, v3, vcc\n"
        "v_xor_b32 v136, v135, v3\n"
        "s_mul_i32 s80, s48, 81920\n"
        "s_add_u32 s80, s80, 884736\n"
        "s_lshl_b32 s81, s45, 14\n"
        "s_add_u32 s49, s80, s81\n"
        "s_lshl_b32 s81, s83, 13\n"
        "s_add_u32 s49, s49, s81\n"
        "s_and_b32 s81, s45, 1\n"
        "s_lshl_b32 s81, s81, 8\n"
        "s_add_u32 s50, s80, s81\n"
        "s_lshr_b32 s81, s45, 1\n"
        "s_lshl_b32 s81, s81, 13\n"
        "s_add_u32 s50, s50, s81\n"
        "s_add_u32 s50, s50, 65536\n"
        "s_add_u32 s80, s80, 163840\n"
        "s_lshl_b32 s69, s83, 11\n"
        "s_add_u32 s83, s80, s36\n"
        "s_mov_b32 s86, 0\n"
        "s_mov_b32 s34, 0\n"
        "s_mov_b32 s53, 0\n"
        "s_add_u32 s80, s80, s66\n"
        "s_lshr_b32 s81, s45, 1\n"
        "s_lshl1_add_u32 s81, s46, s81\n"
        "s_lshl_b32 s81, s81, 14\n"
        "s_add_u32 s51, s80, s81\n"
        "s_and_b32 s81, s45, 1\n"
        "s_lshl_b32 s81, s81, 8\n"
        "s_add_u32 s51, s51, s81\n"
        "s_and_b32 s81, s47, 2\n"
        "s_lshl_b32 s81, s81, 12\n"
        "s_add_u32 s51, s51, s81\n"
        "s_lshl_b32 s81, s46, 5\n"
        "s_add_u32 s81, s81, s45\n"
        "s_lshl_b32 s81, s81, 8\n"
        "s_add_u32 s81, s81, s80\n"
        "s_add_u32 s81, s81, 65536\n"
        "s_cmp_lt_u32 s45, 2\n"
        "s_cselect_b32 s52, s81, 0x40000000\n"
        "s_lshl1_add_u32 s81, s47, s46\n"
        "s_mov_b32 s99, s81\n"
        "s_mov_b32 s28, 0\n"
        "s_lshl_b32 s30, s81, 4\n"
        "s_mul_i32 s29, s48, 69632\n"
        "s_add_u32 s29, s29, 150528\n"
        "s_mul_i32 s80, s48, 2112\n"
        "s_add_u32 s80, s80, s47\n"
        "s_mul_i32 s81, s80, 24576\n"
        "s_mul_i32 s85, s45, 6144\n"
        "s_add_u32 s81, s81, s85\n"
        "s_add_u32 s81, s81, 103153664\n"
        "s_add_u32 s55, s81, 2048\n"
        "s_add_u32 s56, s81, s100\n"
        "s_add_u32 s64, s56, 12288\n"
        "s_mul_i32 s85, s46, 3072\n"
        "s_add_u32 s81, s81, s85\n"
        "s_add_u32 s54, s81, 786432\n"
        "s_lshl_b32 s80, s48, 11\n"
        "s_add_u32 s80, s80, s47\n"
        "s_lshl_b32 s80, s80, 12\n"
        "s_add_u32 s80, s80, 208273408\n"
        "s_bfe_u32 s81, %5, 0x20001\n"
        "s_cmp_eq_u32 s81, 1\n"
        "s_cselect_b32 s59, s80, 0x40000000\n"
        "s_add_u32 s80, s80, 2048\n"
        "s_mul_i32 s85, s45, 1536\n"
        "s_sub_u32 s80, s80, s85\n"
        "s_cmp_eq_u32 s46, 1\n"
        "s_cselect_b32 s60, s80, 0x40000000\n"
        "s_lshl_b32 s80, s48, 5\n"
        "s_add_u32 s80, s80, s47\n"
        "s_mul_i32 s80, s80, 544\n"
        "s_xor_b32 s81, s46, 1\n"
        "s_lshl1_add_u32 s80, s81, s80\n"
        "s_add_u32 s80, s80, s45\n"
        "s_lshl_b32 s80, s80, 2\n"
        "s_add_u32 s80, s80, 12288\n"
        "s_and_b32 s81, s59, s60\n"
        "s_bitcmp1_b32 s81, 30\n"
        "s_cselect_b32 s61, 0x40000000, s80\n"
        "s_lshl1_add_u32 s80, s48, s46\n"
        "s_lshl_b32 s57, s80, 13\n"
        "s_and_b32 s80, s47, 3\n"
        "s_lshl_b32 s80, s80, 8\n"
        "s_add_u32 s57, s57, s80\n"
        "s_add_u32 s57, s57, 884736\n"
        "s_mov_b32 s31, 0\n"
        "s_mul_i32 s80, s48, 2359296\n"
        "s_mul_i32 s81, s46, 18432\n"
        "s_add_u32 s81, s81, s82\n"
        "s_add_u32 s80, s80, s81\n"
        "s_add_u32 s58, s80, 16908288\n"
        "s_mov_b32 s82, s69\n"
        "s_mov_b32 s70, 0x41f40000\n"
        "s_mov_b32 s71, 0x41f40000\n"
        "s_mov_b32 s78, 0x41f40000\n"
        "s_mov_b32 s79, 0\n"
        "s_mul_i32 s80, s48, 81920\n"
        "s_mul_i32 s81, s99, 1280\n"
        "s_add_u32 s80, s80, s81\n"
        "s_add_u32 s47, s80, 1376256\n"
        "s_lshl_b32 s80, s99, 7\n"
        "s_add_u32 s80, s80, 2048\n"
        "s_cmp_lt_u32 s99, 8\n"
        "s_cselect_b32 s80, s80, 0x40000000\n"
        "s_cmp_eq_u32 s45, 2\n"
        "s_cselect_b32 s47, s80, s47\n"
        "s_mov_b32 s44, -2\n"
        "s_waitcnt lgkmcnt(0)\n"
        "s_cmp_lg_u32 s45, 0\n"
        "s_cbranch_scc1 L_x1%=\n"
        "s_branch L_xf2%=\n"
        "L_xf1%=:\n"
        "s_add_u32 s63, s63, 1\n"
        "s_cmp_gt_u32 s63, 0x40000\n"
        "s_cbranch_scc1 L_abort%=\n"
        "s_sleep 2\n"
        "L_xf2%=:\n"
        "v_mov_b32 v140, 0\n"
        "buffer_atomic_add v140, v143, s[36:39], s57 offen sc0\n"
        "s_waitcnt vmcnt(0)\n"
        "v_cmp_ge_u32 vcc, s31, v140\n"
        "s_and_b32 vcc_lo, vcc_lo, 0x1ff\n"
        "s_cbranch_scc1 L_xf1%=\n"
        "L_x1%=:\n"
        "s_bitset1_b32 s49, 30\n"
        "s_bitset1_b32 s50, 30\n"
        "s_bitset1_b32 s56, 30\n"
        "s_bitset1_b32 s64, 30\n"
        "s_add_u32 s29, s29, 1024\n"
        "s_add_u32 s80, s29, s30\n"
        "v_mov_b32 v140, 1\n"
        "s_barrier\n"
        "s_memrealtime s[26:27]\n"
        "s_waitcnt lgkmcnt(0)\n"
        "s_sub_u32 s28, s28, s26\n"
        "s_cmp_eq_u32 s45, 0\n"
        "s_cbranch_scc1 L_x2_0%=\n"
        "s_cmp_eq_u32 s45, 1\n"
        "s_cbranch_scc1 L_x2_1%=\n"
        "s_cmp_eq_u32 s45, 2\n"
        "s_cbranch_scc1 L_x2_2%=\n"
        "s_cmp_eq_u32 s45, 3\n"
        "s_cbranch_scc1 L_x2_3%=\n"
        "L_light_0%=:\n"
        "v_mfma_f32_16x16x32_bf16 v[52:55], v[252:255], v[104:107], v[52:55]\n"
        "v_mfma_f32_16x16x32_bf16 v[84:87], v[96:99], a[112:115], v[84:87]\n"
        "v_mfma_f32_16x16x32_bf16 v[88:91], v[96:99], a[120:123], v[88:91]\n"
        "v_cvt_pk_bf16_f32 v48, v48, v52\n"
        "v_cvt_pk_bf16_f32 v49, v49, v53\n"
        "v_mfma_f32_16x16x32_bf16 v[84:87], v[100:103], a[116:119], v[84:87]\n"
        "v_cvt_pk_bf16_f32 v50, v50, v54\n"
        "v_cvt_pk_bf16_f32 v51, v51, v55\n"
        "v_mfma_f32_16x16x32_bf16 v[88:91], v[100:103], a[124:127], v[88:91]\n"
        "buffer_store_dwordx4 v[48:51], v124, s[36:39], s54 offen offset:1024 sc1\n"
        "v_mfma_f32_16x16x32_bf16 v[84:87], v[92:95], a[128:131], v[84:87]\n"
        "v_mfma_f32_16x16x32_bf16 v[88:91], v[92:95], a[136:139], v[88:91]\n"
        "v_mfma_f32_16x16x32_bf16 v[84:87], v[104:107], a[132:135], v[84:87]\n"
        "v_mfma_f32_16x16x32_bf16 v[88:91], v[104:107], a[140:143], v[88:91]\n"
        "v_mfma_f32_16x16x32_bf16 v[56:59], v[32:35], v[92:95], v[56:59]\n"
        "v_mfma_f32_16x16x32_bf16 v[60:63], v[40:43], v[92:95], v[60:63]\n"
        "v_cvt_pk_bf16_f32 v84, v84, v85\n"
        "v_cvt_pk_bf16_f32 v85, v86, v87\n"
        "v_mfma_f32_16x16x32_bf16 v[56:59], v[16:19], v[96:99], v[56:59]\n"
        "v_cvt_pk_bf16_f32 v86, v88, v89\n"
        "v_cvt_pk_bf16_f32 v87, v90, v91\n"
        "v_mfma_f32_16x16x32_bf16 v[60:63], v[24:27], v[96:99], v[60:63]\n"
        "buffer_store_dwordx4 v[84:87], v124, s[36:39], s54 offen offset:2048 sc1\n"
        "v_mfma_f32_16x16x32_bf16 v[56:59], v[20:23], v[100:103], v[56:59]\n"
        "v_mov_b64 v[92:93], 0\n"
        "v_mov_b64 v[94:95], 0\n"
        "v_mfma_f32_16x16x32_bf16 v[60:63], v[28:31], v[100:103], v[60:63]\n"
        "v_mov_b64 v[96:97], 0\n"
        "v_mov_b64 v[98:99], 0\n"
        "v_mfma_f32_16x16x32_bf16 v[56:59], v[36:39], v[104:107], v[56:59]\n"
        "v_mfma_f32_16x16x32_bf16 v[60:63], v[44:47], v[104:107], v[60:63]\n"
        "s_nop 7\n"
        "v_cvt_pk_bf16_f32 v100, v56, v60\n"
        "v_cvt_pk_bf16_f32 v101, v57, v61\n"
        "v_cvt_pk_bf16_f32 v102, v58, v62\n"
        "v_cvt_pk_bf16_f32 v103, v59, v63\n"
        "buffer_store_dwordx4 v[100:103], v124, s[36:39], s54 offen offset:0 sc1\n"
        "s_barrier\n"
        "s_add_u32 s69, s58, s82\n"
        "s_sub_u32 s72, s58, s82\n"
        "s_waitcnt vmcnt(6)\n"
        "buffer_load_dwordx4 v[64:67], v124, s[36:39], s69 offen offset:0 sc1\n"
        "s_waitcnt vmcnt(6)\n"
        "buffer_load_dwordx4 v[68:71], v124, s[36:39], s69 offen offset:1024 sc1\n"
        "s_waitcnt vmcnt(6)\n"
        "buffer_load_dwordx4 v[72:75], v124, s[36:39], s72 offen offset:2048 sc1\n"
        "s_waitcnt vmcnt(6)\n"
        "buffer_load_dwordx4 v[76:79], v124, s[36:39], s72 offen offset:3072 sc1\n"
        "s_mov_b32 s81, 64\n"
        "buffer_load_dword v107, off, s[36:39], s81 sc1\n"
        "s_memrealtime s[26:27]\n"
        "s_waitcnt vmcnt(5)\n"
        "s_waitcnt lgkmcnt(0)\n"
        "s_barrier\n"
        "s_add_u32 s29, s29, 1024\n"
        "s_waitcnt vmcnt(0)\n"
        "L_par_0%=:\n"
        "v_readfirstlane_b32 s80, v107\n"
        "s_nop 0\n"
        "s_cmp_lt_i32 s80, 0\n"
        "s_cbranch_scc1 L_par2_0%=\n"
        "s_add_u32 s63, s63, 1\n"
        "s_cmp_gt_u32 s63, 0x40000\n"
        "s_cbranch_scc1 L_abort%=\n"
        "s_sleep 4\n"
        "buffer_load_dword v107, off, s[36:39], s81 sc1\n"
        "s_waitcnt vmcnt(0)\n"
        "s_branch L_par_0%=\n"
        "L_par2_0%=:\n"
        "s_bfe_u32 s46, s80, 0x10018\n"
        "s_lshl_b32 s46, s46, 21\n"
        "s_mov_b32 s53, 0x200000\n"
        "s_bfe_u32 s84, s80, 0x80010\n"
        "s_sub_u32 s84, 255, s84\n"
        "s_bfe_u32 s76, s80, 0x80008\n"
        "s_sub_u32 s76, s76, 32\n"
        "s_max_i32 s76, s76, 66\n"
        "s_bfe_u32 s77, s80, 0x80000\n"
        "s_sub_u32 s77, s77, 158\n"
        "s_bitset0_b32 s49, 30\n"
        "s_bitset0_b32 s50, 30\n"
        "s_bitset0_b32 s56, 30\n"
        "s_bitset0_b32 s64, 30\n"
        "v_mov_b32 v140, 0\n"
        "s_mov_b32 s28, 1000\n"
        "s_branch L_xok_0%=\n"
        "L_x2_0%=:\n"
        "s_add_u32 s69, s58, s82\n"
        "s_sub_u32 s72, s58, s82\n"
        "buffer_load_dwordx4 v[64:67], v124, s[36:39], s69 offen offset:0 sc1\n"
        "buffer_load_dwordx4 v[68:71], v124, s[36:39], s69 offen offset:1024 sc1\n"
        "buffer_load_dwordx4 v[72:75], v124, s[36:39], s72 offen offset:2048 sc1\n"
        "buffer_load_dwordx4 v[76:79], v124, s[36:39], s72 offen offset:3072 sc1\n"
        "s_waitcnt vmcnt(0)\n"
        "s_memrealtime s[26:27]\n"
        "s_waitcnt lgkmcnt(0)\n"
        "s_add_u32 s28, s28, s26\n"
        "L_xok_0%=:\n"
        "v_mfma_f32_16x16x32_bf16 v[84:87], v[64:67], a[0:3], 0\n"
        "v_mfma_f32_16x16x32_bf16 v[88:91], v[64:67], a[8:11], 0\n"
        "v_mfma_f32_16x16x32_bf16 v[56:59], a[144:147], v[64:67], 0\n"
        "s_branch L_win_0%=\n"
        "L_retry_0%=:\n"
        "s_add_u32 s63, s63, 1\n"
        "s_cmp_gt_u32 s63, 0x40000\n"
        "s_cbranch_scc1 L_abort%=\n"
        "s_sleep 2\n"
        "s_waitcnt vmcnt(0)\n"
        "s_waitcnt lgkmcnt(0)\n"
        "buffer_load_dwordx4 v[80:83], v138, s[36:39], s50 offen offset:0 sc1\n"
        "buffer_load_dwordx4 v[64:67], v138, s[36:39], s49 offen offset:0 sc1\n"
        "buffer_load_dwordx4 v[68:71], v138, s[36:39], s49 offen offset:256 sc1\n"
        "buffer_load_dwordx4 v[72:75], v138, s[36:39], s65 offen offset:0 sc1\n"
        "buffer_load_dwordx4 v[76:79], v138, s[36:39], s65 offen offset:256 sc1\n"
        "s_waitcnt vmcnt(4)\n"
        "v_bitop3_b32 v123, 32, v80, v82 bitop3:0x80\n"
        "v_add_u32_dpp v80, v81, v80 row_half_mirror row_mask:0xf bank_mask:0xf\n"
        "v_add_u32_dpp v82, v83, v82 row_half_mirror row_mask:0xf bank_mask:0xf\n"
        "v_cvt_f32_i32 v80, v80\n"
        "v_cvt_f32_i32 v82, v82\n"
        "v_cvt_pk_bf16_f32 v52, v80, v82\n"
        "ds_write_b32 v137, v52\n"
        "s_waitcnt vmcnt(3)\n"
        "v_bitop3_b32 v123, v123, v64, v66 bitop3:0x80\n"
        "v_cvt_f32_i32 v64, v64\n"
        "v_cvt_f32_i32 v65, v65\n"
        "v_cvt_f32_i32 v66, v66\n"
        "v_cvt_f32_i32 v67, v67\n"
        "v_cvt_pk_bf16_f32 v52, v64, v65\n"
        "v_cvt_pk_bf16_f32 v53, v66, v67\n"
        "ds_write_b64 v126, v[52:53] offset:0\n"
        "s_waitcnt vmcnt(2)\n"
        "v_bitop3_b32 v123, v123, v68, v70 bitop3:0x80\n"
        "v_cvt_f32_i32 v68, v68\n"
        "v_cvt_f32_i32 v69, v69\n"
        "v_cvt_f32_i32 v70, v70\n"
        "v_cvt_f32_i32 v71, v71\n"
        "v_cvt_pk_bf16_f32 v54, v68, v69\n"
        "v_cvt_pk_bf16_f32 v55, v70, v71\n"
        "ds_write_b64 v126, v[54:55] offset:512\n"
        "s_waitcnt vmcnt(1)\n"
        "v_bitop3_b32 v123, v123, v72, v74 bitop3:0x80\n"
        "v_cvt_f32_i32 v72, v72\n"
        "v_cvt_f32_i32 v73, v73\n"
        "ds_read_b128 v[48:51], v124 offset:0\n"
        "v_cvt_f32_i32 v74, v74\n"
        "v_cvt_f32_i32 v75, v75\n"
        "v_cvt_pk_bf16_f32 v52, v72, v73\n"
        "v_cvt_pk_bf16_f32 v53, v74, v75\n"
        "ds_write_b64 v126, v[52:53] offset:1024\n"
        "s_waitcnt vmcnt(0)\n"
        "v_bitop3_b32 v123, v123, v76, v78 bitop3:0x80\n"
        "v_cvt_f32_i32 v76, v76\n"
        "v_cvt_f32_i32 v77, v77\n"
        "v_cvt_f32_i32 v78, v78\n"
        "v_cvt_f32_i32 v79, v79\n"
        "v_cvt_pk_bf16_f32 v54, v76, v77\n"
        "v_cvt_pk_bf16_f32 v55, v78, v79\n"
        "ds_write_b64 v126, v[54:55] offset:1536\n"
        "v_cmp_eq_u32 vcc, 0, v123\n"
        "v_mov_b64 v[122:123], 0\n"
        "v_mov_b64 v[92:93], 0\n"
        "v_mov_b64 v[94:95], 0\n"
        "v_mov_b64 v[96:97], 0\n"
        "v_mov_b64 v[98:99], 0\n"
        "s_waitcnt lgkmcnt(0)\n"
        "s_branch L_dec_0%=\n"
        "L_sw0_0%=:\n"
        "s_memrealtime s[26:27]\n"
        "s_waitcnt lgkmcnt(0)\n"
        "s_sub_u32 s28, s28, s26\n"
        "L_sw_0%=:\n"
        "s_add_u32 s63, s63, 1\n"
        "s_cmp_gt_u32 s63, 0x40000\n"
        "s_cbranch_scc1 L_abort%=\n"
        "s_sleep 2\n"
        "buffer_load_dword v140, v124, s[36:39], s29 offen sc1\n"
        "s_waitcnt vmcnt(0)\n"
        "v_cmp_eq_u32 vcc, 0, v140\n"
        "s_cbranch_vccnz L_sw_0%=\n"
        "s_memrealtime s[26:27]\n"
        "s_waitcnt lgkmcnt(0)\n"
        "s_add_u32 s28, s28, s26\n"
        "s_branch L_sok_0%=\n"
        ".p2align 6\n"
        "L_rot_0%=:\n"
        "v_mfma_f32_16x16x32_bf16 v[60:63], a[236:239], v[100:103], v[60:63]\n"
        "v_mfma_f32_16x16x32_bf16 v[48:51], v[196:199], v[100:103], v[48:51]\n"
        "v_mfma_f32_16x16x32_bf16 v[52:55], v[204:207], v[100:103], v[52:55]\n"
        "s_waitcnt lgkmcnt(8)\n"
        "v_mfma_f32_16x16x32_bf16 v[84:87], v[92:95], a[96:99], v[84:87]\n"
        "ds_read_b128 v[100:103], v124 offset:15360\n"
        "v_mfma_f32_16x16x32_bf16 v[88:91], v[92:95], a[104:107], v[88:91]\n"
        "v_mfma_f32_16x16x32_bf16 v[56:59], v[0:3], v[92:95], v[56:59]\n"
        "v_mfma_f32_16x16x32_bf16 v[60:63], v[8:11], v[92:95], v[60:63]\n"
        "v_mfma_f32_16x16x32_bf16 v[48:51], v[208:211], v[92:95], v[48:51]\n"
        "s_waitcnt lgkmcnt(0)\n"
        "s_barrier\n"
        "v_mfma_f32_16x16x32_bf16 v[52:55], v[216:219], v[92:95], v[52:55]\n"
        "v_mov_b64 v[116:117], 0\n"
        "v_mfma_f32_16x16x32_bf16 v[84:87], v[104:107], a[100:103], v[84:87]\n"
        "ds_read_b128 v[92:95], v124 offset:16384\n"
        "v_mov_b64 v[118:119], 0\n"
        "v_mfma_f32_16x16x32_bf16 v[88:91], v[104:107], a[108:111], v[88:91]\n"
        "v_mov_b64 v[120:121], 0\n"
        "v_mfma_f32_16x16x32_bf16 v[56:59], v[4:7], v[104:107], v[56:59]\n"
        "v_mfma_f32_16x16x32_bf16 v[60:63], v[12:15], v[104:107], v[60:63]\n"
        "v_mfma_f32_16x16x32_bf16 v[48:51], v[212:215], v[104:107], v[48:51]\n"
        "v_mfma_f32_16x16x32_bf16 v[52:55], v[220:223], v[104:107], v[52:55]\n"
        "v_mfma_f32_16x16x32_bf16 v[48:51], v[224:227], v[96:99], v[48:51]\n"
        "ds_read_b128 v[104:107], v124 offset:17408\n"
        "v_mfma_f32_16x16x32_bf16 v[52:55], v[232:235], v[96:99], v[52:55]\n"
        "v_mfma_f32_16x16x32_bf16 v[48:51], v[228:231], v[100:103], v[48:51]\n"
        "v_mfma_f32_16x16x32_bf16 v[52:55], v[236:239], v[100:103], v[52:55]\n"
        "s_waitcnt lgkmcnt(1)\n"
        "v_mfma_f32_16x16x32_bf16 v[48:51], v[240:243], v[92:95], v[48:51]\n"
        "v_mfma_f32_16x16x32_bf16 v[52:55], v[248:251], v[92:95], v[52:55]\n"
        "s_waitcnt lgkmcnt(0)\n"
        "v_mfma_f32_16x16x32_bf16 v[48:51], v[244:247], v[104:107], v[48:51]\n"
        "s_cmp_lt_i32 s44, 0\n"
        "s_cbranch_scc1 L_light_0%=\n"
        "v_mfma_f32_16x16x32_bf16 v[52:55], v[252:255], v[104:107], v[52:55]\n"
        "s_waitcnt vmcnt(4)\n"
        "v_bitop3_b32 v123, 32, v80, v82 bitop3:0x80\n"
        "s_nop 0\n"
        "v_add_u32_dpp v80, v81, v80 row_half_mirror row_mask:0xf bank_mask:0xf\n"
        "v_mfma_f32_16x16x32_bf16 v[84:87], v[96:99], a[112:115], v[84:87]\n"
        "s_nop 0\n"
        "v_add_u32_dpp v82, v83, v82 row_half_mirror row_mask:0xf bank_mask:0xf\n"
        "v_mfma_f32_16x16x32_bf16 v[88:91], v[96:99], a[120:123], v[88:91]\n"
        "v_cvt_pk_bf16_f32 v48, v48, v52\n"
        "v_cvt_pk_bf16_f32 v49, v49, v53\n"
        "s_nop 0\n"
        "v_mfma_f32_16x16x32_bf16 v[84:87], v[100:103], a[116:119], v[84:87]\n"
        "v_cvt_pk_bf16_f32 v50, v50, v54\n"
        "v_cvt_pk_bf16_f32 v51, v51, v55\n"
        "v_mfma_f32_16x16x32_bf16 v[88:91], v[100:103], a[124:127], v[88:91]\n"
        "buffer_store_dwordx4 v[48:51], v124, s[36:39], s54 offen offset:1024 sc1\n"
        "v_cvt_f32_i32 v80, v80\n"
        "v_mfma_f32_16x16x32_bf16 v[84:87], v[92:95], a[128:131], v[84:87]\n"
        "s_nop 0\n"
        "v_cvt_f32_i32 v82, v82\n"
        "v_mfma_f32_16x16x32_bf16 v[88:91], v[92:95], a[136:139], v[88:91]\n"
        "s_nop 0\n"
        "v_cvt_pk_bf16_f32 v52, v80, v82\n"
        "v_mfma_f32_16x16x32_bf16 v[84:87], v[104:107], a[132:135], v[84:87]\n"
        "s_nop 0\n"
        "v_mfma_f32_16x16x32_bf16 v[88:91], v[104:107], a[140:143], v[88:91]\n"
        "ds_write_b32 v137, v52\n"
        "s_waitcnt vmcnt(4)\n"
        "v_bitop3_b32 v123, v123, v64, v66 bitop3:0x80\n"
        "v_cvt_f32_i32 v64, v64\n"
        "v_mfma_f32_16x16x32_bf16 v[56:59], v[32:35], v[92:95], v[56:59]\n"
        "v_cvt_f32_i32 v65, v65\n"
        "v_cvt_f32_i32 v66, v66\n"
        "v_mfma_f32_16x16x32_bf16 v[60:63], v[40:43], v[92:95], v[60:63]\n"
        "v_cvt_pk_bf16_f32 v84, v84, v85\n"
        "v_cvt_pk_bf16_f32 v85, v86, v87\n"
        "v_mfma_f32_16x16x32_bf16 v[56:59], v[16:19], v[96:99], v[56:59]\n"
        "v_cvt_pk_bf16_f32 v86, v88, v89\n"
        "v_cvt_pk_bf16_f32 v87, v90, v91\n"
        "v_mfma_f32_16x16x32_bf16 v[60:63], v[24:27], v[96:99], v[60:63]\n"
        "buffer_store_dwordx4 v[84:87], v124, s[36:39], s54 offen offset:2048 sc1\n"
        "v_cvt_f32_i32 v67, v67\n"
        "v_cvt_pk_bf16_f32 v52, v64, v65\n"
        "v_mfma_f32_16x16x32_bf16 v[56:59], v[20:23], v[100:103], v[56:59]\n"
        "v_mov_b64 v[92:93], 0\n"
        "v_mov_b64 v[94:95], 0\n"
        "v_mfma_f32_16x16x32_bf16 v[60:63], v[28:31], v[100:103], v[60:63]\n"
        "v_mov_b64 v[96:97], 0\n"
        "v_mov_b64 v[98:99], 0\n"
        "v_mfma_f32_16x16x32_bf16 v[56:59], v[36:39], v[104:107], v[56:59]\n"
        "v_cvt_pk_bf16_f32 v53, v66, v67\n"
        "ds_write_b64 v126, v[52:53] offset:0\n"
        "s_waitcnt vmcnt(4)\n"
        "v_bitop3_b32 v123, v123, v68, v70 bitop3:0x80\n"
        "v_mfma_f32_16x16x32_bf16 v[60:63], v[44:47], v[104:107], v[60:63]\n"
        "v_cvt_f32_i32 v68, v68\n"
        "v_cvt_f32_i32 v69, v69\n"
        "v_cvt_f32_i32 v70, v70\n"
        "v_cvt_f32_i32 v71, v71\n"
        "v_cvt_pk_bf16_f32 v54, v68, v69\n"
        "v_cvt_pk_bf16_f32 v55, v70, v71\n"
        "ds_write_b64 v126, v[54:55] offset:512\n"
        "s_waitcnt vmcnt(3)\n"
        "v_bitop3_b32 v123, v123, v72, v74 bitop3:0x80\n"
        "v_cvt_f32_i32 v72, v72\n"
        "v_cvt_pk_bf16_f32 v100, v56, v60\n"
        "v_cvt_pk_bf16_f32 v101, v57, v61\n"
        "v_cvt_pk_bf16_f32 v102, v58, v62\n"
        "v_cvt_pk_bf16_f32 v103, v59, v63\n"
        "v_cvt_f32_i32 v73, v73\n"
        "ds_read_b128 v[48:51], v124 offset:0\n"
        "v_cvt_f32_i32 v74, v74\n"
        "v_cvt_f32_i32 v75, v75\n"
        "v_cvt_pk_bf16_f32 v52, v72, v73\n"
        "v_cvt_pk_bf16_f32 v53, v74, v75\n"
        "ds_write_b64 v126, v[52:53] offset:1024\n"
        "s_waitcnt vmcnt(2)\n"
        "v_bitop3_b32 v123, v123, v76, v78 bitop3:0x80\n"
        "v_cvt_f32_i32 v76, v76\n"
        "v_cvt_f32_i32 v77, v77\n"
        "v_cvt_f32_i32 v78, v78\n"
        "v_cvt_f32_i32 v79, v79\n"
        "v_cvt_pk_bf16_f32 v54, v76, v77\n"
        "v_cvt_pk_bf16_f32 v55, v78, v79\n"
        "ds_write_b64 v126, v[54:55] offset:1536\n"
        "v_cmp_eq_u32 vcc, 0, v123\n"
        "v_mov_b64 v[122:123], 0\n"
        "L_dec_0%=:\n"
        "s_waitcnt lgkmcnt(2)\n"
        "v_smfmac_f32_16x16x64_bf16 v[92:95], v[48:51], a[144:151], v134\n"
        "ds_read_b128 v[52:55], v124 offset:1024\n"
        "v_max3_f32 v104, |v80|, |v82|, |v64|\n"
        "v_smfmac_f32_16x16x64_bf16 v[96:99], v[48:51], a[152:159], v134\n"
        "s_cbranch_vccnz L_retry_0%=\n"
        "v_max3_f32 v104, |v65|, |v66|, v104\n"
        "v_smfmac_f32_16x16x64_bf16 v[116:119], v[48:51], a[240:247], v134\n"
        "buffer_load_dwordx4 v[88:91], v124, s[36:39], s55 offen offset:3072 sc1\n"
        "s_add_u32 s29, s29, 1024\n"
        "v_max3_f32 v104, |v67|, |v68|, v104\n"
        "v_smfmac_f32_16x16x64_bf16 v[120:123], v[48:51], a[248:255], v134\n"
        "s_waitcnt lgkmcnt(0)\n"
        "v_max3_f32 v104, |v69|, |v70|, v104\n"
        "s_barrier\n"
        "ds_read_b128 v[56:59], v124 offset:6144\n"
        "v_smfmac_f32_16x16x64_bf16 v[92:95], v[52:55], a[160:167], v134\n"
        "ds_read_b128 v[60:63], v124 offset:5120\n"
        "v_max3_f32 v104, |v71|, |v72|, v104\n"
        "v_smfmac_f32_16x16x64_bf16 v[96:99], v[52:55], a[168:175], v134\n"
        "buffer_load_dwordx4 v[84:87], v124, s[36:39], s55 offen offset:0 sc1\n"
        "ds_read_b128 v[80:83], v124 offset:7168\n"
        "v_max3_f32 v104, |v73|, |v74|, v104\n"
        "v_smfmac_f32_16x16x64_bf16 v[116:119], v[52:55], v[176:183], v134\n"
        "ds_read_b128 v[64:67], v124 offset:8192\n"
        "v_max3_f32 v104, |v75|, |v76|, v104\n"
        "v_smfmac_f32_16x16x64_bf16 v[120:123], v[52:55], v[184:191], v134\n"
        "ds_read_b128 v[68:71], v124 offset:2048\n"
        "v_max3_f32 v104, |v77|, |v78|, v104\n"
        "s_waitcnt lgkmcnt(4)\n"
        "v_smfmac_f32_16x16x64_bf16 v[92:95], v[56:59], v[0:7], v134\n"
        "ds_read_b128 v[72:75], v124 offset:3072\n"
        "v_max_f32_e64 v104, |v79|, v104\n"
        "v_smfmac_f32_16x16x64_bf16 v[96:99], v[56:59], v[8:15], v134\n"
        "ds_read_b128 v[0:3], v125 offset:12288\n"
        "v_max_f32_dpp v104, v104, v104 row_shr:1 row_mask:0xf bank_mask:0xf\n"
        "s_waitcnt lgkmcnt(3)\n"
        "v_smfmac_f32_16x16x64_bf16 v[92:95], v[60:63], a[224:231], v134\n"
        "ds_read_b128 v[76:79], v124 offset:4096\n"
        "v_max_f32_dpp v104, v104, v104 row_shr:2 row_mask:0xf bank_mask:0xf\n"
        "v_smfmac_f32_16x16x64_bf16 v[96:99], v[60:63], a[232:239], v134\n"
        "ds_read_b128 v[4:7], v125 offset:13312\n"
        "v_max_f32_dpp v104, v104, v104 row_shr:4 row_mask:0xf bank_mask:0xf\n"
        "v_smfmac_f32_16x16x64_bf16 v[92:95], v[80:83], v[16:23], v134\n"
        "ds_read_b128 v[8:11], v125 offset:14336\n"
        "v_max_f32_dpp v104, v104, v104 row_shr:8 row_mask:0xf bank_mask:0xf\n"
        "v_smfmac_f32_16x16x64_bf16 v[96:99], v[80:83], v[24:31], v134\n"
        "ds_read_b128 v[12:15], v125 offset:15360\n"
        "v_max_f32_dpp v104, v104, v104 row_bcast:15 row_mask:0xa bank_mask:0xf\n"
        "v_smfmac_f32_16x16x64_bf16 v[92:95], v[64:67], v[32:39], v134\n"
        "ds_read_b128 v[16:19], v125 offset:16384\n"
        "v_max_f32_dpp v104, v104, v104 row_bcast:31 row_mask:0xc bank_mask:0xf\n"
        "v_smfmac_f32_16x16x64_bf16 v[96:99], v[64:67], v[40:47], v134\n"
        "ds_read_b128 v[20:23], v125 offset:17408\n"
        "v_readlane_b32 s85, v104, 63\n"
        "s_waitcnt lgkmcnt(5)\n"
        "v_smfmac_f32_16x16x64_bf16 v[92:95], v[68:71], a[176:183], v134\n"
        "ds_read_b128 v[24:27], v125 offset:18432\n"
        "v_smfmac_f32_16x16x64_bf16 v[96:99], v[68:71], a[184:191], v134\n"
        "ds_read_b128 v[28:31], v125 offset:19456\n"
        "v_mov_b32 v104, s85\n"
        "v_smfmac_f32_16x16x64_bf16 v[92:95], v[72:75], a[192:199], v134\n"
        "ds_read_b128 v[32:35], v125 offset:20480\n"
        "v_smfmac_f32_16x16x64_bf16 v[96:99], v[72:75], a[200:207], v134\n"
        "ds_read_b128 v[36:39], v125 offset:21504\n"
        "s_add_u32 s69, s58, s82\n"
        "v_smfmac_f32_16x16x64_bf16 v[92:95], v[76:79], a[208:215], v134\n"
        "ds_read_b128 v[40:43], v125 offset:22528\n"
        "s_sub_u32 s72, s58, s82\n"
        "v_smfmac_f32_16x16x64_bf16 v[96:99], v[76:79], a[216:223], v134\n"
        "ds_read_b128 v[44:47], v125 offset:23552\n"
        "v_smfmac_f32_16x16x64_bf16 v[116:119], v[64:67], v[240:247], v134\n"
        "ds_write_b32 v142, v104\n"
        "v_add_f32 v92, v92, v94\n"
        "v_add_f32 v93, v93, v95\n"
        "v_smfmac_f32_16x16x64_bf16 v[120:123], v[64:67], v[248:255], v134\n"
        "v_add_f32 v96, v96, v98\n"
        "v_add_f32 v97, v97, v99\n"
        "v_cvt_pk_bf16_f32 v92, v92, v96\n"
        "v_cvt_pk_bf16_f32 v93, v93, v97\n"
        "s_waitcnt lgkmcnt(3)\n"
        "v_smfmac_f32_16x16x64_bf16 v[116:119], v[68:71], v[0:7], v134\n"
        "ds_write2_b32 v127, v92, v93 offset1:72\n"
        "v_smfmac_f32_16x16x64_bf16 v[120:123], v[68:71], v[8:15], v134\n"
        "buffer_store_dwordx4 v[100:103], v124, s[36:39], s54 offen offset:0 sc1\n"
        "v_mov_b64 v[98:99], 0\n"
        "v_smfmac_f32_16x16x64_bf16 v[116:119], v[72:75], v[16:23], v134\n"
        "v_mov_b64 v[96:97], 0\n"
        "v_smfmac_f32_16x16x64_bf16 v[120:123], v[72:75], v[24:31], v134\n"
        "s_waitcnt vmcnt(4)\n"
        "s_waitcnt lgkmcnt(0)\n"
        "v_mov_b64 v[106:107], 0\n"
        "s_barrier\n"
        "ds_read_b128 v[0:3], v128 offset:0\n"
        "v_smfmac_f32_16x16x64_bf16 v[116:119], v[76:79], v[32:39], v134\n"
        "ds_read_b128 v[24:27], v124 offset:20480\n"
        "v_mov_b64 v[104:105], 0\n"
        "v_smfmac_f32_16x16x64_bf16 v[120:123], v[76:79], v[40:47], v134\n"
        "ds_read_b128 v[4:7], v128 offset:64\n"
        "v_mov_b64 v[94:95], 0\n"
        "v_smfmac_f32_16x16x64_bf16 v[116:119], v[60:63], v[192:199], v134\n"
        "ds_read_b128 v[8:11], v128 offset:128\n"
        "v_mov_b64 v[92:93], 0\n"
        "v_smfmac_f32_16x16x64_bf16 v[120:123], v[60:63], v[200:207], v134\n"
        "ds_read_b128 v[12:15], v128 offset:192\n"
        "v_mov_b64 v[20:21], 0\n"
        "v_smfmac_f32_16x16x64_bf16 v[116:119], v[56:59], v[208:215], v134\n"
        "s_waitcnt lgkmcnt(3)\n"
        "v_max3_f32 v24, v24, v25, v26\n"
        "s_waitcnt lgkmcnt(2)\n"
        "v_smfmac_f32_16x16x64_bf16 v[104:107], v[108:111], v[0:7], v134\n"
        "v_max_f32 v24, v24, v27\n"
        "v_smfmac_f32_16x16x64_bf16 v[120:123], v[56:59], v[216:223], v134\n"
        "v_readfirstlane_b32 s84, v24\n"
        "s_waitcnt lgkmcnt(0)\n"
        "v_smfmac_f32_16x16x64_bf16 v[104:107], v[112:115], v[8:15], v134\n"
        "v_mov_b64 v[22:23], 0\n"
        "v_smfmac_f32_16x16x64_bf16 v[116:119], v[80:83], v[224:231], v134\n"
        "s_lshr_b32 s84, s84, 23\n"
        "s_add_u32 s84, s84, s101\n"
        "v_smfmac_f32_16x16x64_bf16 v[120:123], v[80:83], v[232:239], v134\n"
        "s_max_i32 s84, s84, s76\n"
        "s_min_i32 s84, s84, 186\n"
        "v_add_f32 v104, v104, v106\n"
        "v_add_f32 v105, v105, v107\n"
        "v_smfmac_f32_16x16x64_bf16 v[92:95], v[64:67], a[128:135], v134\n"
        "ds_write_b64 v131, v[104:105]\n"
        "v_add_f32 v116, v116, v118\n"
        "v_add_f32 v117, v117, v119\n"
        "v_pk_add_f32 v[120:121], v[120:121], v[122:123]\n"
        "v_cvt_pk_bf16_f32 v116, v116, v120\n"
        "v_cvt_pk_bf16_f32 v117, v117, v121\n"
        "ds_write2_b32 v129, v116, v117 offset1:8\n"
        "v_smfmac_f32_16x16x64_bf16 v[96:99], v[64:67], a[136:143], v134\n"
        "s_mul_i32 s88, s84, 0x800080\n"
        "s_mov_b32 s89, s88\n"
        "v_smfmac_f32_16x16x64_bf16 v[92:95], v[68:71], a[32:39], v134\n"
        "buffer_load_dwordx4 v[64:67], v124, s[36:39], s69 offen offset:0 sc1\n"
        "v_mov_b64 v[104:105], s[88:89]\n"
        "v_smfmac_f32_16x16x64_bf16 v[96:99], v[68:71], a[40:47], v134\n"
        "s_add_u32 s90, s88, s35\n"
        "s_mov_b32 s91, s90\n"
        "v_mov_b64 v[106:107], s[90:91]\n"
        "v_smfmac_f32_16x16x64_bf16 v[92:95], v[48:51], a[0:7], v134\n"
        "s_waitcnt lgkmcnt(0)\n"
        "v_mov_b64 v[100:101], s[88:89]\n"
        "s_barrier\n"
        "ds_read_b128 v[24:27], v130\n"
        "v_smfmac_f32_16x16x64_bf16 v[96:99], v[48:51], a[8:15], v134\n"
        "ds_read_b128 v[28:31], v130 offset:1024\n"
        "v_mov_b64 v[102:103], s[90:91]\n"
        "v_smfmac_f32_16x16x64_bf16 v[92:95], v[52:55], a[16:23], v134\n"
        "ds_read_b128 v[116:119], v124 offset:13568\n"
        "v_mov_b64 v[48:49], 0\n"
        "v_smfmac_f32_16x16x64_bf16 v[96:99], v[52:55], a[24:31], v134\n"
        "ds_read_b128 v[32:35], v141 offset:0\n"
        "v_mov_b64 v[50:51], 0\n"
        "v_smfmac_f32_16x16x64_bf16 v[92:95], v[56:59], a[96:103], v134\n"
        "ds_read_b128 v[36:39], v141 offset:1024\n"
        "v_mov_b64 v[52:53], 0\n"
        "v_smfmac_f32_16x16x64_bf16 v[96:99], v[56:59], a[104:111], v134\n"
        "ds_read_b128 v[40:43], v141 offset:2048\n"
        "v_mov_b64 v[54:55], 0\n"
        "s_waitcnt lgkmcnt(5)\n"
        "v_smfmac_f32_16x16x64_bf16 v[20:23], v[24:27], v[0:7], v134\n"
        "ds_read_b128 v[44:47], v141 offset:3072\n"
        "v_mov_b64 v[56:57], 0\n"
        "s_waitcnt lgkmcnt(4)\n"
        "v_smfmac_f32_16x16x64_bf16 v[20:23], v[28:31], v[8:15], v134\n"
        "buffer_load_dwordx4 v[68:71], v124, s[36:39], s69 offen offset:1024 sc1\n"
        "v_max3_f32 v121, v116, v117, v118\n"
        "v_smfmac_f32_16x16x64_bf16 v[92:95], v[72:75], a[48:55], v134\n"
        "ds_read_b128 v[0:3], v125 offset:24576\n"
        "v_mov_b64 v[58:59], 0\n"
        "v_smfmac_f32_16x16x64_bf16 v[96:99], v[72:75], a[56:63], v134\n"
        "buffer_load_dwordx4 v[72:75], v124, s[36:39], s72 offen offset:2048 sc1\n"
        "v_add_f32 v20, v20, v22\n"
        "v_add_f32 v21, v21, v23\n"
        "v_cndmask_b32 v120, v20, v21, s[92:93]\n"
        "v_mul_f32 v120, s74, v120\n"
        "v_max3_f32 v121, v121, v119, v120\n"
        "v_smfmac_f32_16x16x64_bf16 v[92:95], v[76:79], a[64:71], v134\n"
        "ds_read_b128 v[4:7], v125 offset:25600\n"
        "v_max_f32_dpp v121, v121, v121 quad_perm:[2,3,0,1] row_mask:0xf bank_mask:0xf\n"
        "v_mov_b32 v122, v121\n"
        "v_smfmac_f32_16x16x64_bf16 v[96:99], v[76:79], a[72:79], v134\n"
        "buffer_load_dwordx4 v[76:79], v124, s[36:39], s72 offen offset:3072 sc1\n"
        "v_permlane16_swap_b32 v121, v122\n"
        "v_max_f32 v121, v121, v122\n"
        "v_mov_b32 v122, v121\n"
        "v_smfmac_f32_16x16x64_bf16 v[92:95], v[80:83], a[112:119], v134\n"
        "ds_read_b128 v[8:11], v125 offset:26624\n"
        "v_permlane32_swap_b32 v121, v122\n"
        "v_smfmac_f32_16x16x64_bf16 v[96:99], v[80:83], a[120:127], v134\n"
        "v_max_f32 v121, v121, v122\n"
        "v_mul_f32_e64 v123, -s73, v121\n"
        "v_fma_f32 v120, v120, s73, v123\n"
        "v_fma_f32 v116, v116, s73, v123\n"
        "v_smfmac_f32_16x16x64_bf16 v[92:95], v[60:63], a[80:87], v134\n"
        "ds_read_b128 v[12:15], v125 offset:27648\n"
        "v_fma_f32 v117, v117, s73, v123\n"
        "v_fma_f32 v118, v118, s73, v123\n"
        "v_fma_f32 v119, v119, s73, v123\n"
        "v_exp_f32 v120, v120\n"
        "v_smfmac_f32_16x16x64_bf16 v[96:99], v[60:63], a[88:95], v134\n"
        "ds_read_b128 v[16:19], v125 offset:28672\n"
        "v_exp_f32 v116, v116\n"
        "v_mul_f32 v120, s74, v120\n"
        "v_exp_f32 v117, v117\n"
        "v_exp_f32 v118, v118\n"
        "v_exp_f32 v119, v119\n"
        "s_waitcnt vmcnt(5)\n"
        "v_mov_b64 v[108:109], v[86:87]\n"
        "v_mov_b64 v[112:113], v[90:91]\n"
        "v_cvt_pk_bf16_f32 v120, v120, v120\n"
        "v_cvt_pk_bf16_f32 v60, v116, v117\n"
        "v_cvt_pk_bf16_f32 v62, v118, v119\n"
        "v_and_b32 v61, v120, v135\n"
        "v_and_b32 v63, v120, v136\n"
        "v_pk_add_f32 v[92:93], v[92:93], v[94:95]\n"
        "v_pk_add_f32 v[96:97], v[96:97], v[98:99]\n"
        "v_smfmac_f32_16x16x64_bf16 v[56:59], v[60:63], v[100:107], v134\n"
        "ds_read_b128 v[20:23], v125 offset:29696\n"
        "v_cvt_pk_bf16_f32 v86, v92, v93\n"
        "v_cvt_pk_bf16_f32 v87, v92, v93\n"
        "v_mov_b64 v[90:91], v[86:87]\n"
        "v_cvt_pk_bf16_f32 v110, v96, v97\n"
        "v_cvt_pk_bf16_f32 v111, v96, v97\n"
        "v_smfmac_f32_16x16x64_bf16 v[48:51], v[60:63], v[84:91], v134\n"
        "ds_read_b128 v[24:27], v125 offset:30720\n"
        "v_mov_b64 v[114:115], v[110:111]\n"
        "v_add_f32 v56, v56, v58\n"
        "v_add_f32 v57, v57, v59\n"
        "v_smfmac_f32_16x16x64_bf16 v[52:55], v[60:63], v[108:115], v134\n"
        "ds_read_b128 v[28:31], v125 offset:31744\n"
        "s_nop 0\n"
        "v_rcp_f32 v56, v56\n"
        "v_rcp_f32 v57, v57\n"
        "v_pk_add_f32 v[48:49], v[48:49], v[50:51]\n"
        "v_pk_mul_f32 v[48:49], v[48:49], v[56:57]\n"
        "v_mov_b32 v110, v86\n"
        "v_mov_b64 v[92:93], s[70:71]\n"
        "v_pk_add_f32 v[52:53], v[52:53], v[54:55]\n"
        "v_pk_mul_f32 v[52:53], v[52:53], v[56:57]\n"
        "v_cvt_pk_bf16_f32 v48, v48, v52\n"
        "v_cvt_pk_bf16_f32 v49, v49, v53\n"
        "ds_write2_b32 v139, v48, v49 offset1:4\n"
        "v_mov_b64 v[94:95], 0\n"
        "buffer_store_dwordx2 v[110:111], v126, s[36:39], s60 offen sc1\n"
        "v_mov_b64 v[96:97], 0\n"
        "v_mov_b64 v[98:99], 0\n"
        "v_mov_b64 v[100:101], s[70:71]\n"
        "v_mov_b64 v[102:103], 0\n"
        "v_mov_b64 v[104:105], 0\n"
        "s_waitcnt lgkmcnt(0)\n"
        "v_mov_b64 v[106:107], 0\n"
        "s_barrier\n"
        "ds_read_b128 v[108:111], v124 offset:18432\n"
        "v_mov_b64 v[118:119], 0\n"
        "ds_read_b128 v[112:115], v124 offset:19456\n"
        "v_mov_b64 v[118:119], 0\n"
        "s_nop 0\n"
        "v_mov_b64 v[116:117], s[78:79]\n"
        "s_nop 0\n"
        "v_mov_b64 v[118:119], 0\n"
        "s_nop 0\n"
        "v_mov_b64 v[116:117], s[78:79]\n"
        "s_nop 0\n"
        "s_waitcnt lgkmcnt(1)\n"
        "v_smfmac_f32_16x16x64_bf16 v[116:119], v[108:111], v[32:39], v134\n"
        "s_waitcnt lgkmcnt(0)\n"
        "v_smfmac_f32_16x16x64_bf16 v[116:119], v[112:115], v[40:47], v134\n"
        "v_smfmac_f32_16x16x64_bf16 v[92:95], v[108:111], v[144:151], v134\n"
        "v_smfmac_f32_16x16x64_bf16 v[96:99], v[108:111], v[152:159], v134\n"
        "buffer_load_dword v140, v124, s[36:39], s29 offen sc1\n"
        "v_add_f32 v116, v116, v118\n"
        "v_add_f32 v117, v117, v119\n"
        "v_smfmac_f32_16x16x64_bf16 v[92:95], v[112:115], v[160:167], v134\n"
        "v_cvt_rpi_i32_f32 v118, v116\n"
        "v_bfi_b32 v118, 63, 2, v118\n"
        "v_cvt_rpi_i32_f32 v119, v117\n"
        "v_smfmac_f32_16x16x64_bf16 v[96:99], v[112:115], v[168:175], v134\n"
        "buffer_atomic_add_x2 v[118:119], v133, s[36:39], s52 offen\n"
        "v_smfmac_f32_16x16x64_bf16 v[100:103], v[108:111], v[0:7], v134\n"
        "v_add_f32 v92, v92, v94\n"
        "v_add_f32 v93, v93, v95\n"
        "v_cvt_rpi_i32_f32 v94, v92\n"
        "v_smfmac_f32_16x16x64_bf16 v[104:107], v[108:111], v[8:15], v134\n"
        "v_bfi_b32 v94, 63, 1, v94\n"
        "v_add_f32 v96, v96, v98\n"
        "v_smfmac_f32_16x16x64_bf16 v[100:103], v[112:115], v[16:23], v134\n"
        "v_cvt_rpi_i32_f32 v95, v96\n"
        "v_smfmac_f32_16x16x64_bf16 v[104:107], v[112:115], v[24:31], v134\n"
        "buffer_atomic_add_x2 v[94:95], v133, s[36:39], s51 offen\n"
        "v_add_f32 v97, v97, v99\n"
        "v_cvt_rpi_i32_f32 v98, v93\n"
        "v_bfi_b32 v98, 63, 1, v98\n"
        "v_cvt_rpi_i32_f32 v99, v97\n"
        "s_waitcnt vmcnt(7)\n"
        "v_mfma_f32_16x16x32_bf16 v[84:87], v[64:67], a[0:3], 0\n"
        "buffer_atomic_add_x2 v[98:99], v133, s[36:39], s66 offen\n"
        "v_add_f32 v100, v100, v102\n"
        "v_add_f32 v101, v101, v103\n"
        "v_add_f32 v104, v104, v106\n"
        "v_cvt_rpi_i32_f32 v102, v100\n"
        "v_bfi_b32 v102, 63, 1, v102\n"
        "v_cvt_rpi_i32_f32 v103, v104\n"
        "v_mfma_f32_16x16x32_bf16 v[88:91], v[64:67], a[8:11], 0\n"
        "buffer_atomic_add_x2 v[102:103], v133, s[36:39], s67 offen\n"
        "v_add_f32 v105, v105, v107\n"
        "v_cvt_rpi_i32_f32 v106, v101\n"
        "v_bfi_b32 v106, 63, 1, v106\n"
        "v_cvt_rpi_i32_f32 v107, v105\n"
        "v_mfma_f32_16x16x32_bf16 v[56:59], a[144:147], v[64:67], 0\n"
        "buffer_atomic_add_x2 v[106:107], v133, s[36:39], s68 offen\n"
        "L_win_0%=:\n"
        "v_mfma_f32_16x16x32_bf16 v[60:63], a[152:155], v[64:67], 0\n"
        "v_mfma_f32_16x16x32_bf16 v[48:51], a[240:243], v[64:67], 0\n"
        "ds_write_b128 v124, v[64:67] offset:0\n"
        "v_mfma_f32_16x16x32_bf16 v[52:55], a[248:251], v[64:67], 0\n"
        "s_waitcnt vmcnt(9)\n"
        "v_mfma_f32_16x16x32_bf16 v[84:87], v[68:71], a[4:7], v[84:87]\n"
        "ds_write_b128 v124, v[68:71] offset:1024\n"
        "v_mfma_f32_16x16x32_bf16 v[88:91], v[68:71], a[12:15], v[88:91]\n"
        "v_mfma_f32_16x16x32_bf16 v[56:59], a[148:151], v[68:71], v[56:59]\n"
        "s_waitcnt vmcnt(8)\n"
        "ds_write_b128 v124, v[72:75] offset:2048\n"
        "v_mfma_f32_16x16x32_bf16 v[60:63], a[156:159], v[68:71], v[60:63]\n"
        "v_mfma_f32_16x16x32_bf16 v[48:51], a[244:247], v[68:71], v[48:51]\n"
        "s_waitcnt vmcnt(7)\n"
        "ds_write_b128 v124, v[76:79] offset:3072\n"
        "v_mfma_f32_16x16x32_bf16 v[52:55], a[252:255], v[68:71], v[52:55]\n"
        "v_mfma_f32_16x16x32_bf16 v[84:87], v[72:75], a[16:19], v[84:87]\n"
        "v_mfma_f32_16x16x32_bf16 v[88:91], v[72:75], a[24:27], v[88:91]\n"
        "v_mfma_f32_16x16x32_bf16 v[56:59], a[160:163], v[72:75], v[56:59]\n"
        "v_mfma_f32_16x16x32_bf16 v[60:63], a[168:171], v[72:75], v[60:63]\n"
        "v_mfma_f32_16x16x32_bf16 v[48:51], v[176:179], v[72:75], v[48:51]\n"
        "ds_read_b128 v[0:3], v125 offset:12288\n"
        "v_mfma_f32_16x16x32_bf16 v[52:55], v[184:187], v[72:75], v[52:55]\n"
        "ds_read_b128 v[8:11], v125 offset:14336\n"
        "v_mfma_f32_16x16x32_bf16 v[84:87], v[76:79], a[20:23], v[84:87]\n"
        "s_waitcnt lgkmcnt(2)\n"
        "s_barrier\n"
        "v_mfma_f32_16x16x32_bf16 v[88:91], v[76:79], a[28:31], v[88:91]\n"
        "ds_read_b128 v[92:95], v124 offset:4096\n"
        "v_mov_b32 v116, s62\n"
        "v_mfma_f32_16x16x32_bf16 v[56:59], a[164:167], v[76:79], v[56:59]\n"
        "s_add_u32 s49, s49, 163840\n"
        "ds_read_b128 v[104:107], v124 offset:5120\n"
        "v_mfma_f32_16x16x32_bf16 v[60:63], a[172:175], v[76:79], v[60:63]\n"
        "s_add_u32 s50, s50, 163840\n"
        "ds_read_b128 v[4:7], v125 offset:13312\n"
        "v_mfma_f32_16x16x32_bf16 v[48:51], v[180:183], v[76:79], v[48:51]\n"
        "s_xor_b32 s49, s49, s86\n"
        "ds_read_b128 v[12:15], v125 offset:15360\n"
        "v_mfma_f32_16x16x32_bf16 v[52:55], v[188:191], v[76:79], v[52:55]\n"
        "s_xor_b32 s65, s49, 8192\n"
        "s_add_u32 s56, s56, 786432\n"
        "s_waitcnt lgkmcnt(3)\n"
        "v_mfma_f32_16x16x32_bf16 v[84:87], v[92:95], a[32:35], v[84:87]\n"
        "s_add_u32 s64, s64, 786432\n"
        "ds_read_b128 v[96:99], v124 offset:6144\n"
        "v_mfma_f32_16x16x32_bf16 v[88:91], v[92:95], a[40:43], v[88:91]\n"
        "ds_read_b128 v[100:103], v124 offset:7168\n"
        "v_mfma_f32_16x16x32_bf16 v[56:59], a[176:179], v[92:95], v[56:59]\n"
        "ds_read_b128 v[16:19], v125 offset:16384\n"
        "v_mfma_f32_16x16x32_bf16 v[60:63], a[184:187], v[92:95], v[60:63]\n"
        "s_add_u32 s51, s51, 163840\n"
        "ds_read_b128 v[20:23], v125 offset:17408\n"
        "v_mfma_f32_16x16x32_bf16 v[48:51], v[0:3], v[92:95], v[48:51]\n"
        "s_add_u32 s52, s52, 163840\n"
        "ds_read_b128 v[24:27], v125 offset:18432\n"
        "v_mfma_f32_16x16x32_bf16 v[52:55], v[8:11], v[92:95], v[52:55]\n"
        "ds_read_b128 v[28:31], v125 offset:19456\n"
        "s_waitcnt lgkmcnt(8)\n"
        "v_mfma_f32_16x16x32_bf16 v[84:87], v[104:107], a[36:39], v[84:87]\n"
        "s_add_u32 s83, s83, 163840\n"
        "ds_read_b128 v[92:95], v124 offset:8192\n"
        "v_mfma_f32_16x16x32_bf16 v[88:91], v[104:107], a[44:47], v[88:91]\n"
        "s_waitcnt vmcnt(6)\n"
        "s_mov_b64 exec, 1\n"
        "buffer_atomic_umax v116, off, s[36:39], s61\n"
        "s_mov_b64 exec, -1\n"
        "v_mfma_f32_16x16x32_bf16 v[56:59], a[180:183], v[104:107], v[56:59]\n"
        "s_cmp_eq_u32 s44, 63\n"
        "s_cbranch_scc1 L_end%=\n"
        "v_mfma_f32_16x16x32_bf16 v[60:63], a[188:191], v[104:107], v[60:63]\n"
        "s_xor_b32 s86, s83, s46\n"
        "ds_read_b128 v[32:35], v125 offset:20480\n"
        "s_waitcnt lgkmcnt(9)\n"
        "v_mfma_f32_16x16x32_bf16 v[48:51], v[4:7], v[104:107], v[48:51]\n"
        "s_and_b32 s86, s86, s53\n"
        "ds_read_b128 v[36:39], v125 offset:21504\n"
        "s_waitcnt lgkmcnt(9)\n"
        "v_mfma_f32_16x16x32_bf16 v[52:55], v[12:15], v[104:107], v[52:55]\n"
        "ds_read_b128 v[40:43], v125 offset:22528\n"
        "s_waitcnt lgkmcnt(9)\n"
        "v_mfma_f32_16x16x32_bf16 v[84:87], v[96:99], a[48:51], v[84:87]\n"
        "s_lshr_b32 s86, s86, 8\n"
        "ds_read_b128 v[104:107], v124 offset:9216\n"
        "v_mfma_f32_16x16x32_bf16 v[88:91], v[96:99], a[56:59], v[88:91]\n"
        "s_xor_b32 s86, s86, s34\n"
        "ds_read_b128 v[44:47], v125 offset:23552\n"
        "v_mfma_f32_16x16x32_bf16 v[56:59], a[192:195], v[96:99], v[56:59]\n"
        "s_xor_b32 s34, s34, s86\n"
        "ds_read_b128 v[0:3], v125 offset:0\n"
        "v_mfma_f32_16x16x32_bf16 v[60:63], a[200:203], v[96:99], v[60:63]\n"
        "ds_read_b128 v[4:7], v125 offset:1024\n"
        "s_waitcnt vmcnt(6)\n"
        "v_cmp_eq_u32 vcc, 0, v140\n"
        "s_waitcnt lgkmcnt(11)\n"
        "v_mfma_f32_16x16x32_bf16 v[48:51], v[16:19], v[96:99], v[48:51]\n"
        "s_cbranch_vccnz L_sw0_0%=\n"
        "L_sok_0%=:\n"
        "s_waitcnt lgkmcnt(9)\n"
        "v_mfma_f32_16x16x32_bf16 v[52:55], v[24:27], v[96:99], v[52:55]\n"
        "v_readlane_b32 s81, v140, s99\n"
        "v_max_u32_dpp v140, v140, v140 row_shr:1 row_mask:0xf bank_mask:0xf\n"
        "v_mfma_f32_16x16x32_bf16 v[84:87], v[100:103], a[52:55], v[84:87]\n"
        "ds_read_b128 v[96:99], v124 offset:10240\n"
        "v_max_u32_dpp v140, v140, v140 row_shr:2 row_mask:0xf bank_mask:0xf\n"
        "v_mfma_f32_16x16x32_bf16 v[88:91], v[100:103], a[60:63], v[88:91]\n"
        "buffer_load_dwordx4 v[108:111], v132, s[36:39], s56 offen offset:0 sc1\n"
        "v_max_u32_dpp v140, v140, v140 row_shr:4 row_mask:0xf bank_mask:0xf\n"
        "v_mfma_f32_16x16x32_bf16 v[56:59], a[196:199], v[100:103], v[56:59]\n"
        "ds_read_b128 v[8:11], v125 offset:2048\n"
        "v_max_u32_dpp v140, v140, v140 row_shr:8 row_mask:0xf bank_mask:0xf\n"
        "v_mfma_f32_16x16x32_bf16 v[60:63], a[204:207], v[100:103], v[60:63]\n"
        "buffer_load_dwordx4 v[112:115], v132, s[36:39], s64 offen offset:0 sc1\n"
        "v_max_u32_dpp v140, v140, v140 row_bcast:15 row_mask:0xa bank_mask:0xf\n"
        "v_mfma_f32_16x16x32_bf16 v[48:51], v[20:23], v[100:103], v[48:51]\n"
        "ds_read_b128 v[12:15], v125 offset:3072\n"
        "v_max_u32_dpp v140, v140, v140 row_bcast:31 row_mask:0xc bank_mask:0xf\n"
        "s_waitcnt lgkmcnt(11)\n"
        "v_mfma_f32_16x16x32_bf16 v[52:55], v[28:31], v[100:103], v[52:55]\n"
        "v_readlane_b32 s33, v140, 63\n"
        "s_lshl1_add_u32 s28, s28, s81\n"
        "s_waitcnt lgkmcnt(10)\n"
        "v_mfma_f32_16x16x32_bf16 v[84:87], v[92:95], a[64:67], v[84:87]\n"
        "ds_read_b128 v[100:103], v124 offset:11264\n"
        "s_add_u32 s28, s28, 36\n"
        "s_sub_i32 s33, s33, s28\n"
        "v_mfma_f32_16x16x32_bf16 v[88:91], v[92:95], a[72:75], v[88:91]\n"
        "s_min_i32 s33, s33, 500\n"
        "s_mov_b32 s28, 0\n"
        "L_dl_0%=:\n"
        "s_sub_i32 s33, s33, 3\n"
        "s_cmp_gt_i32 s33, 0\n"
        "s_cbranch_scc1 L_dl_0%=\n"
        "v_mfma_f32_16x16x32_bf16 v[56:59], a[208:211], v[92:95], v[56:59]\n"
        "s_xor_b32 s51, s51, s86\n"
        "buffer_load_dwordx4 v[80:83], v138, s[36:39], s50 offen offset:0 sc1\n"
        "v_mfma_f32_16x16x32_bf16 v[60:63], a[216:219], v[92:95], v[60:63]\n"
        "s_add_u32 s55, s55, 786432\n"
        "ds_read_b128 v[16:19], v125 offset:4096\n"
        "s_waitcnt lgkmcnt(11)\n"
        "v_mfma_f32_16x16x32_bf16 v[48:51], v[32:35], v[92:95], v[48:51]\n"
        "s_add_u32 s54, s54, 786432\n"
        "buffer_load_dwordx4 v[64:67], v138, s[36:39], s49 offen offset:0 sc1\n"
        "s_waitcnt lgkmcnt(9)\n"
        "v_mfma_f32_16x16x32_bf16 v[52:55], v[40:43], v[92:95], v[52:55]\n"
        "ds_read_b128 v[20:23], v125 offset:5120\n"
        "s_waitcnt lgkmcnt(9)\n"
        "v_mfma_f32_16x16x32_bf16 v[84:87], v[104:107], a[68:71], v[84:87]\n"
        "s_add_u32 s60, s60, 131072\n"
        "ds_read_b128 v[92:95], v124 offset:12288\n"
        "v_mfma_f32_16x16x32_bf16 v[88:91], v[104:107], a[76:79], v[88:91]\n"
        "s_add_u32 s58, s58, 36864\n"
        "buffer_load_dwordx4 v[68:71], v138, s[36:39], s49 offen offset:256 sc1\n"
        "v_mfma_f32_16x16x32_bf16 v[56:59], a[212:215], v[104:107], v[56:59]\n"
        "s_xor_b32 s66, s51, 1024\n"
        "ds_read_b128 v[24:27], v125 offset:6144\n"
        "v_mfma_f32_16x16x32_bf16 v[60:63], a[220:223], v[104:107], v[60:63]\n"
        "s_xor_b32 s67, s51, 8192\n"
        "buffer_load_dwordx4 v[72:75], v138, s[36:39], s65 offen offset:0 sc1\n"
        "v_mfma_f32_16x16x32_bf16 v[48:51], v[36:39], v[104:107], v[48:51]\n"
        "s_xor_b32 s68, s51, 9216\n"
        "ds_read_b128 v[28:31], v125 offset:7168\n"
        "s_waitcnt lgkmcnt(11)\n"
        "v_mfma_f32_16x16x32_bf16 v[52:55], v[44:47], v[104:107], v[52:55]\n"
        "buffer_load_dwordx4 v[76:79], v138, s[36:39], s65 offen offset:256 sc1\n"
        "s_waitcnt lgkmcnt(8)\n"
        "v_mfma_f32_16x16x32_bf16 v[84:87], v[96:99], a[80:83], v[84:87]\n"
        "s_sub_u32 s75, 127, s84\n"
        "ds_read_b128 v[104:107], v124 offset:13312\n"
        "v_mfma_f32_16x16x32_bf16 v[88:91], v[96:99], a[88:91], v[88:91]\n"
        "s_sub_u32 s101, s77, s75\n"
        "ds_read_b128 v[32:35], v125 offset:8192\n"
        "v_mfma_f32_16x16x32_bf16 v[56:59], a[224:227], v[96:99], v[56:59]\n"
        "s_mul_i32 s35, s75, 0x800080\n"
        "ds_read_b128 v[36:39], v125 offset:9216\n"
        "v_mfma_f32_16x16x32_bf16 v[60:63], a[232:235], v[96:99], v[60:63]\n"
        "s_lshl_b32 s80, s75, 23\n"
        "ds_read_b128 v[40:43], v125 offset:10240\n"
        "v_mfma_f32_16x16x32_bf16 v[48:51], v[192:195], v[96:99], v[48:51]\n"
        "s_sub_u32 s73, 0x3e0293ee, s80\n"
        "ds_read_b128 v[44:47], v125 offset:11264\n"
        "v_mfma_f32_16x16x32_bf16 v[52:55], v[200:203], v[96:99], v[52:55]\n"
        "s_sub_u32 s74, 0x3f800000, s80\n"
        "s_add_u32 s80, s75, 128\n"
        "s_waitcnt lgkmcnt(10)\n"
        "v_mfma_f32_16x16x32_bf16 v[84:87], v[100:103], a[84:87], v[84:87]\n"
        "s_lshl_b32 s80, s80, 8\n"
        "ds_read_b128 v[96:99], v124 offset:14336\n"
        "v_mfma_f32_16x16x32_bf16 v[88:91], v[100:103], a[92:95], v[88:91]\n"
        "s_add_u32 s44, s44, 1\n"
        "s_add_u32 s61, s61, 32\n"
        "s_add_u32 s62, s80, 1\n"
        "v_mfma_f32_16x16x32_bf16 v[56:59], a[228:231], v[100:103], v[56:59]\n"
        "s_branch L_rot_0%=\n"
        "L_light_1%=:\n"
        "v_mfma_f32_16x16x32_bf16 v[52:55], v[252:255], v[104:107], v[52:55]\n"
        "v_mfma_f32_16x16x32_bf16 v[84:87], v[96:99], a[112:115], v[84:87]\n"
        "v_mfma_f32_16x16x32_bf16 v[88:91], v[96:99], a[120:123], v[88:91]\n"
        "v_cvt_pk_bf16_f32 v48, v48, v52\n"
        "v_cvt_pk_bf16_f32 v49, v49, v53\n"
        "v_mfma_f32_16x16x32_bf16 v[84:87], v[100:103], a[116:119], v[84:87]\n"
        "v_cvt_pk_bf16_f32 v50, v50, v54\n"
        "v_cvt_pk_bf16_f32 v51, v51, v55\n"
        "v_mfma_f32_16x16x32_bf16 v[88:91], v[100:103], a[124:127], v[88:91]\n"
        "v_mfma_f32_16x16x32_bf16 v[84:87], v[92:95], a[128:131], v[84:87]\n"
        "buffer_store_dwordx4 v[48:51], v124, s[36:39], s54 offen offset:1024 sc1\n"
        "v_mfma_f32_16x16x32_bf16 v[88:91], v[92:95], a[136:139], v[88:91]\n"
        "v_mfma_f32_16x16x32_bf16 v[84:87], v[104:107], a[132:135], v[84:87]\n"
        "v_mfma_f32_16x16x32_bf16 v[88:91], v[104:107], a[140:143], v[88:91]\n"
        "v_mfma_f32_16x16x32_bf16 v[56:59], v[32:35], v[92:95], v[56:59]\n"
        "v_mfma_f32_16x16x32_bf16 v[60:63], v[40:43], v[92:95], v[60:63]\n"
        "v_cvt_pk_bf16_f32 v84, v84, v85\n"
        "v_cvt_pk_bf16_f32 v85, v86, v87\n"
        "v_mfma_f32_16x16x32_bf16 v[56:59], v[16:19], v[96:99], v[56:59]\n"
        "v_cvt_pk_bf16_f32 v86, v88, v89\n"
        "v_cvt_pk_bf16_f32 v87, v90, v91\n"
        "v_mfma_f32_16x16x32_bf16 v[60:63], v[24:27], v[96:99], v[60:63]\n"
        "v_mfma_f32_16x16x32_bf16 v[56:59], v[20:23], v[100:103], v[56:59]\n"
        "buffer_store_dwordx4 v[84:87], v124, s[36:39], s54 offen offset:2048 sc1\n"
        "v_mov_b64 v[92:93], 0\n"
        "v_mfma_f32_16x16x32_bf16 v[60:63], v[28:31], v[100:103], v[60:63]\n"
        "v_mov_b64 v[94:95], 0\n"
        "v_mov_b64 v[96:97], 0\n"
        "v_mfma_f32_16x16x32_bf16 v[56:59], v[36:39], v[104:107], v[56:59]\n"
        "v_mov_b64 v[98:99], 0\n"
        "v_mfma_f32_16x16x32_bf16 v[60:63], v[44:47], v[104:107], v[60:63]\n"
        "s_nop 7\n"
        "v_cvt_pk_bf16_f32 v100, v56, v60\n"
        "v_cvt_pk_bf16_f32 v101, v57, v61\n"
        "v_cvt_pk_bf16_f32 v102, v58, v62\n"
        "v_cvt_pk_bf16_f32 v103, v59, v63\n"
        "buffer_store_dwordx4 v[100:103], v124, s[36:39], s54 offen offset:0 sc1\n"
        "s_barrier\n"
        "s_add_u32 s69, s58, s82\n"
        "s_sub_u32 s72, s58, s82\n"
        "s_waitcnt vmcnt(7)\n"
        "buffer_load_dwordx4 v[64:67], v124, s[36:39], s69 offen offset:0 sc1\n"
        "s_waitcnt vmcnt(7)\n"
        "buffer_load_dwordx4 v[68:71], v124, s[36:39], s69 offen offset:1024 sc1\n"
        "s_waitcnt vmcnt(7)\n"
        "buffer_load_dwordx4 v[72:75], v124, s[36:39], s72 offen offset:2048 sc1\n"
        "s_waitcnt vmcnt(7)\n"
        "buffer_load_dwordx4 v[76:79], v124, s[36:39], s72 offen offset:3072 sc1\n"
        "s_mov_b32 s81, 64\n"
        "buffer_load_dword v107, off, s[36:39], s81 sc1\n"
        "s_memrealtime s[26:27]\n"
        "s_waitcnt vmcnt(5)\n"
        "s_waitcnt lgkmcnt(0)\n"
        "s_barrier\n"
        "s_add_u32 s29, s29, 1024\n"
        "s_waitcnt vmcnt(0)\n"
        "L_par_1%=:\n"
        "v_readfirstlane_b32 s80, v107\n"
        "s_nop 0\n"
        "s_cmp_lt_i32 s80, 0\n"
        "s_cbranch_scc1 L_par2_1%=\n"
        "s_add_u32 s63, s63, 1\n"
        "s_cmp_gt_u32 s63, 0x40000\n"
        "s_cbranch_scc1 L_abort%=\n"
        "s_sleep 4\n"
        "buffer_load_dword v107, off, s[36:39], s81 sc1\n"
        "s_waitcnt vmcnt(0)\n"
        "s_branch L_par_1%=\n"
        "L_par2_1%=:\n"
        "s_bfe_u32 s46, s80, 0x10018\n"
        "s_lshl_b32 s46, s46, 21\n"
        "s_mov_b32 s53, 0x200000\n"
        "s_bfe_u32 s84, s80, 0x80010\n"
        "s_sub_u32 s84, 255, s84\n"
        "s_bfe_u32 s76, s80, 0x80008\n"
        "s_sub_u32 s76, s76, 32\n"
        "s_max_i32 s76, s76, 66\n"
        "s_bfe_u32 s77, s80, 0x80000\n"
        "s_sub_u32 s77, s77, 158\n"
        "s_bitset0_b32 s49, 30\n"
        "s_bitset0_b32 s50, 30\n"
        "s_bitset0_b32 s56, 30\n"
        "s_bitset0_b32 s64, 30\n"
        "v_mov_b32 v140, 0\n"
        "s_mov_b32 s28, 1000\n"
        "s_branch L_xok_1%=\n"
        "L_x2_1%=:\n"
        "s_add_u32 s69, s58, s82\n"
        "s_sub_u32 s72, s58, s82\n"
        "buffer_load_dwordx4 v[64:67], v124, s[36:39], s69 offen offset:0 sc1\n"
        "buffer_load_dwordx4 v[68:71], v124, s[36:39], s69 offen offset:1024 sc1\n"
        "buffer_load_dwordx4 v[72:75], v124, s[36:39], s72 offen offset:2048 sc1\n"
        "buffer_load_dwordx4 v[76:79], v124, s[36:39], s72 offen offset:3072 sc1\n"
        "s_waitcnt vmcnt(0)\n"
        "s_memrealtime s[26:27]\n"
        "s_waitcnt lgkmcnt(0)\n"
        "s_add_u32 s28, s28, s26\n"
        "L_xok_1%=:\n"
        "v_mfma_f32_16x16x32_bf16 v[84:87], v[64:67], a[0:3], 0\n"
        "v_mfma_f32_16x16x32_bf16 v[88:91], v[64:67], a[8:11], 0\n"
        "v_mfma_f32_16x16x32_bf16 v[56:59], a[144:147], v[64:67], 0\n"
        "s_branch L_win_1%=\n"
        "L_retry_1%=:\n"
        "s_add_u32 s63, s63, 1\n"
        "s_cmp_gt_u32 s63, 0x40000\n"
        "s_cbranch_scc1 L_abort%=\n"
        "s_sleep 2\n"
        "s_waitcnt vmcnt(0)\n"
        "s_waitcnt lgkmcnt(0)\n"
        "buffer_load_dwordx4 v[80:83], v138, s[36:39], s50 offen offset:0 sc1\n"
        "buffer_load_dwordx4 v[64:67], v138, s[36:39], s49 offen offset:0 sc1\n"
        "buffer_load_dwordx4 v[68:71], v138, s[36:39], s49 offen offset:256 sc1\n"
        "buffer_load_dwordx4 v[72:75], v138, s[36:39], s65 offen offset:0 sc1\n"
        "buffer_load_dwordx4 v[76:79], v138, s[36:39], s65 offen offset:256 sc1\n"
        "s_waitcnt vmcnt(4)\n"
        "v_bitop3_b32 v123, 32, v80, v82 bitop3:0x80\n"
        "v_add_u32_dpp v80, v81, v80 row_half_mirror row_mask:0xf bank_mask:0xf\n"
        "v_add_u32_dpp v82, v83, v82 row_half_mirror row_mask:0xf bank_mask:0xf\n"
        "v_cvt_f32_i32 v80, v80\n"
        "v_cvt_f32_i32 v82, v82\n"
        "v_cvt_pk_bf16_f32 v52, v80, v82\n"
        "ds_write_b32 v137, v52\n"
        "s_waitcnt vmcnt(3)\n"
        "v_bitop3_b32 v123, v123, v64, v66 bitop3:0x80\n"
        "v_cvt_f32_i32 v64, v64\n"
        "v_cvt_f32_i32 v65, v65\n"
        "v_cvt_f32_i32 v66, v66\n"
        "v_cvt_f32_i32 v67, v67\n"
        "v_cvt_pk_bf16_f32 v52, v64, v65\n"
        "v_cvt_pk_bf16_f32 v53, v66, v67\n"
        "ds_write_b64 v126, v[52:53] offset:0\n"
        "s_waitcnt vmcnt(2)\n"
        "v_bitop3_b32 v123, v123, v68, v70 bitop3:0x80\n"
        "v_cvt_f32_i32 v68, v68\n"
        "v_cvt_f32_i32 v69, v69\n"
        "v_cvt_f32_i32 v70, v70\n"
        "v_cvt_f32_i32 v71, v71\n"
        "v_cvt_pk_bf16_f32 v54, v68, v69\n"
        "v_cvt_pk_bf16_f32 v55, v70, v71\n"
        "ds_write_b64 v126, v[54:55] offset:512\n"
        "s_waitcnt vmcnt(1)\n"
        "v_bitop3_b32 v123, v123, v72, v74 bitop3:0x80\n"
        "v_cvt_f32_i32 v72, v72\n"
        "v_cvt_f32_i32 v73, v73\n"
        "ds_read_b128 v[48:51], v124 offset:2048\n"
        "v_cvt_f32_i32 v74, v74\n"
        "v_cvt_f32_i32 v75, v75\n"
        "v_cvt_pk_bf16_f32 v52, v72, v73\n"
        "v_cvt_pk_bf16_f32 v53, v74, v75\n"
        "ds_write_b64 v126, v[52:53] offset:1024\n"
        "s_waitcnt vmcnt(0)\n"
        "v_bitop3_b32 v123, v123, v76, v78 bitop3:0x80\n"
        "v_cvt_f32_i32 v76, v76\n"
        "v_cvt_f32_i32 v77, v77\n"
        "v_cvt_f32_i32 v78, v78\n"
        "v_cvt_f32_i32 v79, v79\n"
        "v_cvt_pk_bf16_f32 v54, v76, v77\n"
        "v_cvt_pk_bf16_f32 v55, v78, v79\n"
        "ds_write_b64 v126, v[54:55] offset:1536\n"
        "v_cmp_eq_u32 vcc, 0, v123\n"
        "v_mov_b64 v[122:123], 0\n"
        "v_mov_b64 v[92:93], 0\n"
        "v_mov_b64 v[94:95], 0\n"
        "v_mov_b64 v[96:97], 0\n"
        "v_mov_b64 v[98:99], 0\n"
        "s_waitcnt lgkmcnt(0)\n"
        "s_branch L_dec_1%=\n"
        "L_sw0_1%=:\n"
        "s_memrealtime s[26:27]\n"
        "s_waitcnt lgkmcnt(0)\n"
        "s_sub_u32 s28, s28, s26\n"
        "L_sw_1%=:\n"
        "s_add_u32 s63, s63, 1\n"
        "s_cmp_gt_u32 s63, 0x40000\n"
        "s_cbranch_scc1 L_abort%=\n"
        "s_sleep 2\n"
        "buffer_load_dword v140, v124, s[36:39], s29 offen sc1\n"
        "s_waitcnt vmcnt(0)\n"
        "v_cmp_eq_u32 vcc, 0, v140\n"
        "s_cbranch_vccnz L_sw_1%=\n"
        "s_memrealtime s[26:27]\n"
        "s_waitcnt lgkmcnt(0)\n"
        "s_add_u32 s28, s28, s26\n"
        "s_branch L_sok_1%=\n"
        ".p2align 6\n"
        "L_rot_1%=:\n"
        "v_mfma_f32_16x16x32_bf16 v[60:63], a[236:239], v[100:103], v[60:63]\n"
        "v_mov_b64 v[118:119], 0\n"
        "v_mov_b64 v[120:121], 0\n"
        "v_mfma_f32_16x16x32_bf16 v[48:51], v[196:199], v[100:103], v[48:51]\n"
        "v_and_b32 v122, 0x70, v124\n"
        "v_lshrrev_b32 v123, 7, v124\n"
        "v_mfma_f32_16x16x32_bf16 v[52:55], v[204:207], v[100:103], v[52:55]\n"
        "v_lshl_or_b32 v122, v123, 8, v122\n"
        "s_waitcnt lgkmcnt(9)\n"
        "v_mfma_f32_16x16x32_bf16 v[84:87], v[92:95], a[96:99], v[84:87]\n"
        "ds_read_b128 v[100:103], v124 offset:11264\n"
        "s_cmp_gt_i32 s44, 2\n"
        "s_cselect_b32 s48, s47, 0x40000000\n"
        "v_mfma_f32_16x16x32_bf16 v[88:91], v[92:95], a[104:107], v[88:91]\n"
        "s_bfm_b64 exec, 40, 0\n"
        "buffer_store_dwordx4 v[118:121], v122, s[36:39], s48 offen sc1\n"
        "s_mov_b64 exec, -1\n"
        "v_mfma_f32_16x16x32_bf16 v[56:59], v[0:3], v[92:95], v[56:59]\n"
        "s_add_u32 s47, s47, 163840\n"
        "v_mfma_f32_16x16x32_bf16 v[60:63], v[8:11], v[92:95], v[60:63]\n"
        "v_mfma_f32_16x16x32_bf16 v[48:51], v[208:211], v[92:95], v[48:51]\n"
        "s_waitcnt lgkmcnt(0)\n"
        "s_barrier\n"
        "v_mfma_f32_16x16x32_bf16 v[52:55], v[216:219], v[92:95], v[52:55]\n"
        "v_mov_b64 v[116:117], 0\n"
        "v_mfma_f32_16x16x32_bf16 v[84:87], v[104:107], a[100:103], v[84:87]\n"
        "ds_read_b128 v[92:95], v124 offset:16384\n"
        "v_mfma_f32_16x16x32_bf16 v[88:91], v[104:107], a[108:111], v[88:91]\n"
        "v_mfma_f32_16x16x32_bf16 v[56:59], v[4:7], v[104:107], v[56:59]\n"
        "v_mfma_f32_16x16x32_bf16 v[60:63], v[12:15], v[104:107], v[60:63]\n"
        "v_mfma_f32_16x16x32_bf16 v[48:51], v[212:215], v[104:107], v[48:51]\n"
        "v_mfma_f32_16x16x32_bf16 v[52:55], v[220:223], v[104:107], v[52:55]\n"
        "v_mfma_f32_16x16x32_bf16 v[48:51], v[224:227], v[96:99], v[48:51]\n"
        "ds_read_b128 v[104:107], v124 offset:17408\n"
        "v_mfma_f32_16x16x32_bf16 v[52:55], v[232:235], v[96:99], v[52:55]\n"
        "v_mfma_f32_16x16x32_bf16 v[48:51], v[228:231], v[100:103], v[48:51]\n"
        "v_mfma_f32_16x16x32_bf16 v[52:55], v[236:239], v[100:103], v[52:55]\n"
        "s_waitcnt lgkmcnt(1)\n"
        "v_mfma_f32_16x16x32_bf16 v[48:51], v[240:243], v[92:95], v[48:51]\n"
        "v_mfma_f32_16x16x32_bf16 v[52:55], v[248:251], v[92:95], v[52:55]\n"
        "s_waitcnt lgkmcnt(0)\n"
        "v_mfma_f32_16x16x32_bf16 v[48:51], v[244:247], v[104:107], v[48:51]\n"
        "s_cmp_lt_i32 s44, 0\n"
        "s_cbranch_scc1 L_light_1%=\n"
        "v_mfma_f32_16x16x32_bf16 v[52:55], v[252:255], v[104:107], v[52:55]\n"
        "s_waitcnt vmcnt(5)\n"
        "v_bitop3_b32 v123, 32, v80, v82 bitop3:0x80\n"
        "s_nop 0\n"
        "v_add_u32_dpp v80, v81, v80 row_half_mirror row_mask:0xf bank_mask:0xf\n"
        "v_mfma_f32_16x16x32_bf16 v[84:87], v[96:99], a[112:115], v[84:87]\n"
        "s_nop 0\n"
        "v_add_u32_dpp v82, v83, v82 row_half_mirror row_mask:0xf bank_mask:0xf\n"
        "v_mfma_f32_16x16x32_bf16 v[88:91], v[96:99], a[120:123], v[88:91]\n"
        "v_cvt_pk_bf16_f32 v48, v48, v52\n"
        "v_cvt_pk_bf16_f32 v49, v49, v53\n"
        "s_nop 0\n"
        "v_mfma_f32_16x16x32_bf16 v[84:87], v[100:103], a[116:119], v[84:87]\n"
        "v_cvt_pk_bf16_f32 v50, v50, v54\n"
        "v_cvt_pk_bf16_f32 v51, v51, v55\n"
        "v_mfma_f32_16x16x32_bf16 v[88:91], v[100:103], a[124:127], v[88:91]\n"
        "v_cvt_f32_i32 v80, v80\n"
        "s_nop 0\n"
        "v_cvt_f32_i32 v82, v82\n"
        "v_mfma_f32_16x16x32_bf16 v[84:87], v[92:95], a[128:131], v[84:87]\n"
        "buffer_store_dwordx4 v[48:51], v124, s[36:39], s54 offen offset:1024 sc1\n"
        "v_mfma_f32_16x16x32_bf16 v[88:91], v[92:95], a[136:139], v[88:91]\n"
        "s_nop 0\n"
        "v_cvt_pk_bf16_f32 v52, v80, v82\n"
        "v_mfma_f32_16x16x32_bf16 v[84:87], v[104:107], a[132:135], v[84:87]\n"
        "s_nop 0\n"
        "v_mfma_f32_16x16x32_bf16 v[88:91], v[104:107], a[140:143], v[88:91]\n"
        "ds_write_b32 v137, v52\n"
        "s_waitcnt vmcnt(5)\n"
        "v_bitop3_b32 v123, v123, v64, v66 bitop3:0x80\n"
        "v_cvt_f32_i32 v64, v64\n"
        "v_mfma_f32_16x16x32_bf16 v[56:59], v[32:35], v[92:95], v[56:59]\n"
        "v_cvt_f32_i32 v65, v65\n"
        "v_cvt_f32_i32 v66, v66\n"
        "v_mfma_f32_16x16x32_bf16 v[60:63], v[40:43], v[92:95], v[60:63]\n"
        "v_cvt_pk_bf16_f32 v84, v84, v85\n"
        "v_cvt_pk_bf16_f32 v85, v86, v87\n"
        "v_mfma_f32_16x16x32_bf16 v[56:59], v[16:19], v[96:99], v[56:59]\n"
        "v_cvt_pk_bf16_f32 v86, v88, v89\n"
        "v_cvt_pk_bf16_f32 v87, v90, v91\n"
        "v_mfma_f32_16x16x32_bf16 v[60:63], v[24:27], v[96:99], v[60:63]\n"
        "v_cvt_f32_i32 v67, v67\n"
        "v_cvt_pk_bf16_f32 v52, v64, v65\n"
        "v_mfma_f32_16x16x32_bf16 v[56:59], v[20:23], v[100:103], v[56:59]\n"
        "buffer_store_dwordx4 v[84:87], v124, s[36:39], s54 offen offset:2048 sc1\n"
        "v_mov_b64 v[92:93], 0\n"
        "v_cvt_pk_bf16_f32 v53, v66, v67\n"
        "v_mfma_f32_16x16x32_bf16 v[60:63], v[28:31], v[100:103], v[60:63]\n"
        "v_mov_b64 v[94:95], 0\n"
        "v_mov_b64 v[96:97], 0\n"
        "ds_write_b64 v126, v[52:53] offset:0\n"
        "v_mfma_f32_16x16x32_bf16 v[56:59], v[36:39], v[104:107], v[56:59]\n"
        "v_mov_b64 v[98:99], 0\n"
        "s_waitcnt vmcnt(5)\n"
        "v_bitop3_b32 v123, v123, v68, v70 bitop3:0x80\n"
        "v_mfma_f32_16x16x32_bf16 v[60:63], v[44:47], v[104:107], v[60:63]\n"
        "v_cvt_f32_i32 v68, v68\n"
        "v_cvt_f32_i32 v69, v69\n"
        "v_cvt_f32_i32 v70, v70\n"
        "v_cvt_f32_i32 v71, v71\n"
        "v_cvt_pk_bf16_f32 v54, v68, v69\n"
        "v_cvt_pk_bf16_f32 v55, v70, v71\n"
        "ds_write_b64 v126, v[54:55] offset:512\n"
        "s_waitcnt vmcnt(4)\n"
        "v_bitop3_b32 v123, v123, v72, v74 bitop3:0x80\n"
        "v_cvt_f32_i32 v72, v72\n"
        "v_cvt_pk_bf16_f32 v100, v56, v60\n"
        "v_cvt_pk_bf16_f32 v101, v57, v61\n"
        "v_cvt_pk_bf16_f32 v102, v58, v62\n"
        "v_cvt_pk_bf16_f32 v103, v59, v63\n"
        "v_cvt_f32_i32 v73, v73\n"
        "ds_read_b128 v[48:51], v124 offset:2048\n"
        "v_cvt_f32_i32 v74, v74\n"
        "v_cvt_f32_i32 v75, v75\n"
        "v_cvt_pk_bf16_f32 v52, v72, v73\n"
        "v_cvt_pk_bf16_f32 v53, v74, v75\n"
        "ds_write_b64 v126, v[52:53] offset:1024\n"
        "s_waitcnt vmcnt(3)\n"
        "v_bitop3_b32 v123, v123, v76, v78 bitop3:0x80\n"
        "v_cvt_f32_i32 v76, v76\n"
        "v_cvt_f32_i32 v77, v77\n"
        "v_cvt_f32_i32 v78, v78\n"
        "v_cvt_f32_i32 v79, v79\n"
        "v_cvt_pk_bf16_f32 v54, v76, v77\n"
        "v_cvt_pk_bf16_f32 v55, v78, v79\n"
        "ds_write_b64 v126, v[54:55] offset:1536\n"
        "v_cmp_eq_u32 vcc, 0, v123\n"
        "v_mov_b64 v[122:123], 0\n"
        "L_dec_1%=:\n"
        "s_waitcnt lgkmcnt(2)\n"
        "v_smfmac_f32_16x16x64_bf16 v[92:95], v[48:51], a[144:151], v134\n"
        "ds_read_b128 v[52:55], v124 offset:3072\n"
        "v_max3_f32 v104, |v80|, |v82|, |v64|\n"
        "v_smfmac_f32_16x16x64_bf16 v[96:99], v[48:51], a[152:159], v134\n"
        "s_cbranch_vccnz L_retry_1%=\n"
        "v_max3_f32 v104, |v65|, |v66|, v104\n"
        "v_smfmac_f32_16x16x64_bf16 v[116:119], v[48:51], a[240:247], v134\n"
        "s_add_u32 s29, s29, 1024\n"
        "v_max3_f32 v104, |v67|, |v68|, v104\n"
        "v_smfmac_f32_16x16x64_bf16 v[120:123], v[48:51], a[248:255], v134\n"
        "s_waitcnt lgkmcnt(0)\n"
        "v_max3_f32 v104, |v69|, |v70|, v104\n"
        "s_barrier\n"
        "ds_read_b128 v[56:59], v124 offset:4096\n"
        "v_smfmac_f32_16x16x64_bf16 v[92:95], v[52:55], a[160:167], v134\n"
        "buffer_load_dwordx4 v[88:91], v124, s[36:39], s55 offen offset:3072 sc1\n"
        "ds_read_b128 v[60:63], v124 offset:7168\n"
        "v_max3_f32 v104, |v71|, |v72|, v104\n"
        "v_smfmac_f32_16x16x64_bf16 v[96:99], v[52:55], a[168:175], v134\n"
        "ds_read_b128 v[80:83], v124 offset:5120\n"
        "v_max3_f32 v104, |v73|, |v74|, v104\n"
        "v_smfmac_f32_16x16x64_bf16 v[116:119], v[52:55], v[176:183], v134\n"
        "buffer_load_dwordx4 v[84:87], v124, s[36:39], s55 offen offset:0 sc1\n"
        "ds_read_b128 v[64:67], v124 offset:8192\n"
        "v_max3_f32 v104, |v75|, |v76|, v104\n"
        "v_smfmac_f32_16x16x64_bf16 v[120:123], v[52:55], v[184:191], v134\n"
        "ds_read_b128 v[68:71], v124 offset:0\n"
        "v_max3_f32 v104, |v77|, |v78|, v104\n"
        "s_waitcnt lgkmcnt(4)\n"
        "v_smfmac_f32_16x16x64_bf16 v[92:95], v[56:59], v[0:7], v134\n"
        "ds_read_b128 v[72:75], v124 offset:1024\n"
        "v_max_f32_e64 v104, |v79|, v104\n"
        "v_smfmac_f32_16x16x64_bf16 v[96:99], v[56:59], v[8:15], v134\n"
        "ds_read_b128 v[0:3], v125 offset:12288\n"
        "v_max_f32_dpp v104, v104, v104 row_shr:1 row_mask:0xf bank_mask:0xf\n"
        "s_waitcnt lgkmcnt(3)\n"
        "v_smfmac_f32_16x16x64_bf16 v[92:95], v[60:63], a[224:231], v134\n"
        "ds_read_b128 v[76:79], v124 offset:6144\n"
        "v_max_f32_dpp v104, v104, v104 row_shr:2 row_mask:0xf bank_mask:0xf\n"
        "v_smfmac_f32_16x16x64_bf16 v[96:99], v[60:63], a[232:239], v134\n"
        "ds_read_b128 v[4:7], v125 offset:13312\n"
        "v_max_f32_dpp v104, v104, v104 row_shr:4 row_mask:0xf bank_mask:0xf\n"
        "v_smfmac_f32_16x16x64_bf16 v[92:95], v[80:83], v[16:23], v134\n"
        "ds_read_b128 v[8:11], v125 offset:14336\n"
        "v_max_f32_dpp v104, v104, v104 row_shr:8 row_mask:0xf bank_mask:0xf\n"
        "v_smfmac_f32_16x16x64_bf16 v[96:99], v[80:83], v[24:31], v134\n"
        "ds_read_b128 v[12:15], v125 offset:15360\n"
        "v_max_f32_dpp v104, v104, v104 row_bcast:15 row_mask:0xa bank_mask:0xf\n"
        "v_smfmac_f32_16x16x64_bf16 v[92:95], v[64:67], v[32:39], v134\n"
        "ds_read_b128 v[16:19], v125 offset:16384\n"
        "v_max_f32_dpp v104, v104, v104 row_bcast:31 row_mask:0xc bank_mask:0xf\n"
        "v_smfmac_f32_16x16x64_bf16 v[96:99], v[64:67], v[40:47], v134\n"
        "ds_read_b128 v[20:23], v125 offset:17408\n"
        "v_readlane_b32 s85, v104, 63\n"
        "s_waitcnt lgkmcnt(5)\n"
        "v_smfmac_f32_16x16x64_bf16 v[92:95], v[68:71], a[176:183], v134\n"
        "ds_read_b128 v[24:27], v125 offset:18432\n"
        "v_smfmac_f32_16x16x64_bf16 v[96:99], v[68:71], a[184:191], v134\n"
        "ds_read_b128 v[28:31], v125 offset:19456\n"
        "v_mov_b32 v104, s85\n"
        "v_smfmac_f32_16x16x64_bf16 v[92:95], v[72:75], a[192:199], v134\n"
        "ds_read_b128 v[32:35], v125 offset:20480\n"
        "v_smfmac_f32_16x16x64_bf16 v[96:99], v[72:75], a[200:207], v134\n"
        "ds_read_b128 v[36:39], v125 offset:21504\n"
        "s_add_u32 s69, s58, s82\n"
        "v_smfmac_f32_16x16x64_bf16 v[92:95], v[76:79], a[208:215], v134\n"
        "ds_read_b128 v[40:43], v125 offset:22528\n"
        "s_sub_u32 s72, s58, s82\n"
        "v_smfmac_f32_16x16x64_bf16 v[96:99], v[76:79], a[216:223], v134\n"
        "ds_read_b128 v[44:47], v125 offset:23552\n"
        "v_smfmac_f32_16x16x64_bf16 v[116:119], v[64:67], v[240:247], v134\n"
        "ds_write_b32 v142, v104\n"
        "v_add_f32 v92, v92, v94\n"
        "v_add_f32 v93, v93, v95\n"
        "v_smfmac_f32_16x16x64_bf16 v[120:123], v[64:67], v[248:255], v134\n"
        "v_add_f32 v96, v96, v98\n"
        "v_add_f32 v97, v97, v99\n"
        "v_cvt_pk_bf16_f32 v92, v92, v96\n"
        "v_cvt_pk_bf16_f32 v93, v93, v97\n"
        "s_waitcnt lgkmcnt(3)\n"
        "v_smfmac_f32_16x16x64_bf16 v[116:119], v[68:71], v[0:7], v134\n"
        "ds_write2_b32 v127, v92, v93 offset1:72\n"
        "v_smfmac_f32_16x16x64_bf16 v[120:123], v[68:71], v[8:15], v134\n"
        "v_mov_b64 v[98:99], 0\n"
        "v_smfmac_f32_16x16x64_bf16 v[116:119], v[72:75], v[16:23], v134\n"
        "buffer_store_dwordx4 v[100:103], v124, s[36:39], s54 offen offset:0 sc1\n"
        "v_mov_b64 v[96:97], 0\n"
        "v_smfmac_f32_16x16x64_bf16 v[120:123], v[72:75], v[24:31], v134\n"
        "s_waitcnt vmcnt(4)\n"
        "s_waitcnt lgkmcnt(0)\n"
        "v_mov_b64 v[106:107], 0\n"
        "s_barrier\n"
        "ds_read_b128 v[0:3], v128 offset:0\n"
        "v_smfmac_f32_16x16x64_bf16 v[116:119], v[76:79], v[32:39], v134\n"
        "ds_read_b128 v[24:27], v124 offset:20480\n"
        "v_mov_b64 v[104:105], 0\n"
        "v_smfmac_f32_16x16x64_bf16 v[120:123], v[76:79], v[40:47], v134\n"
        "ds_read_b128 v[4:7], v128 offset:64\n"
        "v_mov_b64 v[94:95], 0\n"
        "v_smfmac_f32_16x16x64_bf16 v[116:119], v[60:63], v[192:199], v134\n"
        "ds_read_b128 v[8:11], v128 offset:128\n"
        "v_mov_b64 v[92:93], 0\n"
        "v_smfmac_f32_16x16x64_bf16 v[120:123], v[60:63], v[200:207], v134\n"
        "ds_read_b128 v[12:15], v128 offset:192\n"
        "v_mov_b64 v[20:21], 0\n"
        "v_smfmac_f32_16x16x64_bf16 v[116:119], v[56:59], v[208:215], v134\n"
        "s_waitcnt lgkmcnt(3)\n"
        "v_max3_f32 v24, v24, v25, v26\n"
        "s_waitcnt lgkmcnt(2)\n"
        "v_smfmac_f32_16x16x64_bf16 v[104:107], v[108:111], v[0:7], v134\n"
        "v_max_f32 v24, v24, v27\n"
        "v_smfmac_f32_16x16x64_bf16 v[120:123], v[56:59], v[216:223], v134\n"
        "v_readfirstlane_b32 s84, v24\n"
        "s_waitcnt lgkmcnt(0)\n"
        "v_smfmac_f32_16x16x64_bf16 v[104:107], v[112:115], v[8:15], v134\n"
        "v_mov_b64 v[22:23], 0\n"
        "v_smfmac_f32_16x16x64_bf16 v[116:119], v[80:83], v[224:231], v134\n"
        "s_lshr_b32 s84, s84, 23\n"
        "s_add_u32 s84, s84, s101\n"
        "v_smfmac_f32_16x16x64_bf16 v[120:123], v[80:83], v[232:239], v134\n"
        "s_max_i32 s84, s84, s76\n"
        "s_min_i32 s84, s84, 186\n"
        "v_add_f32 v104, v104, v106\n"
        "v_add_f32 v105, v105, v107\n"
        "v_smfmac_f32_16x16x64_bf16 v[92:95], v[64:67], a[128:135], v134\n"
        "ds_write_b64 v131, v[104:105]\n"
        "v_add_f32 v116, v116, v118\n"
        "v_add_f32 v117, v117, v119\n"
        "v_pk_add_f32 v[120:121], v[120:121], v[122:123]\n"
        "v_cvt_pk_bf16_f32 v116, v116, v120\n"
        "v_cvt_pk_bf16_f32 v117, v117, v121\n"
        "ds_write2_b32 v129, v116, v117 offset1:8\n"
        "v_smfmac_f32_16x16x64_bf16 v[96:99], v[64:67], a[136:143], v134\n"
        "s_mul_i32 s88, s84, 0x800080\n"
        "s_mov_b32 s89, s88\n"
        "v_smfmac_f32_16x16x64_bf16 v[92:95], v[68:71], a[32:39], v134\n"
        "s_add_u32 s90, s88, s35\n"
        "s_mov_b32 s91, s90\n"
        "v_mov_b64 v[104:105], s[88:89]\n"
        "v_smfmac_f32_16x16x64_bf16 v[96:99], v[68:71], a[40:47], v134\n"
        "buffer_load_dwordx4 v[64:67], v124, s[36:39], s69 offen offset:0 sc1\n"
        "v_mov_b64 v[106:107], s[90:91]\n"
        "v_smfmac_f32_16x16x64_bf16 v[92:95], v[48:51], a[0:7], v134\n"
        "s_waitcnt lgkmcnt(0)\n"
        "v_mov_b64 v[100:101], s[88:89]\n"
        "s_barrier\n"
        "ds_read_b128 v[24:27], v130\n"
        "v_smfmac_f32_16x16x64_bf16 v[96:99], v[48:51], a[8:15], v134\n"
        "ds_read_b128 v[28:31], v130 offset:1024\n"
        "v_mov_b64 v[102:103], s[90:91]\n"
        "v_smfmac_f32_16x16x64_bf16 v[92:95], v[52:55], a[16:23], v134\n"
        "ds_read_b128 v[116:119], v124 offset:13568\n"
        "v_mov_b64 v[48:49], 0\n"
        "v_smfmac_f32_16x16x64_bf16 v[96:99], v[52:55], a[24:31], v134\n"
        "ds_read_b128 v[32:35], v141 offset:0\n"
        "v_mov_b64 v[50:51], 0\n"
        "v_smfmac_f32_16x16x64_bf16 v[92:95], v[56:59], a[96:103], v134\n"
        "ds_read_b128 v[36:39], v141 offset:1024\n"
        "v_mov_b64 v[52:53], 0\n"
        "v_smfmac_f32_16x16x64_bf16 v[96:99], v[56:59], a[104:111], v134\n"
        "ds_read_b128 v[40:43], v141 offset:2048\n"
        "v_mov_b64 v[54:55], 0\n"
        "s_waitcnt lgkmcnt(5)\n"
        "v_smfmac_f32_16x16x64_bf16 v[20:23], v[24:27], v[0:7], v134\n"
        "ds_read_b128 v[44:47], v141 offset:3072\n"
        "v_mov_b64 v[56:57], 0\n"
        "s_waitcnt lgkmcnt(4)\n"
        "v_smfmac_f32_16x16x64_bf16 v[20:23], v[28:31], v[8:15], v134\n"
        "ds_read_b128 v[0:3], v125 offset:24576\n"
        "v_max3_f32 v121, v116, v117, v118\n"
        "v_smfmac_f32_16x16x64_bf16 v[92:95], v[72:75], a[48:55], v134\n"
        "buffer_load_dwordx4 v[68:71], v124, s[36:39], s69 offen offset:1024 sc1\n"
        "v_mov_b64 v[58:59], 0\n"
        "v_smfmac_f32_16x16x64_bf16 v[96:99], v[72:75], a[56:63], v134\n"
        "ds_read_b128 v[4:7], v125 offset:25600\n"
        "v_add_f32 v20, v20, v22\n"
        "v_add_f32 v21, v21, v23\n"
        "v_cndmask_b32 v120, v20, v21, s[92:93]\n"
        "v_mul_f32 v120, s74, v120\n"
        "v_max3_f32 v121, v121, v119, v120\n"
        "v_smfmac_f32_16x16x64_bf16 v[92:95], v[76:79], a[64:71], v134\n"
        "buffer_load_dwordx4 v[72:75], v124, s[36:39], s72 offen offset:2048 sc1\n"
        "v_max_f32_dpp v121, v121, v121 quad_perm:[2,3,0,1] row_mask:0xf bank_mask:0xf\n"
        "v_mov_b32 v122, v121\n"
        "v_smfmac_f32_16x16x64_bf16 v[96:99], v[76:79], a[72:79], v134\n"
        "ds_read_b128 v[8:11], v125 offset:26624\n"
        "v_permlane16_swap_b32 v121, v122\n"
        "v_max_f32 v121, v121, v122\n"
        "v_mov_b32 v122, v121\n"
        "v_smfmac_f32_16x16x64_bf16 v[92:95], v[80:83], a[112:119], v134\n"
        "buffer_load_dwordx4 v[76:79], v124, s[36:39], s72 offen offset:3072 sc1\n"
        "v_permlane32_swap_b32 v121, v122\n"
        "v_smfmac_f32_16x16x64_bf16 v[96:99], v[80:83], a[120:127], v134\n"
        "ds_read_b128 v[12:15], v125 offset:27648\n"
        "v_max_f32 v121, v121, v122\n"
        "v_mul_f32_e64 v123, -s73, v121\n"
        "v_fma_f32 v120, v120, s73, v123\n"
        "v_fma_f32 v116, v116, s73, v123\n"
        "v_smfmac_f32_16x16x64_bf16 v[92:95], v[60:63], a[80:87], v134\n"
        "v_fma_f32 v117, v117, s73, v123\n"
        "v_fma_f32 v118, v118, s73, v123\n"
        "v_fma_f32 v119, v119, s73, v123\n"
        "v_exp_f32 v120, v120\n"
        "v_smfmac_f32_16x16x64_bf16 v[96:99], v[60:63], a[88:95], v134\n"
        "ds_read_b128 v[16:19], v125 offset:28672\n"
        "v_exp_f32 v116, v116\n"
        "v_mul_f32 v120, s74, v120\n"
        "v_exp_f32 v117, v117\n"
        "v_exp_f32 v118, v118\n"
        "v_exp_f32 v119, v119\n"
        "s_waitcnt vmcnt(5)\n"
        "v_mov_b64 v[108:109], v[86:87]\n"
        "v_mov_b64 v[112:113], v[90:91]\n"
        "v_cvt_pk_bf16_f32 v120, v120, v120\n"
        "v_cvt_pk_bf16_f32 v60, v116, v117\n"
        "v_cvt_pk_bf16_f32 v62, v118, v119\n"
        "v_and_b32 v61, v120, v135\n"
        "v_and_b32 v63, v120, v136\n"
        "v_pk_add_f32 v[92:93], v[92:93], v[94:95]\n"
        "v_pk_add_f32 v[96:97], v[96:97], v[98:99]\n"
        "v_smfmac_f32_16x16x64_bf16 v[56:59], v[60:63], v[100:107], v134\n"
        "ds_read_b128 v[20:23], v125 offset:29696\n"
        "v_cvt_pk_bf16_f32 v86, v92, v93\n"
        "v_cvt_pk_bf16_f32 v87, v92, v93\n"
        "v_mov_b64 v[90:91], v[86:87]\n"
        "v_cvt_pk_bf16_f32 v110, v96, v97\n"
        "v_cvt_pk_bf16_f32 v111, v96, v97\n"
        "v_smfmac_f32_16x16x64_bf16 v[48:51], v[60:63], v[84:91], v134\n"
        "ds_read_b128 v[24:27], v125 offset:30720\n"
        "v_mov_b64 v[114:115], v[110:111]\n"
        "v_add_f32 v56, v56, v58\n"
        "v_add_f32 v57, v57, v59\n"
        "v_smfmac_f32_16x16x64_bf16 v[52:55], v[60:63], v[108:115], v134\n"
        "ds_read_b128 v[28:31], v125 offset:31744\n"
        "s_nop 0\n"
        "v_rcp_f32 v56, v56\n"
        "v_rcp_f32 v57, v57\n"
        "v_pk_add_f32 v[48:49], v[48:49], v[50:51]\n"
        "v_pk_mul_f32 v[48:49], v[48:49], v[56:57]\n"
        "v_mov_b32 v110, v86\n"
        "v_mov_b64 v[92:93], s[70:71]\n"
        "v_pk_add_f32 v[52:53], v[52:53], v[54:55]\n"
        "v_pk_mul_f32 v[52:53], v[52:53], v[56:57]\n"
        "v_cvt_pk_bf16_f32 v48, v48, v52\n"
        "v_cvt_pk_bf16_f32 v49, v49, v53\n"
        "ds_write2_b32 v139, v48, v49 offset1:4\n"
        "v_mov_b64 v[94:95], 0\n"
        "v_mov_b64 v[96:97], 0\n"
        "v_mov_b64 v[98:99], 0\n"
        "buffer_store_dwordx2 v[110:111], v126, s[36:39], s60 offen sc1\n"
        "v_mov_b64 v[100:101], s[70:71]\n"
        "v_mov_b64 v[102:103], 0\n"
        "v_mov_b64 v[104:105], 0\n"
        "s_waitcnt lgkmcnt(0)\n"
        "v_mov_b64 v[106:107], 0\n"
        "s_barrier\n"
        "ds_read_b128 v[108:111], v124 offset:18432\n"
        "v_mov_b64 v[118:119], 0\n"
        "ds_read_b128 v[112:115], v124 offset:19456\n"
        "v_mov_b64 v[118:119], 0\n"
        "s_nop 0\n"
        "v_mov_b64 v[116:117], s[78:79]\n"
        "s_nop 0\n"
        "v_mov_b64 v[118:119], 0\n"
        "s_nop 0\n"
        "v_mov_b64 v[116:117], s[78:79]\n"
        "s_nop 0\n"
        "s_waitcnt lgkmcnt(1)\n"
        "v_smfmac_f32_16x16x64_bf16 v[116:119], v[108:111], v[32:39], v134\n"
        "s_waitcnt lgkmcnt(0)\n"
        "v_smfmac_f32_16x16x64_bf16 v[116:119], v[112:115], v[40:47], v134\n"
        "v_smfmac_f32_16x16x64_bf16 v[92:95], v[108:111], v[144:151], v134\n"
        "v_smfmac_f32_16x16x64_bf16 v[96:99], v[108:111], v[152:159], v134\n"
        "v_add_f32 v116, v116, v118\n"
        "v_add_f32 v117, v117, v119\n"
        "v_smfmac_f32_16x16x64_bf16 v[92:95], v[112:115], v[160:167], v134\n"
        "buffer_load_dword v140, v124, s[36:39], s29 offen sc1\n"
        "v_cvt_rpi_i32_f32 v118, v116\n"
        "v_bfi_b32 v118, 63, 2, v118\n"
        "v_cvt_rpi_i32_f32 v119, v117\n"
        "v_smfmac_f32_16x16x64_bf16 v[96:99], v[112:115], v[168:175], v134\n"
        "buffer_atomic_add_x2 v[118:119], v133, s[36:39], s52 offen\n"
        "v_smfmac_f32_16x16x64_bf16 v[100:103], v[108:111], v[0:7], v134\n"
        "v_add_f32 v92, v92, v94\n"
        "v_add_f32 v93, v93, v95\n"
        "v_cvt_rpi_i32_f32 v94, v92\n"
        "v_smfmac_f32_16x16x64_bf16 v[104:107], v[108:111], v[8:15], v134\n"
        "v_bfi_b32 v94, 63, 1, v94\n"
        "v_add_f32 v96, v96, v98\n"
        "v_smfmac_f32_16x16x64_bf16 v[100:103], v[112:115], v[16:23], v134\n"
        "v_cvt_rpi_i32_f32 v95, v96\n"
        "v_smfmac_f32_16x16x64_bf16 v[104:107], v[112:115], v[24:31], v134\n"
        "buffer_atomic_add_x2 v[94:95], v133, s[36:39], s51 offen\n"
        "v_add_f32 v97, v97, v99\n"
        "v_cvt_rpi_i32_f32 v98, v93\n"
        "v_bfi_b32 v98, 63, 1, v98\n"
        "v_cvt_rpi_i32_f32 v99, v97\n"
        "s_waitcnt vmcnt(7)\n"
        "v_mfma_f32_16x16x32_bf16 v[84:87], v[64:67], a[0:3], 0\n"
        "buffer_atomic_add_x2 v[98:99], v133, s[36:39], s66 offen\n"
        "v_add_f32 v100, v100, v102\n"
        "v_add_f32 v101, v101, v103\n"
        "v_add_f32 v104, v104, v106\n"
        "v_cvt_rpi_i32_f32 v102, v100\n"
        "v_bfi_b32 v102, 63, 1, v102\n"
        "v_cvt_rpi_i32_f32 v103, v104\n"
        "v_mfma_f32_16x16x32_bf16 v[88:91], v[64:67], a[8:11], 0\n"
        "buffer_atomic_add_x2 v[102:103], v133, s[36:39], s67 offen\n"
        "v_add_f32 v105, v105, v107\n"
        "v_cvt_rpi_i32_f32 v106, v101\n"
        "v_bfi_b32 v106, 63, 1, v106\n"
        "v_cvt_rpi_i32_f32 v107, v105\n"
        "v_mfma_f32_16x16x32_bf16 v[56:59], a[144:147], v[64:67], 0\n"
        "buffer_atomic_add_x2 v[106:107], v133, s[36:39], s68 offen\n"
        "L_win_1%=:\n"
        "v_mfma_f32_16x16x32_bf16 v[60:63], a[152:155], v[64:67], 0\n"
        "v_mfma_f32_16x16x32_bf16 v[48:51], a[240:243], v[64:67], 0\n"
        "v_mfma_f32_16x16x32_bf16 v[52:55], a[248:251], v[64:67], 0\n"
        "ds_write_b128 v124, v[64:67] offset:4096\n"
        "s_waitcnt vmcnt(9)\n"
        "v_mfma_f32_16x16x32_bf16 v[84:87], v[68:71], a[4:7], v[84:87]\n"
        "v_mfma_f32_16x16x32_bf16 v[88:91], v[68:71], a[12:15], v[88:91]\n"
        "ds_write_b128 v124, v[68:71] offset:5120\n"
        "v_mfma_f32_16x16x32_bf16 v[56:59], a[148:151], v[68:71], v[56:59]\n"
        "v_mfma_f32_16x16x32_bf16 v[60:63], a[156:159], v[68:71], v[60:63]\n"
        "s_waitcnt vmcnt(8)\n"
        "ds_write_b128 v124, v[72:75] offset:6144\n"
        "v_mfma_f32_16x16x32_bf16 v[48:51], a[244:247], v[68:71], v[48:51]\n"
        "v_mfma_f32_16x16x32_bf16 v[52:55], a[252:255], v[68:71], v[52:55]\n"
        "s_waitcnt vmcnt(7)\n"
        "ds_write_b128 v124, v[76:79] offset:7168\n"
        "v_mfma_f32_16x16x32_bf16 v[84:87], v[72:75], a[16:19], v[84:87]\n"
        "v_mfma_f32_16x16x32_bf16 v[88:91], v[72:75], a[24:27], v[88:91]\n"
        "v_mfma_f32_16x16x32_bf16 v[56:59], a[160:163], v[72:75], v[56:59]\n"
        "v_mfma_f32_16x16x32_bf16 v[60:63], a[168:171], v[72:75], v[60:63]\n"
        "v_mfma_f32_16x16x32_bf16 v[48:51], v[176:179], v[72:75], v[48:51]\n"
        "ds_read_b128 v[0:3], v125 offset:12288\n"
        "v_mfma_f32_16x16x32_bf16 v[52:55], v[184:187], v[72:75], v[52:55]\n"
        "ds_read_b128 v[8:11], v125 offset:14336\n"
        "v_mfma_f32_16x16x32_bf16 v[84:87], v[76:79], a[20:23], v[84:87]\n"
        "s_waitcnt lgkmcnt(2)\n"
        "s_barrier\n"
        "v_mfma_f32_16x16x32_bf16 v[88:91], v[76:79], a[28:31], v[88:91]\n"
        "ds_read_b128 v[92:95], v124 offset:0\n"
        "v_mov_b32 v116, s62\n"
        "v_mfma_f32_16x16x32_bf16 v[56:59], a[164:167], v[76:79], v[56:59]\n"
        "s_add_u32 s49, s49, 163840\n"
        "ds_read_b128 v[104:107], v124 offset:1024\n"
        "v_mfma_f32_16x16x32_bf16 v[60:63], a[172:175], v[76:79], v[60:63]\n"
        "s_add_u32 s50, s50, 163840\n"
        "ds_read_b128 v[4:7], v125 offset:13312\n"
        "v_mfma_f32_16x16x32_bf16 v[48:51], v[180:183], v[76:79], v[48:51]\n"
        "s_xor_b32 s49, s49, s86\n"
        "ds_read_b128 v[12:15], v125 offset:15360\n"
        "v_mfma_f32_16x16x32_bf16 v[52:55], v[188:191], v[76:79], v[52:55]\n"
        "s_xor_b32 s65, s49, 8192\n"
        "s_add_u32 s56, s56, 786432\n"
        "s_waitcnt lgkmcnt(3)\n"
        "v_mfma_f32_16x16x32_bf16 v[84:87], v[92:95], a[32:35], v[84:87]\n"
        "s_add_u32 s64, s64, 786432\n"
        "ds_read_b128 v[96:99], v124 offset:2048\n"
        "v_mfma_f32_16x16x32_bf16 v[88:91], v[92:95], a[40:43], v[88:91]\n"
        "ds_read_b128 v[100:103], v124 offset:3072\n"
        "v_mfma_f32_16x16x32_bf16 v[56:59], a[176:179], v[92:95], v[56:59]\n"
        "ds_read_b128 v[16:19], v125 offset:16384\n"
        "v_mfma_f32_16x16x32_bf16 v[60:63], a[184:187], v[92:95], v[60:63]\n"
        "s_add_u32 s51, s51, 163840\n"
        "ds_read_b128 v[20:23], v125 offset:17408\n"
        "v_mfma_f32_16x16x32_bf16 v[48:51], v[0:3], v[92:95], v[48:51]\n"
        "s_add_u32 s52, s52, 163840\n"
        "ds_read_b128 v[24:27], v125 offset:18432\n"
        "v_mfma_f32_16x16x32_bf16 v[52:55], v[8:11], v[92:95], v[52:55]\n"
        "ds_read_b128 v[28:31], v125 offset:19456\n"
        "s_waitcnt lgkmcnt(8)\n"
        "v_mfma_f32_16x16x32_bf16 v[84:87], v[104:107], a[36:39], v[84:87]\n"
        "s_add_u32 s83, s83, 163840\n"
        "ds_read_b128 v[92:95], v124 offset:12288\n"
        "v_mfma_f32_16x16x32_bf16 v[88:91], v[104:107], a[44:47], v[88:91]\n"
        "s_xor_b32 s86, s83, s46\n"
        "s_and_b32 s86, s86, s53\n"
        "s_lshr_b32 s86, s86, 8\n"
        "v_mfma_f32_16x16x32_bf16 v[56:59], a[180:183], v[104:107], v[56:59]\n"
        "s_waitcnt vmcnt(6)\n"
        "s_mov_b64 exec, 1\n"
        "buffer_atomic_umax v116, off, s[36:39], s61\n"
        "s_mov_b64 exec, -1\n"
        "v_mfma_f32_16x16x32_bf16 v[60:63], a[188:191], v[104:107], v[60:63]\n"
        "s_cmp_eq_u32 s44, 63\n"
        "s_cbranch_scc1 L_end%=\n"
        "s_waitcnt lgkmcnt(8)\n"
        "v_mfma_f32_16x16x32_bf16 v[48:51], v[4:7], v[104:107], v[48:51]\n"
        "s_xor_b32 s86, s86, s34\n"
        "ds_read_b128 v[32:35], v125 offset:20480\n"
        "s_waitcnt lgkmcnt(8)\n"
        "v_mfma_f32_16x16x32_bf16 v[52:55], v[12:15], v[104:107], v[52:55]\n"
        "ds_read_b128 v[36:39], v125 offset:21504\n"
        "s_waitcnt lgkmcnt(8)\n"
        "v_mfma_f32_16x16x32_bf16 v[84:87], v[96:99], a[48:51], v[84:87]\n"
        "s_xor_b32 s34, s34, s86\n"
        "ds_read_b128 v[104:107], v124 offset:13312\n"
        "v_mfma_f32_16x16x32_bf16 v[88:91], v[96:99], a[56:59], v[88:91]\n"
        "s_xor_b32 s51, s51, s86\n"
        "ds_read_b128 v[40:43], v125 offset:22528\n"
        "v_mfma_f32_16x16x32_bf16 v[56:59], a[192:195], v[96:99], v[56:59]\n"
        "s_add_u32 s55, s55, 786432\n"
        "ds_read_b128 v[44:47], v125 offset:23552\n"
        "v_mfma_f32_16x16x32_bf16 v[60:63], a[200:203], v[96:99], v[60:63]\n"
        "ds_read_b128 v[0:3], v125 offset:0\n"
        "s_waitcnt vmcnt(6)\n"
        "v_cmp_eq_u32 vcc, 0, v140\n"
        "s_waitcnt lgkmcnt(10)\n"
        "v_mfma_f32_16x16x32_bf16 v[48:51], v[16:19], v[96:99], v[48:51]\n"
        "s_cbranch_vccnz L_sw0_1%=\n"
        "L_sok_1%=:\n"
        "s_waitcnt lgkmcnt(8)\n"
        "v_mfma_f32_16x16x32_bf16 v[52:55], v[24:27], v[96:99], v[52:55]\n"
        "v_readlane_b32 s81, v140, s99\n"
        "v_max_u32_dpp v140, v140, v140 row_shr:1 row_mask:0xf bank_mask:0xf\n"
        "v_mfma_f32_16x16x32_bf16 v[84:87], v[100:103], a[52:55], v[84:87]\n"
        "ds_read_b128 v[96:99], v124 offset:14336\n"
        "v_max_u32_dpp v140, v140, v140 row_shr:2 row_mask:0xf bank_mask:0xf\n"
        "v_mfma_f32_16x16x32_bf16 v[88:91], v[100:103], a[60:63], v[88:91]\n"
        "ds_read_b128 v[4:7], v125 offset:1024\n"
        "v_max_u32_dpp v140, v140, v140 row_shr:4 row_mask:0xf bank_mask:0xf\n"
        "v_mfma_f32_16x16x32_bf16 v[56:59], a[196:199], v[100:103], v[56:59]\n"
        "buffer_load_dwordx4 v[108:111], v132, s[36:39], s56 offen offset:0 sc1\n"
        "v_max_u32_dpp v140, v140, v140 row_shr:8 row_mask:0xf bank_mask:0xf\n"
        "v_mfma_f32_16x16x32_bf16 v[60:63], a[204:207], v[100:103], v[60:63]\n"
        "ds_read_b128 v[8:11], v125 offset:2048\n"
        "v_max_u32_dpp v140, v140, v140 row_bcast:15 row_mask:0xa bank_mask:0xf\n"
        "v_mfma_f32_16x16x32_bf16 v[48:51], v[20:23], v[100:103], v[48:51]\n"
        "buffer_load_dwordx4 v[112:115], v132, s[36:39], s64 offen offset:0 sc1\n"
        "v_max_u32_dpp v140, v140, v140 row_bcast:31 row_mask:0xc bank_mask:0xf\n"
        "s_waitcnt lgkmcnt(10)\n"
        "v_mfma_f32_16x16x32_bf16 v[52:55], v[28:31], v[100:103], v[52:55]\n"
        "v_readlane_b32 s33, v140, 63\n"
        "s_lshl1_add_u32 s28, s28, s81\n"
        "s_waitcnt lgkmcnt(9)\n"
        "v_mfma_f32_16x16x32_bf16 v[84:87], v[92:95], a[64:67], v[84:87]\n"
        "ds_read_b128 v[100:103], v124 offset:15360\n"
        "s_add_u32 s28, s28, 36\n"
        "s_sub_i32 s33, s33, s28\n"
        "v_mfma_f32_16x16x32_bf16 v[88:91], v[92:95], a[72:75], v[88:91]\n"
        "s_min_i32 s33, s33, 500\n"
        "s_mov_b32 s28, 0\n"
        "L_dl_1%=:\n"
        "s_sub_i32 s33, s33, 3\n"
        "s_cmp_gt_i32 s33, 0\n"
        "s_cbranch_scc1 L_dl_1%=\n"
        "v_mfma_f32_16x16x32_bf16 v[56:59], a[208:211], v[92:95], v[56:59]\n"
        "s_add_u32 s54, s54, 786432\n"
        "ds_read_b128 v[12:15], v125 offset:3072\n"
        "v_mfma_f32_16x16x32_bf16 v[60:63], a[216:219], v[92:95], v[60:63]\n"
        "s_add_u32 s60, s60, 131072\n"
        "buffer_load_dwordx4 v[80:83], v138, s[36:39], s50 offen offset:0 sc1\n"
        "s_waitcnt lgkmcnt(10)\n"
        "v_mfma_f32_16x16x32_bf16 v[48:51], v[32:35], v[92:95], v[48:51]\n"
        "s_add_u32 s58, s58, 36864\n"
        "ds_read_b128 v[16:19], v125 offset:4096\n"
        "s_waitcnt lgkmcnt(8)\n"
        "v_mfma_f32_16x16x32_bf16 v[52:55], v[40:43], v[92:95], v[52:55]\n"
        "buffer_load_dwordx4 v[64:67], v138, s[36:39], s49 offen offset:0 sc1\n"
        "v_mfma_f32_16x16x32_bf16 v[84:87], v[104:107], a[68:71], v[84:87]\n"
        "s_xor_b32 s66, s51, 1024\n"
        "ds_read_b128 v[92:95], v124 offset:8192\n"
        "v_mfma_f32_16x16x32_bf16 v[88:91], v[104:107], a[76:79], v[88:91]\n"
        "s_xor_b32 s67, s51, 8192\n"
        "buffer_load_dwordx4 v[68:71], v138, s[36:39], s49 offen offset:256 sc1\n"
        "v_mfma_f32_16x16x32_bf16 v[56:59], a[212:215], v[104:107], v[56:59]\n"
        "s_xor_b32 s68, s51, 9216\n"
        "ds_read_b128 v[20:23], v125 offset:5120\n"
        "v_mfma_f32_16x16x32_bf16 v[60:63], a[220:223], v[104:107], v[60:63]\n"
        "s_sub_u32 s75, 127, s84\n"
        "buffer_load_dwordx4 v[72:75], v138, s[36:39], s65 offen offset:0 sc1\n"
        "v_mfma_f32_16x16x32_bf16 v[48:51], v[36:39], v[104:107], v[48:51]\n"
        "s_sub_u32 s101, s77, s75\n"
        "ds_read_b128 v[24:27], v125 offset:6144\n"
        "s_waitcnt lgkmcnt(10)\n"
        "v_mfma_f32_16x16x32_bf16 v[52:55], v[44:47], v[104:107], v[52:55]\n"
        "buffer_load_dwordx4 v[76:79], v138, s[36:39], s65 offen offset:256 sc1\n"
        "s_waitcnt lgkmcnt(8)\n"
        "v_mfma_f32_16x16x32_bf16 v[84:87], v[96:99], a[80:83], v[84:87]\n"
        "s_mul_i32 s35, s75, 0x800080\n"
        "ds_read_b128 v[104:107], v124 offset:9216\n"
        "v_mfma_f32_16x16x32_bf16 v[88:91], v[96:99], a[88:91], v[88:91]\n"
        "s_lshl_b32 s80, s75, 23\n"
        "ds_read_b128 v[28:31], v125 offset:7168\n"
        "v_mfma_f32_16x16x32_bf16 v[56:59], a[224:227], v[96:99], v[56:59]\n"
        "s_sub_u32 s73, 0x3e0293ee, s80\n"
        "ds_read_b128 v[32:35], v125 offset:8192\n"
        "v_mfma_f32_16x16x32_bf16 v[60:63], a[232:235], v[96:99], v[60:63]\n"
        "s_sub_u32 s74, 0x3f800000, s80\n"
        "ds_read_b128 v[36:39], v125 offset:9216\n"
        "v_mfma_f32_16x16x32_bf16 v[48:51], v[192:195], v[96:99], v[48:51]\n"
        "s_add_u32 s80, s75, 128\n"
        "ds_read_b128 v[40:43], v125 offset:10240\n"
        "v_mfma_f32_16x16x32_bf16 v[52:55], v[200:203], v[96:99], v[52:55]\n"
        "ds_read_b128 v[44:47], v125 offset:11264\n"
        "s_waitcnt lgkmcnt(11)\n"
        "v_mfma_f32_16x16x32_bf16 v[84:87], v[100:103], a[84:87], v[84:87]\n"
        "s_lshl_b32 s80, s80, 8\n"
        "ds_read_b128 v[96:99], v124 offset:10240\n"
        "v_mfma_f32_16x16x32_bf16 v[88:91], v[100:103], a[92:95], v[88:91]\n"
        "s_add_u32 s44, s44, 1\n"
        "s_add_u32 s61, s61, 32\n"
        "s_add_u32 s62, s80, 1\n"
        "v_mfma_f32_16x16x32_bf16 v[56:59], a[228:231], v[100:103], v[56:59]\n"
        "s_branch L_rot_1%=\n"
        "L_light_2%=:\n"
        "v_mfma_f32_16x16x32_bf16 v[52:55], v[252:255], v[104:107], v[52:55]\n"
        "v_mfma_f32_16x16x32_bf16 v[84:87], v[96:99], a[112:115], v[84:87]\n"
        "v_mfma_f32_16x16x32_bf16 v[88:91], v[96:99], a[120:123], v[88:91]\n"
        "v_cvt_pk_bf16_f32 v48, v48, v52\n"
        "v_cvt_pk_bf16_f32 v49, v49, v53\n"
        "v_mfma_f32_16x16x32_bf16 v[84:87], v[100:103], a[116:119], v[84:87]\n"
        "v_cvt_pk_bf16_f32 v50, v50, v54\n"
        "v_cvt_pk_bf16_f32 v51, v51, v55\n"
        "v_mfma_f32_16x16x32_bf16 v[88:91], v[100:103], a[124:127], v[88:91]\n"
        "v_mfma_f32_16x16x32_bf16 v[84:87], v[92:95], a[128:131], v[84:87]\n"
        "v_mfma_f32_16x16x32_bf16 v[88:91], v[92:95], a[136:139], v[88:91]\n"
        "buffer_store_dwordx4 v[48:51], v124, s[36:39], s54 offen offset:1024 sc1\n"
        "v_mfma_f32_16x16x32_bf16 v[84:87], v[104:107], a[132:135], v[84:87]\n"
        "v_mfma_f32_16x16x32_bf16 v[88:91], v[104:107], a[140:143], v[88:91]\n"
        "v_mfma_f32_16x16x32_bf16 v[56:59], v[32:35], v[92:95], v[56:59]\n"
        "v_mfma_f32_16x16x32_bf16 v[60:63], v[40:43], v[92:95], v[60:63]\n"
        "v_cvt_pk_bf16_f32 v84, v84, v85\n"
        "v_cvt_pk_bf16_f32 v85, v86, v87\n"
        "v_mfma_f32_16x16x32_bf16 v[56:59], v[16:19], v[96:99], v[56:59]\n"
        "v_cvt_pk_bf16_f32 v86, v88, v89\n"
        "v_cvt_pk_bf16_f32 v87, v90, v91\n"
        "v_mfma_f32_16x16x32_bf16 v[60:63], v[24:27], v[96:99], v[60:63]\n"
        "v_mfma_f32_16x16x32_bf16 v[56:59], v[20:23], v[100:103], v[56:59]\n"
        "v_mov_b64 v[92:93], 0\n"
        "v_mov_b64 v[94:95], 0\n"
        "v_mfma_f32_16x16x32_bf16 v[60:63], v[28:31], v[100:103], v[60:63]\n"
        "buffer_store_dwordx4 v[84:87], v124, s[36:39], s54 offen offset:2048 sc1\n"
        "v_mov_b64 v[96:97], 0\n"
        "v_mfma_f32_16x16x32_bf16 v[56:59], v[36:39], v[104:107], v[56:59]\n"
        "v_mov_b64 v[98:99], 0\n"
        "v_mfma_f32_16x16x32_bf16 v[60:63], v[44:47], v[104:107], v[60:63]\n"
        "s_nop 7\n"
        "v_cvt_pk_bf16_f32 v100, v56, v60\n"
        "v_cvt_pk_bf16_f32 v101, v57, v61\n"
        "v_cvt_pk_bf16_f32 v102, v58, v62\n"
        "v_cvt_pk_bf16_f32 v103, v59, v63\n"
        "buffer_store_dwordx4 v[100:103], v124, s[36:39], s54 offen offset:0 sc1\n"
        "s_barrier\n"
        "s_add_u32 s69, s58, s82\n"
        "s_sub_u32 s72, s58, s82\n"
        "s_add_u32 s87, s58, 8192\n"
        "s_waitcnt vmcnt(8)\n"
        "buffer_load_dwordx4 v[64:67], v124, s[36:39], s69 offen offset:0 sc1\n"
        "s_waitcnt vmcnt(8)\n"
        "buffer_load_dwordx4 v[68:71], v124, s[36:39], s69 offen offset:1024 sc1\n"
        "s_waitcnt vmcnt(8)\n"
        "buffer_load_dwordx4 v[72:75], v124, s[36:39], s72 offen offset:2048 sc1\n"
        "s_waitcnt vmcnt(8)\n"
        "buffer_load_dwordx4 v[76:79], v124, s[36:39], s72 offen offset:3072 sc1\n"
        "buffer_load_dwordx4 v[80:83], v124, s[36:39], s87 offen offset:0 sc1\n"
        "s_mov_b32 s81, 64\n"
        "buffer_load_dword v107, off, s[36:39], s81 sc1\n"
        "s_memrealtime s[26:27]\n"
        "s_waitcnt vmcnt(6)\n"
        "s_waitcnt lgkmcnt(0)\n"
        "s_barrier\n"
        "s_add_u32 s29, s29, 1024\n"
        "s_waitcnt vmcnt(0)\n"
        "L_par_2%=:\n"
        "v_readfirstlane_b32 s80, v107\n"
        "s_nop 0\n"
        "s_cmp_lt_i32 s80, 0\n"
        "s_cbranch_scc1 L_par2_2%=\n"
        "s_add_u32 s63, s63, 1\n"
        "s_cmp_gt_u32 s63, 0x40000\n"
        "s_cbranch_scc1 L_abort%=\n"
        "s_sleep 4\n"
        "buffer_load_dword v107, off, s[36:39], s81 sc1\n"
        "s_waitcnt vmcnt(0)\n"
        "s_branch L_par_2%=\n"
        "L_par2_2%=:\n"
        "s_bfe_u32 s46, s80, 0x10018\n"
        "s_lshl_b32 s46, s46, 21\n"
        "s_mov_b32 s53, 0x200000\n"
        "s_bfe_u32 s84, s80, 0x80010\n"
        "s_sub_u32 s84, 255, s84\n"
        "s_bfe_u32 s76, s80, 0x80008\n"
        "s_sub_u32 s76, s76, 32\n"
        "s_max_i32 s76, s76, 66\n"
        "s_bfe_u32 s77, s80, 0x80000\n"
        "s_sub_u32 s77, s77, 158\n"
        "s_bitset0_b32 s49, 30\n"
        "s_bitset0_b32 s50, 30\n"
        "s_bitset0_b32 s56, 30\n"
        "s_bitset0_b32 s64, 30\n"
        "v_mov_b32 v140, 0\n"
        "s_mov_b32 s28, 1000\n"
        "s_branch L_xok_2%=\n"
        "L_x2_2%=:\n"
        "s_add_u32 s69, s58, s82\n"
        "s_sub_u32 s72, s58, s82\n"
        "s_add_u32 s87, s58, 8192\n"
        "buffer_load_dwordx4 v[64:67], v124, s[36:39], s69 offen offset:0 sc1\n"
        "buffer_load_dwordx4 v[68:71], v124, s[36:39], s69 offen offset:1024 sc1\n"
        "buffer_load_dwordx4 v[72:75], v124, s[36:39], s72 offen offset:2048 sc1\n"
        "buffer_load_dwordx4 v[76:79], v124, s[36:39], s72 offen offset:3072 sc1\n"
        "buffer_load_dwordx4 v[80:83], v124, s[36:39], s87 offen offset:0 sc1\n"
        "s_waitcnt vmcnt(0)\n"
        "s_memrealtime s[26:27]\n"
        "s_waitcnt lgkmcnt(0)\n"
        "s_add_u32 s28, s28, s26\n"
        "L_xok_2%=:\n"
        "v_mfma_f32_16x16x32_bf16 v[84:87], v[64:67], a[0:3], 0\n"
        "v_mfma_f32_16x16x32_bf16 v[88:91], v[64:67], a[8:11], 0\n"
        "v_mfma_f32_16x16x32_bf16 v[56:59], a[144:147], v[64:67], 0\n"
        "s_branch L_win_2%=\n"
        "L_retry_2%=:\n"
        "s_add_u32 s63, s63, 1\n"
        "s_cmp_gt_u32 s63, 0x40000\n"
        "s_cbranch_scc1 L_abort%=\n"
        "s_sleep 2\n"
        "s_waitcnt vmcnt(0)\n"
        "s_waitcnt lgkmcnt(0)\n"
        "buffer_load_dwordx4 v[80:83], v138, s[36:39], s50 offen offset:0 sc1\n"
        "buffer_load_dwordx4 v[64:67], v138, s[36:39], s49 offen offset:0 sc1\n"
        "buffer_load_dwordx4 v[68:71], v138, s[36:39], s49 offen offset:256 sc1\n"
        "buffer_load_dwordx4 v[72:75], v138, s[36:39], s65 offen offset:0 sc1\n"
        "buffer_load_dwordx4 v[76:79], v138, s[36:39], s65 offen offset:256 sc1\n"
        "s_waitcnt vmcnt(4)\n"
        "v_bitop3_b32 v123, 32, v80, v82 bitop3:0x80\n"
        "v_add_u32_dpp v80, v81, v80 row_half_mirror row_mask:0xf bank_mask:0xf\n"
        "v_add_u32_dpp v82, v83, v82 row_half_mirror row_mask:0xf bank_mask:0xf\n"
        "v_cvt_f32_i32 v80, v80\n"
        "v_cvt_f32_i32 v82, v82\n"
        "v_cvt_pk_bf16_f32 v52, v80, v82\n"
        "ds_write_b32 v137, v52\n"
        "s_waitcnt vmcnt(3)\n"
        "v_bitop3_b32 v123, v123, v64, v66 bitop3:0x80\n"
        "v_cvt_f32_i32 v64, v64\n"
        "v_cvt_f32_i32 v65, v65\n"
        "v_cvt_f32_i32 v66, v66\n"
        "v_cvt_f32_i32 v67, v67\n"
        "v_cvt_pk_bf16_f32 v52, v64, v65\n"
        "v_cvt_pk_bf16_f32 v53, v66, v67\n"
        "ds_write_b64 v126, v[52:53] offset:0\n"
        "s_waitcnt vmcnt(2)\n"
        "v_bitop3_b32 v123, v123, v68, v70 bitop3:0x80\n"
        "v_cvt_f32_i32 v68, v68\n"
        "v_cvt_f32_i32 v69, v69\n"
        "v_cvt_f32_i32 v70, v70\n"
        "v_cvt_f32_i32 v71, v71\n"
        "v_cvt_pk_bf16_f32 v54, v68, v69\n"
        "v_cvt_pk_bf16_f32 v55, v70, v71\n"
        "ds_write_b64 v126, v[54:55] offset:512\n"
        "s_waitcnt vmcnt(1)\n"
        "v_bitop3_b32 v123, v123, v72, v74 bitop3:0x80\n"
        "v_cvt_f32_i32 v72, v72\n"
        "v_cvt_f32_i32 v73, v73\n"
        "ds_read_b128 v[48:51], v124 offset:4096\n"
        "v_cvt_f32_i32 v74, v74\n"
        "v_cvt_f32_i32 v75, v75\n"
        "v_cvt_pk_bf16_f32 v52, v72, v73\n"
        "v_cvt_pk_bf16_f32 v53, v74, v75\n"
        "ds_write_b64 v126, v[52:53] offset:1024\n"
        "s_waitcnt vmcnt(0)\n"
        "v_bitop3_b32 v123, v123, v76, v78 bitop3:0x80\n"
        "v_cvt_f32_i32 v76, v76\n"
        "v_cvt_f32_i32 v77, v77\n"
        "v_cvt_f32_i32 v78, v78\n"
        "v_cvt_f32_i32 v79, v79\n"
        "v_cvt_pk_bf16_f32 v54, v76, v77\n"
        "v_cvt_pk_bf16_f32 v55, v78, v79\n"
        "ds_write_b64 v126, v[54:55] offset:1536\n"
        "v_cmp_eq_u32 vcc, 0, v123\n"
        "v_mov_b64 v[122:123], 0\n"
        "v_mov_b64 v[92:93], 0\n"
        "v_mov_b64 v[94:95], 0\n"
        "v_mov_b64 v[96:97], 0\n"
        "v_mov_b64 v[98:99], 0\n"
        "s_waitcnt lgkmcnt(0)\n"
        "s_branch L_dec_2%=\n"
        "L_sw0_2%=:\n"
        "s_memrealtime s[26:27]\n"
        "s_waitcnt lgkmcnt(0)\n"
        "s_sub_u32 s28, s28, s26\n"
        "L_sw_2%=:\n"
        "s_add_u32 s63, s63, 1\n"
        "s_cmp_gt_u32 s63, 0x40000\n"
        "s_cbranch_scc1 L_abort%=\n"
        "s_sleep 2\n"
        "buffer_load_dword v140, v124, s[36:39], s29 offen sc1\n"
        "s_waitcnt vmcnt(0)\n"
        "v_cmp_eq_u32 vcc, 0, v140\n"
        "s_cbranch_vccnz L_sw_2%=\n"
        "s_memrealtime s[26:27]\n"
        "s_waitcnt lgkmcnt(0)\n"
        "s_add_u32 s28, s28, s26\n"
        "s_branch L_sok_2%=\n"
        ".p2align 6\n"
        "L_rot_2%=:\n"
        "v_mfma_f32_16x16x32_bf16 v[60:63], a[236:239], v[100:103], v[60:63]\n"
        "v_mov_b64 v[118:119], 0\n"
        "v_mov_b64 v[120:121], 0\n"
        "v_mfma_f32_16x16x32_bf16 v[48:51], v[196:199], v[100:103], v[48:51]\n"
        "s_add_u32 s48, s29, s47\n"
        "s_cmp_gt_i32 s44, 0\n"
        "s_cselect_b32 s48, s48, 0x40000000\n"
        "v_mfma_f32_16x16x32_bf16 v[52:55], v[204:207], v[100:103], v[52:55]\n"
        "s_add_u32 s69, s61, 32\n"
        "s_and_b32 s69, s69, 0xffffff80\n"
        "s_waitcnt lgkmcnt(10)\n"
        "v_mfma_f32_16x16x32_bf16 v[84:87], v[92:95], a[96:99], v[84:87]\n"
        "ds_read_b128 v[100:103], v124 offset:7168\n"
        "s_and_b32 s72, s44, 3\n"
        "v_mfma_f32_16x16x32_bf16 v[88:91], v[92:95], a[104:107], v[88:91]\n"
        "s_cmp_eq_u32 s72, 1\n"
        "s_cselect_b32 s69, s69, 0x40000000\n"
        "v_mfma_f32_16x16x32_bf16 v[56:59], v[0:3], v[92:95], v[56:59]\n"
        "s_mov_b64 exec, 0xff\n"
        "buffer_store_dwordx4 v[118:121], v124, s[36:39], s48 offen sc1\n"
        "s_mov_b64 exec, -1\n"
        "v_mfma_f32_16x16x32_bf16 v[60:63], v[8:11], v[92:95], v[60:63]\n"
        "s_mov_b64 exec, 0xff\n"
        "buffer_store_dwordx4 v[118:121], v124, s[36:39], s69 offen sc1\n"
        "s_mov_b64 exec, -1\n"
        "v_mfma_f32_16x16x32_bf16 v[48:51], v[208:211], v[92:95], v[48:51]\n"
        "s_waitcnt lgkmcnt(0)\n"
        "s_barrier\n"
        "v_mfma_f32_16x16x32_bf16 v[52:55], v[216:219], v[92:95], v[52:55]\n"
        "v_mov_b64 v[116:117], 0\n"
        "v_mfma_f32_16x16x32_bf16 v[84:87], v[104:107], a[100:103], v[84:87]\n"
        "ds_read_b128 v[92:95], v124 offset:16384\n"
        "v_mfma_f32_16x16x32_bf16 v[88:91], v[104:107], a[108:111], v[88:91]\n"
        "v_mfma_f32_16x16x32_bf16 v[56:59], v[4:7], v[104:107], v[56:59]\n"
        "v_mfma_f32_16x16x32_bf16 v[60:63], v[12:15], v[104:107], v[60:63]\n"
        "v_mfma_f32_16x16x32_bf16 v[48:51], v[212:215], v[104:107], v[48:51]\n"
        "v_mfma_f32_16x16x32_bf16 v[52:55], v[220:223], v[104:107], v[52:55]\n"
        "v_mfma_f32_16x16x32_bf16 v[48:51], v[224:227], v[96:99], v[48:51]\n"
        "ds_read_b128 v[104:107], v124 offset:17408\n"
        "v_mfma_f32_16x16x32_bf16 v[52:55], v[232:235], v[96:99], v[52:55]\n"
        "v_mfma_f32_16x16x32_bf16 v[48:51], v[228:231], v[100:103], v[48:51]\n"
        "v_mfma_f32_16x16x32_bf16 v[52:55], v[236:239], v[100:103], v[52:55]\n"
        "s_waitcnt lgkmcnt(1)\n"
        "v_mfma_f32_16x16x32_bf16 v[48:51], v[240:243], v[92:95], v[48:51]\n"
        "v_mfma_f32_16x16x32_bf16 v[52:55], v[248:251], v[92:95], v[52:55]\n"
        "s_waitcnt lgkmcnt(0)\n"
        "v_mfma_f32_16x16x32_bf16 v[48:51], v[244:247], v[104:107], v[48:51]\n"
        "s_cmp_lt_i32 s44, 0\n"
        "s_cbranch_scc1 L_light_2%=\n"
        "v_mfma_f32_16x16x32_bf16 v[52:55], v[252:255], v[104:107], v[52:55]\n"
        "s_waitcnt vmcnt(6)\n"
        "v_bitop3_b32 v123, 32, v80, v82 bitop3:0x80\n"
        "s_nop 0\n"
        "v_add_u32_dpp v80, v81, v80 row_half_mirror row_mask:0xf bank_mask:0xf\n"
        "v_mfma_f32_16x16x32_bf16 v[84:87], v[96:99], a[112:115], v[84:87]\n"
        "s_nop 0\n"
        "v_add_u32_dpp v82, v83, v82 row_half_mirror row_mask:0xf bank_mask:0xf\n"
        "v_mfma_f32_16x16x32_bf16 v[88:91], v[96:99], a[120:123], v[88:91]\n"
        "v_cvt_pk_bf16_f32 v48, v48, v52\n"
        "v_cvt_pk_bf16_f32 v49, v49, v53\n"
        "s_nop 0\n"
        "v_mfma_f32_16x16x32_bf16 v[84:87], v[100:103], a[116:119], v[84:87]\n"
        "v_cvt_pk_bf16_f32 v50, v50, v54\n"
        "v_cvt_pk_bf16_f32 v51, v51, v55\n"
        "v_mfma_f32_16x16x32_bf16 v[88:91], v[100:103], a[124:127], v[88:91]\n"
        "v_cvt_f32_i32 v80, v80\n"
        "s_nop 0\n"
        "v_cvt_f32_i32 v82, v82\n"
        "v_mfma_f32_16x16x32_bf16 v[84:87], v[92:95], a[128:131], v[84:87]\n"
        "s_nop 0\n"
        "v_cvt_pk_bf16_f32 v52, v80, v82\n"
        "v_mfma_f32_16x16x32_bf16 v[88:91], v[92:95], a[136:139], v[88:91]\n"
        "buffer_store_dwordx4 v[48:51], v124, s[36:39], s54 offen offset:1024 sc1\n"
        "v_mfma_f32_16x16x32_bf16 v[84:87], v[104:107], a[132:135], v[84:87]\n"
        "s_nop 0\n"
        "v_mfma_f32_16x16x32_bf16 v[88:91], v[104:107], a[140:143], v[88:91]\n"
        "ds_write_b32 v137, v52\n"
        "s_waitcnt vmcnt(6)\n"
        "v_bitop3_b32 v123, v123, v64, v66 bitop3:0x80\n"
        "v_cvt_f32_i32 v64, v64\n"
        "v_mfma_f32_16x16x32_bf16 v[56:59], v[32:35], v[92:95], v[56:59]\n"
        "v_cvt_f32_i32 v65, v65\n"
        "v_cvt_f32_i32 v66, v66\n"
        "v_mfma_f32_16x16x32_bf16 v[60:63], v[40:43], v[92:95], v[60:63]\n"
        "v_cvt_pk_bf16_f32 v84, v84, v85\n"
        "v_cvt_pk_bf16_f32 v85, v86, v87\n"
        "v_mfma_f32_16x16x32_bf16 v[56:59], v[16:19], v[96:99], v[56:59]\n"
        "v_cvt_pk_bf16_f32 v86, v88, v89\n"
        "v_cvt_pk_bf16_f32 v87, v90, v91\n"
        "v_mfma_f32_16x16x32_bf16 v[60:63], v[24:27], v[96:99], v[60:63]\n"
        "v_cvt_f32_i32 v67, v67\n"
        "v_cvt_pk_bf16_f32 v52, v64, v65\n"
        "v_mfma_f32_16x16x32_bf16 v[56:59], v[20:23], v[100:103], v[56:59]\n"
        "v_mov_b64 v[92:93], 0\n"
        "v_mov_b64 v[94:95], 0\n"
        "v_mfma_f32_16x16x32_bf16 v[60:63], v[28:31], v[100:103], v[60:63]\n"
        "buffer_store_dwordx4 v[84:87], v124, s[36:39], s54 offen offset:2048 sc1\n"
        "v_mov_b64 v[96:97], 0\n"
        "v_cvt_pk_bf16_f32 v53, v66, v67\n"
        "v_mfma_f32_16x16x32_bf16 v[56:59], v[36:39], v[104:107], v[56:59]\n"
        "v_mov_b64 v[98:99], 0\n"
        "ds_write_b64 v126, v[52:53] offset:0\n"
        "s_waitcnt vmcnt(6)\n"
        "v_bitop3_b32 v123, v123, v68, v70 bitop3:0x80\n"
        "v_mfma_f32_16x16x32_bf16 v[60:63], v[44:47], v[104:107], v[60:63]\n"
        "v_cvt_f32_i32 v68, v68\n"
        "v_cvt_f32_i32 v69, v69\n"
        "v_cvt_f32_i32 v70, v70\n"
        "v_cvt_f32_i32 v71, v71\n"
        "v_cvt_pk_bf16_f32 v54, v68, v69\n"
        "v_cvt_pk_bf16_f32 v55, v70, v71\n"
        "ds_write_b64 v126, v[54:55] offset:512\n"
        "s_waitcnt vmcnt(5)\n"
        "v_bitop3_b32 v123, v123, v72, v74 bitop3:0x80\n"
        "v_cvt_f32_i32 v72, v72\n"
        "v_cvt_pk_bf16_f32 v100, v56, v60\n"
        "v_cvt_pk_bf16_f32 v101, v57, v61\n"
        "v_cvt_pk_bf16_f32 v102, v58, v62\n"
        "v_cvt_pk_bf16_f32 v103, v59, v63\n"
        "v_cvt_f32_i32 v73, v73\n"
        "ds_read_b128 v[48:51], v124 offset:4096\n"
        "v_cvt_f32_i32 v74, v74\n"
        "v_cvt_f32_i32 v75, v75\n"
        "v_cvt_pk_bf16_f32 v52, v72, v73\n"
        "v_cvt_pk_bf16_f32 v53, v74, v75\n"
        "ds_write_b64 v126, v[52:53] offset:1024\n"
        "s_waitcnt vmcnt(4)\n"
        "v_bitop3_b32 v123, v123, v76, v78 bitop3:0x80\n"
        "v_cvt_f32_i32 v76, v76\n"
        "v_cvt_f32_i32 v77, v77\n"
        "v_cvt_f32_i32 v78, v78\n"
        "v_cvt_f32_i32 v79, v79\n"
        "v_cvt_pk_bf16_f32 v54, v76, v77\n"
        "v_cvt_pk_bf16_f32 v55, v78, v79\n"
        "ds_write_b64 v126, v[54:55] offset:1536\n"
        "v_cmp_eq_u32 vcc, 0, v123\n"
        "v_mov_b64 v[122:123], 0\n"
        "L_dec_2%=:\n"
        "s_waitcnt lgkmcnt(2)\n"
        "v_smfmac_f32_16x16x64_bf16 v[92:95], v[48:51], a[144:151], v134\n"
        "ds_read_b128 v[52:55], v124 offset:5120\n"
        "v_max3_f32 v104, |v80|, |v82|, |v64|\n"
        "v_smfmac_f32_16x16x64_bf16 v[96:99], v[48:51], a[152:159], v134\n"
        "s_cbranch_vccnz L_retry_2%=\n"
        "v_max3_f32 v104, |v65|, |v66|, v104\n"
        "v_smfmac_f32_16x16x64_bf16 v[116:119], v[48:51], a[240:247], v134\n"
        "s_add_u32 s29, s29, 1024\n"
        "v_max3_f32 v104, |v67|, |v68|, v104\n"
        "v_smfmac_f32_16x16x64_bf16 v[120:123], v[48:51], a[248:255], v134\n"
        "s_waitcnt lgkmcnt(0)\n"
        "v_max3_f32 v104, |v69|, |v70|, v104\n"
        "s_barrier\n"
        "ds_read_b128 v[56:59], v124 offset:2048\n"
        "v_smfmac_f32_16x16x64_bf16 v[92:95], v[52:55], a[160:167], v134\n"
        "ds_read_b128 v[60:63], v124 offset:1024\n"
        "v_max3_f32 v104, |v71|, |v72|, v104\n"
        "v_smfmac_f32_16x16x64_bf16 v[96:99], v[52:55], a[168:175], v134\n"
        "buffer_load_dwordx4 v[88:91], v124, s[36:39], s55 offen offset:3072 sc1\n"
        "ds_read_b128 v[80:83], v124 offset:3072\n"
        "v_max3_f32 v104, |v73|, |v74|, v104\n"
        "v_smfmac_f32_16x16x64_bf16 v[116:119], v[52:55], v[176:183], v134\n"
        "ds_read_b128 v[64:67], v124 offset:8192\n"
        "v_max3_f32 v104, |v75|, |v76|, v104\n"
        "v_smfmac_f32_16x16x64_bf16 v[120:123], v[52:55], v[184:191], v134\n"
        "buffer_load_dwordx4 v[84:87], v124, s[36:39], s55 offen offset:0 sc1\n"
        "ds_read_b128 v[68:71], v124 offset:6144\n"
        "v_max3_f32 v104, |v77|, |v78|, v104\n"
        "s_waitcnt lgkmcnt(4)\n"
        "v_smfmac_f32_16x16x64_bf16 v[92:95], v[56:59], v[0:7], v134\n"
        "ds_read_b128 v[72:75], v124 offset:7168\n"
        "v_max_f32_e64 v104, |v79|, v104\n"
        "v_smfmac_f32_16x16x64_bf16 v[96:99], v[56:59], v[8:15], v134\n"
        "ds_read_b128 v[0:3], v125 offset:12288\n"
        "v_max_f32_dpp v104, v104, v104 row_shr:1 row_mask:0xf bank_mask:0xf\n"
        "s_waitcnt lgkmcnt(3)\n"
        "v_smfmac_f32_16x16x64_bf16 v[92:95], v[60:63], a[224:231], v134\n"
        "ds_read_b128 v[76:79], v124 offset:0\n"
        "v_max_f32_dpp v104, v104, v104 row_shr:2 row_mask:0xf bank_mask:0xf\n"
        "v_smfmac_f32_16x16x64_bf16 v[96:99], v[60:63], a[232:239], v134\n"
        "ds_read_b128 v[4:7], v125 offset:13312\n"
        "v_max_f32_dpp v104, v104, v104 row_shr:4 row_mask:0xf bank_mask:0xf\n"
        "v_smfmac_f32_16x16x64_bf16 v[92:95], v[80:83], v[16:23], v134\n"
        "ds_read_b128 v[8:11], v125 offset:14336\n"
        "v_max_f32_dpp v104, v104, v104 row_shr:8 row_mask:0xf bank_mask:0xf\n"
        "v_smfmac_f32_16x16x64_bf16 v[96:99], v[80:83], v[24:31], v134\n"
        "ds_read_b128 v[12:15], v125 offset:15360\n"
        "v_max_f32_dpp v104, v104, v104 row_bcast:15 row_mask:0xa bank_mask:0xf\n"
        "v_smfmac_f32_16x16x64_bf16 v[92:95], v[64:67], v[32:39], v134\n"
        "ds_read_b128 v[16:19], v125 offset:16384\n"
        "v_max_f32_dpp v104, v104, v104 row_bcast:31 row_mask:0xc bank_mask:0xf\n"
        "v_smfmac_f32_16x16x64_bf16 v[96:99], v[64:67], v[40:47], v134\n"
        "ds_read_b128 v[20:23], v125 offset:17408\n"
        "v_readlane_b32 s85, v104, 63\n"
        "s_waitcnt lgkmcnt(5)\n"
        "v_smfmac_f32_16x16x64_bf16 v[92:95], v[68:71], a[176:183], v134\n"
        "ds_read_b128 v[24:27], v125 offset:18432\n"
        "v_smfmac_f32_16x16x64_bf16 v[96:99], v[68:71], a[184:191], v134\n"
        "ds_read_b128 v[28:31], v125 offset:19456\n"
        "v_mov_b32 v104, s85\n"
        "v_smfmac_f32_16x16x64_bf16 v[92:95], v[72:75], a[192:199], v134\n"
        "ds_read_b128 v[32:35], v125 offset:20480\n"
        "v_smfmac_f32_16x16x64_bf16 v[96:99], v[72:75], a[200:207], v134\n"
        "ds_read_b128 v[36:39], v125 offset:21504\n"
        "s_add_u32 s69, s58, s82\n"
        "v_smfmac_f32_16x16x64_bf16 v[92:95], v[76:79], a[208:215], v134\n"
        "ds_read_b128 v[40:43], v125 offset:22528\n"
        "s_sub_u32 s72, s58, s82\n"
        "v_smfmac_f32_16x16x64_bf16 v[96:99], v[76:79], a[216:223], v134\n"
        "ds_read_b128 v[44:47], v125 offset:23552\n"
        "s_add_u32 s87, s58, 8192\n"
        "v_smfmac_f32_16x16x64_bf16 v[116:119], v[64:67], v[240:247], v134\n"
        "ds_write_b32 v142, v104\n"
        "v_add_f32 v92, v92, v94\n"
        "v_add_f32 v93, v93, v95\n"
        "v_smfmac_f32_16x16x64_bf16 v[120:123], v[64:67], v[248:255], v134\n"
        "v_add_f32 v96, v96, v98\n"
        "v_add_f32 v97, v97, v99\n"
        "v_cvt_pk_bf16_f32 v92, v92, v96\n"
        "v_cvt_pk_bf16_f32 v93, v93, v97\n"
        "s_waitcnt lgkmcnt(3)\n"
        "v_smfmac_f32_16x16x64_bf16 v[116:119], v[68:71], v[0:7], v134\n"
        "ds_write2_b32 v127, v92, v93 offset1:72\n"
        "v_smfmac_f32_16x16x64_bf16 v[120:123], v[68:71], v[8:15], v134\n"
        "v_mov_b64 v[98:99], 0\n"
        "v_smfmac_f32_16x16x64_bf16 v[116:119], v[72:75], v[16:23], v134\n"
        "v_mov_b64 v[96:97], 0\n"
        "v_smfmac_f32_16x16x64_bf16 v[120:123], v[72:75], v[24:31], v134\n"
        "s_waitcnt vmcnt(3)\n"
        "s_waitcnt lgkmcnt(0)\n"
        "v_mov_b64 v[106:107], 0\n"
        "s_barrier\n"
        "ds_read_b128 v[0:3], v128 offset:0\n"
        "v_smfmac_f32_16x16x64_bf16 v[116:119], v[76:79], v[32:39], v134\n"
        "ds_read_b128 v[24:27], v124 offset:20480\n"
        "v_mov_b64 v[104:105], 0\n"
        "v_smfmac_f32_16x16x64_bf16 v[120:123], v[76:79], v[40:47], v134\n"
        "ds_read_b128 v[4:7], v128 offset:64\n"
        "v_mov_b64 v[94:95], 0\n"
        "v_smfmac_f32_16x16x64_bf16 v[116:119], v[60:63], v[192:199], v134\n"
        "ds_read_b128 v[8:11], v128 offset:128\n"
        "v_mov_b64 v[92:93], 0\n"
        "v_smfmac_f32_16x16x64_bf16 v[120:123], v[60:63], v[200:207], v134\n"
        "ds_read_b128 v[12:15], v128 offset:192\n"
        "v_mov_b64 v[20:21], 0\n"
        "v_smfmac_f32_16x16x64_bf16 v[116:119], v[56:59], v[208:215], v134\n"
        "s_waitcnt lgkmcnt(3)\n"
        "v_max3_f32 v24, v24, v25, v26\n"
        "s_waitcnt lgkmcnt(2)\n"
        "v_smfmac_f32_16x16x64_bf16 v[104:107], v[108:111], v[0:7], v134\n"
        "v_max_f32 v24, v24, v27\n"
        "v_smfmac_f32_16x16x64_bf16 v[120:123], v[56:59], v[216:223], v134\n"
        "v_readfirstlane_b32 s84, v24\n"
        "s_waitcnt lgkmcnt(0)\n"
        "v_smfmac_f32_16x16x64_bf16 v[104:107], v[112:115], v[8:15], v134\n"
        "v_mov_b64 v[22:23], 0\n"
        "v_smfmac_f32_16x16x64_bf16 v[116:119], v[80:83], v[224:231], v134\n"
        "s_lshr_b32 s84, s84, 23\n"
        "s_add_u32 s84, s84, s101\n"
        "v_smfmac_f32_16x16x64_bf16 v[120:123], v[80:83], v[232:239], v134\n"
        "s_max_i32 s84, s84, s76\n"
        "s_min_i32 s84, s84, 186\n"
        "v_add_f32 v104, v104, v106\n"
        "v_add_f32 v105, v105, v107\n"
        "v_smfmac_f32_16x16x64_bf16 v[92:95], v[64:67], a[128:135], v134\n"
        "ds_write_b64 v131, v[104:105]\n"
        "v_add_f32 v116, v116, v118\n"
        "v_add_f32 v117, v117, v119\n"
        "v_pk_add_f32 v[120:121], v[120:121], v[122:123]\n"
        "v_cvt_pk_bf16_f32 v116, v116, v120\n"
        "v_cvt_pk_bf16_f32 v117, v117, v121\n"
        "ds_write2_b32 v129, v116, v117 offset1:8\n"
        "v_smfmac_f32_16x16x64_bf16 v[96:99], v[64:67], a[136:143], v134\n"
        "s_mul_i32 s88, s84, 0x800080\n"
        "s_mov_b32 s89, s88\n"
        "v_smfmac_f32_16x16x64_bf16 v[92:95], v[68:71], a[32:39], v134\n"
        "buffer_store_dwordx4 v[100:103], v124, s[36:39], s54 offen offset:0 sc1\n"
        "v_mov_b64 v[104:105], s[88:89]\n"
        "v_smfmac_f32_16x16x64_bf16 v[96:99], v[68:71], a[40:47], v134\n"
        "s_add_u32 s90, s88, s35\n"
        "s_mov_b32 s91, s90\n"
        "v_mov_b64 v[106:107], s[90:91]\n"
        "v_smfmac_f32_16x16x64_bf16 v[92:95], v[48:51], a[0:7], v134\n"
        "s_waitcnt lgkmcnt(0)\n"
        "v_mov_b64 v[100:101], s[88:89]\n"
        "s_barrier\n"
        "ds_read_b128 v[24:27], v130\n"
        "v_smfmac_f32_16x16x64_bf16 v[96:99], v[48:51], a[8:15], v134\n"
        "ds_read_b128 v[28:31], v130 offset:1024\n"
        "v_mov_b64 v[102:103], s[90:91]\n"
        "v_smfmac_f32_16x16x64_bf16 v[92:95], v[52:55], a[16:23], v134\n"
        "ds_read_b128 v[116:119], v124 offset:13568\n"
        "v_mov_b64 v[48:49], 0\n"
        "v_smfmac_f32_16x16x64_bf16 v[96:99], v[52:55], a[24:31], v134\n"
        "buffer_load_dwordx4 v[64:67], v124, s[36:39], s69 offen offset:0 sc1\n"
        "v_mov_b64 v[50:51], 0\n"
        "v_smfmac_f32_16x16x64_bf16 v[92:95], v[56:59], a[96:103], v134\n"
        "v_mov_b64 v[52:53], 0\n"
        "v_smfmac_f32_16x16x64_bf16 v[96:99], v[56:59], a[104:111], v134\n"
        "buffer_load_dwordx4 v[68:71], v124, s[36:39], s69 offen offset:1024 sc1\n"
        "v_mov_b64 v[54:55], 0\n"
        "s_waitcnt lgkmcnt(2)\n"
        "v_smfmac_f32_16x16x64_bf16 v[20:23], v[24:27], v[0:7], v134\n"
        "v_mov_b64 v[56:57], 0\n"
        "s_waitcnt lgkmcnt(0)\n"
        "v_smfmac_f32_16x16x64_bf16 v[20:23], v[28:31], v[8:15], v134\n"
        "v_max3_f32 v121, v116, v117, v118\n"
        "v_smfmac_f32_16x16x64_bf16 v[92:95], v[72:75], a[48:55], v134\n"
        "ds_read_b128 v[0:3], v125 offset:24576\n"
        "v_mov_b64 v[58:59], 0\n"
        "v_smfmac_f32_16x16x64_bf16 v[96:99], v[72:75], a[56:63], v134\n"
        "buffer_load_dwordx4 v[72:75], v124, s[36:39], s72 offen offset:2048 sc1\n"
        "v_add_f32 v20, v20, v22\n"
        "v_add_f32 v21, v21, v23\n"
        "v_cndmask_b32 v120, v20, v21, s[92:93]\n"
        "v_mul_f32 v120, s74, v120\n"
        "v_max3_f32 v121, v121, v119, v120\n"
        "v_smfmac_f32_16x16x64_bf16 v[92:95], v[76:79], a[64:71], v134\n"
        "ds_read_b128 v[4:7], v125 offset:25600\n"
        "v_max_f32_dpp v121, v121, v121 quad_perm:[2,3,0,1] row_mask:0xf bank_mask:0xf\n"
        "v_mov_b32 v122, v121\n"
        "v_smfmac_f32_16x16x64_bf16 v[96:99], v[76:79], a[72:79], v134\n"
        "buffer_load_dwordx4 v[76:79], v124, s[36:39], s72 offen offset:3072 sc1\n"
        "v_permlane16_swap_b32 v121, v122\n"
        "v_max_f32 v121, v121, v122\n"
        "v_mov_b32 v122, v121\n"
        "v_smfmac_f32_16x16x64_bf16 v[92:95], v[80:83], a[112:119], v134\n"
        "ds_read_b128 v[8:11], v125 offset:26624\n"
        "v_permlane32_swap_b32 v121, v122\n"
        "v_smfmac_f32_16x16x64_bf16 v[96:99], v[80:83], a[120:127], v134\n"
        "buffer_load_dwordx4 v[80:83], v124, s[36:39], s87 offen offset:0 sc1\n"
        "v_max_f32 v121, v121, v122\n"
        "v_mul_f32_e64 v123, -s73, v121\n"
        "v_fma_f32 v120, v120, s73, v123\n"
        "v_fma_f32 v116, v116, s73, v123\n"
        "v_smfmac_f32_16x16x64_bf16 v[92:95], v[60:63], a[80:87], v134\n"
        "ds_read_b128 v[12:15], v125 offset:27648\n"
        "v_fma_f32 v117, v117, s73, v123\n"
        "v_fma_f32 v118, v118, s73, v123\n"
        "v_fma_f32 v119, v119, s73, v123\n"
        "v_exp_f32 v120, v120\n"
        "v_smfmac_f32_16x16x64_bf16 v[96:99], v[60:63], a[88:95], v134\n"
        "ds_read_b128 v[16:19], v125 offset:28672\n"
        "v_exp_f32 v116, v116\n"
        "v_mul_f32 v120, s74, v120\n"
        "v_exp_f32 v117, v117\n"
        "v_exp_f32 v118, v118\n"
        "v_exp_f32 v119, v119\n"
        "s_waitcnt vmcnt(6)\n"
        "v_mov_b64 v[108:109], v[86:87]\n"
        "v_mov_b64 v[112:113], v[90:91]\n"
        "v_cvt_pk_bf16_f32 v120, v120, v120\n"
        "v_cvt_pk_bf16_f32 v60, v116, v117\n"
        "v_cvt_pk_bf16_f32 v62, v118, v119\n"
        "v_and_b32 v61, v120, v135\n"
        "v_and_b32 v63, v120, v136\n"
        "v_pk_add_f32 v[92:93], v[92:93], v[94:95]\n"
        "v_pk_add_f32 v[96:97], v[96:97], v[98:99]\n"
        "v_smfmac_f32_16x16x64_bf16 v[56:59], v[60:63], v[100:107], v134\n"
        "ds_read_b128 v[20:23], v125 offset:29696\n"
        "v_cvt_pk_bf16_f32 v86, v92, v93\n"
        "v_cvt_pk_bf16_f32 v87, v92, v93\n"
        "v_mov_b64 v[90:91], v[86:87]\n"
        "v_cvt_pk_bf16_f32 v110, v96, v97\n"
        "v_cvt_pk_bf16_f32 v111, v96, v97\n"
        "v_smfmac_f32_16x16x64_bf16 v[48:51], v[60:63], v[84:91], v134\n"
        "ds_read_b128 v[28:31], v125 offset:31744\n"
        "v_mov_b64 v[114:115], v[110:111]\n"
        "v_add_f32 v56, v56, v58\n"
        "v_add_f32 v57, v57, v59\n"
        "v_smfmac_f32_16x16x64_bf16 v[52:55], v[60:63], v[108:115], v134\n"
        "s_nop 1\n"
        "v_rcp_f32 v56, v56\n"
        "v_rcp_f32 v57, v57\n"
        "v_pk_add_f32 v[48:49], v[48:49], v[50:51]\n"
        "v_pk_mul_f32 v[48:49], v[48:49], v[56:57]\n"
        "v_mov_b32 v110, v86\n"
        "v_mov_b64 v[92:93], s[70:71]\n"
        "v_pk_add_f32 v[52:53], v[52:53], v[54:55]\n"
        "v_pk_mul_f32 v[52:53], v[52:53], v[56:57]\n"
        "v_cvt_pk_bf16_f32 v48, v48, v52\n"
        "v_cvt_pk_bf16_f32 v49, v49, v53\n"
        "ds_write2_b32 v139, v48, v49 offset1:4\n"
        "v_mov_b64 v[94:95], 0\n"
        "v_mov_b64 v[96:97], 0\n"
        "v_mov_b64 v[98:99], 0\n"
        "v_mov_b64 v[100:101], s[70:71]\n"
        "v_mov_b64 v[102:103], 0\n"
        "buffer_store_dwordx2 v[110:111], v126, s[36:39], s60 offen sc1\n"
        "v_mov_b64 v[104:105], 0\n"
        "s_waitcnt lgkmcnt(0)\n"
        "v_mov_b64 v[106:107], 0\n"
        "s_barrier\n"
        "ds_read_b128 v[108:111], v124 offset:18432\n"
        "v_mov_b64 v[106:107], 0\n"
        "ds_read_b128 v[112:115], v124 offset:19456\n"
        "v_mov_b64 v[106:107], 0\n"
        "s_bitcmp1_b32 s59, 30\n"
        "s_cbranch_scc1 L_s1_2%=\n"
        "buffer_store_dwordx4 v[24:27], v124, s[36:39], s59 offen offset:0 sc1\n"
        "L_s1_2%=:\n"
        "v_mov_b64 v[106:107], 0\n"
        "v_mov_b64 v[106:107], 0\n"
        "ds_read_b128 v[24:27], v125 offset:30720\n"
        "v_mov_b64 v[106:107], 0\n"
        "s_waitcnt lgkmcnt(2)\n"
        "v_smfmac_f32_16x16x64_bf16 v[92:95], v[108:111], v[144:151], v134\n"
        "v_smfmac_f32_16x16x64_bf16 v[96:99], v[108:111], v[152:159], v134\n"
        "s_waitcnt lgkmcnt(1)\n"
        "v_smfmac_f32_16x16x64_bf16 v[92:95], v[112:115], v[160:167], v134\n"
        "v_smfmac_f32_16x16x64_bf16 v[96:99], v[112:115], v[168:175], v134\n"
        "v_smfmac_f32_16x16x64_bf16 v[100:103], v[108:111], v[0:7], v134\n"
        "v_add_f32 v92, v92, v94\n"
        "v_add_f32 v93, v93, v95\n"
        "v_cvt_rpi_i32_f32 v94, v92\n"
        "v_smfmac_f32_16x16x64_bf16 v[104:107], v[108:111], v[8:15], v134\n"
        "buffer_load_dword v140, v124, s[36:39], s29 offen sc1\n"
        "v_bfi_b32 v94, 63, 1, v94\n"
        "v_add_f32 v96, v96, v98\n"
        "v_smfmac_f32_16x16x64_bf16 v[100:103], v[112:115], v[16:23], v134\n"
        "v_cvt_rpi_i32_f32 v95, v96\n"
        "s_waitcnt lgkmcnt(0)\n"
        "v_smfmac_f32_16x16x64_bf16 v[104:107], v[112:115], v[24:31], v134\n"
        "buffer_atomic_add_x2 v[94:95], v133, s[36:39], s51 offen\n"
        "v_add_f32 v97, v97, v99\n"
        "v_cvt_rpi_i32_f32 v98, v93\n"
        "v_bfi_b32 v98, 63, 1, v98\n"
        "v_cvt_rpi_i32_f32 v99, v97\n"
        "s_waitcnt vmcnt(7)\n"
        "v_mfma_f32_16x16x32_bf16 v[84:87], v[64:67], a[0:3], 0\n"
        "buffer_atomic_add_x2 v[98:99], v133, s[36:39], s66 offen\n"
        "v_add_f32 v100, v100, v102\n"
        "v_add_f32 v101, v101, v103\n"
        "v_add_f32 v104, v104, v106\n"
        "v_cvt_rpi_i32_f32 v102, v100\n"
        "v_bfi_b32 v102, 63, 1, v102\n"
        "v_cvt_rpi_i32_f32 v103, v104\n"
        "v_mfma_f32_16x16x32_bf16 v[88:91], v[64:67], a[8:11], 0\n"
        "buffer_atomic_add_x2 v[102:103], v133, s[36:39], s67 offen\n"
        "v_add_f32 v105, v105, v107\n"
        "v_cvt_rpi_i32_f32 v106, v101\n"
        "v_bfi_b32 v106, 63, 1, v106\n"
        "v_cvt_rpi_i32_f32 v107, v105\n"
        "v_mfma_f32_16x16x32_bf16 v[56:59], a[144:147], v[64:67], 0\n"
        "buffer_atomic_add_x2 v[106:107], v133, s[36:39], s68 offen\n"
        "L_win_2%=:\n"
        "v_mfma_f32_16x16x32_bf16 v[60:63], a[152:155], v[64:67], 0\n"
        "v_mfma_f32_16x16x32_bf16 v[48:51], a[240:243], v[64:67], 0\n"
        "ds_write_b128 v124, v[64:67] offset:8192\n"
        "v_mfma_f32_16x16x32_bf16 v[52:55], a[248:251], v[64:67], 0\n"
        "s_waitcnt vmcnt(9)\n"
        "v_mfma_f32_16x16x32_bf16 v[84:87], v[68:71], a[4:7], v[84:87]\n"
        "ds_write_b128 v124, v[68:71] offset:9216\n"
        "v_mfma_f32_16x16x32_bf16 v[88:91], v[68:71], a[12:15], v[88:91]\n"
        "v_mfma_f32_16x16x32_bf16 v[56:59], a[148:151], v[68:71], v[56:59]\n"
        "s_waitcnt vmcnt(8)\n"
        "ds_write_b128 v124, v[72:75] offset:10240\n"
        "v_mfma_f32_16x16x32_bf16 v[60:63], a[156:159], v[68:71], v[60:63]\n"
        "v_mfma_f32_16x16x32_bf16 v[48:51], a[244:247], v[68:71], v[48:51]\n"
        "s_waitcnt vmcnt(7)\n"
        "ds_write_b128 v124, v[76:79] offset:11264\n"
        "v_mfma_f32_16x16x32_bf16 v[52:55], a[252:255], v[68:71], v[52:55]\n"
        "v_mfma_f32_16x16x32_bf16 v[84:87], v[72:75], a[16:19], v[84:87]\n"
        "s_waitcnt vmcnt(6)\n"
        "ds_write_b128 v124, v[80:83] offset:16384\n"
        "v_mfma_f32_16x16x32_bf16 v[88:91], v[72:75], a[24:27], v[88:91]\n"
        "v_mfma_f32_16x16x32_bf16 v[56:59], a[160:163], v[72:75], v[56:59]\n"
        "v_mfma_f32_16x16x32_bf16 v[60:63], a[168:171], v[72:75], v[60:63]\n"
        "v_mfma_f32_16x16x32_bf16 v[48:51], v[176:179], v[72:75], v[48:51]\n"
        "ds_read_b128 v[0:3], v125 offset:12288\n"
        "v_mfma_f32_16x16x32_bf16 v[52:55], v[184:187], v[72:75], v[52:55]\n"
        "ds_read_b128 v[8:11], v125 offset:14336\n"
        "v_mfma_f32_16x16x32_bf16 v[84:87], v[76:79], a[20:23], v[84:87]\n"
        "s_waitcnt lgkmcnt(2)\n"
        "s_barrier\n"
        "v_mfma_f32_16x16x32_bf16 v[88:91], v[76:79], a[28:31], v[88:91]\n"
        "ds_read_b128 v[92:95], v124 offset:12288\n"
        "v_mov_b32 v116, s62\n"
        "v_mfma_f32_16x16x32_bf16 v[56:59], a[164:167], v[76:79], v[56:59]\n"
        "s_add_u32 s49, s49, 163840\n"
        "ds_read_b128 v[104:107], v124 offset:13312\n"
        "v_mfma_f32_16x16x32_bf16 v[60:63], a[172:175], v[76:79], v[60:63]\n"
        "s_add_u32 s50, s50, 163840\n"
        "ds_read_b128 v[4:7], v125 offset:13312\n"
        "v_mfma_f32_16x16x32_bf16 v[48:51], v[180:183], v[76:79], v[48:51]\n"
        "s_xor_b32 s49, s49, s86\n"
        "ds_read_b128 v[12:15], v125 offset:15360\n"
        "v_mfma_f32_16x16x32_bf16 v[52:55], v[188:191], v[76:79], v[52:55]\n"
        "s_xor_b32 s65, s49, 8192\n"
        "s_add_u32 s56, s56, 786432\n"
        "s_waitcnt lgkmcnt(3)\n"
        "v_mfma_f32_16x16x32_bf16 v[84:87], v[92:95], a[32:35], v[84:87]\n"
        "s_add_u32 s64, s64, 786432\n"
        "ds_read_b128 v[96:99], v124 offset:14336\n"
        "v_mfma_f32_16x16x32_bf16 v[88:91], v[92:95], a[40:43], v[88:91]\n"
        "ds_read_b128 v[100:103], v124 offset:15360\n"
        "v_mfma_f32_16x16x32_bf16 v[56:59], a[176:179], v[92:95], v[56:59]\n"
        "ds_read_b128 v[16:19], v125 offset:16384\n"
        "v_mfma_f32_16x16x32_bf16 v[60:63], a[184:187], v[92:95], v[60:63]\n"
        "s_add_u32 s51, s51, 163840\n"
        "ds_read_b128 v[20:23], v125 offset:17408\n"
        "v_mfma_f32_16x16x32_bf16 v[48:51], v[0:3], v[92:95], v[48:51]\n"
        "s_add_u32 s83, s83, 163840\n"
        "ds_read_b128 v[24:27], v125 offset:18432\n"
        "v_mfma_f32_16x16x32_bf16 v[52:55], v[8:11], v[92:95], v[52:55]\n"
        "ds_read_b128 v[28:31], v125 offset:19456\n"
        "s_waitcnt lgkmcnt(8)\n"
        "v_mfma_f32_16x16x32_bf16 v[84:87], v[104:107], a[36:39], v[84:87]\n"
        "s_xor_b32 s86, s83, s46\n"
        "ds_read_b128 v[92:95], v124 offset:0\n"
        "v_mfma_f32_16x16x32_bf16 v[88:91], v[104:107], a[44:47], v[88:91]\n"
        "s_and_b32 s86, s86, s53\n"
        "s_lshr_b32 s86, s86, 8\n"
        "s_xor_b32 s86, s86, s34\n"
        "v_mfma_f32_16x16x32_bf16 v[56:59], a[180:183], v[104:107], v[56:59]\n"
        "s_xor_b32 s34, s34, s86\n"
        "s_xor_b32 s51, s51, s86\n"
        "s_add_u32 s55, s55, 786432\n"
        "v_mfma_f32_16x16x32_bf16 v[60:63], a[188:191], v[104:107], v[60:63]\n"
        "s_waitcnt vmcnt(5)\n"
        "s_mov_b64 exec, 1\n"
        "buffer_atomic_umax v116, off, s[36:39], s61\n"
        "s_mov_b64 exec, -1\n"
        "s_waitcnt lgkmcnt(8)\n"
        "v_mfma_f32_16x16x32_bf16 v[48:51], v[4:7], v[104:107], v[48:51]\n"
        "s_cmp_eq_u32 s44, 63\n"
        "s_cbranch_scc1 L_end%=\n"
        "s_waitcnt lgkmcnt(7)\n"
        "v_mfma_f32_16x16x32_bf16 v[52:55], v[12:15], v[104:107], v[52:55]\n"
        "ds_read_b128 v[32:35], v125 offset:20480\n"
        "s_waitcnt lgkmcnt(7)\n"
        "v_mfma_f32_16x16x32_bf16 v[84:87], v[96:99], a[48:51], v[84:87]\n"
        "s_add_u32 s54, s54, 786432\n"
        "ds_read_b128 v[104:107], v124 offset:1024\n"
        "v_mfma_f32_16x16x32_bf16 v[88:91], v[96:99], a[56:59], v[88:91]\n"
        "s_add_u32 s59, s59, 131072\n"
        "ds_read_b128 v[36:39], v125 offset:21504\n"
        "v_mfma_f32_16x16x32_bf16 v[56:59], a[192:195], v[96:99], v[56:59]\n"
        "s_add_u32 s60, s60, 131072\n"
        "ds_read_b128 v[40:43], v125 offset:22528\n"
        "v_mfma_f32_16x16x32_bf16 v[60:63], a[200:203], v[96:99], v[60:63]\n"
        "ds_read_b128 v[44:47], v125 offset:23552\n"
        "s_waitcnt vmcnt(5)\n"
        "v_cmp_eq_u32 vcc, 0, v140\n"
        "s_waitcnt lgkmcnt(9)\n"
        "v_mfma_f32_16x16x32_bf16 v[48:51], v[16:19], v[96:99], v[48:51]\n"
        "s_cbranch_vccnz L_sw0_2%=\n"
        "L_sok_2%=:\n"
        "s_waitcnt lgkmcnt(7)\n"
        "v_mfma_f32_16x16x32_bf16 v[52:55], v[24:27], v[96:99], v[52:55]\n"
        "v_readlane_b32 s81, v140, s99\n"
        "v_max_u32_dpp v140, v140, v140 row_shr:1 row_mask:0xf bank_mask:0xf\n"
        "v_mfma_f32_16x16x32_bf16 v[84:87], v[100:103], a[52:55], v[84:87]\n"
        "ds_read_b128 v[96:99], v124 offset:2048\n"
        "v_max_u32_dpp v140, v140, v140 row_shr:2 row_mask:0xf bank_mask:0xf\n"
        "v_mfma_f32_16x16x32_bf16 v[88:91], v[100:103], a[60:63], v[88:91]\n"
        "buffer_load_dwordx4 v[108:111], v132, s[36:39], s56 offen offset:0 sc1\n"
        "v_max_u32_dpp v140, v140, v140 row_shr:4 row_mask:0xf bank_mask:0xf\n"
        "v_mfma_f32_16x16x32_bf16 v[56:59], a[196:199], v[100:103], v[56:59]\n"
        "ds_read_b128 v[0:3], v125 offset:0\n"
        "v_max_u32_dpp v140, v140, v140 row_shr:8 row_mask:0xf bank_mask:0xf\n"
        "v_mfma_f32_16x16x32_bf16 v[60:63], a[204:207], v[100:103], v[60:63]\n"
        "buffer_load_dwordx4 v[112:115], v132, s[36:39], s64 offen offset:0 sc1\n"
        "v_max_u32_dpp v140, v140, v140 row_bcast:15 row_mask:0xa bank_mask:0xf\n"
        "v_mfma_f32_16x16x32_bf16 v[48:51], v[20:23], v[100:103], v[48:51]\n"
        "ds_read_b128 v[4:7], v125 offset:1024\n"
        "v_max_u32_dpp v140, v140, v140 row_bcast:31 row_mask:0xc bank_mask:0xf\n"
        "s_waitcnt lgkmcnt(9)\n"
        "v_mfma_f32_16x16x32_bf16 v[52:55], v[28:31], v[100:103], v[52:55]\n"
        "v_readlane_b32 s33, v140, 63\n"
        "s_lshl1_add_u32 s28, s28, s81\n"
        "s_waitcnt lgkmcnt(8)\n"
        "v_mfma_f32_16x16x32_bf16 v[84:87], v[92:95], a[64:67], v[84:87]\n"
        "ds_read_b128 v[100:103], v124 offset:3072\n"
        "s_add_u32 s28, s28, 36\n"
        "s_sub_i32 s33, s33, s28\n"
        "v_mfma_f32_16x16x32_bf16 v[88:91], v[92:95], a[72:75], v[88:91]\n"
        "s_min_i32 s33, s33, 500\n"
        "s_mov_b32 s28, 0\n"
        "L_dl_2%=:\n"
        "s_sub_i32 s33, s33, 3\n"
        "s_cmp_gt_i32 s33, 0\n"
        "s_cbranch_scc1 L_dl_2%=\n"
        "v_mfma_f32_16x16x32_bf16 v[56:59], a[208:211], v[92:95], v[56:59]\n"
        "s_add_u32 s58, s58, 36864\n"
        "buffer_load_dwordx4 v[80:83], v138, s[36:39], s50 offen offset:0 sc1\n"
        "v_mfma_f32_16x16x32_bf16 v[60:63], a[216:219], v[92:95], v[60:63]\n"
        "s_xor_b32 s66, s51, 1024\n"
        "ds_read_b128 v[8:11], v125 offset:2048\n"
        "s_waitcnt lgkmcnt(9)\n"
        "v_mfma_f32_16x16x32_bf16 v[48:51], v[32:35], v[92:95], v[48:51]\n"
        "s_xor_b32 s67, s51, 8192\n"
        "buffer_load_dwordx4 v[64:67], v138, s[36:39], s49 offen offset:0 sc1\n"
        "s_waitcnt lgkmcnt(6)\n"
        "v_mfma_f32_16x16x32_bf16 v[52:55], v[40:43], v[92:95], v[52:55]\n"
        "ds_read_b128 v[12:15], v125 offset:3072\n"
        "v_mfma_f32_16x16x32_bf16 v[84:87], v[104:107], a[68:71], v[84:87]\n"
        "s_xor_b32 s68, s51, 9216\n"
        "ds_read_b128 v[92:95], v124 offset:4096\n"
        "v_mfma_f32_16x16x32_bf16 v[88:91], v[104:107], a[76:79], v[88:91]\n"
        "s_sub_u32 s75, 127, s84\n"
        "buffer_load_dwordx4 v[68:71], v138, s[36:39], s49 offen offset:256 sc1\n"
        "v_mfma_f32_16x16x32_bf16 v[56:59], a[212:215], v[104:107], v[56:59]\n"
        "s_sub_u32 s101, s77, s75\n"
        "ds_read_b128 v[16:19], v125 offset:4096\n"
        "v_mfma_f32_16x16x32_bf16 v[60:63], a[220:223], v[104:107], v[60:63]\n"
        "s_mul_i32 s35, s75, 0x800080\n"
        "buffer_load_dwordx4 v[72:75], v138, s[36:39], s65 offen offset:0 sc1\n"
        "v_mfma_f32_16x16x32_bf16 v[48:51], v[36:39], v[104:107], v[48:51]\n"
        "s_lshl_b32 s80, s75, 23\n"
        "ds_read_b128 v[20:23], v125 offset:5120\n"
        "s_waitcnt lgkmcnt(9)\n"
        "v_mfma_f32_16x16x32_bf16 v[52:55], v[44:47], v[104:107], v[52:55]\n"
        "buffer_load_dwordx4 v[76:79], v138, s[36:39], s65 offen offset:256 sc1\n"
        "s_waitcnt lgkmcnt(8)\n"
        "v_mfma_f32_16x16x32_bf16 v[84:87], v[96:99], a[80:83], v[84:87]\n"
        "s_sub_u32 s73, 0x3e0293ee, s80\n"
        "ds_read_b128 v[104:107], v124 offset:5120\n"
        "v_mfma_f32_16x16x32_bf16 v[88:91], v[96:99], a[88:91], v[88:91]\n"
        "s_sub_u32 s74, 0x3f800000, s80\n"
        "ds_read_b128 v[24:27], v125 offset:6144\n"
        "v_mfma_f32_16x16x32_bf16 v[56:59], a[224:227], v[96:99], v[56:59]\n"
        "s_add_u32 s80, s75, 128\n"
        "ds_read_b128 v[28:31], v125 offset:7168\n"
        "v_mfma_f32_16x16x32_bf16 v[60:63], a[232:235], v[96:99], v[60:63]\n"
        "s_lshl_b32 s80, s80, 8\n"
        "ds_read_b128 v[32:35], v125 offset:8192\n"
        "v_mfma_f32_16x16x32_bf16 v[48:51], v[192:195], v[96:99], v[48:51]\n"
        "s_add_u32 s62, s80, 1\n"
        "ds_read_b128 v[36:39], v125 offset:9216\n"
        "v_mfma_f32_16x16x32_bf16 v[52:55], v[200:203], v[96:99], v[52:55]\n"
        "ds_read_b128 v[40:43], v125 offset:10240\n"
        "s_waitcnt lgkmcnt(11)\n"
        "v_mfma_f32_16x16x32_bf16 v[84:87], v[100:103], a[84:87], v[84:87]\n"
        "s_add_u32 s44, s44, 1\n"
        "ds_read_b128 v[96:99], v124 offset:6144\n"
        "v_mfma_f32_16x16x32_bf16 v[88:91], v[100:103], a[92:95], v[88:91]\n"
        "s_add_u32 s61, s61, 32\n"
        "ds_read_b128 v[44:47], v125 offset:11264\n"
        "v_mfma_f32_16x16x32_bf16 v[56:59], a[228:231], v[100:103], v[56:59]\n"
        "s_branch L_rot_2%=\n"
        "L_light_3%=:\n"
        "v_mfma_f32_16x16x32_bf16 v[52:55], v[252:255], v[104:107], v[52:55]\n"
        "v_mfma_f32_16x16x32_bf16 v[84:87], v[96:99], a[112:115], v[84:87]\n"
        "v_mfma_f32_16x16x32_bf16 v[88:91], v[96:99], a[120:123], v[88:91]\n"
        "v_cvt_pk_bf16_f32 v48, v48, v52\n"
        "v_cvt_pk_bf16_f32 v49, v49, v53\n"
        "v_mfma_f32_16x16x32_bf16 v[84:87], v[100:103], a[116:119], v[84:87]\n"
        "v_cvt_pk_bf16_f32 v50, v50, v54\n"
        "v_cvt_pk_bf16_f32 v51, v51, v55\n"
        "v_mfma_f32_16x16x32_bf16 v[88:91], v[100:103], a[124:127], v[88:91]\n"
        "v_mfma_f32_16x16x32_bf16 v[84:87], v[92:95], a[128:131], v[84:87]\n"
        "v_mfma_f32_16x16x32_bf16 v[88:91], v[92:95], a[136:139], v[88:91]\n"
        "v_mfma_f32_16x16x32_bf16 v[84:87], v[104:107], a[132:135], v[84:87]\n"
        "buffer_store_dwordx4 v[48:51], v124, s[36:39], s54 offen offset:1024 sc1\n"
        "v_mfma_f32_16x16x32_bf16 v[88:91], v[104:107], a[140:143], v[88:91]\n"
        "v_mfma_f32_16x16x32_bf16 v[56:59], v[32:35], v[92:95], v[56:59]\n"
        "v_mfma_f32_16x16x32_bf16 v[60:63], v[40:43], v[92:95], v[60:63]\n"
        "v_cvt_pk_bf16_f32 v84, v84, v85\n"
        "v_cvt_pk_bf16_f32 v85, v86, v87\n"
        "v_mfma_f32_16x16x32_bf16 v[56:59], v[16:19], v[96:99], v[56:59]\n"
        "v_cvt_pk_bf16_f32 v86, v88, v89\n"
        "v_cvt_pk_bf16_f32 v87, v90, v91\n"
        "v_mfma_f32_16x16x32_bf16 v[60:63], v[24:27], v[96:99], v[60:63]\n"
        "v_mfma_f32_16x16x32_bf16 v[56:59], v[20:23], v[100:103], v[56:59]\n"
        "v_mov_b64 v[92:93], 0\n"
        "v_mov_b64 v[94:95], 0\n"
        "v_mfma_f32_16x16x32_bf16 v[60:63], v[28:31], v[100:103], v[60:63]\n"
        "v_mov_b64 v[96:97], 0\n"
        "v_mov_b64 v[98:99], 0\n"
        "v_mfma_f32_16x16x32_bf16 v[56:59], v[36:39], v[104:107], v[56:59]\n"
        "buffer_store_dwordx4 v[84:87], v124, s[36:39], s54 offen offset:2048 sc1\n"
        "v_mfma_f32_16x16x32_bf16 v[60:63], v[44:47], v[104:107], v[60:63]\n"
        "s_nop 7\n"
        "v_cvt_pk_bf16_f32 v100, v56, v60\n"
        "v_cvt_pk_bf16_f32 v101, v57, v61\n"
        "v_cvt_pk_bf16_f32 v102, v58, v62\n"
        "v_cvt_pk_bf16_f32 v103, v59, v63\n"
        "buffer_store_dwordx4 v[100:103], v124, s[36:39], s54 offen offset:0 sc1\n"
        "s_waitcnt vmcnt(3)\n"
        "v_cmp_ge_u32 vcc, s31, v140\n"
        "s_and_b32 vcc_lo, vcc_lo, 0x1ff\n"
        "s_cbranch_scc0 L_xl_3%=\n"
        "s_branch L_xl2_3%=\n"
        "L_xl1_3%=:\n"
        "s_add_u32 s63, s63, 1\n"
        "s_cmp_gt_u32 s63, 0x40000\n"
        "s_cbranch_scc1 L_abort%=\n"
        "s_sleep 4\n"
        "L_xl2_3%=:\n"
        "v_mov_b32 v140, 0\n"
        "buffer_atomic_add v140, v143, s[36:39], s57 offen sc0\n"
        "s_waitcnt vmcnt(0)\n"
        "v_cmp_ge_u32 vcc, s31, v140\n"
        "s_and_b32 vcc_lo, vcc_lo, 0x1ff\n"
        "s_cbranch_scc1 L_xl1_3%=\n"
        "L_xl_3%=:\n"
        "s_barrier\n"
        "s_add_u32 s69, s58, s82\n"
        "s_sub_u32 s72, s58, s82\n"
        "s_add_u32 s87, s58, 5120\n"
        "buffer_load_dwordx4 v[64:67], v124, s[36:39], s69 offen offset:0 sc1\n"
        "buffer_load_dwordx4 v[68:71], v124, s[36:39], s69 offen offset:1024 sc1\n"
        "buffer_load_dwordx4 v[72:75], v124, s[36:39], s72 offen offset:2048 sc1\n"
        "buffer_load_dwordx4 v[76:79], v124, s[36:39], s72 offen offset:3072 sc1\n"
        "buffer_load_dwordx4 v[80:83], v124, s[36:39], s87 offen offset:0 sc1\n"
        "s_mov_b32 s81, 64\n"
        "buffer_load_dword v107, off, s[36:39], s81 sc1\n"
        "s_memrealtime s[26:27]\n"
        "s_waitcnt vmcnt(6)\n"
        "s_waitcnt lgkmcnt(0)\n"
        "s_barrier\n"
        "v_lshl_or_b32 v20, s26, 1, 1\n"
        "s_add_u32 s29, s29, 1024\n"
        "s_add_u32 s80, s29, s30\n"
        "s_mov_b64 exec, 1\n"
        "buffer_atomic_umax v20, off, s[36:39], s80\n"
        "s_mov_b64 exec, -1\n"
        "s_waitcnt vmcnt(1)\n"
        "L_par_3%=:\n"
        "v_readfirstlane_b32 s80, v107\n"
        "s_nop 0\n"
        "s_cmp_lt_i32 s80, 0\n"
        "s_cbranch_scc1 L_par2_3%=\n"
        "s_add_u32 s63, s63, 1\n"
        "s_cmp_gt_u32 s63, 0x40000\n"
        "s_cbranch_scc1 L_abort%=\n"
        "s_sleep 4\n"
        "buffer_load_dword v107, off, s[36:39], s81 sc1\n"
        "s_waitcnt vmcnt(0)\n"
        "s_branch L_par_3%=\n"
        "L_par2_3%=:\n"
        "s_bfe_u32 s46, s80, 0x10018\n"
        "s_lshl_b32 s46, s46, 21\n"
        "s_mov_b32 s53, 0x200000\n"
        "s_bfe_u32 s84, s80, 0x80010\n"
        "s_sub_u32 s84, 255, s84\n"
        "s_bfe_u32 s76, s80, 0x80008\n"
        "s_sub_u32 s76, s76, 32\n"
        "s_max_i32 s76, s76, 66\n"
        "s_bfe_u32 s77, s80, 0x80000\n"
        "s_sub_u32 s77, s77, 158\n"
        "s_bitset0_b32 s49, 30\n"
        "s_bitset0_b32 s50, 30\n"
        "s_bitset0_b32 s56, 30\n"
        "s_bitset0_b32 s64, 30\n"
        "v_mov_b32 v140, 0\n"
        "s_mov_b32 s28, 1000\n"
        "s_branch L_xok_3%=\n"
        "L_x2_3%=:\n"
        "s_add_u32 s69, s58, s82\n"
        "s_sub_u32 s72, s58, s82\n"
        "s_add_u32 s87, s58, 5120\n"
        "buffer_load_dwordx4 v[64:67], v124, s[36:39], s69 offen offset:0 sc1\n"
        "buffer_load_dwordx4 v[68:71], v124, s[36:39], s69 offen offset:1024 sc1\n"
        "buffer_load_dwordx4 v[72:75], v124, s[36:39], s72 offen offset:2048 sc1\n"
        "buffer_load_dwordx4 v[76:79], v124, s[36:39], s72 offen offset:3072 sc1\n"
        "buffer_load_dwordx4 v[80:83], v124, s[36:39], s87 offen offset:0 sc1\n"
        "s_waitcnt vmcnt(0)\n"
        "s_memrealtime s[26:27]\n"
        "s_waitcnt lgkmcnt(0)\n"
        "s_add_u32 s28, s28, s26\n"
        "L_xok_3%=:\n"
        "v_mfma_f32_16x16x32_bf16 v[84:87], v[64:67], a[0:3], 0\n"
        "v_mfma_f32_16x16x32_bf16 v[88:91], v[64:67], a[8:11], 0\n"
        "v_mfma_f32_16x16x32_bf16 v[56:59], a[144:147], v[64:67], 0\n"
        "s_branch L_win_3%=\n"
        "L_retry_3%=:\n"
        "s_add_u32 s63, s63, 1\n"
        "s_cmp_gt_u32 s63, 0x40000\n"
        "s_cbranch_scc1 L_abort%=\n"
        "s_sleep 2\n"
        "s_waitcnt vmcnt(0)\n"
        "s_waitcnt lgkmcnt(0)\n"
        "buffer_load_dwordx4 v[80:83], v138, s[36:39], s50 offen offset:0 sc1\n"
        "buffer_load_dwordx4 v[64:67], v138, s[36:39], s49 offen offset:0 sc1\n"
        "buffer_load_dwordx4 v[68:71], v138, s[36:39], s49 offen offset:256 sc1\n"
        "buffer_load_dwordx4 v[72:75], v138, s[36:39], s65 offen offset:0 sc1\n"
        "buffer_load_dwordx4 v[76:79], v138, s[36:39], s65 offen offset:256 sc1\n"
        "s_waitcnt vmcnt(4)\n"
        "v_bitop3_b32 v123, 32, v80, v82 bitop3:0x80\n"
        "v_add_u32_dpp v80, v81, v80 row_half_mirror row_mask:0xf bank_mask:0xf\n"
        "v_add_u32_dpp v82, v83, v82 row_half_mirror row_mask:0xf bank_mask:0xf\n"
        "v_cvt_f32_i32 v80, v80\n"
        "v_cvt_f32_i32 v82, v82\n"
        "v_cvt_pk_bf16_f32 v52, v80, v82\n"
        "ds_write_b32 v137, v52\n"
        "s_waitcnt vmcnt(3)\n"
        "v_bitop3_b32 v123, v123, v64, v66 bitop3:0x80\n"
        "v_cvt_f32_i32 v64, v64\n"
        "v_cvt_f32_i32 v65, v65\n"
        "v_cvt_f32_i32 v66, v66\n"
        "v_cvt_f32_i32 v67, v67\n"
        "v_cvt_pk_bf16_f32 v52, v64, v65\n"
        "v_cvt_pk_bf16_f32 v53, v66, v67\n"
        "ds_write_b64 v126, v[52:53] offset:0\n"
        "s_waitcnt vmcnt(2)\n"
        "v_bitop3_b32 v123, v123, v68, v70 bitop3:0x80\n"
        "v_cvt_f32_i32 v68, v68\n"
        "v_cvt_f32_i32 v69, v69\n"
        "v_cvt_f32_i32 v70, v70\n"
        "v_cvt_f32_i32 v71, v71\n"
        "v_cvt_pk_bf16_f32 v54, v68, v69\n"
        "v_cvt_pk_bf16_f32 v55, v70, v71\n"
        "ds_write_b64 v126, v[54:55] offset:512\n"
        "s_waitcnt vmcnt(1)\n"
        "v_bitop3_b32 v123, v123, v72, v74 bitop3:0x80\n"
        "v_cvt_f32_i32 v72, v72\n"
        "v_cvt_f32_i32 v73, v73\n"
        "ds_read_b128 v[48:51], v124 offset:6144\n"
        "v_cvt_f32_i32 v74, v74\n"
        "v_cvt_f32_i32 v75, v75\n"
        "v_cvt_pk_bf16_f32 v52, v72, v73\n"
        "v_cvt_pk_bf16_f32 v53, v74, v75\n"
        "ds_write_b64 v126, v[52:53] offset:1024\n"
        "s_waitcnt vmcnt(0)\n"
        "v_bitop3_b32 v123, v123, v76, v78 bitop3:0x80\n"
        "v_cvt_f32_i32 v76, v76\n"
        "v_cvt_f32_i32 v77, v77\n"
        "v_cvt_f32_i32 v78, v78\n"
        "v_cvt_f32_i32 v79, v79\n"
        "v_cvt_pk_bf16_f32 v54, v76, v77\n"
        "v_cvt_pk_bf16_f32 v55, v78, v79\n"
        "ds_write_b64 v126, v[54:55] offset:1536\n"
        "v_cmp_eq_u32 vcc, 0, v123\n"
        "v_mov_b64 v[122:123], 0\n"
        "v_mov_b64 v[92:93], 0\n"
        "v_mov_b64 v[94:95], 0\n"
        "v_mov_b64 v[96:97], 0\n"
        "v_mov_b64 v[98:99], 0\n"
        "s_waitcnt lgkmcnt(0)\n"
        "s_branch L_dec_3%=\n"
        "L_sw0_3%=:\n"
        "s_memrealtime s[26:27]\n"
        "s_waitcnt lgkmcnt(0)\n"
        "s_sub_u32 s28, s28, s26\n"
        "L_sw_3%=:\n"
        "s_add_u32 s63, s63, 1\n"
        "s_cmp_gt_u32 s63, 0x40000\n"
        "s_cbranch_scc1 L_abort%=\n"
        "s_sleep 2\n"
        "buffer_load_dword v140, v124, s[36:39], s29 offen sc1\n"
        "s_waitcnt vmcnt(0)\n"
        "v_cmp_eq_u32 vcc, 0, v140\n"
        "s_cbranch_vccnz L_sw_3%=\n"
        "s_memrealtime s[26:27]\n"
        "s_waitcnt lgkmcnt(0)\n"
        "s_add_u32 s28, s28, s26\n"
        "s_branch L_sok_3%=\n"
        "L_xs_3%=:\n"
        "s_branch L_xs2_3%=\n"
        "L_xs1_3%=:\n"
        "s_add_u32 s63, s63, 1\n"
        "s_cmp_gt_u32 s63, 0x40000\n"
        "s_cbranch_scc1 L_abort%=\n"
        "s_sleep 4\n"
        "L_xs2_3%=:\n"
        "v_mov_b32 v140, 0\n"
        "buffer_atomic_add v140, v143, s[36:39], s57 offen sc0\n"
        "s_waitcnt vmcnt(0)\n"
        "v_cmp_ge_u32 vcc, s31, v140\n"
        "s_and_b32 vcc_lo, vcc_lo, 0x1ff\n"
        "s_cbranch_scc1 L_xs1_3%=\n"
        "s_branch L_xr_3%=\n"
        ".p2align 6\n"
        "L_rot_3%=:\n"
        "v_mfma_f32_16x16x32_bf16 v[48:51], v[196:199], v[100:103], v[48:51]\n"
        "v_mfma_f32_16x16x32_bf16 v[52:55], v[204:207], v[100:103], v[52:55]\n"
        "s_waitcnt lgkmcnt(10)\n"
        "v_mfma_f32_16x16x32_bf16 v[84:87], v[92:95], a[96:99], v[84:87]\n"
        "ds_read_b128 v[100:103], v124 offset:3072\n"
        "v_mfma_f32_16x16x32_bf16 v[88:91], v[92:95], a[104:107], v[88:91]\n"
        "v_mfma_f32_16x16x32_bf16 v[56:59], v[0:3], v[92:95], v[56:59]\n"
        "v_mfma_f32_16x16x32_bf16 v[60:63], v[8:11], v[92:95], v[60:63]\n"
        "v_mfma_f32_16x16x32_bf16 v[48:51], v[208:211], v[92:95], v[48:51]\n"
        "s_waitcnt lgkmcnt(0)\n"
        "s_barrier\n"
        "v_mfma_f32_16x16x32_bf16 v[52:55], v[216:219], v[92:95], v[52:55]\n"
        "v_mov_b64 v[116:117], 0\n"
        "v_mfma_f32_16x16x32_bf16 v[84:87], v[104:107], a[100:103], v[84:87]\n"
        "ds_read_b128 v[92:95], v124 offset:16384\n"
        "v_mov_b32 v140, 0\n"
        "v_mfma_f32_16x16x32_bf16 v[88:91], v[104:107], a[108:111], v[88:91]\n"
        "buffer_atomic_add v140, v143, s[36:39], s57 offen sc0\n"
        "v_mov_b64 v[118:119], 0\n"
        "v_mfma_f32_16x16x32_bf16 v[56:59], v[4:7], v[104:107], v[56:59]\n"
        "v_mov_b64 v[120:121], 0\n"
        "v_mfma_f32_16x16x32_bf16 v[60:63], v[12:15], v[104:107], v[60:63]\n"
        "v_mfma_f32_16x16x32_bf16 v[48:51], v[212:215], v[104:107], v[48:51]\n"
        "v_mfma_f32_16x16x32_bf16 v[52:55], v[220:223], v[104:107], v[52:55]\n"
        "v_mfma_f32_16x16x32_bf16 v[48:51], v[224:227], v[96:99], v[48:51]\n"
        "ds_read_b128 v[104:107], v124 offset:17408\n"
        "v_mfma_f32_16x16x32_bf16 v[52:55], v[232:235], v[96:99], v[52:55]\n"
        "v_mfma_f32_16x16x32_bf16 v[48:51], v[228:231], v[100:103], v[48:51]\n"
        "v_mfma_f32_16x16x32_bf16 v[52:55], v[236:239], v[100:103], v[52:55]\n"
        "s_waitcnt lgkmcnt(1)\n"
        "v_mfma_f32_16x16x32_bf16 v[48:51], v[240:243], v[92:95], v[48:51]\n"
        "v_mfma_f32_16x16x32_bf16 v[52:55], v[248:251], v[92:95], v[52:55]\n"
        "s_waitcnt lgkmcnt(0)\n"
        "v_mfma_f32_16x16x32_bf16 v[48:51], v[244:247], v[104:107], v[48:51]\n"
        "s_cmp_lt_i32 s44, 0\n"
        "s_cbranch_scc1 L_light_3%=\n"
        "v_mfma_f32_16x16x32_bf16 v[52:55], v[252:255], v[104:107], v[52:55]\n"
        "s_waitcnt vmcnt(5)\n"
        "v_bitop3_b32 v123, 32, v80, v82 bitop3:0x80\n"
        "s_nop 0\n"
        "v_add_u32_dpp v80, v81, v80 row_half_mirror row_mask:0xf bank_mask:0xf\n"
        "v_mfma_f32_16x16x32_bf16 v[84:87], v[96:99], a[112:115], v[84:87]\n"
        "s_nop 0\n"
        "v_add_u32_dpp v82, v83, v82 row_half_mirror row_mask:0xf bank_mask:0xf\n"
        "v_mfma_f32_16x16x32_bf16 v[88:91], v[96:99], a[120:123], v[88:91]\n"
        "v_cvt_pk_bf16_f32 v48, v48, v52\n"
        "v_cvt_pk_bf16_f32 v49, v49, v53\n"
        "s_nop 0\n"
        "v_mfma_f32_16x16x32_bf16 v[84:87], v[100:103], a[116:119], v[84:87]\n"
        "v_cvt_pk_bf16_f32 v50, v50, v54\n"
        "v_cvt_pk_bf16_f32 v51, v51, v55\n"
        "v_mfma_f32_16x16x32_bf16 v[88:91], v[100:103], a[124:127], v[88:91]\n"
        "v_cvt_f32_i32 v80, v80\n"
        "s_nop 0\n"
        "v_cvt_f32_i32 v82, v82\n"
        "v_mfma_f32_16x16x32_bf16 v[84:87], v[92:95], a[128:131], v[84:87]\n"
        "s_nop 0\n"
        "v_cvt_pk_bf16_f32 v52, v80, v82\n"
        "v_mfma_f32_16x16x32_bf16 v[88:91], v[92:95], a[136:139], v[88:91]\n"
        "s_nop 0\n"
        "v_mfma_f32_16x16x32_bf16 v[84:87], v[104:107], a[132:135], v[84:87]\n"
        "buffer_store_dwordx4 v[48:51], v124, s[36:39], s54 offen offset:1024 sc1\n"
        "v_mfma_f32_16x16x32_bf16 v[88:91], v[104:107], a[140:143], v[88:91]\n"
        "ds_write_b32 v137, v52\n"
        "s_waitcnt vmcnt(5)\n"
        "v_bitop3_b32 v123, v123, v64, v66 bitop3:0x80\n"
        "v_cvt_f32_i32 v64, v64\n"
        "v_mfma_f32_16x16x32_bf16 v[56:59], v[32:35], v[92:95], v[56:59]\n"
        "v_cvt_f32_i32 v65, v65\n"
        "v_cvt_f32_i32 v66, v66\n"
        "v_mfma_f32_16x16x32_bf16 v[60:63], v[40:43], v[92:95], v[60:63]\n"
        "v_cvt_pk_bf16_f32 v84, v84, v85\n"
        "v_cvt_pk_bf16_f32 v85, v86, v87\n"
        "v_mfma_f32_16x16x32_bf16 v[56:59], v[16:19], v[96:99], v[56:59]\n"
        "v_cvt_pk_bf16_f32 v86, v88, v89\n"
        "v_cvt_pk_bf16_f32 v87, v90, v91\n"
        "v_mfma_f32_16x16x32_bf16 v[60:63], v[24:27], v[96:99], v[60:63]\n"
        "v_cvt_f32_i32 v67, v67\n"
        "v_cvt_pk_bf16_f32 v52, v64, v65\n"
        "v_mfma_f32_16x16x32_bf16 v[56:59], v[20:23], v[100:103], v[56:59]\n"
        "v_mov_b64 v[92:93], 0\n"
        "v_mov_b64 v[94:95], 0\n"
        "v_mfma_f32_16x16x32_bf16 v[60:63], v[28:31], v[100:103], v[60:63]\n"
        "v_mov_b64 v[96:97], 0\n"
        "v_mov_b64 v[98:99], 0\n"
        "v_mfma_f32_16x16x32_bf16 v[56:59], v[36:39], v[104:107], v[56:59]\n"
        "buffer_store_dwordx4 v[84:87], v124, s[36:39], s54 offen offset:2048 sc1\n"
        "v_cvt_pk_bf16_f32 v53, v66, v67\n"
        "v_mfma_f32_16x16x32_bf16 v[60:63], v[44:47], v[104:107], v[60:63]\n"
        "ds_write_b64 v126, v[52:53] offset:0\n"
        "s_waitcnt vmcnt(5)\n"
        "v_bitop3_b32 v123, v123, v68, v70 bitop3:0x80\n"
        "v_cvt_f32_i32 v68, v68\n"
        "v_cvt_f32_i32 v69, v69\n"
        "v_cvt_f32_i32 v70, v70\n"
        "v_cvt_f32_i32 v71, v71\n"
        "v_cvt_pk_bf16_f32 v54, v68, v69\n"
        "v_cvt_pk_bf16_f32 v55, v70, v71\n"
        "ds_write_b64 v126, v[54:55] offset:512\n"
        "s_waitcnt vmcnt(4)\n"
        "v_bitop3_b32 v123, v123, v72, v74 bitop3:0x80\n"
        "v_cvt_pk_bf16_f32 v100, v56, v60\n"
        "v_cvt_pk_bf16_f32 v101, v57, v61\n"
        "v_cvt_pk_bf16_f32 v102, v58, v62\n"
        "v_cvt_pk_bf16_f32 v103, v59, v63\n"
        "v_cvt_f32_i32 v72, v72\n"
        "v_cvt_f32_i32 v73, v73\n"
        "ds_read_b128 v[48:51], v124 offset:6144\n"
        "v_cvt_f32_i32 v74, v74\n"
        "v_cvt_f32_i32 v75, v75\n"
        "v_cvt_pk_bf16_f32 v52, v72, v73\n"
        "v_cvt_pk_bf16_f32 v53, v74, v75\n"
        "ds_write_b64 v126, v[52:53] offset:1024\n"
        "s_waitcnt vmcnt(3)\n"
        "v_bitop3_b32 v123, v123, v76, v78 bitop3:0x80\n"
        "v_cvt_f32_i32 v76, v76\n"
        "v_cvt_f32_i32 v77, v77\n"
        "v_cvt_f32_i32 v78, v78\n"
        "v_cvt_f32_i32 v79, v79\n"
        "v_cvt_pk_bf16_f32 v54, v76, v77\n"
        "v_cvt_pk_bf16_f32 v55, v78, v79\n"
        "ds_write_b64 v126, v[54:55] offset:1536\n"
        "v_cmp_eq_u32 vcc, 0, v123\n"
        "v_mov_b64 v[122:123], 0\n"
        "L_dec_3%=:\n"
        "s_waitcnt lgkmcnt(2)\n"
        "v_smfmac_f32_16x16x64_bf16 v[92:95], v[48:51], a[144:151], v134\n"
        "ds_read_b128 v[52:55], v124 offset:7168\n"
        "v_max3_f32 v104, |v80|, |v82|, |v64|\n"
        "v_smfmac_f32_16x16x64_bf16 v[96:99], v[48:51], a[152:159], v134\n"
        "s_cbranch_vccnz L_retry_3%=\n"
        "v_max3_f32 v104, |v65|, |v66|, v104\n"
        "v_smfmac_f32_16x16x64_bf16 v[116:119], v[48:51], a[240:247], v134\n"
        "s_add_u32 s29, s29, 1024\n"
        "s_add_u32 s80, s29, s30\n"
        "v_max3_f32 v104, |v67|, |v68|, v104\n"
        "v_smfmac_f32_16x16x64_bf16 v[120:123], v[48:51], a[248:255], v134\n"
        "s_waitcnt lgkmcnt(0)\n"
        "v_max3_f32 v104, |v69|, |v70|, v104\n"
        "s_barrier\n"
        "ds_read_b128 v[56:59], v124 offset:0\n"
        "v_smfmac_f32_16x16x64_bf16 v[92:95], v[52:55], a[160:167], v134\n"
        "ds_read_b128 v[60:63], v124 offset:3072\n"
        "v_max3_f32 v104, |v71|, |v72|, v104\n"
        "v_smfmac_f32_16x16x64_bf16 v[96:99], v[52:55], a[168:175], v134\n"
        "ds_read_b128 v[80:83], v124 offset:1024\n"
        "v_max3_f32 v104, |v73|, |v74|, v104\n"
        "v_smfmac_f32_16x16x64_bf16 v[116:119], v[52:55], v[176:183], v134\n"
        "buffer_load_dwordx4 v[88:91], v124, s[36:39], s55 offen offset:3072 sc1\n"
        "ds_read_b128 v[64:67], v124 offset:8192\n"
        "v_max3_f32 v104, |v75|, |v76|, v104\n"
        "v_smfmac_f32_16x16x64_bf16 v[120:123], v[52:55], v[184:191], v134\n"
        "ds_read_b128 v[68:71], v124 offset:4096\n"
        "v_max3_f32 v104, |v77|, |v78|, v104\n"
        "s_waitcnt lgkmcnt(4)\n"
        "v_smfmac_f32_16x16x64_bf16 v[92:95], v[56:59], v[0:7], v134\n"
        "buffer_load_dwordx4 v[84:87], v124, s[36:39], s55 offen offset:0 sc1\n"
        "ds_read_b128 v[72:75], v124 offset:5120\n"
        "v_max_f32_e64 v104, |v79|, v104\n"
        "v_smfmac_f32_16x16x64_bf16 v[96:99], v[56:59], v[8:15], v134\n"
        "ds_read_b128 v[0:3], v125 offset:12288\n"
        "v_max_f32_dpp v104, v104, v104 row_shr:1 row_mask:0xf bank_mask:0xf\n"
        "s_waitcnt lgkmcnt(3)\n"
        "v_smfmac_f32_16x16x64_bf16 v[92:95], v[60:63], a[224:231], v134\n"
        "ds_read_b128 v[76:79], v124 offset:2048\n"
        "v_max_f32_dpp v104, v104, v104 row_shr:2 row_mask:0xf bank_mask:0xf\n"
        "v_smfmac_f32_16x16x64_bf16 v[96:99], v[60:63], a[232:239], v134\n"
        "ds_read_b128 v[4:7], v125 offset:13312\n"
        "v_max_f32_dpp v104, v104, v104 row_shr:4 row_mask:0xf bank_mask:0xf\n"
        "v_smfmac_f32_16x16x64_bf16 v[92:95], v[80:83], v[16:23], v134\n"
        "ds_read_b128 v[8:11], v125 offset:14336\n"
        "v_max_f32_dpp v104, v104, v104 row_shr:8 row_mask:0xf bank_mask:0xf\n"
        "v_smfmac_f32_16x16x64_bf16 v[96:99], v[80:83], v[24:31], v134\n"
        "ds_read_b128 v[12:15], v125 offset:15360\n"
        "v_max_f32_dpp v104, v104, v104 row_bcast:15 row_mask:0xa bank_mask:0xf\n"
        "v_smfmac_f32_16x16x64_bf16 v[92:95], v[64:67], v[32:39], v134\n"
        "ds_read_b128 v[16:19], v125 offset:16384\n"
        "v_max_f32_dpp v104, v104, v104 row_bcast:31 row_mask:0xc bank_mask:0xf\n"
        "v_smfmac_f32_16x16x64_bf16 v[96:99], v[64:67], v[40:47], v134\n"
        "ds_read_b128 v[20:23], v125 offset:17408\n"
        "v_readlane_b32 s85, v104, 63\n"
        "s_waitcnt lgkmcnt(5)\n"
        "v_smfmac_f32_16x16x64_bf16 v[92:95], v[68:71], a[176:183], v134\n"
        "ds_read_b128 v[24:27], v125 offset:18432\n"
        "v_smfmac_f32_16x16x64_bf16 v[96:99], v[68:71], a[184:191], v134\n"
        "ds_read_b128 v[28:31], v125 offset:19456\n"
        "v_mov_b32 v104, s85\n"
        "v_smfmac_f32_16x16x64_bf16 v[92:95], v[72:75], a[192:199], v134\n"
        "ds_read_b128 v[32:35], v125 offset:20480\n"
        "v_smfmac_f32_16x16x64_bf16 v[96:99], v[72:75], a[200:207], v134\n"
        "ds_read_b128 v[36:39], v125 offset:21504\n"
        "s_add_u32 s69, s58, s82\n"
        "v_smfmac_f32_16x16x64_bf16 v[92:95], v[76:79], a[208:215], v134\n"
        "ds_read_b128 v[40:43], v125 offset:22528\n"
        "s_sub_u32 s72, s58, s82\n"
        "v_smfmac_f32_16x16x64_bf16 v[96:99], v[76:79], a[216:223], v134\n"
        "ds_read_b128 v[44:47], v125 offset:23552\n"
        "s_add_u32 s87, s58, 5120\n"
        "v_smfmac_f32_16x16x64_bf16 v[116:119], v[64:67], v[240:247], v134\n"
        "ds_write_b32 v142, v104\n"
        "v_add_f32 v92, v92, v94\n"
        "v_add_f32 v93, v93, v95\n"
        "v_smfmac_f32_16x16x64_bf16 v[120:123], v[64:67], v[248:255], v134\n"
        "v_add_f32 v96, v96, v98\n"
        "v_add_f32 v97, v97, v99\n"
        "v_cvt_pk_bf16_f32 v92, v92, v96\n"
        "v_cvt_pk_bf16_f32 v93, v93, v97\n"
        "s_waitcnt lgkmcnt(3)\n"
        "v_smfmac_f32_16x16x64_bf16 v[116:119], v[68:71], v[0:7], v134\n"
        "ds_write2_b32 v127, v92, v93 offset1:72\n"
        "s_waitcnt vmcnt(4)\n"
        "v_cmp_ge_u32 vcc, s31, v140\n"
        "v_smfmac_f32_16x16x64_bf16 v[120:123], v[68:71], v[8:15], v134\n"
        "s_and_b32 vcc_lo, vcc_lo, 0x1ff\n"
        "s_cbranch_scc1 L_xs_3%=\n"
        "L_xr_3%=:\n"
        "s_memrealtime s[26:27]\n"
        "v_smfmac_f32_16x16x64_bf16 v[116:119], v[72:75], v[16:23], v134\n"
        "v_mov_b64 v[96:97], 0\n"
        "v_mov_b64 v[106:107], 0\n"
        "v_smfmac_f32_16x16x64_bf16 v[120:123], v[72:75], v[24:31], v134\n"
        "s_waitcnt vmcnt(3)\n"
        "s_waitcnt lgkmcnt(0)\n"
        "v_lshl_or_b32 v20, s26, 1, 1\n"
        "s_barrier\n"
        "ds_read_b128 v[0:3], v128 offset:0\n"
        "v_smfmac_f32_16x16x64_bf16 v[116:119], v[76:79], v[32:39], v134\n"
        "s_mov_b64 exec, 1\n"
        "buffer_atomic_umax v20, off, s[36:39], s80\n"
        "s_mov_b64 exec, -1\n"
        "ds_read_b128 v[24:27], v124 offset:20480\n"
        "v_mov_b64 v[104:105], 0\n"
        "v_smfmac_f32_16x16x64_bf16 v[120:123], v[76:79], v[40:47], v134\n"
        "ds_read_b128 v[4:7], v128 offset:64\n"
        "v_mov_b64 v[94:95], 0\n"
        "v_smfmac_f32_16x16x64_bf16 v[116:119], v[60:63], v[192:199], v134\n"
        "ds_read_b128 v[8:11], v128 offset:128\n"
        "v_mov_b64 v[92:93], 0\n"
        "v_smfmac_f32_16x16x64_bf16 v[120:123], v[60:63], v[200:207], v134\n"
        "ds_read_b128 v[12:15], v128 offset:192\n"
        "v_mov_b64 v[20:21], 0\n"
        "v_smfmac_f32_16x16x64_bf16 v[116:119], v[56:59], v[208:215], v134\n"
        "s_waitcnt lgkmcnt(3)\n"
        "v_max3_f32 v24, v24, v25, v26\n"
        "v_mov_b64 v[98:99], 0\n"
        "s_waitcnt lgkmcnt(2)\n"
        "v_smfmac_f32_16x16x64_bf16 v[104:107], v[108:111], v[0:7], v134\n"
        "v_max_f32 v24, v24, v27\n"
        "v_smfmac_f32_16x16x64_bf16 v[120:123], v[56:59], v[216:223], v134\n"
        "v_readfirstlane_b32 s84, v24\n"
        "s_waitcnt lgkmcnt(0)\n"
        "v_smfmac_f32_16x16x64_bf16 v[104:107], v[112:115], v[8:15], v134\n"
        "v_mov_b64 v[22:23], 0\n"
        "v_smfmac_f32_16x16x64_bf16 v[116:119], v[80:83], v[224:231], v134\n"
        "s_lshr_b32 s84, s84, 23\n"
        "s_add_u32 s84, s84, s101\n"
        "v_smfmac_f32_16x16x64_bf16 v[120:123], v[80:83], v[232:239], v134\n"
        "s_max_i32 s84, s84, s76\n"
        "s_min_i32 s84, s84, 186\n"
        "v_add_f32 v104, v104, v106\n"
        "v_add_f32 v105, v105, v107\n"
        "v_smfmac_f32_16x16x64_bf16 v[92:95], v[64:67], a[128:135], v134\n"
        "ds_write_b64 v131, v[104:105]\n"
        "v_add_f32 v116, v116, v118\n"
        "v_add_f32 v117, v117, v119\n"
        "v_pk_add_f32 v[120:121], v[120:121], v[122:123]\n"
        "v_cvt_pk_bf16_f32 v116, v116, v120\n"
        "v_cvt_pk_bf16_f32 v117, v117, v121\n"
        "ds_write2_b32 v129, v116, v117 offset1:8\n"
        "v_smfmac_f32_16x16x64_bf16 v[96:99], v[64:67], a[136:143], v134\n"
        "s_mul_i32 s88, s84, 0x800080\n"
        "s_mov_b32 s89, s88\n"
        "v_smfmac_f32_16x16x64_bf16 v[92:95], v[68:71], a[32:39], v134\n"
        "s_add_u32 s90, s88, s35\n"
        "s_mov_b32 s91, s90\n"
        "v_mov_b64 v[104:105], s[88:89]\n"
        "v_smfmac_f32_16x16x64_bf16 v[96:99], v[68:71], a[40:47], v134\n"
        "buffer_store_dwordx4 v[100:103], v124, s[36:39], s54 offen offset:0 sc1\n"
        "v_mov_b64 v[106:107], s[90:91]\n"
        "v_smfmac_f32_16x16x64_bf16 v[92:95], v[48:51], a[0:7], v134\n"
        "s_waitcnt lgkmcnt(0)\n"
        "v_mov_b64 v[100:101], s[88:89]\n"
        "s_barrier\n"
        "ds_read_b128 v[24:27], v130\n"
        "v_smfmac_f32_16x16x64_bf16 v[96:99], v[48:51], a[8:15], v134\n"
        "ds_read_b128 v[28:31], v130 offset:1024\n"
        "v_mov_b64 v[102:103], s[90:91]\n"
        "v_smfmac_f32_16x16x64_bf16 v[92:95], v[52:55], a[16:23], v134\n"
        "ds_read_b128 v[116:119], v124 offset:13568\n"
        "v_mov_b64 v[48:49], 0\n"
        "v_smfmac_f32_16x16x64_bf16 v[96:99], v[52:55], a[24:31], v134\n"
        "v_mov_b64 v[50:51], 0\n"
        "v_smfmac_f32_16x16x64_bf16 v[92:95], v[56:59], a[96:103], v134\n"
        "buffer_load_dwordx4 v[64:67], v124, s[36:39], s69 offen offset:0 sc1\n"
        "v_mov_b64 v[52:53], 0\n"
        "v_smfmac_f32_16x16x64_bf16 v[96:99], v[56:59], a[104:111], v134\n"
        "v_mov_b64 v[54:55], 0\n"
        "s_waitcnt lgkmcnt(2)\n"
        "v_smfmac_f32_16x16x64_bf16 v[20:23], v[24:27], v[0:7], v134\n"
        "buffer_load_dwordx4 v[68:71], v124, s[36:39], s69 offen offset:1024 sc1\n"
        "v_mov_b64 v[56:57], 0\n"
        "s_waitcnt lgkmcnt(0)\n"
        "v_smfmac_f32_16x16x64_bf16 v[20:23], v[28:31], v[8:15], v134\n"
        "ds_read_b128 v[0:3], v125 offset:24576\n"
        "v_max3_f32 v121, v116, v117, v118\n"
        "v_smfmac_f32_16x16x64_bf16 v[92:95], v[72:75], a[48:55], v134\n"
        "v_mov_b64 v[58:59], 0\n"
        "v_smfmac_f32_16x16x64_bf16 v[96:99], v[72:75], a[56:63], v134\n"
        "ds_read_b128 v[4:7], v125 offset:25600\n"
        "v_add_f32 v20, v20, v22\n"
        "v_add_f32 v21, v21, v23\n"
        "v_cndmask_b32 v120, v20, v21, s[92:93]\n"
        "v_mul_f32 v120, s74, v120\n"
        "v_max3_f32 v121, v121, v119, v120\n"
        "v_smfmac_f32_16x16x64_bf16 v[92:95], v[76:79], a[64:71], v134\n"
        "buffer_load_dwordx4 v[72:75], v124, s[36:39], s72 offen offset:2048 sc1\n"
        "v_max_f32_dpp v121, v121, v121 quad_perm:[2,3,0,1] row_mask:0xf bank_mask:0xf\n"
        "v_mov_b32 v122, v121\n"
        "v_smfmac_f32_16x16x64_bf16 v[96:99], v[76:79], a[72:79], v134\n"
        "ds_read_b128 v[8:11], v125 offset:26624\n"
        "v_permlane16_swap_b32 v121, v122\n"
        "v_max_f32 v121, v121, v122\n"
        "v_mov_b32 v122, v121\n"
        "v_smfmac_f32_16x16x64_bf16 v[92:95], v[80:83], a[112:119], v134\n"
        "buffer_load_dwordx4 v[76:79], v124, s[36:39], s72 offen offset:3072 sc1\n"
        "v_permlane32_swap_b32 v121, v122\n"
        "v_smfmac_f32_16x16x64_bf16 v[96:99], v[80:83], a[120:127], v134\n"
        "ds_read_b128 v[12:15], v125 offset:27648\n"
        "v_max_f32 v121, v121, v122\n"
        "v_mul_f32_e64 v123, -s73, v121\n"
        "v_fma_f32 v120, v120, s73, v123\n"
        "v_fma_f32 v116, v116, s73, v123\n"
        "v_smfmac_f32_16x16x64_bf16 v[92:95], v[60:63], a[80:87], v134\n"
        "buffer_load_dwordx4 v[80:83], v124, s[36:39], s87 offen offset:0 sc1\n"
        "v_fma_f32 v117, v117, s73, v123\n"
        "v_fma_f32 v118, v118, s73, v123\n"
        "v_fma_f32 v119, v119, s73, v123\n"
        "v_exp_f32 v120, v120\n"
        "v_smfmac_f32_16x16x64_bf16 v[96:99], v[60:63], a[88:95], v134\n"
        "ds_read_b128 v[16:19], v125 offset:28672\n"
        "v_exp_f32 v116, v116\n"
        "v_mul_f32 v120, s74, v120\n"
        "v_exp_f32 v117, v117\n"
        "v_exp_f32 v118, v118\n"
        "v_exp_f32 v119, v119\n"
        "s_waitcnt vmcnt(7)\n"
        "v_mov_b64 v[108:109], v[86:87]\n"
        "v_mov_b64 v[112:113], v[90:91]\n"
        "v_cvt_pk_bf16_f32 v120, v120, v120\n"
        "v_cvt_pk_bf16_f32 v60, v116, v117\n"
        "v_cvt_pk_bf16_f32 v62, v118, v119\n"
        "v_and_b32 v61, v120, v135\n"
        "v_and_b32 v63, v120, v136\n"
        "v_pk_add_f32 v[92:93], v[92:93], v[94:95]\n"
        "v_pk_add_f32 v[96:97], v[96:97], v[98:99]\n"
        "v_smfmac_f32_16x16x64_bf16 v[56:59], v[60:63], v[100:107], v134\n"
        "ds_read_b128 v[20:23], v125 offset:29696\n"
        "v_cvt_pk_bf16_f32 v86, v92, v93\n"
        "v_cvt_pk_bf16_f32 v87, v92, v93\n"
        "v_mov_b64 v[90:91], v[86:87]\n"
        "v_cvt_pk_bf16_f32 v110, v96, v97\n"
        "v_cvt_pk_bf16_f32 v111, v96, v97\n"
        "v_smfmac_f32_16x16x64_bf16 v[48:51], v[60:63], v[84:91], v134\n"
        "ds_read_b128 v[24:27], v125 offset:30720\n"
        "v_mov_b64 v[114:115], v[110:111]\n"
        "v_add_f32 v56, v56, v58\n"
        "v_add_f32 v57, v57, v59\n"
        "v_smfmac_f32_16x16x64_bf16 v[52:55], v[60:63], v[108:115], v134\n"
        "s_nop 1\n"
        "v_rcp_f32 v56, v56\n"
        "v_rcp_f32 v57, v57\n"
        "v_pk_add_f32 v[48:49], v[48:49], v[50:51]\n"
        "v_pk_mul_f32 v[48:49], v[48:49], v[56:57]\n"
        "v_mov_b32 v110, v86\n"
        "v_mov_b64 v[92:93], s[70:71]\n"
        "v_pk_add_f32 v[52:53], v[52:53], v[54:55]\n"
        "v_pk_mul_f32 v[52:53], v[52:53], v[56:57]\n"
        "v_cvt_pk_bf16_f32 v48, v48, v52\n"
        "v_cvt_pk_bf16_f32 v49, v49, v53\n"
        "ds_write2_b32 v139, v48, v49 offset1:4\n"
        "v_mov_b64 v[94:95], 0\n"
        "v_mov_b64 v[96:97], 0\n"
        "v_mov_b64 v[98:99], 0\n"
        "v_mov_b64 v[100:101], s[70:71]\n"
        "v_mov_b64 v[102:103], 0\n"
        "v_mov_b64 v[104:105], 0\n"
        "buffer_store_dwordx2 v[110:111], v126, s[36:39], s60 offen sc1\n"
        "s_waitcnt lgkmcnt(0)\n"
        "v_mov_b64 v[106:107], 0\n"
        "s_barrier\n"
        "ds_read_b128 v[108:111], v124 offset:18432\n"
        "v_mov_b64 v[106:107], 0\n"
        "ds_read_b128 v[112:115], v124 offset:19456\n"
        "v_mov_b64 v[106:107], 0\n"
        "s_bitcmp1_b32 s59, 30\n"
        "s_cbranch_scc1 L_s1_3%=\n"
        "buffer_store_dwordx4 v[28:31], v124, s[36:39], s59 offen offset:1024 sc1\n"
        "L_s1_3%=:\n"
        "v_mov_b64 v[106:107], 0\n"
        "v_mov_b64 v[106:107], 0\n"
        "ds_read_b128 v[28:31], v125 offset:31744\n"
        "v_mov_b64 v[106:107], 0\n"
        "s_waitcnt lgkmcnt(2)\n"
        "v_smfmac_f32_16x16x64_bf16 v[92:95], v[108:111], v[144:151], v134\n"
        "v_smfmac_f32_16x16x64_bf16 v[96:99], v[108:111], v[152:159], v134\n"
        "s_waitcnt lgkmcnt(1)\n"
        "v_smfmac_f32_16x16x64_bf16 v[92:95], v[112:115], v[160:167], v134\n"
        "v_smfmac_f32_16x16x64_bf16 v[96:99], v[112:115], v[168:175], v134\n"
        "v_smfmac_f32_16x16x64_bf16 v[100:103], v[108:111], v[0:7], v134\n"
        "v_add_f32 v92, v92, v94\n"
        "v_add_f32 v93, v93, v95\n"
        "v_cvt_rpi_i32_f32 v94, v92\n"
        "v_smfmac_f32_16x16x64_bf16 v[104:107], v[108:111], v[8:15], v134\n"
        "v_bfi_b32 v94, 63, 1, v94\n"
        "v_add_f32 v96, v96, v98\n"
        "v_smfmac_f32_16x16x64_bf16 v[100:103], v[112:115], v[16:23], v134\n"
        "buffer_load_dword v140, v124, s[36:39], s29 offen sc1\n"
        "v_cvt_rpi_i32_f32 v95, v96\n"
        "s_waitcnt lgkmcnt(0)\n"
        "v_smfmac_f32_16x16x64_bf16 v[104:107], v[112:115], v[24:31], v134\n"
        "buffer_atomic_add_x2 v[94:95], v133, s[36:39], s51 offen\n"
        "v_add_f32 v97, v97, v99\n"
        "v_cvt_rpi_i32_f32 v98, v93\n"
        "v_bfi_b32 v98, 63, 1, v98\n"
        "v_cvt_rpi_i32_f32 v99, v97\n"
        "s_waitcnt vmcnt(7)\n"
        "v_mfma_f32_16x16x32_bf16 v[84:87], v[64:67], a[0:3], 0\n"
        "buffer_atomic_add_x2 v[98:99], v133, s[36:39], s66 offen\n"
        "v_add_f32 v100, v100, v102\n"
        "v_add_f32 v101, v101, v103\n"
        "v_add_f32 v104, v104, v106\n"
        "v_cvt_rpi_i32_f32 v102, v100\n"
        "v_bfi_b32 v102, 63, 1, v102\n"
        "v_cvt_rpi_i32_f32 v103, v104\n"
        "v_mfma_f32_16x16x32_bf16 v[88:91], v[64:67], a[8:11], 0\n"
        "buffer_atomic_add_x2 v[102:103], v133, s[36:39], s67 offen\n"
        "v_add_f32 v105, v105, v107\n"
        "v_cvt_rpi_i32_f32 v106, v101\n"
        "v_bfi_b32 v106, 63, 1, v106\n"
        "v_cvt_rpi_i32_f32 v107, v105\n"
        "v_mfma_f32_16x16x32_bf16 v[56:59], a[144:147], v[64:67], 0\n"
        "buffer_atomic_add_x2 v[106:107], v133, s[36:39], s68 offen\n"
        "L_win_3%=:\n"
        "v_mfma_f32_16x16x32_bf16 v[60:63], a[152:155], v[64:67], 0\n"
        "v_mfma_f32_16x16x32_bf16 v[48:51], a[240:243], v[64:67], 0\n"
        "v_mfma_f32_16x16x32_bf16 v[52:55], a[248:251], v[64:67], 0\n"
        "ds_write_b128 v124, v[64:67] offset:12288\n"
        "s_waitcnt vmcnt(9)\n"
        "v_mfma_f32_16x16x32_bf16 v[84:87], v[68:71], a[4:7], v[84:87]\n"
        "v_mfma_f32_16x16x32_bf16 v[88:91], v[68:71], a[12:15], v[88:91]\n"
        "ds_write_b128 v124, v[68:71] offset:13312\n"
        "v_mfma_f32_16x16x32_bf16 v[56:59], a[148:151], v[68:71], v[56:59]\n"
        "v_mfma_f32_16x16x32_bf16 v[60:63], a[156:159], v[68:71], v[60:63]\n"
        "s_waitcnt vmcnt(8)\n"
        "ds_write_b128 v124, v[72:75] offset:14336\n"
        "v_mfma_f32_16x16x32_bf16 v[48:51], a[244:247], v[68:71], v[48:51]\n"
        "v_mfma_f32_16x16x32_bf16 v[52:55], a[252:255], v[68:71], v[52:55]\n"
        "s_waitcnt vmcnt(7)\n"
        "ds_write_b128 v124, v[76:79] offset:15360\n"
        "v_mfma_f32_16x16x32_bf16 v[84:87], v[72:75], a[16:19], v[84:87]\n"
        "v_mfma_f32_16x16x32_bf16 v[88:91], v[72:75], a[24:27], v[88:91]\n"
        "s_waitcnt vmcnt(6)\n"
        "ds_write_b128 v124, v[80:83] offset:17408\n"
        "v_mfma_f32_16x16x32_bf16 v[56:59], a[160:163], v[72:75], v[56:59]\n"
        "v_mfma_f32_16x16x32_bf16 v[60:63], a[168:171], v[72:75], v[60:63]\n"
        "v_mfma_f32_16x16x32_bf16 v[48:51], v[176:179], v[72:75], v[48:51]\n"
        "ds_read_b128 v[0:3], v125 offset:12288\n"
        "v_mfma_f32_16x16x32_bf16 v[52:55], v[184:187], v[72:75], v[52:55]\n"
        "ds_read_b128 v[8:11], v125 offset:14336\n"
        "v_mfma_f32_16x16x32_bf16 v[84:87], v[76:79], a[20:23], v[84:87]\n"
        "s_waitcnt lgkmcnt(2)\n"
        "s_barrier\n"
        "v_mfma_f32_16x16x32_bf16 v[88:91], v[76:79], a[28:31], v[88:91]\n"
        "ds_read_b128 v[92:95], v124 offset:8192\n"
        "v_mov_b32 v116, s62\n"
        "v_mfma_f32_16x16x32_bf16 v[56:59], a[164:167], v[76:79], v[56:59]\n"
        "s_add_u32 s49, s49, 163840\n"
        "ds_read_b128 v[104:107], v124 offset:9216\n"
        "v_mfma_f32_16x16x32_bf16 v[60:63], a[172:175], v[76:79], v[60:63]\n"
        "s_add_u32 s50, s50, 163840\n"
        "ds_read_b128 v[4:7], v125 offset:13312\n"
        "v_mfma_f32_16x16x32_bf16 v[48:51], v[180:183], v[76:79], v[48:51]\n"
        "s_xor_b32 s49, s49, s86\n"
        "ds_read_b128 v[12:15], v125 offset:15360\n"
        "v_mfma_f32_16x16x32_bf16 v[52:55], v[188:191], v[76:79], v[52:55]\n"
        "s_xor_b32 s65, s49, 8192\n"
        "s_add_u32 s56, s56, 786432\n"
        "s_waitcnt lgkmcnt(3)\n"
        "v_mfma_f32_16x16x32_bf16 v[84:87], v[92:95], a[32:35], v[84:87]\n"
        "s_add_u32 s64, s64, 786432\n"
        "ds_read_b128 v[96:99], v124 offset:10240\n"
        "v_mfma_f32_16x16x32_bf16 v[88:91], v[92:95], a[40:43], v[88:91]\n"
        "ds_read_b128 v[100:103], v124 offset:11264\n"
        "v_mfma_f32_16x16x32_bf16 v[56:59], a[176:179], v[92:95], v[56:59]\n"
        "ds_read_b128 v[16:19], v125 offset:16384\n"
        "v_mfma_f32_16x16x32_bf16 v[60:63], a[184:187], v[92:95], v[60:63]\n"
        "s_add_u32 s51, s51, 163840\n"
        "ds_read_b128 v[20:23], v125 offset:17408\n"
        "v_mfma_f32_16x16x32_bf16 v[48:51], v[0:3], v[92:95], v[48:51]\n"
        "s_add_u32 s83, s83, 163840\n"
        "ds_read_b128 v[24:27], v125 offset:18432\n"
        "v_mfma_f32_16x16x32_bf16 v[52:55], v[8:11], v[92:95], v[52:55]\n"
        "ds_read_b128 v[28:31], v125 offset:19456\n"
        "s_waitcnt lgkmcnt(8)\n"
        "v_mfma_f32_16x16x32_bf16 v[84:87], v[104:107], a[36:39], v[84:87]\n"
        "s_xor_b32 s86, s83, s46\n"
        "ds_read_b128 v[92:95], v124 offset:4096\n"
        "v_mfma_f32_16x16x32_bf16 v[88:91], v[104:107], a[44:47], v[88:91]\n"
        "s_and_b32 s86, s86, s53\n"
        "s_lshr_b32 s86, s86, 8\n"
        "s_xor_b32 s86, s86, s34\n"
        "v_mfma_f32_16x16x32_bf16 v[56:59], a[180:183], v[104:107], v[56:59]\n"
        "s_xor_b32 s34, s34, s86\n"
        "s_xor_b32 s51, s51, s86\n"
        "s_add_u32 s55, s55, 786432\n"
        "v_mfma_f32_16x16x32_bf16 v[60:63], a[188:191], v[104:107], v[60:63]\n"
        "s_add_u32 s54, s54, 786432\n"
        "ds_read_b128 v[32:35], v125 offset:20480\n"
        "s_waitcnt lgkmcnt(9)\n"
        "v_mfma_f32_16x16x32_bf16 v[48:51], v[4:7], v[104:107], v[48:51]\n"
        "s_waitcnt vmcnt(5)\n"
        "s_mov_b64 exec, 1\n"
        "buffer_atomic_umax v116, off, s[36:39], s61\n"
        "s_mov_b64 exec, -1\n"
        "s_waitcnt lgkmcnt(8)\n"
        "v_mfma_f32_16x16x32_bf16 v[52:55], v[12:15], v[104:107], v[52:55]\n"
        "ds_read_b128 v[36:39], v125 offset:21504\n"
        "s_waitcnt lgkmcnt(8)\n"
        "v_mfma_f32_16x16x32_bf16 v[84:87], v[96:99], a[48:51], v[84:87]\n"
        "s_add_u32 s59, s59, 131072\n"
        "ds_read_b128 v[104:107], v124 offset:5120\n"
        "v_mfma_f32_16x16x32_bf16 v[88:91], v[96:99], a[56:59], v[88:91]\n"
        "s_cmp_eq_u32 s44, 63\n"
        "s_cbranch_scc1 L_end%=\n"
        "v_mfma_f32_16x16x32_bf16 v[56:59], a[192:195], v[96:99], v[56:59]\n"
        "s_add_u32 s60, s60, 131072\n"
        "ds_read_b128 v[40:43], v125 offset:22528\n"
        "v_mfma_f32_16x16x32_bf16 v[60:63], a[200:203], v[96:99], v[60:63]\n"
        "ds_read_b128 v[44:47], v125 offset:23552\n"
        "s_waitcnt vmcnt(5)\n"
        "v_cmp_eq_u32 vcc, 0, v140\n"
        "s_waitcnt lgkmcnt(9)\n"
        "v_mfma_f32_16x16x32_bf16 v[48:51], v[16:19], v[96:99], v[48:51]\n"
        "s_cbranch_vccnz L_sw0_3%=\n"
        "L_sok_3%=:\n"
        "s_waitcnt lgkmcnt(7)\n"
        "v_mfma_f32_16x16x32_bf16 v[52:55], v[24:27], v[96:99], v[52:55]\n"
        "v_readlane_b32 s81, v140, s99\n"
        "v_max_u32_dpp v140, v140, v140 row_shr:1 row_mask:0xf bank_mask:0xf\n"
        "v_mfma_f32_16x16x32_bf16 v[84:87], v[100:103], a[52:55], v[84:87]\n"
        "ds_read_b128 v[96:99], v124 offset:6144\n"
        "v_max_u32_dpp v140, v140, v140 row_shr:2 row_mask:0xf bank_mask:0xf\n"
        "v_mfma_f32_16x16x32_bf16 v[88:91], v[100:103], a[60:63], v[88:91]\n"
        "ds_read_b128 v[0:3], v125 offset:0\n"
        "v_max_u32_dpp v140, v140, v140 row_shr:4 row_mask:0xf bank_mask:0xf\n"
        "v_mfma_f32_16x16x32_bf16 v[56:59], a[196:199], v[100:103], v[56:59]\n"
        "buffer_load_dwordx4 v[108:111], v132, s[36:39], s56 offen offset:0 sc1\n"
        "v_max_u32_dpp v140, v140, v140 row_shr:8 row_mask:0xf bank_mask:0xf\n"
        "v_mfma_f32_16x16x32_bf16 v[60:63], a[204:207], v[100:103], v[60:63]\n"
        "ds_read_b128 v[4:7], v125 offset:1024\n"
        "v_max_u32_dpp v140, v140, v140 row_bcast:15 row_mask:0xa bank_mask:0xf\n"
        "v_mfma_f32_16x16x32_bf16 v[48:51], v[20:23], v[100:103], v[48:51]\n"
        "buffer_load_dwordx4 v[112:115], v132, s[36:39], s64 offen offset:0 sc1\n"
        "v_max_u32_dpp v140, v140, v140 row_bcast:31 row_mask:0xc bank_mask:0xf\n"
        "s_waitcnt lgkmcnt(9)\n"
        "v_mfma_f32_16x16x32_bf16 v[52:55], v[28:31], v[100:103], v[52:55]\n"
        "v_readlane_b32 s33, v140, 63\n"
        "s_lshl1_add_u32 s28, s28, s81\n"
        "s_waitcnt lgkmcnt(8)\n"
        "v_mfma_f32_16x16x32_bf16 v[84:87], v[92:95], a[64:67], v[84:87]\n"
        "ds_read_b128 v[100:103], v124 offset:7168\n"
        "s_add_u32 s28, s28, 36\n"
        "s_sub_i32 s33, s33, s28\n"
        "v_mfma_f32_16x16x32_bf16 v[88:91], v[92:95], a[72:75], v[88:91]\n"
        "s_min_i32 s33, s33, 500\n"
        "s_mov_b32 s28, 0\n"
        "L_dl_3%=:\n"
        "s_sub_i32 s33, s33, 3\n"
        "s_cmp_gt_i32 s33, 0\n"
        "s_cbranch_scc1 L_dl_3%=\n"
        "v_mfma_f32_16x16x32_bf16 v[56:59], a[208:211], v[92:95], v[56:59]\n"
        "s_add_u32 s58, s58, 36864\n"
        "ds_read_b128 v[8:11], v125 offset:2048\n"
        "v_mfma_f32_16x16x32_bf16 v[60:63], a[216:219], v[92:95], v[60:63]\n"
        "s_xor_b32 s66, s51, 1024\n"
        "buffer_load_dwordx4 v[80:83], v138, s[36:39], s50 offen offset:0 sc1\n"
        "s_waitcnt lgkmcnt(9)\n"
        "v_mfma_f32_16x16x32_bf16 v[48:51], v[32:35], v[92:95], v[48:51]\n"
        "s_xor_b32 s67, s51, 8192\n"
        "ds_read_b128 v[12:15], v125 offset:3072\n"
        "s_waitcnt lgkmcnt(7)\n"
        "v_mfma_f32_16x16x32_bf16 v[52:55], v[40:43], v[92:95], v[52:55]\n"
        "buffer_load_dwordx4 v[64:67], v138, s[36:39], s49 offen offset:0 sc1\n"
        "v_mfma_f32_16x16x32_bf16 v[84:87], v[104:107], a[68:71], v[84:87]\n"
        "s_xor_b32 s68, s51, 9216\n"
        "ds_read_b128 v[92:95], v124 offset:0\n"
        "v_mfma_f32_16x16x32_bf16 v[88:91], v[104:107], a[76:79], v[88:91]\n"
        "s_sub_u32 s75, 127, s84\n"
        "buffer_load_dwordx4 v[68:71], v138, s[36:39], s49 offen offset:256 sc1\n"
        "v_mfma_f32_16x16x32_bf16 v[56:59], a[212:215], v[104:107], v[56:59]\n"
        "s_sub_u32 s101, s77, s75\n"
        "ds_read_b128 v[16:19], v125 offset:4096\n"
        "v_mfma_f32_16x16x32_bf16 v[60:63], a[220:223], v[104:107], v[60:63]\n"
        "s_mul_i32 s35, s75, 0x800080\n"
        "buffer_load_dwordx4 v[72:75], v138, s[36:39], s65 offen offset:0 sc1\n"
        "v_mfma_f32_16x16x32_bf16 v[48:51], v[36:39], v[104:107], v[48:51]\n"
        "s_lshl_b32 s80, s75, 23\n"
        "ds_read_b128 v[20:23], v125 offset:5120\n"
        "s_waitcnt lgkmcnt(9)\n"
        "v_mfma_f32_16x16x32_bf16 v[52:55], v[44:47], v[104:107], v[52:55]\n"
        "buffer_load_dwordx4 v[76:79], v138, s[36:39], s65 offen offset:256 sc1\n"
        "s_waitcnt lgkmcnt(8)\n"
        "v_mfma_f32_16x16x32_bf16 v[84:87], v[96:99], a[80:83], v[84:87]\n"
        "s_sub_u32 s73, 0x3e0293ee, s80\n"
        "ds_read_b128 v[104:107], v124 offset:1024\n"
        "v_mfma_f32_16x16x32_bf16 v[88:91], v[96:99], a[88:91], v[88:91]\n"
        "s_sub_u32 s74, 0x3f800000, s80\n"
        "ds_read_b128 v[24:27], v125 offset:6144\n"
        "v_mfma_f32_16x16x32_bf16 v[56:59], a[224:227], v[96:99], v[56:59]\n"
        "s_add_u32 s80, s75, 128\n"
        "ds_read_b128 v[28:31], v125 offset:7168\n"
        "v_mfma_f32_16x16x32_bf16 v[60:63], a[232:235], v[96:99], v[60:63]\n"
        "s_lshl_b32 s80, s80, 8\n"
        "ds_read_b128 v[32:35], v125 offset:8192\n"
        "v_mfma_f32_16x16x32_bf16 v[48:51], v[192:195], v[96:99], v[48:51]\n"
        "s_add_u32 s62, s80, 1\n"
        "ds_read_b128 v[36:39], v125 offset:9216\n"
        "v_mfma_f32_16x16x32_bf16 v[52:55], v[200:203], v[96:99], v[52:55]\n"
        "ds_read_b128 v[40:43], v125 offset:10240\n"
        "s_waitcnt lgkmcnt(11)\n"
        "v_mfma_f32_16x16x32_bf16 v[84:87], v[100:103], a[84:87], v[84:87]\n"
        "s_add_u32 s44, s44, 1\n"
        "ds_read_b128 v[96:99], v124 offset:2048\n"
        "v_mfma_f32_16x16x32_bf16 v[88:91], v[100:103], a[92:95], v[88:91]\n"
        "s_add_u32 s61, s61, 32\n"
        "ds_read_b128 v[44:47], v125 offset:11264\n"
        "v_mfma_f32_16x16x32_bf16 v[56:59], a[228:231], v[100:103], v[56:59]\n"
        "s_xor_b32 s57, s57, 4096\n"
        "s_add_u32 s31, s31, 1\n"
        "s_and_b32 s31, s31, 63\n"
        "v_mfma_f32_16x16x32_bf16 v[60:63], a[236:239], v[100:103], v[60:63]\n"
        "s_branch L_rot_3%=\n"
        "L_abort%=:\n"
        "v_mov_b32 v0, 1\n"
        "s_mov_b32 s80, 0\n"
        "buffer_store_dword v0, off, s[36:39], s80 sc1\n"
        "L_end%=:\n"
        "s_waitcnt vmcnt(0)\n"
        "s_waitcnt lgkmcnt(0)\n"
        :: "s"(ws), "s"(Wq), "s"(Wk), "s"(Wv), "s"(Wo), "s"(id), "s"(ack)
        : "v0", "v1", "v2", "v3", "v4", "v5", "v6", "v7", "v8", "v9", "v10", "v11", "v12", "v13", "v14", "v15", "v16", "v17", "v18", "v19", "v20", "v21", "v22", "v23",
          "v24", "v25", "v26", "v27", "v28", "v29", "v30", "v31", "v32", "v33", "v34", "v35", "v36", "v37", "v38", "v39", "v40", "v41", "v42", "v43", "v44", "v45", "v46", "v47",
          "v48", "v49", "v50", "v51", "v52", "v53", "v54", "v55", "v56", "v57", "v58", "v59", "v60", "v61", "v62", "v63", "v64", "v65", "v66", "v67", "v68", "v69", "v70", "v71",
          "v72", "v73", "v74", "v75", "v76", "v77", "v78", "v79", "v80", "v81", "v82", "v83", "v84", "v85", "v86", "v87", "v88", "v89", "v90", "v91", "v92", "v93", "v94", "v95",
          "v96", "v97", "v98", "v99", "v100", "v101", "v102", "v103", "v104", "v105", "v106", "v107", "v108", "v109", "v110", "v111", "v112", "v113", "v114", "v115", "v116", "v117", "v118", "v119",
          "v120", "v121", "v122", "v123", "v124", "v125", "v126", "v127", "v128", "v129", "v130", "v131", "v132", "v133", "v134", "v135", "v136", "v137", "v138", "v139", "v140", "v141", "v142", "v143",
          "v144", "v145", "v146", "v147", "v148", "v149", "v150", "v151", "v152", "v153", "v154", "v155", "v156", "v157", "v158", "v159", "v160", "v161", "v162", "v163", "v164", "v165", "v166", "v167",
          "v168", "v169", "v170", "v171", "v172", "v173", "v174", "v175", "v176", "v177", "v178", "v179", "v180", "v181", "v182", "v183", "v184", "v185", "v186", "v187", "v188", "v189", "v190", "v191",
          "v192", "v193", "v194", "v195", "v196", "v197", "v198", "v199", "v200", "v201", "v202", "v203", "v204", "v205", "v206", "v207", "v208", "v209", "v210", "v211", "v212", "v213", "v214", "v215",
          "v216", "v217", "v218", "v219", "v220", "v221", "v222", "v223", "v224", "v225", "v226", "v227", "v228", "v229", "v230", "v231", "v232", "v233", "v234", "v235", "v236", "v237", "v238", "v239",
          "v240", "v241", "v242", "v243", "v244", "v245", "v246", "v247", "v248", "v249", "v250", "v251", "v252", "v253", "v254", "v255", "a0", "a1", "a2", "a3", "a4", "a5", "a6", "a7",
          "a8", "a9", "a10", "a11", "a12", "a13", "a14", "a15", "a16", "a17", "a18", "a19", "a20", "a21", "a22", "a23", "a24", "a25", "a26", "a27", "a28", "a29", "a30", "a31",
          "a32", "a33", "a34", "a35", "a36", "a37", "a38", "a39", "a40", "a41", "a42", "a43", "a44", "a45", "a46", "a47", "a48", "a49", "a50", "a51", "a52", "a53", "a54", "a55",
          "a56", "a57", "a58", "a59", "a60", "a61", "a62", "a63", "a64", "a65", "a66", "a67", "a68", "a69", "a70", "a71", "a72", "a73", "a74", "a75", "a76", "a77", "a78", "a79",
          "a80", "a81", "a82", "a83", "a84", "a85", "a86", "a87", "a88", "a89", "a90", "a91", "a92", "a93", "a94", "a95", "a96", "a97", "a98", "a99", "a100", "a101", "a102", "a103",
          "a104", "a105", "a106", "a107", "a108", "a109", "a110", "a111", "a112", "a113", "a114", "a115", "a116", "a117", "a118", "a119", "a120", "a121", "a122", "a123", "a124", "a125", "a126", "a127",
          "a128", "a129", "a130", "a131", "a132", "a133", "a134", "a135", "a136", "a137", "a138", "a139", "a140", "a141", "a142", "a143", "a144", "a145", "a146", "a147", "a148", "a149", "a150", "a151",
          "a152", "a153", "a154", "a155", "a156", "a157", "a158", "a159", "a160", "a161", "a162", "a163", "a164", "a165", "a166", "a167", "a168", "a169", "a170", "a171", "a172", "a173", "a174", "a175",
          "a176", "a177", "a178", "a179", "a180", "a181", "a182", "a183", "a184", "a185", "a186", "a187", "a188", "a189", "a190", "a191", "a192", "a193", "a194", "a195", "a196", "a197", "a198", "a199",
          "a200", "a201", "a202", "a203", "a204", "a205", "a206", "a207", "a208", "a209", "a210", "a211", "a212", "a213", "a214", "a215", "a216", "a217", "a218", "a219", "a220", "a221", "a222", "a223",
          "a224", "a225", "a226", "a227", "a228", "a229", "a230", "a231", "a232", "a233", "a234", "a235", "a236", "a237", "a238", "a239", "a240", "a241", "a242", "a243", "a244", "a245", "a246", "a247",
          "a248", "a249", "a250", "a251", "a252", "a253", "a254", "a255", "s20", "s21", "s22", "s23", "s24", "s25", "s26", "s27", "s28", "s29", "s30", "s31", "s32", "s33", "s34", "s35",
          "s36", "s37", "s38", "s39", "s40", "s41", "s42", "s43", "s44", "s45", "s46", "s47", "s48", "s49", "s50", "s51", "s52", "s53", "s54", "s55", "s56", "s57", "s58", "s59",
          "s60", "s61", "s62", "s63", "s64", "s65", "s66", "s67", "s68", "s69", "s70", "s71", "s72", "s73", "s74", "s75", "s76", "s77", "s78", "s79", "s80", "s81", "s82", "s83",
          "s84", "s85", "s86", "s87", "s88", "s89", "s90", "s91", "s92", "s93", "s94", "s95", "s96", "s97", "s98", "s99", "s100", "s101", "vcc", "scc", "m0", "memory");
}
constexpr u32 OOB = 0x40000000u;
#define BID (blockIdx.x ^ 248u)

constexpr u32 XNB_W = OFF_XNB, FX_W = 0;
__device__ __forceinline__ float bf2f(u16 v) { return __uint_as_float(((u32)v) << 16); }
__device__ __forceinline__ float bflo(u32 v) { return __uint_as_float(v << 16); }
__device__ __forceinline__ float bfhi(u32 v) { return __uint_as_float(v & 0xffff0000u); }
__device__ __forceinline__ u32 pack2(float a, float b) { bfv2 v = {(__bf16)a, (__bf16)b}; return __builtin_bit_cast(u32, v); }
__device__ __forceinline__ float rbf(float f) { return (float)(__bf16)f; }
__device__ __forceinline__ v4u ld16(rsrc_t r, u32 off) { return __builtin_amdgcn_raw_buffer_load_b128(r, off, 0, 16); }
__device__ __forceinline__ v2u ld8(rsrc_t r, u32 off) { return __builtin_amdgcn_raw_buffer_load_b64(r, off, 0, 16); }
__device__ __forceinline__ void st16(rsrc_t r, u32 off, v4u v) { __builtin_amdgcn_raw_buffer_store_b128(v, r, off, 0, 16); }
__device__ __forceinline__ void st8(rsrc_t r, u32 off, v2u v) { __builtin_amdgcn_raw_buffer_store_b64(v, r, off, 0, 16); }
__device__ __forceinline__ void st4(rsrc_t r, u32 off, u32 v) { __builtin_amdgcn_raw_buffer_store_b32(v, r, off, 0, 16); }
#define DRAIN() asm volatile("s_waitcnt vmcnt(0)" ::: "memory")
#define CBAR() asm volatile("" ::: "memory")
#define SCHED() __builtin_amdgcn_sched_barrier(0)
#define VMW(n) asm volatile("s_waitcnt vmcnt(%0)" :: "n"(n) : "memory")
#define LBAR() asm volatile("s_waitcnt lgkmcnt(0)\n\ts_barrier" ::: "memory")
__device__ __forceinline__ u32 ldw(char* ws, u32 off) { return __hip_atomic_load((gu32*)(ws + off), RLX_AGENT); }
constexpr u32 SPIN_CAP = 1u << 20;
#define SLP_A 2
#define SLP_S 6
__device__ __forceinline__ bool spin_fail(char* ws, u32& spins) {
    ++spins;
    if ((spins & 63u) == 0u) {
        if (ldw(ws, OFF_ABORT)) return true;
        if (spins > SPIN_CAP) { __hip_atomic_store((gu32*)(ws + OFF_ABORT), 1u, RLX_AGENT); return true; }
    }
    return false;
}
#define NAP_A 2
#define NAP_W 2
#define NAP_W0 0
#define NAP_C 1
__device__ __forceinline__ void nap(int n) { for (int i = 0; i < n; ++i) __builtin_amdgcn_s_sleep(127); }
__device__ __forceinline__ f32x4 mfma32(bf16x8 a, bf16x8 b, f32x4 c) { return __builtin_amdgcn_mfma_f32_16x16x32_bf16(a, b, c, 0, 0, 0); }
__device__ __forceinline__ f32x4 mfma16(bf16x4 a, bf16x4 b, f32x4 c) { return __builtin_amdgcn_mfma_f32_16x16x16bf16_1k(a, b, c, 0, 0, 0); }
template <bool AG, bool SW, bool FIRST>
__device__ __forceinline__ void mmaw(f32x4& acc, const bf16x8& w, const bf16x8& x) {
    if constexpr (FIRST) {
        if constexpr (AG && !SW) asm volatile("v_mfma_f32_16x16x32_bf16 %0, %1, %2, 0" : "=&v"(acc) : "a"(w), "v"(x));
        else if constexpr (AG && SW) asm volatile("v_mfma_f32_16x16x32_bf16 %0, %2, %1, 0" : "=&v"(acc) : "a"(w), "v"(x));
        else if constexpr (!SW) asm volatile("v_mfma_f32_16x16x32_bf16 %0, %1, %2, 0" : "=&v"(acc) : "v"(w), "v"(x));
        else asm volatile("v_mfma_f32_16x16x32_bf16 %0, %2, %1, 0" : "=&v"(acc) : "v"(w), "v"(x));
    } else {
        if constexpr (AG && !SW) asm volatile("v_mfma_f32_16x16x32_bf16 %0, %1, %2, %0" : "+v"(acc) : "a"(w), "v"(x));
        else if constexpr (AG && SW) asm volatile("v_mfma_f32_16x16x32_bf16 %0, %2, %1, %0" : "+v"(acc) : "a"(w), "v"(x));
        else if constexpr (!SW) asm volatile("v_mfma_f32_16x16x32_bf16 %0, %1, %2, %0" : "+v"(acc) : "v"(w), "v"(x));
        else asm volatile("v_mfma_f32_16x16x32_bf16 %0, %2, %1, %0" : "+v"(acc) : "v"(w), "v"(x));
    }
}
#define NOP2() asm volatile("s_nop 1")
#define NOP8() asm volatile("s_nop 7")
#define PIN(x) asm volatile("" : "+v"(x))
#define PINA(x) asm volatile("" : "+a"(x))
template <bool PERM = false>
__device__ __forceinline__ void load_pair(const u16* W, int ld, int krow, int ncol, bf16x8& f0, bf16x8& f1) {
    u32 d[8];
#pragma unroll
    for (int j = 0; j < 8; ++j) d[j] = *(const u32*)(W + (size_t)(PERM ? krow + 16 * (j & 1) + (j >> 1) : krow + j) * ld + ncol);
    v4u a, b;
#pragma unroll
    for (int q = 0; q < 4; ++q) {
        a[q] = __builtin_amdgcn_perm(d[2 * q + 1], d[2 * q], 0x05040100u);
        b[q] = __builtin_amdgcn_perm(d[2 * q + 1], d[2 * q], 0x07060302u);
    }
    f0 = __builtin_bit_cast(bf16x8, a);
    f1 = __builtin_bit_cast(bf16x8, b);
}
template <bool PERM = false>
__device__ __forceinline__ void load_quad(const u16* W, int ld, int krow, int ncol, bf16x8& f0, bf16x8& f1, bf16x8& f2, bf16x8& f3) {
    v2u d[8];
#pragma unroll
    for (int j = 0; j < 8; ++j) d[j] = *(const v2u*)(W + (size_t)(PERM ? krow + 16 * (j & 1) + (j >> 1) : krow + j) * ld + ncol);
    v4u a, b, c, e;
#pragma unroll
    for (int q = 0; q < 4; ++q) {
        a[q] = __builtin_amdgcn_perm(d[2 * q + 1][0], d[2 * q][0], 0x05040100u);
        b[q] = __builtin_amdgcn_perm(d[2 * q + 1][0], d[2 * q][0], 0x07060302u);
        c[q] = __builtin_amdgcn_perm(d[2 * q + 1][1], d[2 * q][1], 0x05040100u);
        e[q] = __builtin_amdgcn_perm(d[2 * q + 1][1], d[2 * q][1], 0x07060302u);
    }
    f0 = __builtin_bit_cast(bf16x8, a); f1 = __builtin_bit_cast(bf16x8, b);
    f2 = __builtin_bit_cast(bf16x8, c); f3 = __builtin_bit_cast(bf16x8, e);
}
__device__ __forceinline__ v4u pack_quad(f32x4 t0, f32x4 t1, f32x4 t2, f32x4 t3, bool hf) {
    return v4u{pack2(hf ? t0[2] : t0[0], hf ? t1[2] : t1[0]), pack2(hf ? t2[2] : t2[0], hf ? t3[2] : t3[0]),
               pack2(hf ? t0[3] : t0[1], hf ? t1[3] : t1[1]), pack2(hf ? t2[3] : t2[1], hf ? t3[3] : t3[1])};
}
__device__ __forceinline__ v4u pack_pair(f32x4 t0, f32x4 t1) {
    v4u o;
#pragma unroll
    for (int r = 0; r < 4; ++r) o[r] = pack2(t0[r], t1[r]);
    return o;
}
__device__ __forceinline__ float vmax(float a, float b) { float r; asm("v_max_f32 %0, %1, %2" : "=v"(r) : "v"(a), "v"(b)); return r; }
__device__ __forceinline__ float xmax(float v) {
    auto a = __builtin_amdgcn_permlane16_swap(__float_as_uint(v), __float_as_uint(v), false, false);
    v = vmax(__uint_as_float(a[0]), __uint_as_float(a[1]));
    auto b = __builtin_amdgcn_permlane32_swap(__float_as_uint(v), __float_as_uint(v), false, false);
    return vmax(__uint_as_float(b[0]), __uint_as_float(b[1]));
}
__device__ __forceinline__ float xsum(float v) {
    auto a = __builtin_amdgcn_permlane16_swap(__float_as_uint(v), __float_as_uint(v), false, false);
    v = __uint_as_float(a[0]) + __uint_as_float(a[1]);
    auto b = __builtin_amdgcn_permlane32_swap(__float_as_uint(v), __float_as_uint(v), false, false);
    return __uint_as_float(b[0]) + __uint_as_float(b[1]);
}
template <int CTRL> __device__ __forceinline__ float dpp(float v) {
    return __uint_as_float(__builtin_amdgcn_update_dpp(0u, __float_as_uint(v), CTRL, 0xf, 0xf, false));
}
__device__ __forceinline__ float wave_sum(float v) {
    v += dpp<0xB1>(v);
    v += dpp<0x4E>(v);
    v += dpp<0x141>(v);
    v += dpp<0x140>(v);
    return xsum(v);
}
__device__ __forceinline__ f32x2 un2(u32 w) { return f32x2{bflo(w), bfhi(w)}; }
__device__ __forceinline__ u32 pack2(f32x2 v) { return pack2(v[0], v[1]); }
__device__ __forceinline__ void ln_par(float s, float q, f32x2& a, f32x2& b) {
    const float mean = s * (1.f / D_), inv = __builtin_amdgcn_rcpf(rbf(__builtin_amdgcn_sqrtf(rbf(rbf(fmaxf(q * (1.f / D_) - mean * mean, 0.f)) + 1e-5f))));
    a = f32x2{inv, inv};
    b = -rbf(mean) * a;
}
__device__ __forceinline__ void ln_row(const f32x2 (&v)[6], v2u (&y)[3]) {
    f32x2 s = v[0], q = v[0] * v[0], a, b;
#pragma unroll
    for (int i = 1; i < 6; ++i) { s += v[i]; q += v[i] * v[i]; }
    ln_par(wave_sum(s[0] + s[1]), wave_sum(q[0] + q[1]), a, b);
#pragma unroll
    for (int i = 0; i < 6; ++i) y[i >> 1][i & 1] = pack2(v[i] * a + b);
}

__device__ __forceinline__ u32 prog(u32 k) { return OFF_PROG + (k & 7) * 4096 + (k >> 3) * 256; }
#define CR 2
#define CA 1
#define CP 1
#define CB 1
constexpr u32 FOA = 8 * CX, FR = FOA + 8 * CA, FPB = FR + 2 * CR, FOB = FPB + 16 * CP, NPROG = FOB + 8 * CB;
static_assert(NPROG <= 128);
__device__ __forceinline__ u32 rdw(rsrc_t rs, u32 off) { return (u32)__builtin_amdgcn_raw_ptr_buffer_atomic_add_i32(0, rs, off, 0, 0); }
__device__ int buf_umax(int, rsrc_t, int, int, int) __asm("llvm.amdgcn.raw.ptr.buffer.atomic.umax.i32");
__device__ int buf_swap(int, rsrc_t, int, int, int) __asm("llvm.amdgcn.raw.ptr.buffer.atomic.swap.i32");
__device__ __forceinline__ void put(rsrc_t rs, u32 off, u32 v) { buf_swap(v, rs, off, 0, 0); }
__device__ __forceinline__ void tell(rsrc_t rs, u32 mine, u32 done) { buf_umax(done, rs, mine, 0, 0); }
template <int N> __device__ __forceinline__ u32 words(u32 off) { const u32 lane = threadIdx.x & 63; return lane < N ? off + lane * 4 : OOB; }
template <int N> __device__ __forceinline__ bool reached(u32 f, u32 need) { return ((u32)__ballot(f < need) & (u32)((1ull << N) - 1)) == 0u; }
template <int N> __device__ __forceinline__ u32 wait_prog(char* ws, rsrc_t rs, u32 off, u32 need) {
    u32 spins = 0, f;
    while (!reached<N>(f = rdw(rs, off), need)) {
        if (spin_fail(ws, spins)) break;
        __builtin_amdgcn_s_sleep(SLP_A);
    }
    CBAR();
    return __builtin_amdgcn_readfirstlane(f);
}
#define PS 8
#define AS 0
#define BS 2
template <int N, bool ANN, int P> __device__ __forceinline__ u32 watch(char* ws, rsrc_t rs, u32 mine, u32 done, u32 off, u32 need) {
    u32 f = rdw(rs, off), spins = 0;
    if (ANN) { VMW(1); tell(rs, mine, done); PIN(f); }
    if (!reached<N>(f, need))
        do {
            if (P) __builtin_amdgcn_s_sleep(P);
            f = rdw(rs, off);
        } while (!reached<N>(f, need) && !spin_fail(ws, spins));
    CBAR();
    return __builtin_amdgcn_readfirstlane(f);
}
template <int N> __device__ __forceinline__ void meet(char* ws, rsrc_t rs, bool me, u32 mine, u32 done, u32 off, u32 need) {
    if (me) watch<N, true, BS>(ws, rs, mine, done, off, need);
    else { DRAIN(); tell(rs, mine, done); }
    asm volatile("s_barrier" ::: "memory");
}
__device__ __forceinline__ bool told(const char* p) { return __builtin_amdgcn_readfirstlane(*(const u32*)p); }
__device__ __forceinline__ u32 my_word() { return words<1>(OFF_ACK + (BID * 4 + (threadIdx.x >> 6)) * 4); }
__device__ __forceinline__ u32 ask(rsrc_t rs) { DRAIN(); return rdw(rs, my_word()); }
__device__ __forceinline__ void go(rsrc_t rs, u32 a) {
    for (u32 spins = 0; !reached<1>(a, 1u) && spins < SPIN_CAP; ++spins) { __builtin_amdgcn_s_sleep(SLP_A); a = rdw(rs, my_word()); }
    CBAR();
    put(rs, my_word(), 2u);
}
#define CLR 16
template <int E = 1> __device__ __forceinline__ void clear(rsrc_t rs, u32 off, int n) {
#pragma unroll 5
    for (int i = 0; i < n; ++i) __builtin_amdgcn_raw_buffer_store_b128(v4u{0u, 0u, 0u, 0u}, rs, off + (i * 32 + (threadIdx.x >> 3)) * (128 * E) + (threadIdx.x & 7) * 16, 0, CLR);
}
__device__ __forceinline__ void role_lead(char* ws, rsrc_t rs) {
    const u32 tid = threadIdx.x;
    clear(rs, 0, 3);
#pragma unroll
    for (u32 i = FX_W ? CX / 4 : 0; i < (NPROG + 31) / 32; ++i) st16(rs, prog(i * 32 + (tid >> 3)) + (tid & 7) * 16, v4u{0u, 0u, 0u, 0u});
    DRAIN();
    __syncthreads();
#pragma unroll
    for (int k = 0; k < 4; ++k) put(rs, OFF_ACK + (k * 256 + tid) * 4, 1u);
    DRAIN();
    u32 pend = tid < 248 || tid >= 252 ? 15u : 7u, spins = 0;
    bool z = tid < 128;
    while (__any(pend)) {
        u32 old[4];
#pragma unroll
        for (int k = 0; k < 4; ++k) old[k] = __hip_atomic_fetch_max((gu32*)(ws + OFF_ACK) + k * 256 + tid, 1u, RLX_AGENT);
#pragma unroll
        for (int k = 0; k < 4; ++k) if (old[k] == 2u || (old[k] == 0u && !(k == 2 && tid < 128))) pend &= ~(1u << k);
        if (z && !__any(pend & 4u)) { tell(rs, words<1>(OFF_Z + (tid >> 6) * 4), 1u); z = false; }
        if (spin_fail(ws, spins)) break;
        __builtin_amdgcn_s_sleep(SLP_A);
    }
}
typedef short s16x4 __attribute__((ext_vector_type(4)));
__device__ __forceinline__ v2u tr_read(const char* p) {
    return __builtin_bit_cast(v2u, __builtin_amdgcn_ds_read_tr16_b64_v4i16((__attribute__((address_space(3))) s16x4*)p));
}
__device__ __forceinline__ bf16x8 asf(v4u v) { return __builtin_bit_cast(bf16x8, v); }

constexpr u32 TILE = 18432;
__device__ __forceinline__ u32 quad(int lane) { return ((lane >> 2) & 3) * 4608 + (lane >> 4) * 64 + (lane & 3) * 16; }
__device__ __forceinline__ v4u ldt(rsrc_t r, u32 vo, u32 so) { return __builtin_amdgcn_raw_buffer_load_b128(r, vo, so, 16); }
__device__ __forceinline__ void stt(rsrc_t r, u32 vo, u32 so, v4u v) {
    __builtin_amdgcn_raw_buffer_store_b128(v, r, vo, so, 16);
    asm volatile("s_nop 1" :: "v"(v) : "memory");
}

__device__ __forceinline__ void ld_tile(int tile, const u16* x0, v4u (&t)[18]) {
    const int lane = threadIdx.x & 63;
#pragma unroll
    for (int ks = 0; ks < 18; ++ks) t[ks] = *(const v4u*)(x0 + (size_t)(tile * 16 + (lane & 15)) * D_ + ks * 32 + (lane >> 4) * 8);
}
__device__ __forceinline__ void ln_tile(v4u (&t)[18]) {
    f32x2 s = {0.f, 0.f}, q = {0.f, 0.f}, a, b;
#pragma unroll
    for (int i = 0; i < 72; ++i) { const f32x2 v = un2(t[i >> 2][i & 3]); s += v; q += v * v; }
    ln_par(xsum(s[0] + s[1]), xsum(q[0] + q[1]), a, b);
#pragma unroll
    for (int i = 0; i < 72; ++i) t[i >> 2][i & 3] = pack2(un2(t[i >> 2][i & 3]) * a + b);
}
__device__ __forceinline__ void role_t0(int tile, const u16* x0, char* ws, rsrc_t rs) {
    v4u t[18];
    ld_tile(tile, x0, t);
    const u32 a = ask(rs);
    ln_tile(t);
    go(rs, a);
#pragma unroll
    for (int ks = 0; ks < 18; ++ks) stt(rs, quad(threadIdx.x & 63) + (ks & 15) * 256, OFF_XNA + tile * TILE + (ks >> 4) * 4096, t[ks]);
    DRAIN();
    tell(rs, words<16>(OFF_CXNA + tile * 64), NSEG);
}

#define R_EARLY 1
constexpr int SL = 56;
template <int NR, bool FIN, int S0 = 0, int S1 = NSEG>
__device__ __forceinline__ void role_r(int idx, const u16* x0, u16* out, char* ws, rsrc_t rs, char* smem, const v2u (&x00)[6]) {
    const int tid = threadIdx.x, lane = tid & 63, wave = __builtin_amdgcn_readfirstlane(tid >> 6);
    const bool act = lane < 48;
    const int lc = act ? lane : 0;
    const int rl = (idx * 4 + wave) * NR;
    const u32 po = act ? lane * 16 : OOB, xo = act ? (lane >> 1) * 64 + (lane & 1) * 8 : OOB;
    const u32 fp = words<24>(prog(FPB + idx % CP * 16 + (FIN ? 4 : 0) + (rl >> 3) + (lane < 12 ? 0 : 8)) + 16), fo = lane < CR ? prog(FR + lane * 2 + (rl >> 4)) + (rl & 15) * 4 : OOB;
    if (!FIN) {
        v2u xr[2][3];
#pragma unroll
        for (int i = 0; i < 6; ++i) xr[i / 3][i % 3] = x00[i];
        const u32 a = ask(rs);
#pragma unroll
        for (int u = 0; u < 2; ++u) {
            f32x2 v[6];
#pragma unroll
            for (int i = 0; i < 6; ++i) v[i] = un2(act ? xr[u][i >> 1][i & 1] : 0u);
            v2u y[3];
            ln_row(v, y);
#pragma unroll
            for (int j = 0; j < 3; ++j) if (act) *(v2u*)(smem + 9216 + u * 4608 + (j * 24 + (lc >> 1)) * 64 + wave * 16 + (lc & 1) * 8) = y[j];
        }
        LBAR();
#pragma unroll
        for (int i = 0; i < 3; ++i) {
            const int c = tid + 256 * i;
            if (c < 576) st16(rs, OFF_XNA + (idx >> 2) * TILE + (idx & 3) * 4608 + (c < 288 ? c * 16 : 2 * TILE - 4608 + c * 16), *(const v4u*)(smem + 9216 + c * 16));
        }
        DRAIN();
        go(rs, a);
        tell(rs, lane < 2 ? OFF_CXNA + lane * 128 + rl * 4 : OOB, NSEG);
    }
    bool owe = false;
    const u32 f1 = wave ? OOB : fp;
    meet<24>(ws, rs, !wave, OOB, 0, fp, S0 + 1);
    const rsrc_t ro = __builtin_amdgcn_make_buffer_rsrc(out, 0, XB, 0x00020000);
    v2u xn[NR][3];
    auto ldx = [&](int s) {
#pragma unroll
        for (int i = 0; i < 3 * NR; ++i)
            xn[i / 3][i % 3] = __builtin_amdgcn_raw_buffer_load_b64(rs, xo + (i % 3) * 1536, OFF_X + (FIN ? 2 : 0) * XB + (s * 2 + (rl >> 4)) * TILE + ((rl >> 2) & 3) * 4608 + ((rl + i / 3) & 3) * 16, 16);
    };
    ldx(S0);
#pragma unroll 1
    for (int s = S0; s < S1; ++s) {
        v2u xr[NR][3];
        v4u pr[NR][4][3];
#pragma unroll
        for (int u = 0; u < NR; ++u) {
#pragma unroll
            for (int p = 0; p < 4; ++p)
#pragma unroll
                for (int j = 0; j < 3; ++j) pr[u][p][j] = ldt(rs, po + j * 768, OFF_PB + (((FIN ? 64 + s : s) * 4 + p) * SEG + rl + u) * D_ * 4);
        }
        SCHED();
#pragma unroll
        for (int i = 0; i < 3 * NR; ++i) xr[i / 3][i % 3] = xn[i / 3][i % 3];
        ldx(min(s + 1, NSEG - 1));
        SCHED();
        if (!FIN && owe) { VMW(15 * NR); tell(rs, fo, s); }
        u32 fn;
        if (FIN) { CBAR(); fn = rdw(rs, f1); }
        char* sm = smem + (s & 1) * 9216;
#pragma unroll
        for (int u = 0; u < NR; ++u) {
            f32x2 v[6];
#pragma unroll
            for (int i = 0; i < 6; ++i) {
                auto P = [&](int p) { return f32x2{__uint_as_float(pr[u][p][i >> 1][2 * (i & 1)]), __uint_as_float(pr[u][p][i >> 1][2 * (i & 1) + 1])}; };
                v[i] = ((P(0) + P(1)) + (P(2) + P(3))) + un2(xr[u][i >> 1][i & 1]);
            }
            v2u y[3];
            if (!FIN) { PIN(v[5]); CBAR(); fn = rdw(rs, f1); }
            if (!FIN) ln_row(v, y);
            if (FIN) {
#pragma unroll
                for (int j = 0; j < 3; ++j) __builtin_amdgcn_raw_buffer_store_b64(v2u{pack2(v[2 * j]), pack2(v[2 * j + 1])}, ro, (po >> 1) + j * 384, (s * SEG + rl + u) * (D_ * 2), 0);
            } else if (act) {
#pragma unroll
                for (int j = 0; j < 3; ++j) {
                    const v2u xw = {pack2(v[2 * j]), pack2(v[2 * j + 1])};
                    {
                        *(v2u*)(sm + (j * 24 + (lc >> 1)) * 64 + wave * 16 + (lc & 1) * 8) = y[j];
                        *(v2u*)(sm + 4608 + (j * 24 + (lc >> 1)) * 64 + wave * 16 + (lc & 1) * 8) = xw;
                    }
                }
            }
        }
        SCHED();
        PIN(fn);
        if (!wave) *(u32*)(smem + 32768 + (s & 1) * 4) = reached<24>(fn, s + 2);
        LBAR();
        owe = told(smem + 32768 + (s & 1) * 4);
        if (!FIN) {
#pragma unroll
            for (int i = 0; i < 3; ++i) {
                const int c = tid + 256 * i, c2 = c < 288 ? c : c - 288;
                if (c < 576) st16(rs, (c < 288 ? OFF_XNA : OFF_X) + XB + (s * 2 + (idx >> 2)) * TILE + (idx & 3) * 4608 + c2 * 16, *(const v4u*)(sm + c * 16));
            }
        }
        CBAR();
        if (!owe) {
            if (!FIN) meet<24>(ws, rs, !wave, fo, s + 1, fp, min(s + 2, NSEG));
            else if (s + 1 < S1) { if (!wave) watch<24, false, BS>(ws, rs, 0, 0, fp, s + 2); asm volatile("s_barrier" ::: "memory"); }
        }
    }
}

#define A1_EARLY 1
__device__ __forceinline__ void role_a1(int layer, int hp, const u16* wqkv, char* ws, rsrc_t rs, char* smem) {
    const int tid = threadIdx.x, lane = tid & 63, l15 = lane & 15, g = lane >> 4;
    const int wave = __builtin_amdgcn_readfirstlane(tid >> 6), hd = wave >> 1, m = wave & 1;
    bf16x8 w[18][4];
    v4u x[18];
    u32 a0, f0;
    const u32 fx = wave < 3 ? OOB : words<32>(layer ? prog(FR + hp % CR * 2 + (g & 1)) - (g & 1) * 64 : OFF_CXNA), fs = layer ? 0 : 128, xb = OFF_XNA + layer * XB + m * TILE;
    const u32 fo = lane < CA ? prog(FOA + lane * 8 + (layer * 2 + m) * 2) + hp * 4 : OOB, ql = quad(lane);
    {
        const rsrc_t wr = __builtin_amdgcn_make_buffer_rsrc((void*)wqkv, 0, D_ * NQA * 2, 0x00020000);
        const int c = l15 ^ (2 * (g | hd << 2));
        const u32 vo = c < 15 ? (4 * wave + g) * (NQA * 2) + (c / 5) * (D_ * 2) + ((hp * 72) & ~15) + (c % 5) * 16 : OOB;
#pragma unroll
        for (int i = 0; i < 36; ++i) __builtin_amdgcn_raw_ptr_buffer_load_lds(wr, (__attribute__((address_space(3))) void*)(smem + (4 * i + wave) * 1024), 16, vo, i * 16 * NQA * 2, 0, 0);
        const char* rd[4];
#pragma unroll
        for (int t = 0; t < 4; ++t) {
            const int by = (hp & 1) * 8 + (t == 2 ? 32 + 80 * min(l15 & 3, 2) : hd * 40 + (l15 & 3) * 8 + 80 * min(t, 2));
            rd[t] = smem + (8 * g + (l15 >> 2)) * 256 + (by ^ (32 * ((l15 >> 2) | (g & 1) << 2)));
        }
        a0 = ask(rs);
        LBAR();
        go(rs, a0);
        f0 = rdw(rs, fx);
#pragma unroll
        for (int ks = 0; ks < 18; ++ks)
#pragma unroll
            for (int t = 0; t < 4; ++t) {
                const v2u a = tr_read(rd[t] + ks * 8192), b = tr_read(rd[t] + ks * 8192 + 1024);
                w[ks][t] = asf(v4u{a[0], a[1], b[0], b[1]});
            }
#pragma unroll
        for (int i = 0; i < 72; ++i) if (i < 60) PINA(w[i >> 2][i & 3]); else PIN(w[i >> 2][i & 3]);
        LBAR();
    }
    char* sK = smem + hd * 8192;
    char* sV = smem + 16384 + hd * 8192;
#pragma unroll
    for (int i = 0; i < 8; ++i) *(v4u*)(smem + (i * 256 + tid) * 16) = v4u{0u, 0u, 0u, 0u};
    if (wave == 3) *(u32*)(smem + 36864) = reached<32>(f0, 1);
    LBAR();
    const bool odd = l15 & 1;
    const u32 pm = odd ? 0xffff0000u : 0xffffu;
    bool ok[4];
#pragma unroll
    for (int i = 0; i < 4; ++i) { const int d = 16 + l15 - (16 * (i >> 1) + 4 * g + 2 * (i & 1) + (l15 & 1)); ok[i] = d >= 0 && d <= 14; }
    const u32 vb = (4 * g + (l15 >> 2)) * 48 + (l15 & 3) * 8, vb2 = (4 * g + (l15 >> 2)) * 48 + ((l15 & 3) == 0 ? 32 : 40);
    char* sO = smem + 32768 + m * 1024;
    bool owe = false;
    auto body = [&](int s, auto own) {
        auto ldx = [&](int ks) { if constexpr (!own) x[ks] = ldt(rs, ql + (ks & 15) * 256, xb + s * 2 * TILE + (ks >> 4) * 4096); SCHED(); };
#pragma unroll
        for (int ks = 0; ks < 12; ++ks) ldx(ks);
        if (owe) { VMW(12); tell(rs, fo + (~s & 1) * 4096, s); }
        f32x4 acc[4];
#pragma unroll
        for (int ks = 0; ks < 18; ++ks) {
#pragma unroll
            for (int t = 0; t < 4; ++t) {
                if (ks == 0) mmaw<true, false, true>(acc[t], w[ks][t], asf(x[ks]));
                else if (ks < 15) mmaw<true, false, false>(acc[t], w[ks][t], asf(x[ks]));
                else mmaw<false, false, false>(acc[t], w[ks][t], asf(x[ks]));
            }
            SCHED();
            if (ks < 6) ldx(ks + 12);
        }
        CBAR();
        NOP8();
#pragma unroll
        for (int t = 0; t < 4; ++t) PIN(acc[t]);
        const int T = (2 * s + m) & 7, Tp = (T + 7) & 7;
        const u32 e01 = pack2(hd ? acc[2][2] : acc[2][0], hd ? acc[2][3] : acc[2][1]);
        const v4u qf = {pack2(acc[0][0], acc[0][1]), pack2(acc[0][2], acc[0][3]), g ? 0u : e01, 0u};
        const v4u kf = {pack2(acc[1][0], acc[1][1]), pack2(acc[1][2], acc[1][3]), __builtin_amdgcn_permlane16_swap(e01, e01, false, false)[1], 0u};
        *(v4u*)(sK + T * 1024 + lane * 16) = kf;
        *(v2u*)(sV + T * 1024 + l15 * 48 + g * 8) = v2u{pack2(acc[3][0], acc[3][1]), pack2(acc[3][2], acc[3][3])};
        if (g == 2) *(u32*)(sV + T * 1024 + l15 * 48 + 32) = e01;
        const f32x4 s1 = mfma32(asf(kf), asf(qf), f32x4{0.f, 0.f, 0.f, 0.f});
        LBAR();
        const u32 fn = rdw(rs, fx + (s + 1 < NSEG ? s + 1 : s) * fs);
        CBAR();
        const v4u kp = *(const v4u*)(sK + Tp * 1024 + lane * 16);
        const v2u va0 = tr_read(sV + Tp * 1024 + vb), va1 = tr_read(sV + T * 1024 + vb), vc0 = tr_read(sV + Tp * 1024 + vb2), vc1 = tr_read(sV + T * 1024 + vb2);
        const f32x4 s0 = mfma32(asf(kp), asf(qf), f32x4{0.f, 0.f, 0.f, 0.f});
        float l[4] = {odd ? s0[1] : s0[0], odd ? s0[3] : s0[2], odd ? s1[1] : s1[0], odd ? s1[3] : s1[2]};
        constexpr float SC = 0.23570226039551584f * 1.4426950408889634f;
#pragma unroll
        for (int i = 0; i < 4; ++i) l[i] = ok[i] && (i > 1 || s + m) ? l[i] : -3.0e38f;
        const float mx = -SC * xmax(fmaxf(fmaxf(l[0], l[1]), fmaxf(l[2], l[3])));
        v4u p;
#pragma unroll
        for (int i = 0; i < 4; ++i) { l[i] = __builtin_amdgcn_exp2f(l[i] * SC + mx); p[i] = pack2(l[i], l[i]) & pm; }
        const f32x4 oa = mfma32(asf(v4u{va0[0], va0[1], va1[0], va1[1]}), asf(p), f32x4{0.f, 0.f, 0.f, 0.f});
        const f32x4 ob = mfma32(asf(v4u{vc0[0], vc0[1], vc1[0], vc1[1]}), asf(p), f32x4{0.f, 0.f, 0.f, 0.f});
        const float inv = __builtin_amdgcn_rcpf(xsum((l[0] + l[1]) + (l[2] + l[3])));
        const v2u o = {pack2(oa[0] * inv, oa[1] * inv), pack2(oa[2] * inv, oa[3] * inv)};
        const u32 oe = pack2(ob[0] * inv, ob[1] * inv);
        if (hd) { *(v2u*)(sO + lane * 8) = o; if (g == 0) *(u32*)(sO + 512 + lane * 4) = oe; }
        LBAR();
        if (!hd) {
            const v2u o2 = *(const v2u*)(sO + lane * 8);
            const u32 oe2 = *(const u32*)(sO + 512 + l15 * 4);
            const u32 dst = OFF_OA + layer * XB + (s * 2 + m) * TILE;
            stt(rs, lane * 16, dst + hp * 1024, v4u{o[0], o[1], o2[0], o2[1]});
            if (g == 0) st8(rs, dst + 16384 + hp * 128 + lane * 8, v2u{oe, oe2});
        }
        CBAR();
        if (wave == 3) *(u32*)(smem + 36864) = reached<32>(fn, s + 2);
        LBAR();
        const bool have = told(smem + 36864);
        owe = have && !hd;
        if (!have) meet<32>(ws, rs, wave == 3, hd ? OOB : fo + (s & 1) * 4096, s + 1, fx + (s + 1 < NSEG ? s + 1 : s) * fs, min(s + 2, NSEG));
    };
    if (!told(smem + 36864)) meet<32>(ws, rs, wave == 3, OOB, 0, fx, 1);
    LBAR();
    for (int s = 0; s < NSEG; ++s) body(s, std::false_type());
}

#define JSTAT 1
__device__ __forceinline__ void role_a2(int layer, int nb, const u16* x0, const u16* woA, char* ws, rsrc_t rs, char* smem) {
    const int tid = threadIdx.x, lane = tid & 63, l15 = lane & 15, g = lane >> 4;
    const int wave = __builtin_amdgcn_readfirstlane(tid >> 6), m = wave & 1;
    bf16x8 w[18][4];
    {
        v4u t[18];
#pragma unroll
        for (int i = 0; i < 18; ++i) t[i] = *(const v4u*)(woA + (size_t)(i * 32 + (tid >> 3)) * D_ + nb * 64 + (tid & 7) * 8);
#pragma unroll
        for (int i = 0; i < 18; ++i) *(v4u*)(smem + (i * 32 + (tid >> 3)) * 144 + (tid & 7) * 16) = t[i];
        __syncthreads();
        const int q = l15 >> 2;
        const char* ra = smem + (4 * g + q) * 144 + (l15 & 3) * 16;
        const char* rb = smem + (36 * g + 16 + q) * 144 + (l15 & 3) * 16;
#pragma unroll
        for (int ks = 0; ks < 18; ++ks)
#pragma unroll
            for (int T = 0; T < 4; ++T) {
                const int co = (T >> 1) * 64 + (T & 1) * 8;
                v2u a, b;
                if (ks < 16) { a = tr_read(ra + ks * 36 * 144 + co); b = tr_read(ra + (ks * 36 + 20) * 144 + co); }
                else { a = tr_read(rb + (ks - 16) * 288 * 144 + co); b = tr_read(rb + ((ks - 16) * 288 + 144) * 144 + co); }
                w[ks][T] = asf(v4u{a[0], a[1], b[0], b[1]});
            }
#pragma unroll
        for (int i = 0; i < 72; ++i) if (i < 60) PINA(w[i >> 2][i & 3]); else PIN(w[i >> 2][i & 3]);
    }
    go(rs, ask(rs));
    wait_prog<2>(ws, rs, words<2>(OFF_Z), 1);
    const u32 fi = words<16>(prog(FOA + nb % CA * 8 + (layer * 2 + m) * 2 + (wave >> 1))), fo = lane < CX ? FX_W + prog(lane * 8 + (layer * 2 + m) * 2 + (wave >> 1)) + nb * 4 : OOB;
    v4u xn[2];
    auto ldr = [&](int s) {
#pragma unroll
        for (int P = 0; P < 2; ++P) xn[P] = *(const v4u*)(x0 + (size_t)(s * SEG + m * 16 + l15) * D_ + nb * 64 + P * 32 + g * 8);
    };
    if (layer == 0) ldr(wave >> 1);
    watch<16, false, PS>(ws, rs, 0, 0, fi, (wave >> 1) + 1);
    for (int s = wave >> 1; s < NSEG; s += 2) {
        const u32 task = (layer * 64 + s) * 2 + m;
        const u32 ro = (s * 2 + m) * TILE + nb * 2048, rq = (s * 2 + m) * TILE + nb * 512;
        v4u x[18], xr[2];
#pragma unroll
        for (int ks = 0; ks < 16; ++ks) x[ks] = ldt(rs, lane * 16 + (ks & 3) * 1024, OFF_OA + layer * XB + (s * 2 + m) * TILE + (ks >> 2) * 4096);
#pragma unroll
        for (int e = 0; e < 2; ++e) {
            const v2u a = __builtin_amdgcn_raw_buffer_load_b64(rs, lane * 8 + e * 1024, OFF_OA + layer * XB + (s * 2 + m) * TILE + 16384, 16);
            const v2u b = __builtin_amdgcn_raw_buffer_load_b64(rs, lane * 8 + e * 1024 + 512, OFF_OA + layer * XB + (s * 2 + m) * TILE + 16384, 16);
            x[16 + e] = v4u{a[0], a[1], b[0], b[1]};
        }
        if (layer != 0) {
#pragma unroll
            for (int P = 0; P < 2; ++P) xr[P] = ldt(rs, quad(lane) + P * 256, OFF_X + XB + rq);
        } else {
            xr[0] = xn[0]; xr[1] = xn[1];
            ldr(min(s + 2, NSEG - 1));
        }
        f32x4 acc[4];
        f32x2 xf[8];
#pragma unroll
        for (int ks = 0; ks < 18; ++ks)
#pragma unroll
            for (int t = 0; t < 4; ++t) {
                if (ks == 0) mmaw<true, false, true>(acc[t], w[ks][t], asf(x[ks]));
                else if (ks < 15) mmaw<true, false, false>(acc[t], w[ks][t], asf(x[ks]));
                else mmaw<false, false, false>(acc[t], w[ks][t], asf(x[ks]));
                if (ks >= 16) { u32 xw = xr[ks & 1][t]; PIN(xw); xf[(ks & 1) * 4 + t] = un2(xw); PIN(xf[(ks & 1) * 4 + t]); }
            }
        NOP8();
        f32x2 v[8];
#pragma unroll
        for (int i = 0; i < 8; ++i) { PIN(acc[i >> 1]); v[i] = f32x2{acc[i >> 1][2 * (i & 1)], acc[i >> 1][2 * (i & 1) + 1]} + xf[i]; }
        f32x2 su = v[0], sq = v[0] * v[0], a, b;
#pragma unroll
        for (int i = 1; i < 8; ++i) { su += v[i]; sq += v[i] * v[i]; }
        const u32 so = OFF_STAT + task * 2304 + l15 * 8;
        asm volatile("buffer_atomic_or_x2 %0, %1, %2, 0 offen" :: "v"(v2u{__float_as_uint(xsum(su[0] + su[1])), __float_as_uint(xsum(sq[0] + sq[1])) | 1u}),
                     "v"(g ? OOB : so + nb * 256), "s"(rs) : "memory");
        u64 st[3] = {0, 0, g ? 1ull << 32 : 0};
        u32 spins = 0;
        __builtin_amdgcn_s_sleep(SLP_S);
        for (;;) {
            CBAR();
            u64 z = 0;
            PIN(z);
#pragma unroll
            for (int i = 0; i < 3; ++i) if ((st[i] >> 32) == 0) st[i] = __hip_atomic_fetch_add((GAS u64*)(ws + (so + (g + 4 * i) * 256)), z, RLX_AGENT);
            if (__all((st[0] >> 32) != 0 && (st[1] >> 32) != 0 && (st[2] >> 32) != 0)) break;
            if (spin_fail(ws, spins)) break;
        }
        ln_par(xsum(__uint_as_float((u32)st[0]) + __uint_as_float((u32)st[1]) + __uint_as_float((u32)st[2])),
               xsum(__uint_as_float(st[0] >> 32) + __uint_as_float(st[1] >> 32) + __uint_as_float(st[2] >> 32)), a, b);
#pragma unroll
        for (int P = 0; P < 2; ++P) stt(rs, lane * 16 + P * 1024, XNB_W + layer * XB + ro, v4u{pack2(v[4 * P] * a + b), pack2(v[4 * P + 1] * a + b), pack2(v[4 * P + 2] * a + b), pack2(v[4 * P + 3] * a + b)});
#pragma unroll
        for (int P = 0; P < 2; ++P) stt(rs, quad(lane) + P * 256, OFF_X + 2 * layer * XB + rq, v4u{pack2(v[4 * P]), pack2(v[4 * P + 1]), pack2(v[4 * P + 2]), pack2(v[4 * P + 3])});
        st16(rs, lane < 8 && s + 2 < NSEG ? OFF_STAT + (task + 4) * 2304 + nb * 256 + lane * 16 : OOB, v4u{0u, 0u, 0u, 0u});
        watch<16, true, PS>(ws, rs, fo, s + 1, fi, s + 2 < NSEG ? s + 3 : s + 1);
    }
}

__device__ __forceinline__ void role_w(int layer, int kb, int nb, const u16* Wo, char* ws, rsrc_t rs, char* smem) {
    const int tid = threadIdx.x, lane = tid & 63, l15 = lane & 15, g = lane >> 4;
    const int wave = __builtin_amdgcn_readfirstlane(tid >> 6);
    float* sR = (float*)smem;
    bf16x8 w[3][8][2];
#pragma unroll
    for (int i = 0; i < 3; ++i)
#pragma unroll
        for (int ks = 0; ks < 8; ++ks) load_pair<true>(Wo, D_, kb * 1024 + wave * 256 + ks * 32 + 4 * g, nb * 96 + i * 32 + 2 * l15, w[i][ks][0], w[i][ks][1]);
#pragma unroll
    for (int i = 0; i < 48; ++i) PINA(w[i >> 4][(i >> 1) & 7][i & 1]);
    const u32 fo = wave ? OOB : words<8>(prog(FOB + nb % CB * 8 + layer * 4 + kb)), ob = OFF_OB + (layer * 2048 + kb * 8 + wave * 2) * 8192;
    const u32 fp = lane < CP ? prog(FPB + lane * 16 + layer * 4 + wave + (kb >> 1) * 8) + 16 + (kb * 6 + nb) * 4 : OOB;
    bool owe = false;
    go(rs, ask(rs));
    meet<8>(ws, rs, !wave, OOB, 0, fo, 1);
    for (int s = 0; s < NSEG; ++s) {
        v4u tb[16];
        auto ldb = [&](int i) { tb[i] = ldt(rs, lane * 16 + (i & 3) * 1024, ob + s * 262144 + (i >> 2) * 4096); SCHED(); };
#pragma unroll
        for (int i = 0; i < 10; ++i) ldb(i);
        if (owe) { VMW(10); tell(rs, fp, s); }
        f32x4 acc[3][2][2];
        u32 fn;
#pragma unroll
        for (int ks = 0; ks < 8; ++ks) {
#pragma unroll
            for (int m = 0; m < 2; ++m) {
#pragma unroll
                for (int i = 0; i < 3; ++i)
#pragma unroll
                    for (int t = 0; t < 2; ++t) {
                        if (ks == 0) mmaw<true, false, true>(acc[i][t][m], w[i][ks][t], asf(tb[2 * ks + m]));
                        else mmaw<true, false, false>(acc[i][t][m], w[i][ks][t], asf(tb[2 * ks + m]));
                    }
                SCHED();
                if (ks < 3) ldb(2 * ks + m + 10);
            }
        }
        NOP8();
        float* sB = sR + (s & 1) * 12800;
#pragma unroll
        for (int m = 0; m < 2; ++m)
#pragma unroll
            for (int i = 0; i < 3; ++i) {
                if (m && i == 0) { CBAR(); fn = rdw(rs, fo); CBAR(); }
                PIN(acc[i][0][m]); PIN(acc[i][1][m]);
                const f32x4 a0 = acc[i][0][m], a1 = acc[i][1][m];
                float* dst = sB + (wave * 32 + m * 16 + l15) * 100 + i * 32 + 8 * g;
                *(f32x4*)dst = f32x4{a0[0], a1[0], a0[1], a1[1]};
                *(f32x4*)(dst + 4) = f32x4{a0[2], a1[2], a0[3], a1[3]};
            }
        LBAR();
#pragma unroll
        for (int i = 0; i < 3; ++i) {
            const int c = wave * 192 + lane + 64 * i, tok = c / 24, c4 = (c % 24) * 4;
            f32x4 a = *(const f32x4*)(sB + tok * 100 + c4);
#pragma unroll
            for (int wv = 1; wv < 4; ++wv) a += *(const f32x4*)(sB + (wv * 32 + tok) * 100 + c4);
            st16(rs, OFF_PB + ((((layer * 64 + s) * 4 + kb) * SEG + tok) * D_ + nb * 96 + c4) * 4, __builtin_bit_cast(v4u, a));
        }
        CBAR();
        if (!wave) *(u32*)(smem + 102400) = reached<8>(fn, s + 2);
        LBAR();
        owe = told(smem + 102400);
        if (!owe) meet<8>(ws, rs, !wave, fp, s + 1, fo, min(s + 2, NSEG));
    }
}

__device__ __forceinline__ int exp_above(float x) { return (int)((__float_as_uint(x) >> 23) & 255u) - 126; }
__device__ __forceinline__ float sq2(u32 w, float c) { return __builtin_amdgcn_fdot2_f32_bf16(__builtin_bit_cast(bfv2, w), __builtin_bit_cast(bfv2, w), c, false); }
__device__ __forceinline__ float wave_max(float v) {
    v = vmax(v, dpp<0xB1>(v)); v = vmax(v, dpp<0x4E>(v)); v = vmax(v, dpp<0x141>(v)); v = vmax(v, dpp<0x140>(v));
    return xmax(v);
}
__device__ __forceinline__ void par_done(char* ws, rsrc_t rs) {
    const int tid = threadIdx.x;
    DRAIN();
    __syncthreads();
    if (tid < 64) {
        u32 n = 0;
        if (tid == 0) n = __hip_atomic_fetch_add((gu32*)(ws + OFF_CC), 1u, RLX_AGENT);
        if (__builtin_amdgcn_readfirstlane(n) == 63) {
            const v2u fm = ld8(rs, OFF_CH + (tid & 31) * 8);
            const u32 b0 = ldw(ws, OFF_CH + 256);
            u32 ph, spins = 0;
            while ((ph = ldw(ws, OFF_CH + 384)) == 0u && !spin_fail(ws, spins)) __builtin_amdgcn_s_sleep(2);
            const float c = sqrtf(__uint_as_float(fm[0])) * sqrtf(__uint_as_float(fm[1]));
            const float cs = wave_sum(c) * 0.5f;
            const int e35 = min(max(exp_above(43.75f * cs), -100), 100), ek = min(max(exp_above(30.f * cs), -100), 100);
            if (tid == 0) st4(rs, OFF_PAR, 0x80000000u | (ph & 1u) << 24 | b0 << 16 | (u32)(e35 + 128) << 8 | (u32)(ek + 128));
        }
    }
}
#define PAGE_STEP 0
#define CODE_BYTES 83968
__device__ __forceinline__ void touch(int j, int xc, const u16* wqkv, const u16* Wq, const u16* Wk, const u16* Wv, const u16* Wo, rsrc_t rs) {
    const u32 L = j * 256 + threadIdx.x, o = (L >> 3) * NB + xc * 512 + (L & 7) * 64;
    auto get = [](const u16* p) { asm volatile("" :: "v"(*(const u32*)p) : "memory"); };

    get(wqkv + min(L >> 1, 1727u) * D_ + xc * 72 + (L & 1) * 70);
    get(Wv + o);
    if ((j & ~3) == 8) go(rs, ask(rs));
    get(Wo + (xc * 4608 + L) * 64);
    get(Wq + o);
    get(Wk + o);
}
__device__ __forceinline__ void role_par(int h, int p, const u16* mem0, const u16* Wv, const u16* Wo, char* ws, rsrc_t rs, char* smem) {
    const int tid = threadIdx.x;
    float* sR = (float*)smem;
    float* sW = sR + 1024;
    const u32 a0 = ask(rs);
    if (p == 0) {
        v4u w[36];
#pragma unroll
        for (int i = 0; i < 36; ++i) w[i] = *(const v4u*)(Wv + (size_t)(i * 16 + (tid >> 4)) * NB + h * 128 + (tid & 15) * 8);
        float f[4] = {0.f, 0.f, 0.f, 0.f};
#pragma unroll
        for (int i = 0; i < 144; ++i) f[i & 3] = sq2(w[i >> 2][i & 3], f[i & 3]);
        const float t = wave_sum((f[0] + f[1]) + (f[2] + f[3]));
        if ((tid & 63) == 0) sW[tid >> 6] = t;
        __syncthreads();
        go(rs, a0);
        if (tid == 0) st4(rs, OFF_CH + h * 8, __float_as_uint((sW[0] + sW[1]) + (sW[2] + sW[3])));
    } else {
        u32 me[9];
        v4u w[43];
        const int q = h * 72 + min(tid, 71);
        int i0, i1;
        if (q < 2048) { i0 = ((q >> 3) & 7) * D_ + (q >> 8) * 64 + ((q >> 6) & 3) * 16 + (q & 3) * 4 + ((q >> 2) & 1) * 2; i1 = i0 + 1; }
        else { i0 = ((q >> 4) & 3) * 2 * D_ + 512 + ((q >> 6) & 3) * 16 + (q & 15); i1 = i0 + D_; }
        const u16 m0 = mem0[i0], m1 = mem0[i1];
#pragma unroll
        for (int i = 0; i < 9; ++i) me[i] = ((const u32*)mem0)[i * 256 + tid];
#pragma unroll
        for (int i = 0; i < 43; ++i) w[i] = *(const v4u*)(Wo + (size_t)(h * 128 + min(3 * i + tid / 72, 127)) * D_ + (tid % 72) * 8);
        float mm = 0.f;
#pragma unroll
        for (int i = 0; i < 9; ++i) mm = fmaxf(mm, fmaxf(fabsf(bflo(me[i])), fabsf(bfhi(me[i]))));
        mm = wave_max(mm);
        if ((tid & 63) == 0) sW[tid >> 6] = mm;
        LBAR();
        const int b0 = 28 - min(max(exp_above(fmaxf(fmaxf(sW[0], sW[1]), fmaxf(sW[2], sW[3]))), -30), 90);
        f32x4 c = {0.f, 0.f, 0.f, 0.f};
#pragma unroll
        for (int i = 0; i < 172; ++i) c[i & 3] = sq2(w[i >> 2][i & 3], c[i & 3]);
        *(f32x4*)(sR + tid * 4) = c;
        if (tid < 72) {
            const v2u z = {(u32)(int)rintf(ldexpf(bf2f(m0), b0 - 3)) << 6 | 32u, (u32)(int)rintf(ldexpf(bf2f(m1), b0 + 3))};
            const u32 a = (q < 2048 ? (q >> 9) * 16384 + ((q >> 8) & 1) * 8192 + ((q >> 3) & 7) * 1024 + ((q >> 6) & 1) * 512 + ((q >> 7) & 1) * 256
                                    : 65536 + ((q >> 3) & 1) * 512 + ((q >> 4) & 3) * 2048 + ((q >> 6) & 1) * 256 + ((q >> 7) & 1) * 8192) + (q & 7) * 8;
            for (int i = 0; i < 4; ++i) {
                st8(rs, OFF_ACC + (2 + (i >> 1)) * (ACC_SLOT / 2) + (i & 1) * 64 + a, z);
                st8(rs, q < 2048 ? OOB : OFF_ACC + (2 + (i >> 1)) * (ACC_SLOT / 2) + 1024 + (i & 1) * 64 + a, v2u{32u, 0u});
            }
        }
        LBAR();
        go(rs, a0);
        if (tid < 64) {
            float m = 0.f;
#pragma unroll
            for (int i = 0; i < 5; ++i) { const int d = min(tid + 64 * i, 287); m = fmaxf(m, sR[d] + sR[288 + d] + sR[576 + d]); }
            m = wave_max(m);
            if (tid < 2) st4(rs, tid ? OFF_CH + 256 : OFF_CH + h * 8 + 4, tid ? (u32)(b0 + 3 + 128) : __float_as_uint(m));
        }
    }
    par_done(ws, rs);
}
__device__ __forceinline__ void role_side(char* ws, rsrc_t rs, char* smem) {
    const int lane = threadIdx.x & 63, wave = threadIdx.x >> 6;
    go(rs, ask(rs));
    const u32 base = ((OFF_JUNK + 16383u) & ~16383u) + wave * 32768;
    st16(rs, lane < 32 ? base + (lane >> 3) * 8192 + (lane & 7) * 16 : OOB, v4u{0u, 0u, 0u, 0u});
    int vote = 0;
    DRAIN();
#pragma unroll 1
    for (int r = 0; r < 2; ++r) {
        u32 t[2];
        __builtin_amdgcn_raw_ptr_buffer_atomic_add_i32(1, rs, lane < 2 ? base + r * 16384 + lane * 8192 : OOB, 0, 0);
        DRAIN();
#pragma unroll
        for (int e = 0; e < 2; ++e) {
            const u32 a = base + r * 16384 + e * 8192;
            v4u q;
            u64 t0, t1;
            asm volatile("s_memtime %0\n\ts_waitcnt lgkmcnt(0)\n\tbuffer_load_dwordx4 %2, %3, %4, 0 offen sc1\n\ts_waitcnt vmcnt(0)\n\ts_memtime %1\n\ts_waitcnt lgkmcnt(0)"
                         : "=&s"(t0), "=&s"(t1), "=&v"(q) : "v"(lane < 4 ? a + lane * 16 : OOB), "s"(rs) : "memory");
            t[e] = (u32)(t1 - t0);
        }
        const u32 near = t[1] < t[0], b21 = ((u32)(size_t)ws + base + r * 16384) >> 21 & 1;
        vote += (near ^ (BID >> 2 & 1) ^ b21) ? 1 : -1;
    }
    if (lane == 0) *(int*)(smem + wave * 4) = vote;
    __syncthreads();
    if (threadIdx.x == 0) { const int* v = (const int*)smem; st4(rs, OFF_CH + 384, v[0] + v[1] + v[2] + v[3] > 0 ? 3u : 2u); }
}
using v8u = __attribute__((ext_vector_type(8))) unsigned;
template <bool FIRST> __device__ __forceinline__ void mm16(f32x4& acc, v2u a, v2u b) {
    if constexpr (FIRST) asm volatile("v_mfma_f32_16x16x16_bf16 %0, %1, %2, 0" : "=&v"(acc) : "v"(a), "v"(b));
    else asm volatile("v_mfma_f32_16x16x16_bf16 %0, %1, %2, %0" : "+v"(acc) : "v"(a), "v"(b));
}
__device__ __forceinline__ v4u half(const v8u& v, int i) { return i ? v4u{v[4], v[5], v[6], v[7]} : v4u{v[0], v[1], v[2], v[3]}; }
__device__ __forceinline__ void role_att(int layer, int h, char* ws, rsrc_t rs) {
    const int lane = threadIdx.x & 63, l15 = lane & 15, g = lane >> 4;
    const u32 fl = words<6>(OFF_FKVM + ((layer * 32 + h) * 68 + 2) * 32);
    const u32 kvo = ((g >> 1) * 16 + ((l15 >> 3) | ((l15 >> 1) & 2)) * 4 + (l15 & 1) + (g & 1) * 2) * 16, vmo = (lane & 57) * 8 + (lane & 2) * 16 + (lane & 4) * 4;
    constexpr float SC = 0.08838834764831845f * 1.4426950408889634f;
    const u32 fo = lane < CB ? prog(FOB + lane * 8 + layer * 4 + (h >> 3)) + (h & 7) * 4 : OOB;
    v4u k[2][4], v[2][4];
    auto issue = [&](int s, v8u (&q)[2][2]) {
        const u32 blk = OFF_QKV + ((layer * 66 + s) * 32 + h) * 24576;
        v4u t[2][4];
#pragma unroll
        for (int w = 0; w < 4; ++w)
#pragma unroll
            for (int p = 0; p < 2; ++p) { t[p][w] = ldt(rs, lane * 16, blk + w * 6144 + p * 3072); k[p][w] = ldt(rs, lane * 16 + 1024, blk + w * 6144 + p * 3072); }
#pragma unroll
        for (int p = 0; p < 2; ++p)
#pragma unroll
            for (int i = 0; i < 2; ++i) q[p][i] = __builtin_shufflevector(t[p][2 * i], t[p][2 * i + 1], 0, 1, 2, 3, 4, 5, 6, 7);
    };
    auto issuev = [&](int s) {
        const u32 blk = OFF_QKV + ((layer * 66 + s) * 32 + h) * 24576;
#pragma unroll
        for (int p = 0; p < 2; ++p)
#pragma unroll
            for (int w = 0; w < 4; ++w) v[p][w] = ldt(rs, lane * 16 + 2048, blk + w * 6144 + p * 3072);
    };
    auto body = [&](int s, v8u (&q)[2][2], v8u (&qn)[2][2]) {
        const u32 mb = OFF_KVM + ((layer * 64 + s) * 32 + h) * 4096;
        f32x4 sc[3];
#pragma unroll
        for (int w = 0; w < 4; ++w)
#pragma unroll
            for (int u = 0; u < 3; ++u) {
                if (w == 0) mmaw<false, false, true>(sc[u], asf(k[u >> 1][w]), asf(half(q[u > 0][w >> 1], w & 1)));
                else mmaw<false, false, false>(sc[u], asf(k[u >> 1][w]), asf(half(q[u > 0][w >> 1], w & 1)));
            }
        NOP8();
        float mx[2] = {-3.0e38f, -3.0e38f}, sum[2] = {0.f, 0.f};
#pragma unroll
        for (int u = 0; u < 3; ++u) {
            PIN(sc[u]);
#pragma unroll
            for (int r = 0; r < 4; ++r) {
                if (u != 1) sc[u][r] = 4 * g + r <= l15 ? sc[u][r] : -3.0e38f;
                mx[u > 0] = fmaxf(mx[u > 0], sc[u][r]);
            }
        }
        SCHED();
        u32 f = rdw(rs, fl + s * 32);
        SCHED();
        v2u pt[3];
#pragma unroll
        for (int m = 0; m < 2; ++m) mx[m] = xmax(mx[m]) * SC;
#pragma unroll
        for (int u = 0; u < 3; ++u) {
#pragma unroll
            for (int r = 0; r < 4; ++r) { sc[u][r] = __builtin_amdgcn_exp2f(sc[u][r] * SC - mx[u > 0]); sum[u > 0] += sc[u][r]; }
            pt[u] = v2u{pack2(sc[u][0], sc[u][1]), pack2(sc[u][2], sc[u][3])};
            PIN(pt[u]);
        }
        VMW(9);
        tell(rs, fo, s);
        NOP2();
        f32x4 o[2][4][2], sm[2];
#pragma unroll
        for (int u = 0; u < 3; ++u)
#pragma unroll
            for (int w = 0; w < 4; ++w)
#pragma unroll
                for (int t = 0; t < 2; ++t) {
                    const v2u a = {v[u >> 1][w][2 * t], v[u >> 1][w][2 * t + 1]};
                    if (u < 2) mm16<true>(o[u][w][t], a, pt[u]); else mm16<false>(o[1][w][t], a, pt[u]);
                }
#pragma unroll
        for (int m = 0; m < 2; ++m) { sum[m] = xsum(sum[m]); PIN(sum[m]); PIN(mx[m]); }
        NOP8();
        SCHED();
        PIN(f);
        f = reached<6>(f, 1u) ? __builtin_amdgcn_readfirstlane(f) : watch<6, false, AS>(ws, rs, 0, 0, fl + s * 32, 1u);
        CBAR();
        v4u km[4];
        v2u vm[4];
#pragma unroll
        for (int w = 0; w < 4; ++w) km[w] = ldt(rs, kvo + w * 512, mb);
#pragma unroll
        for (int w = 0; w < 4; ++w) vm[w] = __builtin_amdgcn_raw_buffer_load_b64(rs, vmo, mb + 2048 + w * 512, 16);
        SCHED();
        issue(s + 1 < NSEG ? s + 1 : s, qn);
        SCHED();
        const float si = __uint_as_float((255u - ((f >> 8) & 255u)) << 23);
#pragma unroll
        for (int w = 0; w < 4; ++w)
#pragma unroll
            for (int m = 0; m < 2; ++m) {
                if (w == 0) mmaw<false, false, true>(sm[m], asf(km[w]), asf(half(q[m][w >> 1], w & 1)));
                else mmaw<false, false, false>(sm[m], asf(km[w]), asf(half(q[m][w >> 1], w & 1)));
            }
        NOP8();
        v2u pm[2][2];
        float a[2];
#pragma unroll
        for (int m = 0; m < 2; ++m) {
            PIN(sm[m]);
            float m0 = sm[m][0] * (si * SC), m1 = sm[m][1] * (si * SC);
            const float mm = fmaxf(xmax(fmaxf(m0, m1)), mx[m]);
            m0 = __builtin_amdgcn_exp2f(m0 - mm); m1 = __builtin_amdgcn_exp2f(m1 - mm);
            a[m] = __builtin_amdgcn_exp2f(mx[m] - mm);
            const float inv = __builtin_amdgcn_rcpf(xsum(m0 + m1) + a[m] * sum[m]);
            a[m] *= inv;
            const u32 pp = pack2(m0 * inv * si, m1 * inv * si);
            pm[m][0] = v2u{pp, 0u}; pm[m][1] = v2u{0u, pp};
            PIN(pm[m][0]); PIN(pm[m][1]);
        }
        auto scale = [&](int w) {
#pragma unroll
            for (int m = 0; m < 2; ++m)
#pragma unroll
                for (int t = 0; t < 2; ++t) { o[m][w][t] *= a[m]; PIN(o[m][w][t]); }
        };
        auto put = [&](int w) {
#pragma unroll
            for (int m = 0; m < 2; ++m) { PIN(o[m][w][0]); PIN(o[m][w][1]); stt(rs, lane * 16 + m * 1024, OFF_OB + (((layer * 64 + s) * 32 + h) * 4 + w) * 2048, pack_pair(o[m][w][0], o[m][w][1])); }
        };
        scale(0);
#pragma unroll
        for (int w = 0; w < 4; ++w) {
            if (w < 3) scale(w + 1); else NOP2();
#pragma unroll
            for (int m = 0; m < 2; ++m)
#pragma unroll
                for (int t = 0; t < 2; ++t) mm16<false>(o[m][w][t], vm[w], pm[m][t]);
            if (w) put(w - 1);
        }
        NOP8();
        put(3);
        issuev(s + 1 < NSEG ? s + 1 : s);
    };
    v8u A[2][2], B[2][2];
    go(rs, ask(rs));
    wait_prog<2>(ws, rs, words<2>(OFF_Z), 1);
    watch<6, false, PS>(ws, rs, 0, 0, fl - 32, 1u);
    issue(0, A);
    issuev(0);
    for (int s = 0; s < NSEG; s += 2) { body(s, A, B); body(s + 1, B, A); }
    VMW(8);
    tell(rs, fo, NSEG);
}

__global__ __launch_bounds__(256, 1) void mega_kernel(const u16* __restrict__ x0, const u16* __restrict__ wqkv, const u16* __restrict__ woA,
                                                     const u16* __restrict__ mem0, const u16* __restrict__ Wq, const u16* __restrict__ Wk,
                                                     const u16* __restrict__ Wv, const u16* __restrict__ Wo, u16* out, char* ws) {
    __shared__ __attribute__((aligned(16))) char smem[163840];
    const char* code;
    asm volatile("s_getpc_b64 %0" : "=s"(code));
    u32 ins = 0;
    {
        const u32 i = BID >> 3, L = ((i & 1) | (i >> 2) << 1) * 256 + threadIdx.x;
        if ((i & 2) && L * 128 < CODE_BYTES) ins = *(const GAS u32*)(code + L * 128);
    }
    v2u x00[6] = {};
    if (BID >> 3 == 28) {
        const u16* xp = x0 + (((BID & 7) * 4 + (threadIdx.x >> 6)) * D_ + min((int)(threadIdx.x & 63), 47) * 4);
        asm volatile("" : "+v"(xp));
#pragma unroll
        for (int i = 0; i < 6; ++i) x00[i] = *(const GAS v2u*)(xp + (i / 3) * SEG * D_ + (i % 3) * 192);
    }
    const rsrc_t rs = __builtin_amdgcn_make_buffer_rsrc(ws, 0, WS_BYTES, 0x00020000);
    const int b = BID;
    const int xc = b & 7, ix = b >> 3;
    const int wave = __builtin_amdgcn_readfirstlane(threadIdx.x >> 6);
    put(rs, my_word(), 0u);
    if ((ix & ~3) == 16) {
#pragma unroll
        for (int i = 0; i < 5; ++i) {
            const int u = (b - 128) * 5 + i;
            if (u < 68) clear(rs, OFF_FKVM + u * 4096, 1); else clear<2>(rs, b < 156 ? OFF_STAT + (u - 68) * 8192 : OOB, 1);
        }
        clear<2>(rs, OFF_ACC + 2 * ACC_SLOT + (b - 128) * 40960, 5);
    }
    const u32 late = 0x4b3fccccu;
    if (late >> ix & 1) { touch(__popc(late << (31 - ix)) - 1, xc, wqkv, Wq, Wk, Wv, Wo, rs); asm volatile("" :: "v"(ins)); }
    if (ix == 24 || ix == 25 || ix == 30) nap(NAP_A);
    if (b >= 250 && b < 254) go(rs, ask(rs));
#define ROLE(m)
    if (ix < 16) {
        if (ix & 2) { role_par(4 * xc + (ix >> 2), ix & 1, mem0, Wv, Wo, ws, rs, smem); nap(NAP_C); }
        role_chain(ws, Wq, Wk, Wv, Wo, (u32)(((ix >> 1) & 1) << 8 | (4 * xc + (ix >> 2)) << 3 | (ix & 1) << 2 | wave), OFF_ACK + (b * 4 + wave) * 4);
    }
    else if (ix < 22) {
        if (!(xc & 1)) nap(NAP_W0);
        if (xc & 1) {
            nap(NAP_W);
            ROLE(1) if (ix >= 20) for (int i = 0; i < 2; ++i) role_t0(64 + ((ix - 20) * 8 + xc - i) * 4 + wave, x0, ws, rs);
        }
        ROLE(4) role_w(xc & 1, xc >> 1, ix - 16, Wo, ws, rs, smem);
    }
    else if (ix < 26) { ROLE(ix < 24 ? 1 : 16) role_a1((ix - 22) >> 1, 2 * xc + (ix & 1), wqkv, ws, rs, smem);
        ROLE(8) if (ix == 22) { DRAIN(); role_r<1, true, SL, NSEG>(xc, x0, out, ws, rs, smem, x00); }
    }
    else if (ix < 28) {
        if (ix == 27) nap(NAP_A);
        ROLE(1) if (b != 208) role_t0(((ix - 26) * 8 + xc) * 4 + wave, x0, ws, rs);
        ROLE(2) role_att(ix - 26, 4 * xc + wave, ws, rs);
    }
    else if (ix == 28) { ROLE(8) role_r<1, false>(xc, x0, out, ws, rs, smem, x00); }
    else if (b < 250) { ROLE(b & 8 && b != 249 ? 64 : 32) role_a2(ix < 31 ? ix - 29 : xc, ix < 31 ? xc : 8, x0, woA, ws, rs, smem); }
    else if (b < 254) { ROLE(8) role_r<2, true, 0, SL>(xc - 2, x0, out, ws, rs, smem, x00); }
    else if (b == 254) role_lead(ws, rs);
    else role_side(ws, rs, smem);
}

extern "C" void kernel_launch(void* const* d_in, const int* in_sizes, int n_in, void* d_out, int out_size, void* d_ws,
                              size_t ws_size, hipStream_t stream) {
    mega_kernel<<<256, 256, 0, stream>>>((const u16*)d_in[0], (const u16*)d_in[1], (const u16*)d_in[2], (const u16*)d_in[4],
                                         (const u16*)d_in[5], (const u16*)d_in[6], (const u16*)d_in[7], (const u16*)d_in[8],
                                         (u16*)d_out, (char*)d_ws);
}
```
